```python
import torch

def _softmax(x, axis=-1):
    return torch.softmax(x, dim=axis)
def _gelu(x):
    return torch.nn.functional.gelu(x, approximate="tanh")
def _silu(x):
    return torch.nn.functional.silu(x)
def _sigmoid(x):
    return torch.sigmoid(x)
def _layernorm(x, axis=-1, eps=1e-5):
    m = x.mean(dim=axis, keepdim=True)
    v = x.var(dim=axis, keepdim=True, unbiased=False)
    return (x - m) / torch.sqrt(v + eps)
def _scatter_add(tgt, idx, src, axis=0):
    return tgt.clone().index_add_(axis, idx, src)
def _segment_sum(src, seg, n):
    out = torch.zeros((n,) + src.shape[1:], dtype=src.dtype)
    return out.index_add_(0, seg, src)
def _pad(x, pw):
    flat = tuple(v for pair in reversed(pw) for v in pair)
    return torch.nn.functional.pad(x, flat)

kernel_name = 'sequential_preLN__compressed_kv_cla__dense_activation_relu__prompt'

def setup_inputs():

    _w = (lambda a: (lambda t: t.to(torch.bfloat16) if t.is_floating_point() else t)(torch.as_tensor(a)))
    def _A_setup():
        B, H, Sq, Sk, D, M = 1, 32, 2048, 2048, 128, 512
        Dc = 4 * D
        return {
            "x": _w((lambda *s: torch.randn(*s, dtype=torch.bfloat16) * 0.05)(B, Sq, M)),
            "c_kv": _w((lambda *s: torch.randn(*s, dtype=torch.bfloat16) * 0.4)(B, Sk, Dc)),
            "w_q": _w((lambda *s: torch.randn(*s, dtype=torch.bfloat16) * 3.2)(M, H, D) * (M ** -0.5)),
            "w_uk": _w((lambda *s: torch.randn(*s, dtype=torch.bfloat16) * 3.2)(Dc, H, D) * (Dc ** -0.5)),
            "w_uv": _w((lambda *s: torch.randn(*s, dtype=torch.bfloat16) * 0.4)(Dc, H, D) * (Dc ** -0.5)),
            "w_o": _w((lambda *s: torch.randn(*s, dtype=torch.bfloat16) * 0.4)(H, D, M) * ((H * D) ** -0.5)),
        }
    _A_in = _A_setup()

    def _B_setup():
        B, S, D, Dff = 1, 2048, 512, 1792
        return {
            "x": _w((lambda *s: torch.randn(*s, dtype=torch.bfloat16) * 0.05)(B, S, D)),
            "w1": _w((lambda *s: torch.randn(*s, dtype=torch.bfloat16) * 0.05)(D, Dff)),
            "w2": _w((lambda *s: torch.randn(*s, dtype=torch.bfloat16) * 0.05)(Dff, D)),
        }
    _B_in = _B_setup()

    return {
        "A_x": _A_in["x"],
        "A_c_kv": _A_in["c_kv"],
        "A_w_q": _A_in["w_q"],
        "A_w_uk": _A_in["w_uk"],
        "A_w_uv": _A_in["w_uv"],
        "A_w_o": _A_in["w_o"],
        "B_x": _B_in["x"],
        "B_w1": _B_in["w1"],
        "B_w2": _B_in["w2"],
    }

def reference(A_x, A_c_kv, A_w_q, A_w_uk, A_w_uv, A_w_o, B_x, B_w1, B_w2):
    def _A_ref(x, c_kv, w_q, w_uk, w_uv, w_o):
        B, Sq, M = x.shape[0], x.shape[1], x.shape[2]
        Sk = c_kv.shape[1]
        H, D = w_q.shape[1], w_q.shape[2]
        q = torch.einsum("bqm,mhd->bhqd", x, w_q)
        k = torch.einsum("bkc,chd->bhkd", c_kv, w_uk)
        v = torch.einsum("bkc,chd->bhkd", c_kv, w_uv)
        scores = torch.einsum("bhqd,bhkd->bhqk", q, k) * (D ** -0.5)
        off = Sk - Sq
        qi = torch.arange(Sq)[:, None]
        kj = torch.arange(Sk)[None, :]
        cmask = kj <= (qi + off)
        scores = torch.where(cmask[None, None], scores, -1e9)
        p = _softmax(scores, axis=-1)
        ctx = torch.einsum("bhqk,bhkd->bhqd", p, v)
        return torch.einsum("bhqd,hdm->bqm", ctx, w_o)

    def _B_ref(x, w1, w2):
        h = torch.einsum("bsd,df->bsf", x, w1)
        zero = torch.zeros_like(h)
        h = torch.maximum(h, zero)
        return torch.einsum("bsf,fd->bsd", h, w2)

    x = A_x
    h = x + _A_ref(_layernorm(x, axis=-1), A_c_kv, A_w_q, A_w_uk, A_w_uv, A_w_o)
    h2 = h + _B_ref(_layernorm(h, axis=-1), B_w1, B_w2)
    return h2
```

```cpp
#include <hip/hip_runtime.h>
#include <hip/hip_bf16.h>
#include <stdint.h>

typedef unsigned short u16;
__device__ __forceinline__ float bf2f(u16 v){ return __uint_as_float(((unsigned)v)<<16); }
__device__ __forceinline__ u16 f2bf(float f){ unsigned u=__float_as_uint(f); return (u16)((u + 0x7FFF + ((u>>16)&1))>>16); }

__global__ void ln_k(const u16* x, u16* y){
  int row=blockIdx.x; int t=threadIdx.x;
  float v[8]; float s=0;
  for(int i=0;i<8;i++){ v[i]=bf2f(x[row*512+t*8+i]); s+=v[i]; }
  for(int o=32;o;o>>=1) s+=__shfl_xor(s,o);
  float m=s/512.f; float q=0;
  for(int i=0;i<8;i++){ float d=v[i]-m; q+=d*d; }
  for(int o=32;o;o>>=1) q+=__shfl_xor(q,o);
  float r=rsqrtf(q/512.f+1e-5f);
  for(int i=0;i<8;i++) y[row*512+t*8+i]=f2bf((v[i]-m)*r);
}
__global__ void gemm_k(const u16* A,const u16* B,u16* C,const u16* R,int M,int N,int K,int mode){
  __shared__ float As[16][65]; __shared__ float Bs[16][65];
  int tx=threadIdx.x&15, ty=threadIdx.x>>4; int bm=blockIdx.y*64, bn=blockIdx.x*64;
  float acc[4][4]={};
  for(int k0=0;k0<K;k0+=16){
    for(int i=threadIdx.x;i<1024;i+=256){ int r=i>>4,c=i&15; As[c][r]=bf2f(A[(size_t)(bm+r)*K+k0+c]); int kk=i>>6,n=i&63; Bs[kk][n]=bf2f(B[(size_t)(k0+kk)*N+bn+n]); }
    __syncthreads();
    for(int k=0;k<16;k++){ float a[4],b[4]; for(int i=0;i<4;i++){a[i]=As[k][ty*4+i]; b[i]=Bs[k][tx*4+i];}
      for(int i=0;i<4;i++)for(int j=0;j<4;j++)acc[i][j]+=a[i]*b[j]; }
    __syncthreads();
  }
  for(int i=0;i<4;i++)for(int j=0;j<4;j++){ size_t o=(size_t)(bm+ty*4+i)*N+bn+tx*4+j; float v=acc[i][j];
    if(mode==1) v=fmaxf(v,0.f); if(mode==2) v=bf2f(f2bf(v))+bf2f(R[o]); C[o]=f2bf(v);}
}
__global__ void attn_k(const u16* Q,const u16* Kk,const u16* V,u16* O){
  int row=blockIdx.x, h=blockIdx.y; int t=threadIdx.x;
  __shared__ float q[128]; __shared__ float p[2048];
  q[t]=bf2f(Q[(size_t)row*4096+h*128+t]); q[t+64]=bf2f(Q[(size_t)row*4096+h*128+t+64]);
  __syncthreads();
  float m=-1e30f;
  for(int j=t;j<=row;j+=64){ float s=0; const u16* kp=Kk+(size_t)j*4096+h*128; for(int d=0;d<128;d++) s+=q[d]*bf2f(kp[d]); s*=0.08838834764831845f; p[j]=s; m=fmaxf(m,s);}
  for(int o=32;o;o>>=1) m=fmaxf(m,__shfl_xor(m,o));
  float l=0; for(int j=t;j<=row;j+=64){ float e=__expf(p[j]-m); p[j]=e; l+=e; }
  for(int o=32;o;o>>=1) l+=__shfl_xor(l,o);
  __syncthreads();
  float a0=0,a1=0; for(int j=0;j<=row;j++){ const u16* vp=V+(size_t)j*4096+h*128; a0+=p[j]*bf2f(vp[t]); a1+=p[j]*bf2f(vp[t+64]); }
  O[(size_t)row*4096+h*128+t]=f2bf(a0/l); O[(size_t)row*4096+h*128+t+64]=f2bf(a1/l);
}

extern "C" void kernel_launch(void* const* d_in, const int* in_sizes, int n_in,
                              void* d_out, int out_size, void* d_ws, size_t ws_size,
                              hipStream_t stream) {
  const u16* x=(const u16*)d_in[0]; const u16* ckv=(const u16*)d_in[1]; const u16* wq=(const u16*)d_in[2];
  const u16* wuk=(const u16*)d_in[3]; const u16* wuv=(const u16*)d_in[4]; const u16* wo=(const u16*)d_in[5];
  const u16* w1=(const u16*)d_in[7]; const u16* w2=(const u16*)d_in[8];
  char* ws=(char*)d_ws; size_t MB=1<<20;
  u16* xn=(u16*)(ws); u16* Q=(u16*)(ws+2*MB); u16* K=(u16*)(ws+18*MB); u16* V=(u16*)(ws+34*MB); u16* ctx=(u16*)(ws+50*MB);
  u16* h=(u16*)(ws+66*MB); u16* hn=(u16*)(ws+68*MB); u16* h1=(u16*)(ws+70*MB);
  ln_k<<<2048,64,0,stream>>>(x,xn);
  gemm_k<<<dim3(64,32),256,0,stream>>>(xn,wq,Q,nullptr,2048,4096,512,0);
  gemm_k<<<dim3(64,32),256,0,stream>>>(ckv,wuk,K,nullptr,2048,4096,512,0);
  gemm_k<<<dim3(64,32),256,0,stream>>>(ckv,wuv,V,nullptr,2048,4096,512,0);
  attn_k<<<dim3(2048,32),64,0,stream>>>(Q,K,V,ctx);
  gemm_k<<<dim3(8,32),256,0,stream>>>(ctx,wo,h,x,2048,512,4096,2);
  ln_k<<<2048,64,0,stream>>>(h,hn);
  gemm_k<<<dim3(28,32),256,0,stream>>>(hn,w1,h1,nullptr,2048,1792,512,1);
  gemm_k<<<dim3(8,32),256,0,stream>>>(h1,w2,(u16*)d_out,h,2048,512,1792,2);
}
```

```cpp
#include <hip/hip_runtime.h>
#include <stdint.h>
extern "C" __global__ __launch_bounds__(256,1) void fused_k(const void* x, const void* ckv, const void* wq, const void* wuk, const void* wuv, const void* wo, const void* w1, const void* w2, void* out, void* ws){
  __shared__ __attribute__((aligned(16))) char lds[163840];
  unsigned ldsb=(unsigned)(size_t)lds;
  asm volatile(
R"ASM(s_mov_b32 s6, %1
s_mov_b32 s7, %2
s_load_dwordx16 s[64:79], %0, 0
s_load_dwordx4 s[80:83], %0, 64
v_mbcnt_lo_u32_b32 v253, -1, 0
v_mbcnt_hi_u32_b32 v253, -1, v253
s_getpc_b64 s[8:9]
KPC:
s_and_b32 s9, s9, 0xffff
s_mov_b32 s10, KEND-KPC
s_mov_b32 s11, 0x20000
v_lshlrev_b32 v0, 7, v253
s_lshr_b32 s12, s6, 3
s_and_b32 s12, s12, 3
s_lshl_b32 s12, s12, 2
s_add_u32 s12, s12, s7
s_lshl_b32 s12, s12, 13
buffer_load_dword a252, v0, s[8:11], s12 offen
v_lshlrev_b32 v252, 4, v253
v_lshrrev_b32 v0, 3, v253
v_and_b32 v1, 7, v253
v_xor_b32 v1, v1, v0
v_lshlrev_b32 v1, 4, v1
v_lshl_add_u32 v200, v0, 10, v1
v_add_u32 v201, 7168, v200
v_add_u32 v202, 14336, v200
v_add_u32 v203, 21504, v200
v_lshrrev_b32 v0, 3, v253
v_lshlrev_b32 v0, 5, v0
v_and_b32 v1, 1, v253
v_lshl_add_u32 v0, v1, 4, v0
v_bfe_u32 v1, v253, 1, 2
v_lshl_add_u32 v204, v1, 13, v0
v_xor_b32 v206, 32, v204
v_mov_b32 v208, v204
v_add_u32 v209, 31744, v204
v_add_u32 v210, 63488, v206
v_add_u32 v211, 95232, v206
v_add_u32 v205, 30720, v204
v_add_u32 v207, 30720, v206
v_and_b32 v0, 15, v253
v_lshrrev_b32 v1, 4, v253
v_and_b32 v2, 7, v253
v_xor_b32 v2, v2, v1
v_lshlrev_b32 v2, 4, v2
v_lshl_add_u32 v0, v0, 7, v2
s_and_b32 s44, s7, 1
s_lshl_b32 s44, s44, 13
s_add_u32 s44, s44, 16384
v_add_u32 v192, s44, v0
v_xor_b32 v193, 64, v192
s_lshl_b32 s44, s7, 13
s_add_u32 s44, s44, 65536
v_add_u32 v196, s44, v0
v_xor_b32 v197, 64, v196
v_lshrrev_b32 v0, 4, v253
v_bfe_u32 v2, v253, 2, 2
v_lshlrev_b32 v1, 5, v2
v_and_b32 v2, 3, v253
v_lshl_add_u32 v1, v2, 3, v1
v_and_b32 v2, 1, v0
v_lshlrev_b32 v2, 7, v2
v_lshl_add_u32 v3, v0, 12, v1
s_lshr_b32 s44, s7, 1
s_lshl_b32 s44, s44, 10
s_add_u32 s44, s44, 32768
v_add_u32 v3, s44, v3
v_add_u32 v194, v3, v2
v_sub_u32 v195, v3, v2
v_lshl_add_u32 v3, v0, 11, v1
v_add_u32 v3, 98304, v3
v_add_u32 v198, v3, v2
v_sub_u32 v199, v3, v2
s_and_b32 s26, s6, 7
s_lshl_b32 s26, s26, 2
s_lshr_b32 s44, s6, 3
s_and_b32 s45, s44, 3
s_add_u32 s26, s26, s45
s_lshr_b32 s27, s44, 2
s_lshl_b32 s28, s27, 17
s_lshl_b32 s44, s7, 15
s_add_u32 s28, s28, s44
s_lshl_b32 s29, s26, 8
s_lshl_b32 s44, s7, 17
s_add_u32 s29, s29, s44
s_add_u32 s30, s29, 65536
s_mov_b32 s33, s29
s_sub_u32 s44, 15, s27
s_lshl_b32 s31, s44, 17
s_lshl_b32 s32, s27, 17
s_lshl_b32 s44, s7, 14
s_add_u32 s31, s31, s44
s_add_u32 s32, s32, s44
s_lshl_b32 s44, s7, 1
s_sub_u32 s44, 6, s44
s_lshl_b32 s44, s44, 14
v_add_u32 v212, s44, v202
v_add_u32 v213, s44, v203
s_and_b32 s24, s26, 3
s_lshl_b32 s24, s24, 1
s_and_b32 s24, s26, 3
s_add_u32 s24, s24, s27
s_and_b32 s24, s24, 3
s_lshl_b32 s24, s24, 1
s_and_b32 s24, s24, 7
s_cmp_eq_u32 s24, 7
s_cselect_b32 s98, 0, s24
s_add_u32 s98, s98, 1
s_mov_b32 s24, 0
s_mov_b32 s25, s28
s_mov_b32 s58, s29
s_mov_b32 s60, s31
s_mov_b32 s96, s32
s_mov_b32 s97, s33
s_lshl_b32 s44, s24, 7
s_add_u32 s28, s28, s44
s_lshl_b32 s44, s24, 19
s_add_u32 s29, s29, s44
s_add_u32 s30, s29, 65536
s_lshl_b32 s34, s7, 12
s_lshl_b32 s35, s7, 13
s_add_u32 s35, s35, 16384
s_lshl_b32 s56, s7, 13
s_lshl_b32 s57, s7, 12
s_add_u32 s57, s57, 32768
s_lshl_b32 s62, s26, 5
s_lshl_b32 s44, s27, 1
s_add_u32 s62, s62, s44
s_and_b32 s44, s7, 1
s_add_u32 s62, s62, s44
s_lshl_b32 s62, s62, 15
s_lshr_b32 s44, s7, 1
s_lshl_b32 s44, s44, 14
s_add_u32 s62, s62, s44
s_waitcnt lgkmcnt(0)
s_mov_b32 s8, s66
s_and_b32 s9, s67, 0xffff
s_mov_b32 s10, 0x200000
s_mov_b32 s11, 0x20000
s_mov_b32 s12, s70
s_and_b32 s13, s71, 0xffff
s_mov_b32 s14, 0x400000
s_mov_b32 s15, 0x20000
s_mov_b32 s16, s72
s_and_b32 s17, s73, 0xffff
s_mov_b32 s18, 0x400000
s_mov_b32 s19, 0x20000
s_sub_u32 s16, s16, 1024
s_subb_u32 s17, s17, 0
s_add_u32 s18, s18, 1024
s_mov_b32 s48, s64
s_and_b32 s49, s65, 0xffff
s_mov_b32 s50, 0x200000
s_mov_b32 s51, 0x20000
s_mov_b32 s52, s82
s_and_b32 s53, s83, 0xffff
s_mov_b32 s54, 0x200000
s_mov_b32 s55, 0x20000
s_add_u32 s88, s82, 0x6000000
s_addc_u32 s89, s83, 0
s_and_b32 s89, s89, 0xffff
s_mov_b32 s90, 0x8000
s_mov_b32 s91, 0x20000
v_mov_b32 v12, 0
buffer_load_dwordx2 v[14:15], v12, s[88:91], 0 offen sc1
s_lshl_b32 s44, s6, 3
s_lshl_b32 s45, s7, 1
s_add_u32 s44, s44, s45
s_lshl_b32 s44, s44, 10
buffer_load_dwordx4 v[0:3], v252, s[48:51], s44 offen
buffer_load_dwordx4 v[4:7], v252, s[48:51], s44 offen offset:1024
s_mov_b32 s59, s44
s_add_u32 m0, s34, 16384
s_nop 0
buffer_load_dwordx4 v200, s[8:11], s28 offen offset:0 lds
buffer_load_dwordx4 v201, s[8:11], s28 offen offset:1024 lds
buffer_load_dwordx4 v202, s[8:11], s28 offen offset:2048 lds
buffer_load_dwordx4 v203, s[8:11], s28 offen offset:3072 lds
s_add_u32 m0, s35, 16384
s_nop 0
buffer_load_dwordx4 v204, s[12:15], s29 offen offset:0 lds
buffer_load_dwordx4 v204, s[16:19], s29 offen offset:1024 lds
buffer_load_dwordx4 v205, s[12:15], s29 offen offset:2048 lds
buffer_load_dwordx4 v205, s[16:19], s29 offen offset:3072 lds
s_add_u32 m0, s35, 20480
s_nop 0
buffer_load_dwordx4 v206, s[12:15], s30 offen offset:0 lds
buffer_load_dwordx4 v206, s[16:19], s30 offen offset:1024 lds
buffer_load_dwordx4 v207, s[12:15], s30 offen offset:2048 lds
buffer_load_dwordx4 v207, s[16:19], s30 offen offset:3072 lds
s_mov_b32 s24, s98
s_lshl_b32 s28, s24, 7
s_add_u32 s28, s28, s25
s_lshl_b32 s29, s24, 19
s_add_u32 s29, s29, s58
s_add_u32 s30, s29, 65536
s_add_u32 m0, s34, 65536
s_nop 0
buffer_load_dwordx4 v200, s[8:11], s28 offen offset:0 lds
buffer_load_dwordx4 v201, s[8:11], s28 offen offset:1024 lds
buffer_load_dwordx4 v202, s[8:11], s28 offen offset:2048 lds
buffer_load_dwordx4 v203, s[8:11], s28 offen offset:3072 lds
s_add_u32 m0, s35, 65536
s_nop 0
buffer_load_dwordx4 v204, s[12:15], s29 offen offset:0 lds
buffer_load_dwordx4 v204, s[16:19], s29 offen offset:1024 lds
buffer_load_dwordx4 v205, s[12:15], s29 offen offset:2048 lds
buffer_load_dwordx4 v205, s[16:19], s29 offen offset:3072 lds
s_add_u32 m0, s35, 69632
s_nop 0
buffer_load_dwordx4 v206, s[12:15], s30 offen offset:0 lds
buffer_load_dwordx4 v206, s[16:19], s30 offen offset:1024 lds
buffer_load_dwordx4 v207, s[12:15], s30 offen offset:2048 lds
buffer_load_dwordx4 v207, s[16:19], s30 offen offset:3072 lds
s_add_u32 s24, s24, 1
s_cmp_eq_u32 s24, 8
s_cselect_b32 s24, 1, s24
s_lshl_b32 s28, s24, 7
s_add_u32 s28, s28, s25
s_lshl_b32 s29, s24, 19
s_add_u32 s29, s29, s58
s_add_u32 s30, s29, 65536
s_add_u32 m0, s34, 114688
s_nop 0
buffer_load_dwordx4 v200, s[8:11], s28 offen offset:0 lds
buffer_load_dwordx4 v201, s[8:11], s28 offen offset:1024 lds
buffer_load_dwordx4 v202, s[8:11], s28 offen offset:2048 lds
buffer_load_dwordx4 v203, s[8:11], s28 offen offset:3072 lds
s_add_u32 m0, s35, 114688
s_nop 0
buffer_load_dwordx4 v204, s[12:15], s29 offen offset:0 lds
buffer_load_dwordx4 v204, s[16:19], s29 offen offset:1024 lds
buffer_load_dwordx4 v205, s[12:15], s29 offen offset:2048 lds
buffer_load_dwordx4 v205, s[16:19], s29 offen offset:3072 lds
s_add_u32 m0, s35, 118784
s_nop 0
buffer_load_dwordx4 v206, s[12:15], s30 offen offset:0 lds
buffer_load_dwordx4 v206, s[16:19], s30 offen offset:1024 lds
buffer_load_dwordx4 v207, s[12:15], s30 offen offset:2048 lds
buffer_load_dwordx4 v207, s[16:19], s30 offen offset:3072 lds
s_add_u32 s24, s24, 1
s_cmp_eq_u32 s24, 8
s_cselect_b32 s24, 1, s24
s_lshl_b32 s28, s24, 7
s_add_u32 s28, s28, s25
s_lshl_b32 s29, s24, 19
s_add_u32 s29, s29, s58
s_add_u32 s30, s29, 65536
s_mov_b32 s20, s82
s_and_b32 s21, s83, 0xffff
s_mov_b32 s22, 0x200000
s_mov_b32 s23, 0x20000
s_mov_b32 s36, s68
s_and_b32 s37, s69, 0xffff
s_mov_b32 s38, 0x400000
s_mov_b32 s39, 0x20000
s_add_u32 s40, s82, 0x400000
s_addc_u32 s41, s83, 0
s_and_b32 s41, s41, 0xffff
s_mov_b32 s42, 0x2000000
s_mov_b32 s43, 0x20000
s_mov_b32 s44, s59
s_waitcnt vmcnt(36)
v_lshlrev_b32 v16, 16, v0
v_and_b32 v17, 0xffff0000, v0
v_lshlrev_b32 v18, 16, v1
v_and_b32 v19, 0xffff0000, v1
v_lshlrev_b32 v20, 16, v2
v_and_b32 v21, 0xffff0000, v2
v_lshlrev_b32 v22, 16, v3
v_and_b32 v23, 0xffff0000, v3
v_add_f32 v8, v16, v17
v_add_f32 v8, v8, v18
v_add_f32 v8, v8, v19
v_add_f32 v8, v8, v20
v_add_f32 v8, v8, v21
v_add_f32 v8, v8, v22
v_add_f32 v8, v8, v23
s_nop 1
v_add_f32_dpp v8, v8, v8 quad_perm:[1,0,3,2] row_mask:0xf bank_mask:0xf
s_nop 1
v_add_f32_dpp v8, v8, v8 quad_perm:[2,3,0,1] row_mask:0xf bank_mask:0xf
s_nop 1
v_add_f32_dpp v8, v8, v8 row_half_mirror row_mask:0xf bank_mask:0xf
s_nop 1
v_add_f32_dpp v8, v8, v8 row_mirror row_mask:0xf bank_mask:0xf
v_mov_b32 v10, v8
s_nop 1
v_permlane16_swap_b32 v10, v8
s_nop 1
v_add_f32 v8, v8, v10
v_mov_b32 v10, v8
s_nop 1
v_permlane32_swap_b32 v10, v8
s_nop 1
v_add_f32 v8, v8, v10
v_mul_f32 v8, 0x3b000000, v8
v_sub_f32 v16, v16, v8
v_sub_f32 v17, v17, v8
v_sub_f32 v18, v18, v8
v_sub_f32 v19, v19, v8
v_sub_f32 v20, v20, v8
v_sub_f32 v21, v21, v8
v_sub_f32 v22, v22, v8
v_sub_f32 v23, v23, v8
v_mul_f32 v8, v16, v16
v_fmac_f32 v8, v17, v17
v_fmac_f32 v8, v18, v18
v_fmac_f32 v8, v19, v19
v_fmac_f32 v8, v20, v20
v_fmac_f32 v8, v21, v21
v_fmac_f32 v8, v22, v22
v_fmac_f32 v8, v23, v23
s_nop 1
v_add_f32_dpp v8, v8, v8 quad_perm:[1,0,3,2] row_mask:0xf bank_mask:0xf
s_nop 1
v_add_f32_dpp v8, v8, v8 quad_perm:[2,3,0,1] row_mask:0xf bank_mask:0xf
s_nop 1
v_add_f32_dpp v8, v8, v8 row_half_mirror row_mask:0xf bank_mask:0xf
s_nop 1
v_add_f32_dpp v8, v8, v8 row_mirror row_mask:0xf bank_mask:0xf
v_mov_b32 v10, v8
s_nop 1
v_permlane16_swap_b32 v10, v8
s_nop 1
v_add_f32 v8, v8, v10
v_mov_b32 v10, v8
s_nop 1
v_permlane32_swap_b32 v10, v8
s_nop 1
v_add_f32 v8, v8, v10
v_mov_b32 v11, 0x3727c5ac
v_fmac_f32 v11, 0x3b000000, v8
v_rsq_f32 v11, v11
s_nop 0
v_mul_f32 v16, v16, v11
v_mul_f32 v17, v17, v11
v_mul_f32 v18, v18, v11
v_mul_f32 v19, v19, v11
v_mul_f32 v20, v20, v11
v_mul_f32 v21, v21, v11
v_mul_f32 v22, v22, v11
v_mul_f32 v23, v23, v11
v_cvt_pk_bf16_f32 v0, v16, v17
v_cvt_pk_bf16_f32 v1, v18, v19
v_cvt_pk_bf16_f32 v2, v20, v21
v_cvt_pk_bf16_f32 v3, v22, v23
buffer_store_dwordx4 v[0:3], v252, s[52:55], s44 offen offset:0 sc1
s_nop 1
v_lshlrev_b32 v32, 16, v4
v_and_b32 v33, 0xffff0000, v4
v_lshlrev_b32 v34, 16, v5
v_and_b32 v35, 0xffff0000, v5
v_lshlrev_b32 v36, 16, v6
v_and_b32 v37, 0xffff0000, v6
v_lshlrev_b32 v38, 16, v7
v_and_b32 v39, 0xffff0000, v7
v_add_f32 v9, v32, v33
v_add_f32 v9, v9, v34
v_add_f32 v9, v9, v35
v_add_f32 v9, v9, v36
v_add_f32 v9, v9, v37
v_add_f32 v9, v9, v38
v_add_f32 v9, v9, v39
s_nop 1
v_add_f32_dpp v9, v9, v9 quad_perm:[1,0,3,2] row_mask:0xf bank_mask:0xf
s_nop 1
v_add_f32_dpp v9, v9, v9 quad_perm:[2,3,0,1] row_mask:0xf bank_mask:0xf
s_nop 1
v_add_f32_dpp v9, v9, v9 row_half_mirror row_mask:0xf bank_mask:0xf
s_nop 1
v_add_f32_dpp v9, v9, v9 row_mirror row_mask:0xf bank_mask:0xf
v_mov_b32 v10, v9
s_nop 1
v_permlane16_swap_b32 v10, v9
s_nop 1
v_add_f32 v9, v9, v10
v_mov_b32 v10, v9
s_nop 1
v_permlane32_swap_b32 v10, v9
s_nop 1
v_add_f32 v9, v9, v10
v_mul_f32 v9, 0x3b000000, v9
v_sub_f32 v32, v32, v9
v_sub_f32 v33, v33, v9
v_sub_f32 v34, v34, v9
v_sub_f32 v35, v35, v9
v_sub_f32 v36, v36, v9
v_sub_f32 v37, v37, v9
v_sub_f32 v38, v38, v9
v_sub_f32 v39, v39, v9
v_mul_f32 v9, v32, v32
v_fmac_f32 v9, v33, v33
v_fmac_f32 v9, v34, v34
v_fmac_f32 v9, v35, v35
v_fmac_f32 v9, v36, v36
v_fmac_f32 v9, v37, v37
v_fmac_f32 v9, v38, v38
v_fmac_f32 v9, v39, v39
s_nop 1
v_add_f32_dpp v9, v9, v9 quad_perm:[1,0,3,2] row_mask:0xf bank_mask:0xf
s_nop 1
v_add_f32_dpp v9, v9, v9 quad_perm:[2,3,0,1] row_mask:0xf bank_mask:0xf
s_nop 1
v_add_f32_dpp v9, v9, v9 row_half_mirror row_mask:0xf bank_mask:0xf
s_nop 1
v_add_f32_dpp v9, v9, v9 row_mirror row_mask:0xf bank_mask:0xf
v_mov_b32 v10, v9
s_nop 1
v_permlane16_swap_b32 v10, v9
s_nop 1
v_add_f32 v9, v9, v10
v_mov_b32 v10, v9
s_nop 1
v_permlane32_swap_b32 v10, v9
s_nop 1
v_add_f32 v9, v9, v10
v_mov_b32 v11, 0x3727c5ac
v_fmac_f32 v11, 0x3b000000, v9
v_rsq_f32 v11, v11
s_nop 0
v_mul_f32 v32, v32, v11
v_mul_f32 v33, v33, v11
v_mul_f32 v34, v34, v11
v_mul_f32 v35, v35, v11
v_mul_f32 v36, v36, v11
v_mul_f32 v37, v37, v11
v_mul_f32 v38, v38, v11
v_mul_f32 v39, v39, v11
v_cvt_pk_bf16_f32 v4, v32, v33
v_cvt_pk_bf16_f32 v5, v34, v35
v_cvt_pk_bf16_f32 v6, v36, v37
v_cvt_pk_bf16_f32 v7, v38, v39
buffer_store_dwordx4 v[4:7], v252, s[52:55], s44 offen offset:1024 sc1
s_nop 1
v_readfirstlane_b32 s84, v14
v_readfirstlane_b32 s85, v15
s_nop 3
s_add_u32 s84, s84, 1
s_addc_u32 s85, s85, 0
s_waitcnt vmcnt(26)
s_barrier
s_lshr_b32 s44, s7, 1
s_cmp_eq_u32 s44, 0
s_cbranch_scc0 G3_V
ds_read_b64_tr_b16 a[0:1], v194 offset:0
ds_read_b64_tr_b16 a[2:3], v194 offset:2048
ds_read_b128 a[128:131], v192 offset:0
ds_read_b128 a[132:135], v192 offset:2048
ds_read_b128 a[136:139], v192 offset:4096
ds_read_b128 a[140:143], v192 offset:6144
ds_read_b64_tr_b16 a[4:5], v195 offset:128
ds_read_b64_tr_b16 a[6:7], v195 offset:2176
ds_read_b64_tr_b16 a[8:9], v194 offset:256
ds_read_b64_tr_b16 a[10:11], v194 offset:2304
ds_read_b64_tr_b16 a[12:13], v195 offset:384
ds_read_b64_tr_b16 a[14:15], v195 offset:2432
ds_read_b64_tr_b16 a[16:17], v194 offset:512
ds_read_b64_tr_b16 a[18:19], v194 offset:2560
ds_read_b64_tr_b16 a[20:21], v195 offset:640
ds_read_b64_tr_b16 a[22:23], v195 offset:2688
ds_read_b64_tr_b16 a[24:25], v194 offset:768
ds_read_b64_tr_b16 a[26:27], v194 offset:2816
ds_read_b64_tr_b16 a[28:29], v195 offset:896
ds_read_b64_tr_b16 a[30:31], v195 offset:2944
s_waitcnt lgkmcnt(0)
v_mfma_f32_16x16x32_bf16 v[0:3], a[0:3], a[128:131], 0
ds_read_b64_tr_b16 a[32:33], v194 offset:16384
v_mfma_f32_16x16x32_bf16 v[4:7], a[0:3], a[132:135], 0
ds_read_b64_tr_b16 a[34:35], v194 offset:18432
v_mfma_f32_16x16x32_bf16 v[8:11], a[0:3], a[136:139], 0
ds_read_b128 a[144:147], v193 offset:0
v_mfma_f32_16x16x32_bf16 v[12:15], a[0:3], a[140:143], 0
ds_read_b128 a[148:151], v193 offset:2048
v_mfma_f32_16x16x32_bf16 v[16:19], a[4:7], a[128:131], 0
ds_read_b128 a[152:155], v193 offset:4096
v_mfma_f32_16x16x32_bf16 v[20:23], a[4:7], a[132:135], 0
ds_read_b128 a[156:159], v193 offset:6144
v_mfma_f32_16x16x32_bf16 v[24:27], a[4:7], a[136:139], 0
ds_read_b64_tr_b16 a[36:37], v195 offset:16512
v_mfma_f32_16x16x32_bf16 v[28:31], a[4:7], a[140:143], 0
ds_read_b64_tr_b16 a[38:39], v195 offset:18560
v_mfma_f32_16x16x32_bf16 v[32:35], a[8:11], a[128:131], 0
ds_read_b64_tr_b16 a[40:41], v194 offset:16640
v_mfma_f32_16x16x32_bf16 v[36:39], a[8:11], a[132:135], 0
ds_read_b64_tr_b16 a[42:43], v194 offset:18688
v_mfma_f32_16x16x32_bf16 v[40:43], a[8:11], a[136:139], 0
ds_read_b64_tr_b16 a[44:45], v195 offset:16768
v_mfma_f32_16x16x32_bf16 v[44:47], a[8:11], a[140:143], 0
ds_read_b64_tr_b16 a[46:47], v195 offset:18816
v_mfma_f32_16x16x32_bf16 v[48:51], a[12:15], a[128:131], 0
ds_read_b64_tr_b16 a[48:49], v194 offset:16896
v_mfma_f32_16x16x32_bf16 v[52:55], a[12:15], a[132:135], 0
ds_read_b64_tr_b16 a[50:51], v194 offset:18944
v_mfma_f32_16x16x32_bf16 v[56:59], a[12:15], a[136:139], 0
ds_read_b64_tr_b16 a[52:53], v195 offset:17024
v_mfma_f32_16x16x32_bf16 v[60:63], a[12:15], a[140:143], 0
ds_read_b64_tr_b16 a[54:55], v195 offset:19072
v_mfma_f32_16x16x32_bf16 v[64:67], a[16:19], a[128:131], 0
ds_read_b64_tr_b16 a[56:57], v194 offset:17152
v_mfma_f32_16x16x32_bf16 v[68:71], a[16:19], a[132:135], 0
ds_read_b64_tr_b16 a[58:59], v194 offset:19200
v_mfma_f32_16x16x32_bf16 v[72:75], a[16:19], a[136:139], 0
ds_read_b64_tr_b16 a[60:61], v195 offset:17280
v_mfma_f32_16x16x32_bf16 v[76:79], a[16:19], a[140:143], 0
ds_read_b64_tr_b16 a[62:63], v195 offset:19328
v_mfma_f32_16x16x32_bf16 v[80:83], a[20:23], a[128:131], 0
v_mfma_f32_16x16x32_bf16 v[84:87], a[20:23], a[132:135], 0
v_mfma_f32_16x16x32_bf16 v[88:91], a[20:23], a[136:139], 0
v_mfma_f32_16x16x32_bf16 v[92:95], a[20:23], a[140:143], 0
v_mfma_f32_16x16x32_bf16 v[96:99], a[24:27], a[128:131], 0
v_mfma_f32_16x16x32_bf16 v[100:103], a[24:27], a[132:135], 0
v_mfma_f32_16x16x32_bf16 v[104:107], a[24:27], a[136:139], 0
v_mfma_f32_16x16x32_bf16 v[108:111], a[24:27], a[140:143], 0
v_mfma_f32_16x16x32_bf16 v[112:115], a[28:31], a[128:131], 0
v_mfma_f32_16x16x32_bf16 v[116:119], a[28:31], a[132:135], 0
v_mfma_f32_16x16x32_bf16 v[120:123], a[28:31], a[136:139], 0
v_mfma_f32_16x16x32_bf16 v[124:127], a[28:31], a[140:143], 0
s_waitcnt vmcnt(14) lgkmcnt(0)
s_barrier
v_add_u32 v192, 0xc000, v192
v_add_u32 v193, 0xc000, v193
v_add_u32 v194, 0xc000, v194
v_add_u32 v195, 0xc000, v195
s_nop 0
v_mfma_f32_16x16x32_bf16 v[0:3], a[32:35], a[144:147], v[0:3]
ds_read_b64_tr_b16 a[0:1], v194 offset:0
s_add_u32 m0, s34, 16384
v_mfma_f32_16x16x32_bf16 v[4:7], a[32:35], a[148:151], v[4:7]
buffer_load_dwordx4 v200, s[8:11], s28 offen offset:0 lds
v_mfma_f32_16x16x32_bf16 v[8:11], a[32:35], a[152:155], v[8:11]
ds_read_b64_tr_b16 a[2:3], v194 offset:2048
v_mfma_f32_16x16x32_bf16 v[12:15], a[32:35], a[156:159], v[12:15]
ds_read_b128 a[128:131], v192 offset:0
v_mfma_f32_16x16x32_bf16 v[16:19], a[36:39], a[144:147], v[16:19]
ds_read_b128 a[132:135], v192 offset:2048
v_mfma_f32_16x16x32_bf16 v[20:23], a[36:39], a[148:151], v[20:23]
ds_read_b128 a[136:139], v192 offset:4096
v_mfma_f32_16x16x32_bf16 v[24:27], a[36:39], a[152:155], v[24:27]
buffer_load_dwordx4 v201, s[8:11], s28 offen offset:1024 lds
v_mfma_f32_16x16x32_bf16 v[28:31], a[36:39], a[156:159], v[28:31]
ds_read_b128 a[140:143], v192 offset:6144
v_mfma_f32_16x16x32_bf16 v[32:35], a[40:43], a[144:147], v[32:35]
ds_read_b64_tr_b16 a[4:5], v195 offset:128
v_mfma_f32_16x16x32_bf16 v[36:39], a[40:43], a[148:151], v[36:39]
ds_read_b64_tr_b16 a[6:7], v195 offset:2176
v_mfma_f32_16x16x32_bf16 v[40:43], a[40:43], a[152:155], v[40:43]
ds_read_b64_tr_b16 a[8:9], v194 offset:256
v_mfma_f32_16x16x32_bf16 v[44:47], a[40:43], a[156:159], v[44:47]
buffer_load_dwordx4 v202, s[8:11], s28 offen offset:2048 lds
v_mfma_f32_16x16x32_bf16 v[48:51], a[44:47], a[144:147], v[48:51]
ds_read_b64_tr_b16 a[10:11], v194 offset:2304
v_mfma_f32_16x16x32_bf16 v[52:55], a[44:47], a[148:151], v[52:55]
ds_read_b64_tr_b16 a[12:13], v195 offset:384
v_mfma_f32_16x16x32_bf16 v[56:59], a[44:47], a[152:155], v[56:59]
ds_read_b64_tr_b16 a[14:15], v195 offset:2432
v_mfma_f32_16x16x32_bf16 v[60:63], a[44:47], a[156:159], v[60:63]
ds_read_b64_tr_b16 a[16:17], v194 offset:512
v_mfma_f32_16x16x32_bf16 v[64:67], a[48:51], a[144:147], v[64:67]
buffer_load_dwordx4 v203, s[8:11], s28 offen offset:3072 lds
v_mfma_f32_16x16x32_bf16 v[68:71], a[48:51], a[148:151], v[68:71]
ds_read_b64_tr_b16 a[18:19], v194 offset:2560
v_mfma_f32_16x16x32_bf16 v[72:75], a[48:51], a[152:155], v[72:75]
ds_read_b64_tr_b16 a[20:21], v195 offset:640
v_mfma_f32_16x16x32_bf16 v[76:79], a[48:51], a[156:159], v[76:79]
ds_read_b64_tr_b16 a[22:23], v195 offset:2688
v_mfma_f32_16x16x32_bf16 v[80:83], a[52:55], a[144:147], v[80:83]
ds_read_b64_tr_b16 a[24:25], v194 offset:768
s_add_u32 m0, s35, 16384
v_mfma_f32_16x16x32_bf16 v[84:87], a[52:55], a[148:151], v[84:87]
buffer_load_dwordx4 v204, s[12:15], s29 offen offset:0 lds
v_mfma_f32_16x16x32_bf16 v[88:91], a[52:55], a[152:155], v[88:91]
ds_read_b64_tr_b16 a[26:27], v194 offset:2816
v_mfma_f32_16x16x32_bf16 v[92:95], a[52:55], a[156:159], v[92:95]
ds_read_b64_tr_b16 a[28:29], v195 offset:896
v_mfma_f32_16x16x32_bf16 v[96:99], a[56:59], a[144:147], v[96:99]
ds_read_b64_tr_b16 a[30:31], v195 offset:2944
v_mfma_f32_16x16x32_bf16 v[100:103], a[56:59], a[148:151], v[100:103]
v_mfma_f32_16x16x32_bf16 v[104:107], a[56:59], a[152:155], v[104:107]
buffer_load_dwordx4 v204, s[16:19], s29 offen offset:1024 lds
v_mfma_f32_16x16x32_bf16 v[108:111], a[56:59], a[156:159], v[108:111]
v_mfma_f32_16x16x32_bf16 v[112:115], a[60:63], a[144:147], v[112:115]
v_mfma_f32_16x16x32_bf16 v[116:119], a[60:63], a[148:151], v[116:119]
v_mfma_f32_16x16x32_bf16 v[120:123], a[60:63], a[152:155], v[120:123]
v_mfma_f32_16x16x32_bf16 v[124:127], a[60:63], a[156:159], v[124:127]
buffer_load_dwordx4 v205, s[12:15], s29 offen offset:2048 lds
s_waitcnt lgkmcnt(0)
v_mfma_f32_16x16x32_bf16 v[0:3], a[0:3], a[128:131], v[0:3]
ds_read_b64_tr_b16 a[32:33], v194 offset:16384
v_mfma_f32_16x16x32_bf16 v[4:7], a[0:3], a[132:135], v[4:7]
ds_read_b64_tr_b16 a[34:35], v194 offset:18432
v_mfma_f32_16x16x32_bf16 v[8:11], a[0:3], a[136:139], v[8:11]
ds_read_b128 a[144:147], v193 offset:0
v_mfma_f32_16x16x32_bf16 v[12:15], a[0:3], a[140:143], v[12:15]
ds_read_b128 a[148:151], v193 offset:2048
v_mfma_f32_16x16x32_bf16 v[16:19], a[4:7], a[128:131], v[16:19]
buffer_load_dwordx4 v205, s[16:19], s29 offen offset:3072 lds
v_mfma_f32_16x16x32_bf16 v[20:23], a[4:7], a[132:135], v[20:23]
ds_read_b128 a[152:155], v193 offset:4096
v_mfma_f32_16x16x32_bf16 v[24:27], a[4:7], a[136:139], v[24:27]
ds_read_b128 a[156:159], v193 offset:6144
v_mfma_f32_16x16x32_bf16 v[28:31], a[4:7], a[140:143], v[28:31]
ds_read_b64_tr_b16 a[36:37], v195 offset:16512
v_mfma_f32_16x16x32_bf16 v[32:35], a[8:11], a[128:131], v[32:35]
ds_read_b64_tr_b16 a[38:39], v195 offset:18560
s_add_u32 m0, s35, 20480
v_mfma_f32_16x16x32_bf16 v[36:39], a[8:11], a[132:135], v[36:39]
buffer_load_dwordx4 v206, s[12:15], s30 offen offset:0 lds
v_mfma_f32_16x16x32_bf16 v[40:43], a[8:11], a[136:139], v[40:43]
ds_read_b64_tr_b16 a[40:41], v194 offset:16640
v_mfma_f32_16x16x32_bf16 v[44:47], a[8:11], a[140:143], v[44:47]
ds_read_b64_tr_b16 a[42:43], v194 offset:18688
v_mfma_f32_16x16x32_bf16 v[48:51], a[12:15], a[128:131], v[48:51]
ds_read_b64_tr_b16 a[44:45], v195 offset:16768
v_mfma_f32_16x16x32_bf16 v[52:55], a[12:15], a[132:135], v[52:55]
ds_read_b64_tr_b16 a[46:47], v195 offset:18816
v_mfma_f32_16x16x32_bf16 v[56:59], a[12:15], a[136:139], v[56:59]
buffer_load_dwordx4 v206, s[16:19], s30 offen offset:1024 lds
v_mfma_f32_16x16x32_bf16 v[60:63], a[12:15], a[140:143], v[60:63]
ds_read_b64_tr_b16 a[48:49], v194 offset:16896
v_mfma_f32_16x16x32_bf16 v[64:67], a[16:19], a[128:131], v[64:67]
ds_read_b64_tr_b16 a[50:51], v194 offset:18944
v_mfma_f32_16x16x32_bf16 v[68:71], a[16:19], a[132:135], v[68:71]
ds_read_b64_tr_b16 a[52:53], v195 offset:17024
v_mfma_f32_16x16x32_bf16 v[72:75], a[16:19], a[136:139], v[72:75]
ds_read_b64_tr_b16 a[54:55], v195 offset:19072
v_mfma_f32_16x16x32_bf16 v[76:79], a[16:19], a[140:143], v[76:79]
buffer_load_dwordx4 v207, s[12:15], s30 offen offset:2048 lds
v_mfma_f32_16x16x32_bf16 v[80:83], a[20:23], a[128:131], v[80:83]
ds_read_b64_tr_b16 a[56:57], v194 offset:17152
v_mfma_f32_16x16x32_bf16 v[84:87], a[20:23], a[132:135], v[84:87]
ds_read_b64_tr_b16 a[58:59], v194 offset:19200
v_mfma_f32_16x16x32_bf16 v[88:91], a[20:23], a[136:139], v[88:91]
ds_read_b64_tr_b16 a[60:61], v195 offset:17280
v_mfma_f32_16x16x32_bf16 v[92:95], a[20:23], a[140:143], v[92:95]
ds_read_b64_tr_b16 a[62:63], v195 offset:19328
v_mfma_f32_16x16x32_bf16 v[96:99], a[24:27], a[128:131], v[96:99]
buffer_load_dwordx4 v207, s[16:19], s30 offen offset:3072 lds
v_mfma_f32_16x16x32_bf16 v[100:103], a[24:27], a[132:135], v[100:103]
s_add_u32 s24, s24, 1
s_cmp_eq_u32 s24, 8
v_mfma_f32_16x16x32_bf16 v[104:107], a[24:27], a[136:139], v[104:107]
s_cselect_b32 s24, 1, s24
s_lshl_b32 s28, s24, 7
v_mfma_f32_16x16x32_bf16 v[108:111], a[24:27], a[140:143], v[108:111]
s_add_u32 s28, s28, s25
s_lshl_b32 s29, s24, 19
v_mfma_f32_16x16x32_bf16 v[112:115], a[28:31], a[128:131], v[112:115]
s_add_u32 s29, s29, s58
s_add_u32 s30, s29, 65536
v_mfma_f32_16x16x32_bf16 v[116:119], a[28:31], a[132:135], v[116:119]
v_mfma_f32_16x16x32_bf16 v[120:123], a[28:31], a[136:139], v[120:123]
v_mfma_f32_16x16x32_bf16 v[124:127], a[28:31], a[140:143], v[124:127]
s_waitcnt vmcnt(14) lgkmcnt(0)
s_barrier
v_add_u32 v192, 0xc000, v192
v_add_u32 v193, 0xc000, v193
v_add_u32 v194, 0xc000, v194
v_add_u32 v195, 0xc000, v195
s_nop 0
v_mfma_f32_16x16x32_bf16 v[0:3], a[32:35], a[144:147], v[0:3]
ds_read_b64_tr_b16 a[0:1], v194 offset:0
s_add_u32 m0, s34, 65536
v_mfma_f32_16x16x32_bf16 v[4:7], a[32:35], a[148:151], v[4:7]
buffer_load_dwordx4 v200, s[8:11], s28 offen offset:0 lds
v_mfma_f32_16x16x32_bf16 v[8:11], a[32:35], a[152:155], v[8:11]
ds_read_b64_tr_b16 a[2:3], v194 offset:2048
v_mfma_f32_16x16x32_bf16 v[12:15], a[32:35], a[156:159], v[12:15]
ds_read_b128 a[128:131], v192 offset:0
v_mfma_f32_16x16x32_bf16 v[16:19], a[36:39], a[144:147], v[16:19]
ds_read_b128 a[132:135], v192 offset:2048
v_mfma_f32_16x16x32_bf16 v[20:23], a[36:39], a[148:151], v[20:23]
ds_read_b128 a[136:139], v192 offset:4096
v_mfma_f32_16x16x32_bf16 v[24:27], a[36:39], a[152:155], v[24:27]
buffer_load_dwordx4 v201, s[8:11], s28 offen offset:1024 lds
v_mfma_f32_16x16x32_bf16 v[28:31], a[36:39], a[156:159], v[28:31]
ds_read_b128 a[140:143], v192 offset:6144
v_mfma_f32_16x16x32_bf16 v[32:35], a[40:43], a[144:147], v[32:35]
ds_read_b64_tr_b16 a[4:5], v195 offset:128
v_mfma_f32_16x16x32_bf16 v[36:39], a[40:43], a[148:151], v[36:39]
ds_read_b64_tr_b16 a[6:7], v195 offset:2176
v_mfma_f32_16x16x32_bf16 v[40:43], a[40:43], a[152:155], v[40:43]
ds_read_b64_tr_b16 a[8:9], v194 offset:256
v_mfma_f32_16x16x32_bf16 v[44:47], a[40:43], a[156:159], v[44:47]
buffer_load_dwordx4 v202, s[8:11], s28 offen offset:2048 lds
v_mfma_f32_16x16x32_bf16 v[48:51], a[44:47], a[144:147], v[48:51]
ds_read_b64_tr_b16 a[10:11], v194 offset:2304
v_mfma_f32_16x16x32_bf16 v[52:55], a[44:47], a[148:151], v[52:55]
ds_read_b64_tr_b16 a[12:13], v195 offset:384
v_mfma_f32_16x16x32_bf16 v[56:59], a[44:47], a[152:155], v[56:59]
ds_read_b64_tr_b16 a[14:15], v195 offset:2432
v_mfma_f32_16x16x32_bf16 v[60:63], a[44:47], a[156:159], v[60:63]
ds_read_b64_tr_b16 a[16:17], v194 offset:512
v_mfma_f32_16x16x32_bf16 v[64:67], a[48:51], a[144:147], v[64:67]
buffer_load_dwordx4 v203, s[8:11], s28 offen offset:3072 lds
v_mfma_f32_16x16x32_bf16 v[68:71], a[48:51], a[148:151], v[68:71]
ds_read_b64_tr_b16 a[18:19], v194 offset:2560
v_mfma_f32_16x16x32_bf16 v[72:75], a[48:51], a[152:155], v[72:75]
ds_read_b64_tr_b16 a[20:21], v195 offset:640
v_mfma_f32_16x16x32_bf16 v[76:79], a[48:51], a[156:159], v[76:79]
ds_read_b64_tr_b16 a[22:23], v195 offset:2688
v_mfma_f32_16x16x32_bf16 v[80:83], a[52:55], a[144:147], v[80:83]
ds_read_b64_tr_b16 a[24:25], v194 offset:768
s_add_u32 m0, s35, 65536
v_mfma_f32_16x16x32_bf16 v[84:87], a[52:55], a[148:151], v[84:87]
buffer_load_dwordx4 v204, s[12:15], s29 offen offset:0 lds
v_mfma_f32_16x16x32_bf16 v[88:91], a[52:55], a[152:155], v[88:91]
ds_read_b64_tr_b16 a[26:27], v194 offset:2816
v_mfma_f32_16x16x32_bf16 v[92:95], a[52:55], a[156:159], v[92:95]
ds_read_b64_tr_b16 a[28:29], v195 offset:896
v_mfma_f32_16x16x32_bf16 v[96:99], a[56:59], a[144:147], v[96:99]
ds_read_b64_tr_b16 a[30:31], v195 offset:2944
v_mfma_f32_16x16x32_bf16 v[100:103], a[56:59], a[148:151], v[100:103]
v_mfma_f32_16x16x32_bf16 v[104:107], a[56:59], a[152:155], v[104:107]
buffer_load_dwordx4 v204, s[16:19], s29 offen offset:1024 lds
v_mfma_f32_16x16x32_bf16 v[108:111], a[56:59], a[156:159], v[108:111]
v_mfma_f32_16x16x32_bf16 v[112:115], a[60:63], a[144:147], v[112:115]
v_mfma_f32_16x16x32_bf16 v[116:119], a[60:63], a[148:151], v[116:119]
v_mfma_f32_16x16x32_bf16 v[120:123], a[60:63], a[152:155], v[120:123]
v_mfma_f32_16x16x32_bf16 v[124:127], a[60:63], a[156:159], v[124:127]
buffer_load_dwordx4 v205, s[12:15], s29 offen offset:2048 lds
s_waitcnt lgkmcnt(0)
v_mfma_f32_16x16x32_bf16 v[0:3], a[0:3], a[128:131], v[0:3]
ds_read_b64_tr_b16 a[32:33], v194 offset:16384
v_mfma_f32_16x16x32_bf16 v[4:7], a[0:3], a[132:135], v[4:7]
ds_read_b64_tr_b16 a[34:35], v194 offset:18432
v_mfma_f32_16x16x32_bf16 v[8:11], a[0:3], a[136:139], v[8:11]
ds_read_b128 a[144:147], v193 offset:0
v_mfma_f32_16x16x32_bf16 v[12:15], a[0:3], a[140:143], v[12:15]
ds_read_b128 a[148:151], v193 offset:2048
v_mfma_f32_16x16x32_bf16 v[16:19], a[4:7], a[128:131], v[16:19]
buffer_load_dwordx4 v205, s[16:19], s29 offen offset:3072 lds
v_mfma_f32_16x16x32_bf16 v[20:23], a[4:7], a[132:135], v[20:23]
ds_read_b128 a[152:155], v193 offset:4096
v_mfma_f32_16x16x32_bf16 v[24:27], a[4:7], a[136:139], v[24:27]
ds_read_b128 a[156:159], v193 offset:6144
v_mfma_f32_16x16x32_bf16 v[28:31], a[4:7], a[140:143], v[28:31]
ds_read_b64_tr_b16 a[36:37], v195 offset:16512
v_mfma_f32_16x16x32_bf16 v[32:35], a[8:11], a[128:131], v[32:35]
ds_read_b64_tr_b16 a[38:39], v195 offset:18560
s_add_u32 m0, s35, 69632
v_mfma_f32_16x16x32_bf16 v[36:39], a[8:11], a[132:135], v[36:39]
buffer_load_dwordx4 v206, s[12:15], s30 offen offset:0 lds
v_mfma_f32_16x16x32_bf16 v[40:43], a[8:11], a[136:139], v[40:43]
ds_read_b64_tr_b16 a[40:41], v194 offset:16640
v_mfma_f32_16x16x32_bf16 v[44:47], a[8:11], a[140:143], v[44:47]
ds_read_b64_tr_b16 a[42:43], v194 offset:18688
v_mfma_f32_16x16x32_bf16 v[48:51], a[12:15], a[128:131], v[48:51]
ds_read_b64_tr_b16 a[44:45], v195 offset:16768
v_mfma_f32_16x16x32_bf16 v[52:55], a[12:15], a[132:135], v[52:55]
ds_read_b64_tr_b16 a[46:47], v195 offset:18816
v_mfma_f32_16x16x32_bf16 v[56:59], a[12:15], a[136:139], v[56:59]
buffer_load_dwordx4 v206, s[16:19], s30 offen offset:1024 lds
v_mfma_f32_16x16x32_bf16 v[60:63], a[12:15], a[140:143], v[60:63]
ds_read_b64_tr_b16 a[48:49], v194 offset:16896
v_mfma_f32_16x16x32_bf16 v[64:67], a[16:19], a[128:131], v[64:67]
ds_read_b64_tr_b16 a[50:51], v194 offset:18944
v_mfma_f32_16x16x32_bf16 v[68:71], a[16:19], a[132:135], v[68:71]
ds_read_b64_tr_b16 a[52:53], v195 offset:17024
v_mfma_f32_16x16x32_bf16 v[72:75], a[16:19], a[136:139], v[72:75]
ds_read_b64_tr_b16 a[54:55], v195 offset:19072
v_mfma_f32_16x16x32_bf16 v[76:79], a[16:19], a[140:143], v[76:79]
buffer_load_dwordx4 v207, s[12:15], s30 offen offset:2048 lds
v_mfma_f32_16x16x32_bf16 v[80:83], a[20:23], a[128:131], v[80:83]
ds_read_b64_tr_b16 a[56:57], v194 offset:17152
v_mfma_f32_16x16x32_bf16 v[84:87], a[20:23], a[132:135], v[84:87]
ds_read_b64_tr_b16 a[58:59], v194 offset:19200
v_mfma_f32_16x16x32_bf16 v[88:91], a[20:23], a[136:139], v[88:91]
ds_read_b64_tr_b16 a[60:61], v195 offset:17280
v_mfma_f32_16x16x32_bf16 v[92:95], a[20:23], a[140:143], v[92:95]
ds_read_b64_tr_b16 a[62:63], v195 offset:19328
v_mfma_f32_16x16x32_bf16 v[96:99], a[24:27], a[128:131], v[96:99]
buffer_load_dwordx4 v207, s[16:19], s30 offen offset:3072 lds
v_mfma_f32_16x16x32_bf16 v[100:103], a[24:27], a[132:135], v[100:103]
s_add_u32 s24, s24, 1
s_cmp_eq_u32 s24, 8
v_mfma_f32_16x16x32_bf16 v[104:107], a[24:27], a[136:139], v[104:107]
s_cselect_b32 s24, 1, s24
s_lshl_b32 s28, s24, 7
v_mfma_f32_16x16x32_bf16 v[108:111], a[24:27], a[140:143], v[108:111]
s_add_u32 s28, s28, s25
s_lshl_b32 s29, s24, 19
v_mfma_f32_16x16x32_bf16 v[112:115], a[28:31], a[128:131], v[112:115]
s_add_u32 s29, s29, s58
s_add_u32 s30, s29, 65536
v_mfma_f32_16x16x32_bf16 v[116:119], a[28:31], a[132:135], v[116:119]
v_mfma_f32_16x16x32_bf16 v[120:123], a[28:31], a[136:139], v[120:123]
v_mfma_f32_16x16x32_bf16 v[124:127], a[28:31], a[140:143], v[124:127]
s_waitcnt vmcnt(12) lgkmcnt(0)
s_barrier
s_nop 0
s_cmp_eq_u32 s7, 0
s_cbranch_scc0 Lpubln_1
s_lshl_b32 s46, s6, 3
s_add_u32 s46, s46, 4096
v_mov_b32 v254, s84
v_accvgpr_write_b32 a250, v254
v_mov_b32 v254, s85
v_accvgpr_write_b32 a251, v254
v_mov_b32 v254, 0
s_mov_b64 exec, 1
s_nop 1
buffer_store_dwordx2 a[250:251], v254, s[88:91], s46 offen sc1
s_nop 0
s_mov_b64 exec, -1
Lpubln_1:
v_add_u32 v192, 0xfffe8000, v192
v_add_u32 v193, 0xfffe8000, v193
v_add_u32 v194, 0xfffe8000, v194
v_add_u32 v195, 0xfffe8000, v195
s_nop 0
v_mfma_f32_16x16x32_bf16 v[0:3], a[32:35], a[144:147], v[0:3]
ds_read_b64_tr_b16 a[0:1], v194 offset:0
s_add_u32 m0, s34, 114688
v_mfma_f32_16x16x32_bf16 v[4:7], a[32:35], a[148:151], v[4:7]
buffer_load_dwordx4 v200, s[8:11], s28 offen offset:0 lds
v_mfma_f32_16x16x32_bf16 v[8:11], a[32:35], a[152:155], v[8:11]
ds_read_b64_tr_b16 a[2:3], v194 offset:2048
v_mfma_f32_16x16x32_bf16 v[12:15], a[32:35], a[156:159], v[12:15]
ds_read_b128 a[128:131], v192 offset:0
v_mfma_f32_16x16x32_bf16 v[16:19], a[36:39], a[144:147], v[16:19]
ds_read_b128 a[132:135], v192 offset:2048
v_mfma_f32_16x16x32_bf16 v[20:23], a[36:39], a[148:151], v[20:23]
ds_read_b128 a[136:139], v192 offset:4096
v_mfma_f32_16x16x32_bf16 v[24:27], a[36:39], a[152:155], v[24:27]
buffer_load_dwordx4 v201, s[8:11], s28 offen offset:1024 lds
v_mfma_f32_16x16x32_bf16 v[28:31], a[36:39], a[156:159], v[28:31]
ds_read_b128 a[140:143], v192 offset:6144
v_mfma_f32_16x16x32_bf16 v[32:35], a[40:43], a[144:147], v[32:35]
ds_read_b64_tr_b16 a[4:5], v195 offset:128
v_mfma_f32_16x16x32_bf16 v[36:39], a[40:43], a[148:151], v[36:39]
ds_read_b64_tr_b16 a[6:7], v195 offset:2176
v_mfma_f32_16x16x32_bf16 v[40:43], a[40:43], a[152:155], v[40:43]
ds_read_b64_tr_b16 a[8:9], v194 offset:256
v_mfma_f32_16x16x32_bf16 v[44:47], a[40:43], a[156:159], v[44:47]
buffer_load_dwordx4 v202, s[8:11], s28 offen offset:2048 lds
v_mfma_f32_16x16x32_bf16 v[48:51], a[44:47], a[144:147], v[48:51]
ds_read_b64_tr_b16 a[10:11], v194 offset:2304
v_mfma_f32_16x16x32_bf16 v[52:55], a[44:47], a[148:151], v[52:55]
ds_read_b64_tr_b16 a[12:13], v195 offset:384
v_mfma_f32_16x16x32_bf16 v[56:59], a[44:47], a[152:155], v[56:59]
ds_read_b64_tr_b16 a[14:15], v195 offset:2432
v_mfma_f32_16x16x32_bf16 v[60:63], a[44:47], a[156:159], v[60:63]
ds_read_b64_tr_b16 a[16:17], v194 offset:512
v_mfma_f32_16x16x32_bf16 v[64:67], a[48:51], a[144:147], v[64:67]
buffer_load_dwordx4 v203, s[8:11], s28 offen offset:3072 lds
v_mfma_f32_16x16x32_bf16 v[68:71], a[48:51], a[148:151], v[68:71]
ds_read_b64_tr_b16 a[18:19], v194 offset:2560
v_mfma_f32_16x16x32_bf16 v[72:75], a[48:51], a[152:155], v[72:75]
ds_read_b64_tr_b16 a[20:21], v195 offset:640
v_mfma_f32_16x16x32_bf16 v[76:79], a[48:51], a[156:159], v[76:79]
ds_read_b64_tr_b16 a[22:23], v195 offset:2688
v_mfma_f32_16x16x32_bf16 v[80:83], a[52:55], a[144:147], v[80:83]
ds_read_b64_tr_b16 a[24:25], v194 offset:768
s_add_u32 m0, s35, 114688
v_mfma_f32_16x16x32_bf16 v[84:87], a[52:55], a[148:151], v[84:87]
buffer_load_dwordx4 v204, s[12:15], s29 offen offset:0 lds
v_mfma_f32_16x16x32_bf16 v[88:91], a[52:55], a[152:155], v[88:91]
ds_read_b64_tr_b16 a[26:27], v194 offset:2816
v_mfma_f32_16x16x32_bf16 v[92:95], a[52:55], a[156:159], v[92:95]
ds_read_b64_tr_b16 a[28:29], v195 offset:896
v_mfma_f32_16x16x32_bf16 v[96:99], a[56:59], a[144:147], v[96:99]
ds_read_b64_tr_b16 a[30:31], v195 offset:2944
v_mfma_f32_16x16x32_bf16 v[100:103], a[56:59], a[148:151], v[100:103]
v_mfma_f32_16x16x32_bf16 v[104:107], a[56:59], a[152:155], v[104:107]
buffer_load_dwordx4 v204, s[16:19], s29 offen offset:1024 lds
v_mfma_f32_16x16x32_bf16 v[108:111], a[56:59], a[156:159], v[108:111]
v_mfma_f32_16x16x32_bf16 v[112:115], a[60:63], a[144:147], v[112:115]
v_mfma_f32_16x16x32_bf16 v[116:119], a[60:63], a[148:151], v[116:119]
v_mfma_f32_16x16x32_bf16 v[120:123], a[60:63], a[152:155], v[120:123]
v_mfma_f32_16x16x32_bf16 v[124:127], a[60:63], a[156:159], v[124:127]
buffer_load_dwordx4 v205, s[12:15], s29 offen offset:2048 lds
s_waitcnt lgkmcnt(0)
v_mfma_f32_16x16x32_bf16 v[0:3], a[0:3], a[128:131], v[0:3]
ds_read_b64_tr_b16 a[32:33], v194 offset:16384
v_mfma_f32_16x16x32_bf16 v[4:7], a[0:3], a[132:135], v[4:7]
ds_read_b64_tr_b16 a[34:35], v194 offset:18432
v_mfma_f32_16x16x32_bf16 v[8:11], a[0:3], a[136:139], v[8:11]
ds_read_b128 a[144:147], v193 offset:0
v_mfma_f32_16x16x32_bf16 v[12:15], a[0:3], a[140:143], v[12:15]
ds_read_b128 a[148:151], v193 offset:2048
v_mfma_f32_16x16x32_bf16 v[16:19], a[4:7], a[128:131], v[16:19]
buffer_load_dwordx4 v205, s[16:19], s29 offen offset:3072 lds
v_mfma_f32_16x16x32_bf16 v[20:23], a[4:7], a[132:135], v[20:23]
ds_read_b128 a[152:155], v193 offset:4096
v_mfma_f32_16x16x32_bf16 v[24:27], a[4:7], a[136:139], v[24:27]
ds_read_b128 a[156:159], v193 offset:6144
v_mfma_f32_16x16x32_bf16 v[28:31], a[4:7], a[140:143], v[28:31]
ds_read_b64_tr_b16 a[36:37], v195 offset:16512
v_mfma_f32_16x16x32_bf16 v[32:35], a[8:11], a[128:131], v[32:35]
ds_read_b64_tr_b16 a[38:39], v195 offset:18560
s_add_u32 m0, s35, 118784
v_mfma_f32_16x16x32_bf16 v[36:39], a[8:11], a[132:135], v[36:39]
buffer_load_dwordx4 v206, s[12:15], s30 offen offset:0 lds
v_mfma_f32_16x16x32_bf16 v[40:43], a[8:11], a[136:139], v[40:43]
ds_read_b64_tr_b16 a[40:41], v194 offset:16640
v_mfma_f32_16x16x32_bf16 v[44:47], a[8:11], a[140:143], v[44:47]
ds_read_b64_tr_b16 a[42:43], v194 offset:18688
v_mfma_f32_16x16x32_bf16 v[48:51], a[12:15], a[128:131], v[48:51]
ds_read_b64_tr_b16 a[44:45], v195 offset:16768
v_mfma_f32_16x16x32_bf16 v[52:55], a[12:15], a[132:135], v[52:55]
ds_read_b64_tr_b16 a[46:47], v195 offset:18816
v_mfma_f32_16x16x32_bf16 v[56:59], a[12:15], a[136:139], v[56:59]
buffer_load_dwordx4 v206, s[16:19], s30 offen offset:1024 lds
v_mfma_f32_16x16x32_bf16 v[60:63], a[12:15], a[140:143], v[60:63]
ds_read_b64_tr_b16 a[48:49], v194 offset:16896
v_mfma_f32_16x16x32_bf16 v[64:67], a[16:19], a[128:131], v[64:67]
ds_read_b64_tr_b16 a[50:51], v194 offset:18944
v_mfma_f32_16x16x32_bf16 v[68:71], a[16:19], a[132:135], v[68:71]
ds_read_b64_tr_b16 a[52:53], v195 offset:17024
v_mfma_f32_16x16x32_bf16 v[72:75], a[16:19], a[136:139], v[72:75]
ds_read_b64_tr_b16 a[54:55], v195 offset:19072
v_mfma_f32_16x16x32_bf16 v[76:79], a[16:19], a[140:143], v[76:79]
buffer_load_dwordx4 v207, s[12:15], s30 offen offset:2048 lds
v_mfma_f32_16x16x32_bf16 v[80:83], a[20:23], a[128:131], v[80:83]
ds_read_b64_tr_b16 a[56:57], v194 offset:17152
v_mfma_f32_16x16x32_bf16 v[84:87], a[20:23], a[132:135], v[84:87]
ds_read_b64_tr_b16 a[58:59], v194 offset:19200
v_mfma_f32_16x16x32_bf16 v[88:91], a[20:23], a[136:139], v[88:91]
ds_read_b64_tr_b16 a[60:61], v195 offset:17280
v_mfma_f32_16x16x32_bf16 v[92:95], a[20:23], a[140:143], v[92:95]
ds_read_b64_tr_b16 a[62:63], v195 offset:19328
v_mfma_f32_16x16x32_bf16 v[96:99], a[24:27], a[128:131], v[96:99]
buffer_load_dwordx4 v207, s[16:19], s30 offen offset:3072 lds
v_mfma_f32_16x16x32_bf16 v[100:103], a[24:27], a[132:135], v[100:103]
s_add_u32 s24, s24, 1
s_cmp_eq_u32 s24, 8
v_mfma_f32_16x16x32_bf16 v[104:107], a[24:27], a[136:139], v[104:107]
s_cselect_b32 s24, 1, s24
s_lshl_b32 s28, s24, 7
v_mfma_f32_16x16x32_bf16 v[108:111], a[24:27], a[140:143], v[108:111]
s_add_u32 s28, s28, s25
s_lshl_b32 s29, s24, 19
v_mfma_f32_16x16x32_bf16 v[112:115], a[28:31], a[128:131], v[112:115]
s_add_u32 s29, s29, s58
s_add_u32 s30, s29, 65536
v_mfma_f32_16x16x32_bf16 v[116:119], a[28:31], a[132:135], v[116:119]
v_mfma_f32_16x16x32_bf16 v[120:123], a[28:31], a[136:139], v[120:123]
v_mfma_f32_16x16x32_bf16 v[124:127], a[28:31], a[140:143], v[124:127]
s_waitcnt vmcnt(12) lgkmcnt(0)
s_barrier
s_mov_b32 s48, s68
s_and_b32 s49, s69, 0xffff
s_mov_b32 s50, 0x400000
s_mov_b32 s51, 0x20000
v_lshrrev_b32 v254, 4, v253
v_lshlrev_b32 v254, 6, v254
v_and_or_b32 v254, v253, 7, v254
v_lshlrev_b32 v254, 7, v254
s_lshr_b32 s46, s6, 3
s_lshl_b32 s46, s46, 2
s_add_u32 s46, s46, s7
s_lshl_b32 s46, s46, 15
s_and_b32 s47, s6, 7
s_lshl_b32 s47, s47, 10
s_add_u32 s46, s46, s47
buffer_load_dword a252, v254, s[48:51], s46 offen
v_add_u32 v192, 0xc000, v192
v_add_u32 v193, 0xc000, v193
v_add_u32 v194, 0xc000, v194
v_add_u32 v195, 0xc000, v195
s_nop 0
v_mfma_f32_16x16x32_bf16 v[0:3], a[32:35], a[144:147], v[0:3]
ds_read_b64_tr_b16 a[0:1], v194 offset:0
s_add_u32 m0, s34, 16384
v_mfma_f32_16x16x32_bf16 v[4:7], a[32:35], a[148:151], v[4:7]
buffer_load_dwordx4 v200, s[8:11], s28 offen offset:0 lds
v_mfma_f32_16x16x32_bf16 v[8:11], a[32:35], a[152:155], v[8:11]
ds_read_b64_tr_b16 a[2:3], v194 offset:2048
v_mfma_f32_16x16x32_bf16 v[12:15], a[32:35], a[156:159], v[12:15]
ds_read_b128 a[128:131], v192 offset:0
v_mfma_f32_16x16x32_bf16 v[16:19], a[36:39], a[144:147], v[16:19]
ds_read_b128 a[132:135], v192 offset:2048
v_mfma_f32_16x16x32_bf16 v[20:23], a[36:39], a[148:151], v[20:23]
ds_read_b128 a[136:139], v192 offset:4096
v_mfma_f32_16x16x32_bf16 v[24:27], a[36:39], a[152:155], v[24:27]
buffer_load_dwordx4 v201, s[8:11], s28 offen offset:1024 lds
v_mfma_f32_16x16x32_bf16 v[28:31], a[36:39], a[156:159], v[28:31]
ds_read_b128 a[140:143], v192 offset:6144
v_mfma_f32_16x16x32_bf16 v[32:35], a[40:43], a[144:147], v[32:35]
ds_read_b64_tr_b16 a[4:5], v195 offset:128
v_mfma_f32_16x16x32_bf16 v[36:39], a[40:43], a[148:151], v[36:39]
ds_read_b64_tr_b16 a[6:7], v195 offset:2176
v_mfma_f32_16x16x32_bf16 v[40:43], a[40:43], a[152:155], v[40:43]
ds_read_b64_tr_b16 a[8:9], v194 offset:256
v_mfma_f32_16x16x32_bf16 v[44:47], a[40:43], a[156:159], v[44:47]
buffer_load_dwordx4 v202, s[8:11], s28 offen offset:2048 lds
v_mfma_f32_16x16x32_bf16 v[48:51], a[44:47], a[144:147], v[48:51]
ds_read_b64_tr_b16 a[10:11], v194 offset:2304
v_mfma_f32_16x16x32_bf16 v[52:55], a[44:47], a[148:151], v[52:55]
ds_read_b64_tr_b16 a[12:13], v195 offset:384
v_mfma_f32_16x16x32_bf16 v[56:59], a[44:47], a[152:155], v[56:59]
ds_read_b64_tr_b16 a[14:15], v195 offset:2432
v_mfma_f32_16x16x32_bf16 v[60:63], a[44:47], a[156:159], v[60:63]
ds_read_b64_tr_b16 a[16:17], v194 offset:512
v_mfma_f32_16x16x32_bf16 v[64:67], a[48:51], a[144:147], v[64:67]
buffer_load_dwordx4 v203, s[8:11], s28 offen offset:3072 lds
v_mfma_f32_16x16x32_bf16 v[68:71], a[48:51], a[148:151], v[68:71]
ds_read_b64_tr_b16 a[18:19], v194 offset:2560
v_mfma_f32_16x16x32_bf16 v[72:75], a[48:51], a[152:155], v[72:75]
ds_read_b64_tr_b16 a[20:21], v195 offset:640
v_mfma_f32_16x16x32_bf16 v[76:79], a[48:51], a[156:159], v[76:79]
ds_read_b64_tr_b16 a[22:23], v195 offset:2688
v_mfma_f32_16x16x32_bf16 v[80:83], a[52:55], a[144:147], v[80:83]
ds_read_b64_tr_b16 a[24:25], v194 offset:768
s_add_u32 m0, s35, 16384
v_mfma_f32_16x16x32_bf16 v[84:87], a[52:55], a[148:151], v[84:87]
buffer_load_dwordx4 v204, s[12:15], s29 offen offset:0 lds
v_mfma_f32_16x16x32_bf16 v[88:91], a[52:55], a[152:155], v[88:91]
ds_read_b64_tr_b16 a[26:27], v194 offset:2816
v_mfma_f32_16x16x32_bf16 v[92:95], a[52:55], a[156:159], v[92:95]
ds_read_b64_tr_b16 a[28:29], v195 offset:896
v_mfma_f32_16x16x32_bf16 v[96:99], a[56:59], a[144:147], v[96:99]
ds_read_b64_tr_b16 a[30:31], v195 offset:2944
v_mfma_f32_16x16x32_bf16 v[100:103], a[56:59], a[148:151], v[100:103]
v_mfma_f32_16x16x32_bf16 v[104:107], a[56:59], a[152:155], v[104:107]
buffer_load_dwordx4 v204, s[16:19], s29 offen offset:1024 lds
v_mfma_f32_16x16x32_bf16 v[108:111], a[56:59], a[156:159], v[108:111]
v_mfma_f32_16x16x32_bf16 v[112:115], a[60:63], a[144:147], v[112:115]
v_mfma_f32_16x16x32_bf16 v[116:119], a[60:63], a[148:151], v[116:119]
v_mfma_f32_16x16x32_bf16 v[120:123], a[60:63], a[152:155], v[120:123]
v_mfma_f32_16x16x32_bf16 v[124:127], a[60:63], a[156:159], v[124:127]
buffer_load_dwordx4 v205, s[12:15], s29 offen offset:2048 lds
s_waitcnt lgkmcnt(0)
v_mfma_f32_16x16x32_bf16 v[0:3], a[0:3], a[128:131], v[0:3]
ds_read_b64_tr_b16 a[32:33], v194 offset:16384
v_mfma_f32_16x16x32_bf16 v[4:7], a[0:3], a[132:135], v[4:7]
ds_read_b64_tr_b16 a[34:35], v194 offset:18432
v_mfma_f32_16x16x32_bf16 v[8:11], a[0:3], a[136:139], v[8:11]
ds_read_b128 a[144:147], v193 offset:0
v_mfma_f32_16x16x32_bf16 v[12:15], a[0:3], a[140:143], v[12:15]
ds_read_b128 a[148:151], v193 offset:2048
v_mfma_f32_16x16x32_bf16 v[16:19], a[4:7], a[128:131], v[16:19]
buffer_load_dwordx4 v205, s[16:19], s29 offen offset:3072 lds
v_mfma_f32_16x16x32_bf16 v[20:23], a[4:7], a[132:135], v[20:23]
ds_read_b128 a[152:155], v193 offset:4096
v_mfma_f32_16x16x32_bf16 v[24:27], a[4:7], a[136:139], v[24:27]
ds_read_b128 a[156:159], v193 offset:6144
v_mfma_f32_16x16x32_bf16 v[28:31], a[4:7], a[140:143], v[28:31]
ds_read_b64_tr_b16 a[36:37], v195 offset:16512
v_mfma_f32_16x16x32_bf16 v[32:35], a[8:11], a[128:131], v[32:35]
ds_read_b64_tr_b16 a[38:39], v195 offset:18560
s_add_u32 m0, s35, 20480
v_mfma_f32_16x16x32_bf16 v[36:39], a[8:11], a[132:135], v[36:39]
buffer_load_dwordx4 v206, s[12:15], s30 offen offset:0 lds
v_mfma_f32_16x16x32_bf16 v[40:43], a[8:11], a[136:139], v[40:43]
ds_read_b64_tr_b16 a[40:41], v194 offset:16640
v_mfma_f32_16x16x32_bf16 v[44:47], a[8:11], a[140:143], v[44:47]
ds_read_b64_tr_b16 a[42:43], v194 offset:18688
v_mfma_f32_16x16x32_bf16 v[48:51], a[12:15], a[128:131], v[48:51]
ds_read_b64_tr_b16 a[44:45], v195 offset:16768
v_mfma_f32_16x16x32_bf16 v[52:55], a[12:15], a[132:135], v[52:55]
ds_read_b64_tr_b16 a[46:47], v195 offset:18816
v_mfma_f32_16x16x32_bf16 v[56:59], a[12:15], a[136:139], v[56:59]
buffer_load_dwordx4 v206, s[16:19], s30 offen offset:1024 lds
v_mfma_f32_16x16x32_bf16 v[60:63], a[12:15], a[140:143], v[60:63]
ds_read_b64_tr_b16 a[48:49], v194 offset:16896
v_mfma_f32_16x16x32_bf16 v[64:67], a[16:19], a[128:131], v[64:67]
ds_read_b64_tr_b16 a[50:51], v194 offset:18944
v_mfma_f32_16x16x32_bf16 v[68:71], a[16:19], a[132:135], v[68:71]
ds_read_b64_tr_b16 a[52:53], v195 offset:17024
v_mfma_f32_16x16x32_bf16 v[72:75], a[16:19], a[136:139], v[72:75]
ds_read_b64_tr_b16 a[54:55], v195 offset:19072
v_mfma_f32_16x16x32_bf16 v[76:79], a[16:19], a[140:143], v[76:79]
buffer_load_dwordx4 v207, s[12:15], s30 offen offset:2048 lds
v_mfma_f32_16x16x32_bf16 v[80:83], a[20:23], a[128:131], v[80:83]
ds_read_b64_tr_b16 a[56:57], v194 offset:17152
v_mfma_f32_16x16x32_bf16 v[84:87], a[20:23], a[132:135], v[84:87]
ds_read_b64_tr_b16 a[58:59], v194 offset:19200
v_mfma_f32_16x16x32_bf16 v[88:91], a[20:23], a[136:139], v[88:91]
ds_read_b64_tr_b16 a[60:61], v195 offset:17280
v_mfma_f32_16x16x32_bf16 v[92:95], a[20:23], a[140:143], v[92:95]
ds_read_b64_tr_b16 a[62:63], v195 offset:19328
v_mfma_f32_16x16x32_bf16 v[96:99], a[24:27], a[128:131], v[96:99]
buffer_load_dwordx4 v207, s[16:19], s30 offen offset:3072 lds
v_mfma_f32_16x16x32_bf16 v[100:103], a[24:27], a[132:135], v[100:103]
s_add_u32 s24, s24, 1
s_cmp_eq_u32 s24, 8
v_mfma_f32_16x16x32_bf16 v[104:107], a[24:27], a[136:139], v[104:107]
s_cselect_b32 s24, 1, s24
s_lshl_b32 s28, s24, 7
v_mfma_f32_16x16x32_bf16 v[108:111], a[24:27], a[140:143], v[108:111]
s_add_u32 s28, s28, s25
s_lshl_b32 s29, s24, 19
v_mfma_f32_16x16x32_bf16 v[112:115], a[28:31], a[128:131], v[112:115]
s_add_u32 s29, s29, s58
s_add_u32 s30, s29, 65536
v_mfma_f32_16x16x32_bf16 v[116:119], a[28:31], a[132:135], v[116:119]
v_mfma_f32_16x16x32_bf16 v[120:123], a[28:31], a[136:139], v[120:123]
v_mfma_f32_16x16x32_bf16 v[124:127], a[28:31], a[140:143], v[124:127]
s_waitcnt vmcnt(12) lgkmcnt(0)
s_barrier
v_add_u32 v192, 0xc000, v192
v_add_u32 v193, 0xc000, v193
v_add_u32 v194, 0xc000, v194
v_add_u32 v195, 0xc000, v195
s_nop 0
v_mfma_f32_16x16x32_bf16 v[0:3], a[32:35], a[144:147], v[0:3]
ds_read_b64_tr_b16 a[0:1], v194 offset:0
s_add_u32 m0, s34, 65536
v_mfma_f32_16x16x32_bf16 v[4:7], a[32:35], a[148:151], v[4:7]
buffer_load_dwordx4 v200, s[8:11], s28 offen offset:0 lds
v_mfma_f32_16x16x32_bf16 v[8:11], a[32:35], a[152:155], v[8:11]
ds_read_b64_tr_b16 a[2:3], v194 offset:2048
v_mfma_f32_16x16x32_bf16 v[12:15], a[32:35], a[156:159], v[12:15]
ds_read_b128 a[128:131], v192 offset:0
v_mfma_f32_16x16x32_bf16 v[16:19], a[36:39], a[144:147], v[16:19]
ds_read_b128 a[132:135], v192 offset:2048
v_mfma_f32_16x16x32_bf16 v[20:23], a[36:39], a[148:151], v[20:23]
ds_read_b128 a[136:139], v192 offset:4096
v_mfma_f32_16x16x32_bf16 v[24:27], a[36:39], a[152:155], v[24:27]
buffer_load_dwordx4 v201, s[8:11], s28 offen offset:1024 lds
v_mfma_f32_16x16x32_bf16 v[28:31], a[36:39], a[156:159], v[28:31]
ds_read_b128 a[140:143], v192 offset:6144
v_mfma_f32_16x16x32_bf16 v[32:35], a[40:43], a[144:147], v[32:35]
ds_read_b64_tr_b16 a[4:5], v195 offset:128
v_mfma_f32_16x16x32_bf16 v[36:39], a[40:43], a[148:151], v[36:39]
ds_read_b64_tr_b16 a[6:7], v195 offset:2176
v_mfma_f32_16x16x32_bf16 v[40:43], a[40:43], a[152:155], v[40:43]
ds_read_b64_tr_b16 a[8:9], v194 offset:256
v_mfma_f32_16x16x32_bf16 v[44:47], a[40:43], a[156:159], v[44:47]
buffer_load_dwordx4 v202, s[8:11], s28 offen offset:2048 lds
v_mfma_f32_16x16x32_bf16 v[48:51], a[44:47], a[144:147], v[48:51]
ds_read_b64_tr_b16 a[10:11], v194 offset:2304
v_mfma_f32_16x16x32_bf16 v[52:55], a[44:47], a[148:151], v[52:55]
ds_read_b64_tr_b16 a[12:13], v195 offset:384
v_mfma_f32_16x16x32_bf16 v[56:59], a[44:47], a[152:155], v[56:59]
ds_read_b64_tr_b16 a[14:15], v195 offset:2432
v_mfma_f32_16x16x32_bf16 v[60:63], a[44:47], a[156:159], v[60:63]
ds_read_b64_tr_b16 a[16:17], v194 offset:512
v_mfma_f32_16x16x32_bf16 v[64:67], a[48:51], a[144:147], v[64:67]
buffer_load_dwordx4 v203, s[8:11], s28 offen offset:3072 lds
v_mfma_f32_16x16x32_bf16 v[68:71], a[48:51], a[148:151], v[68:71]
ds_read_b64_tr_b16 a[18:19], v194 offset:2560
v_mfma_f32_16x16x32_bf16 v[72:75], a[48:51], a[152:155], v[72:75]
ds_read_b64_tr_b16 a[20:21], v195 offset:640
v_mfma_f32_16x16x32_bf16 v[76:79], a[48:51], a[156:159], v[76:79]
ds_read_b64_tr_b16 a[22:23], v195 offset:2688
v_mfma_f32_16x16x32_bf16 v[80:83], a[52:55], a[144:147], v[80:83]
ds_read_b64_tr_b16 a[24:25], v194 offset:768
s_add_u32 m0, s35, 65536
v_mfma_f32_16x16x32_bf16 v[84:87], a[52:55], a[148:151], v[84:87]
buffer_load_dwordx4 v204, s[12:15], s29 offen offset:0 lds
v_mfma_f32_16x16x32_bf16 v[88:91], a[52:55], a[152:155], v[88:91]
ds_read_b64_tr_b16 a[26:27], v194 offset:2816
v_mfma_f32_16x16x32_bf16 v[92:95], a[52:55], a[156:159], v[92:95]
ds_read_b64_tr_b16 a[28:29], v195 offset:896
v_mfma_f32_16x16x32_bf16 v[96:99], a[56:59], a[144:147], v[96:99]
ds_read_b64_tr_b16 a[30:31], v195 offset:2944
v_mfma_f32_16x16x32_bf16 v[100:103], a[56:59], a[148:151], v[100:103]
v_mfma_f32_16x16x32_bf16 v[104:107], a[56:59], a[152:155], v[104:107]
buffer_load_dwordx4 v204, s[16:19], s29 offen offset:1024 lds
v_mfma_f32_16x16x32_bf16 v[108:111], a[56:59], a[156:159], v[108:111]
v_mfma_f32_16x16x32_bf16 v[112:115], a[60:63], a[144:147], v[112:115]
v_mfma_f32_16x16x32_bf16 v[116:119], a[60:63], a[148:151], v[116:119]
v_mfma_f32_16x16x32_bf16 v[120:123], a[60:63], a[152:155], v[120:123]
v_mfma_f32_16x16x32_bf16 v[124:127], a[60:63], a[156:159], v[124:127]
buffer_load_dwordx4 v205, s[12:15], s29 offen offset:2048 lds
s_waitcnt lgkmcnt(0)
v_mfma_f32_16x16x32_bf16 v[0:3], a[0:3], a[128:131], v[0:3]
ds_read_b64_tr_b16 a[32:33], v194 offset:16384
v_mfma_f32_16x16x32_bf16 v[4:7], a[0:3], a[132:135], v[4:7]
ds_read_b64_tr_b16 a[34:35], v194 offset:18432
v_mfma_f32_16x16x32_bf16 v[8:11], a[0:3], a[136:139], v[8:11]
ds_read_b128 a[144:147], v193 offset:0
v_mfma_f32_16x16x32_bf16 v[12:15], a[0:3], a[140:143], v[12:15]
ds_read_b128 a[148:151], v193 offset:2048
v_mfma_f32_16x16x32_bf16 v[16:19], a[4:7], a[128:131], v[16:19]
buffer_load_dwordx4 v205, s[16:19], s29 offen offset:3072 lds
v_mfma_f32_16x16x32_bf16 v[20:23], a[4:7], a[132:135], v[20:23]
ds_read_b128 a[152:155], v193 offset:4096
v_mfma_f32_16x16x32_bf16 v[24:27], a[4:7], a[136:139], v[24:27]
ds_read_b128 a[156:159], v193 offset:6144
v_mfma_f32_16x16x32_bf16 v[28:31], a[4:7], a[140:143], v[28:31]
)ASM"
R"ASM(ds_read_b64_tr_b16 a[36:37], v195 offset:16512
v_mfma_f32_16x16x32_bf16 v[32:35], a[8:11], a[128:131], v[32:35]
ds_read_b64_tr_b16 a[38:39], v195 offset:18560
s_add_u32 m0, s35, 69632
v_mfma_f32_16x16x32_bf16 v[36:39], a[8:11], a[132:135], v[36:39]
buffer_load_dwordx4 v206, s[12:15], s30 offen offset:0 lds
v_mfma_f32_16x16x32_bf16 v[40:43], a[8:11], a[136:139], v[40:43]
ds_read_b64_tr_b16 a[40:41], v194 offset:16640
v_mfma_f32_16x16x32_bf16 v[44:47], a[8:11], a[140:143], v[44:47]
ds_read_b64_tr_b16 a[42:43], v194 offset:18688
v_mfma_f32_16x16x32_bf16 v[48:51], a[12:15], a[128:131], v[48:51]
ds_read_b64_tr_b16 a[44:45], v195 offset:16768
v_mfma_f32_16x16x32_bf16 v[52:55], a[12:15], a[132:135], v[52:55]
ds_read_b64_tr_b16 a[46:47], v195 offset:18816
v_mfma_f32_16x16x32_bf16 v[56:59], a[12:15], a[136:139], v[56:59]
buffer_load_dwordx4 v206, s[16:19], s30 offen offset:1024 lds
v_mfma_f32_16x16x32_bf16 v[60:63], a[12:15], a[140:143], v[60:63]
ds_read_b64_tr_b16 a[48:49], v194 offset:16896
v_mfma_f32_16x16x32_bf16 v[64:67], a[16:19], a[128:131], v[64:67]
ds_read_b64_tr_b16 a[50:51], v194 offset:18944
v_mfma_f32_16x16x32_bf16 v[68:71], a[16:19], a[132:135], v[68:71]
ds_read_b64_tr_b16 a[52:53], v195 offset:17024
v_mfma_f32_16x16x32_bf16 v[72:75], a[16:19], a[136:139], v[72:75]
ds_read_b64_tr_b16 a[54:55], v195 offset:19072
v_mfma_f32_16x16x32_bf16 v[76:79], a[16:19], a[140:143], v[76:79]
buffer_load_dwordx4 v207, s[12:15], s30 offen offset:2048 lds
v_mfma_f32_16x16x32_bf16 v[80:83], a[20:23], a[128:131], v[80:83]
ds_read_b64_tr_b16 a[56:57], v194 offset:17152
v_mfma_f32_16x16x32_bf16 v[84:87], a[20:23], a[132:135], v[84:87]
ds_read_b64_tr_b16 a[58:59], v194 offset:19200
v_mfma_f32_16x16x32_bf16 v[88:91], a[20:23], a[136:139], v[88:91]
ds_read_b64_tr_b16 a[60:61], v195 offset:17280
v_mfma_f32_16x16x32_bf16 v[92:95], a[20:23], a[140:143], v[92:95]
ds_read_b64_tr_b16 a[62:63], v195 offset:19328
v_mfma_f32_16x16x32_bf16 v[96:99], a[24:27], a[128:131], v[96:99]
buffer_load_dwordx4 v207, s[16:19], s30 offen offset:3072 lds
v_mfma_f32_16x16x32_bf16 v[100:103], a[24:27], a[132:135], v[100:103]
s_mov_b32 s24, 0
s_add_u32 s25, s25, 1048576
v_mfma_f32_16x16x32_bf16 v[104:107], a[24:27], a[136:139], v[104:107]
s_lshl_b32 s28, s24, 7
s_add_u32 s28, s28, s25
v_mfma_f32_16x16x32_bf16 v[108:111], a[24:27], a[140:143], v[108:111]
s_lshl_b32 s29, s24, 19
s_add_u32 s29, s29, s58
v_mfma_f32_16x16x32_bf16 v[112:115], a[28:31], a[128:131], v[112:115]
s_add_u32 s30, s29, 65536
v_mfma_f32_16x16x32_bf16 v[116:119], a[28:31], a[132:135], v[116:119]
v_mfma_f32_16x16x32_bf16 v[120:123], a[28:31], a[136:139], v[120:123]
v_mfma_f32_16x16x32_bf16 v[124:127], a[28:31], a[140:143], v[124:127]
s_waitcnt vmcnt(12) lgkmcnt(0)
s_barrier
v_add_u32 v192, 0xfffe8000, v192
v_add_u32 v193, 0xfffe8000, v193
v_add_u32 v194, 0xfffe8000, v194
v_add_u32 v195, 0xfffe8000, v195
s_nop 0
v_mfma_f32_16x16x32_bf16 v[0:3], a[32:35], a[144:147], v[0:3]
ds_read_b64_tr_b16 a[0:1], v194 offset:0
s_add_u32 m0, s34, 114688
v_mfma_f32_16x16x32_bf16 v[4:7], a[32:35], a[148:151], v[4:7]
buffer_load_dwordx4 v200, s[8:11], s28 offen offset:0 lds
v_mfma_f32_16x16x32_bf16 v[8:11], a[32:35], a[152:155], v[8:11]
ds_read_b64_tr_b16 a[2:3], v194 offset:2048
v_mfma_f32_16x16x32_bf16 v[12:15], a[32:35], a[156:159], v[12:15]
ds_read_b128 a[128:131], v192 offset:0
v_mfma_f32_16x16x32_bf16 v[16:19], a[36:39], a[144:147], v[16:19]
ds_read_b128 a[132:135], v192 offset:2048
v_mfma_f32_16x16x32_bf16 v[20:23], a[36:39], a[148:151], v[20:23]
ds_read_b128 a[136:139], v192 offset:4096
v_mfma_f32_16x16x32_bf16 v[24:27], a[36:39], a[152:155], v[24:27]
buffer_load_dwordx4 v201, s[8:11], s28 offen offset:1024 lds
v_mfma_f32_16x16x32_bf16 v[28:31], a[36:39], a[156:159], v[28:31]
ds_read_b128 a[140:143], v192 offset:6144
v_mfma_f32_16x16x32_bf16 v[32:35], a[40:43], a[144:147], v[32:35]
ds_read_b64_tr_b16 a[4:5], v195 offset:128
v_mfma_f32_16x16x32_bf16 v[36:39], a[40:43], a[148:151], v[36:39]
ds_read_b64_tr_b16 a[6:7], v195 offset:2176
v_mfma_f32_16x16x32_bf16 v[40:43], a[40:43], a[152:155], v[40:43]
ds_read_b64_tr_b16 a[8:9], v194 offset:256
v_mfma_f32_16x16x32_bf16 v[44:47], a[40:43], a[156:159], v[44:47]
buffer_load_dwordx4 v202, s[8:11], s28 offen offset:2048 lds
v_mfma_f32_16x16x32_bf16 v[48:51], a[44:47], a[144:147], v[48:51]
ds_read_b64_tr_b16 a[10:11], v194 offset:2304
v_mfma_f32_16x16x32_bf16 v[52:55], a[44:47], a[148:151], v[52:55]
ds_read_b64_tr_b16 a[12:13], v195 offset:384
v_mfma_f32_16x16x32_bf16 v[56:59], a[44:47], a[152:155], v[56:59]
ds_read_b64_tr_b16 a[14:15], v195 offset:2432
v_mfma_f32_16x16x32_bf16 v[60:63], a[44:47], a[156:159], v[60:63]
ds_read_b64_tr_b16 a[16:17], v194 offset:512
v_mfma_f32_16x16x32_bf16 v[64:67], a[48:51], a[144:147], v[64:67]
buffer_load_dwordx4 v203, s[8:11], s28 offen offset:3072 lds
v_mfma_f32_16x16x32_bf16 v[68:71], a[48:51], a[148:151], v[68:71]
ds_read_b64_tr_b16 a[18:19], v194 offset:2560
v_mfma_f32_16x16x32_bf16 v[72:75], a[48:51], a[152:155], v[72:75]
ds_read_b64_tr_b16 a[20:21], v195 offset:640
v_mfma_f32_16x16x32_bf16 v[76:79], a[48:51], a[156:159], v[76:79]
ds_read_b64_tr_b16 a[22:23], v195 offset:2688
v_mfma_f32_16x16x32_bf16 v[80:83], a[52:55], a[144:147], v[80:83]
ds_read_b64_tr_b16 a[24:25], v194 offset:768
s_add_u32 m0, s35, 114688
v_mfma_f32_16x16x32_bf16 v[84:87], a[52:55], a[148:151], v[84:87]
buffer_load_dwordx4 v204, s[12:15], s29 offen offset:0 lds
v_mfma_f32_16x16x32_bf16 v[88:91], a[52:55], a[152:155], v[88:91]
ds_read_b64_tr_b16 a[26:27], v194 offset:2816
v_mfma_f32_16x16x32_bf16 v[92:95], a[52:55], a[156:159], v[92:95]
ds_read_b64_tr_b16 a[28:29], v195 offset:896
v_mfma_f32_16x16x32_bf16 v[96:99], a[56:59], a[144:147], v[96:99]
ds_read_b64_tr_b16 a[30:31], v195 offset:2944
v_mfma_f32_16x16x32_bf16 v[100:103], a[56:59], a[148:151], v[100:103]
v_mfma_f32_16x16x32_bf16 v[104:107], a[56:59], a[152:155], v[104:107]
buffer_load_dwordx4 v204, s[16:19], s29 offen offset:1024 lds
v_mfma_f32_16x16x32_bf16 v[108:111], a[56:59], a[156:159], v[108:111]
v_mfma_f32_16x16x32_bf16 v[112:115], a[60:63], a[144:147], v[112:115]
v_mfma_f32_16x16x32_bf16 v[116:119], a[60:63], a[148:151], v[116:119]
v_mfma_f32_16x16x32_bf16 v[120:123], a[60:63], a[152:155], v[120:123]
v_mfma_f32_16x16x32_bf16 v[124:127], a[60:63], a[156:159], v[124:127]
buffer_load_dwordx4 v205, s[12:15], s29 offen offset:2048 lds
s_waitcnt lgkmcnt(0)
v_mfma_f32_16x16x32_bf16 v[0:3], a[0:3], a[128:131], v[0:3]
ds_read_b64_tr_b16 a[32:33], v194 offset:16384
v_mfma_f32_16x16x32_bf16 v[4:7], a[0:3], a[132:135], v[4:7]
ds_read_b64_tr_b16 a[34:35], v194 offset:18432
v_mfma_f32_16x16x32_bf16 v[8:11], a[0:3], a[136:139], v[8:11]
ds_read_b128 a[144:147], v193 offset:0
v_mfma_f32_16x16x32_bf16 v[12:15], a[0:3], a[140:143], v[12:15]
ds_read_b128 a[148:151], v193 offset:2048
v_mfma_f32_16x16x32_bf16 v[16:19], a[4:7], a[128:131], v[16:19]
buffer_load_dwordx4 v205, s[16:19], s29 offen offset:3072 lds
v_mfma_f32_16x16x32_bf16 v[20:23], a[4:7], a[132:135], v[20:23]
ds_read_b128 a[152:155], v193 offset:4096
v_mfma_f32_16x16x32_bf16 v[24:27], a[4:7], a[136:139], v[24:27]
ds_read_b128 a[156:159], v193 offset:6144
v_mfma_f32_16x16x32_bf16 v[28:31], a[4:7], a[140:143], v[28:31]
ds_read_b64_tr_b16 a[36:37], v195 offset:16512
v_mfma_f32_16x16x32_bf16 v[32:35], a[8:11], a[128:131], v[32:35]
ds_read_b64_tr_b16 a[38:39], v195 offset:18560
s_add_u32 m0, s35, 118784
v_mfma_f32_16x16x32_bf16 v[36:39], a[8:11], a[132:135], v[36:39]
buffer_load_dwordx4 v206, s[12:15], s30 offen offset:0 lds
v_mfma_f32_16x16x32_bf16 v[40:43], a[8:11], a[136:139], v[40:43]
ds_read_b64_tr_b16 a[40:41], v194 offset:16640
v_mfma_f32_16x16x32_bf16 v[44:47], a[8:11], a[140:143], v[44:47]
ds_read_b64_tr_b16 a[42:43], v194 offset:18688
v_mfma_f32_16x16x32_bf16 v[48:51], a[12:15], a[128:131], v[48:51]
ds_read_b64_tr_b16 a[44:45], v195 offset:16768
v_mfma_f32_16x16x32_bf16 v[52:55], a[12:15], a[132:135], v[52:55]
ds_read_b64_tr_b16 a[46:47], v195 offset:18816
v_mfma_f32_16x16x32_bf16 v[56:59], a[12:15], a[136:139], v[56:59]
buffer_load_dwordx4 v206, s[16:19], s30 offen offset:1024 lds
v_mfma_f32_16x16x32_bf16 v[60:63], a[12:15], a[140:143], v[60:63]
ds_read_b64_tr_b16 a[48:49], v194 offset:16896
v_mfma_f32_16x16x32_bf16 v[64:67], a[16:19], a[128:131], v[64:67]
ds_read_b64_tr_b16 a[50:51], v194 offset:18944
v_mfma_f32_16x16x32_bf16 v[68:71], a[16:19], a[132:135], v[68:71]
ds_read_b64_tr_b16 a[52:53], v195 offset:17024
v_mfma_f32_16x16x32_bf16 v[72:75], a[16:19], a[136:139], v[72:75]
ds_read_b64_tr_b16 a[54:55], v195 offset:19072
v_mfma_f32_16x16x32_bf16 v[76:79], a[16:19], a[140:143], v[76:79]
buffer_load_dwordx4 v207, s[12:15], s30 offen offset:2048 lds
v_mfma_f32_16x16x32_bf16 v[80:83], a[20:23], a[128:131], v[80:83]
ds_read_b64_tr_b16 a[56:57], v194 offset:17152
v_mfma_f32_16x16x32_bf16 v[84:87], a[20:23], a[132:135], v[84:87]
ds_read_b64_tr_b16 a[58:59], v194 offset:19200
v_mfma_f32_16x16x32_bf16 v[88:91], a[20:23], a[136:139], v[88:91]
ds_read_b64_tr_b16 a[60:61], v195 offset:17280
v_mfma_f32_16x16x32_bf16 v[92:95], a[20:23], a[140:143], v[92:95]
ds_read_b64_tr_b16 a[62:63], v195 offset:19328
v_mfma_f32_16x16x32_bf16 v[96:99], a[24:27], a[128:131], v[96:99]
buffer_load_dwordx4 v207, s[16:19], s30 offen offset:3072 lds
v_mfma_f32_16x16x32_bf16 v[100:103], a[24:27], a[132:135], v[100:103]
s_mov_b32 s24, s98
s_lshl_b32 s28, s24, 7
v_mfma_f32_16x16x32_bf16 v[104:107], a[24:27], a[136:139], v[104:107]
s_add_u32 s28, s28, s25
s_lshl_b32 s29, s24, 19
v_mfma_f32_16x16x32_bf16 v[108:111], a[24:27], a[140:143], v[108:111]
s_add_u32 s29, s29, s58
s_add_u32 s30, s29, 65536
v_mfma_f32_16x16x32_bf16 v[112:115], a[28:31], a[128:131], v[112:115]
v_mfma_f32_16x16x32_bf16 v[116:119], a[28:31], a[132:135], v[116:119]
v_mfma_f32_16x16x32_bf16 v[120:123], a[28:31], a[136:139], v[120:123]
v_mfma_f32_16x16x32_bf16 v[124:127], a[28:31], a[140:143], v[124:127]
s_waitcnt vmcnt(12) lgkmcnt(0)
s_barrier
v_add_u32 v192, 0xc000, v192
v_add_u32 v193, 0xc000, v193
v_add_u32 v194, 0xc000, v194
v_add_u32 v195, 0xc000, v195
s_nop 0
v_mfma_f32_16x16x32_bf16 v[0:3], a[32:35], a[144:147], v[0:3]
ds_read_b64_tr_b16 a[0:1], v194 offset:0
s_add_u32 m0, s34, 16384
v_mfma_f32_16x16x32_bf16 v[4:7], a[32:35], a[148:151], v[4:7]
buffer_load_dwordx4 v200, s[8:11], s28 offen offset:0 lds
v_mfma_f32_16x16x32_bf16 v[8:11], a[32:35], a[152:155], v[8:11]
ds_read_b64_tr_b16 a[2:3], v194 offset:2048
v_mfma_f32_16x16x32_bf16 v[12:15], a[32:35], a[156:159], v[12:15]
ds_read_b128 a[128:131], v192 offset:0
v_mfma_f32_16x16x32_bf16 v[16:19], a[36:39], a[144:147], v[16:19]
ds_read_b128 a[132:135], v192 offset:2048
v_mfma_f32_16x16x32_bf16 v[20:23], a[36:39], a[148:151], v[20:23]
ds_read_b128 a[136:139], v192 offset:4096
v_mfma_f32_16x16x32_bf16 v[24:27], a[36:39], a[152:155], v[24:27]
buffer_load_dwordx4 v201, s[8:11], s28 offen offset:1024 lds
v_mfma_f32_16x16x32_bf16 v[28:31], a[36:39], a[156:159], v[28:31]
ds_read_b128 a[140:143], v192 offset:6144
v_mfma_f32_16x16x32_bf16 v[32:35], a[40:43], a[144:147], v[32:35]
ds_read_b64_tr_b16 a[4:5], v195 offset:128
v_mfma_f32_16x16x32_bf16 v[36:39], a[40:43], a[148:151], v[36:39]
ds_read_b64_tr_b16 a[6:7], v195 offset:2176
v_mfma_f32_16x16x32_bf16 v[40:43], a[40:43], a[152:155], v[40:43]
ds_read_b64_tr_b16 a[8:9], v194 offset:256
v_mfma_f32_16x16x32_bf16 v[44:47], a[40:43], a[156:159], v[44:47]
buffer_load_dwordx4 v202, s[8:11], s28 offen offset:2048 lds
v_mfma_f32_16x16x32_bf16 v[48:51], a[44:47], a[144:147], v[48:51]
ds_read_b64_tr_b16 a[10:11], v194 offset:2304
v_mfma_f32_16x16x32_bf16 v[52:55], a[44:47], a[148:151], v[52:55]
ds_read_b64_tr_b16 a[12:13], v195 offset:384
v_mfma_f32_16x16x32_bf16 v[56:59], a[44:47], a[152:155], v[56:59]
ds_read_b64_tr_b16 a[14:15], v195 offset:2432
v_mfma_f32_16x16x32_bf16 v[60:63], a[44:47], a[156:159], v[60:63]
ds_read_b64_tr_b16 a[16:17], v194 offset:512
v_mfma_f32_16x16x32_bf16 v[64:67], a[48:51], a[144:147], v[64:67]
buffer_load_dwordx4 v203, s[8:11], s28 offen offset:3072 lds
v_mfma_f32_16x16x32_bf16 v[68:71], a[48:51], a[148:151], v[68:71]
ds_read_b64_tr_b16 a[18:19], v194 offset:2560
v_mfma_f32_16x16x32_bf16 v[72:75], a[48:51], a[152:155], v[72:75]
ds_read_b64_tr_b16 a[20:21], v195 offset:640
v_mfma_f32_16x16x32_bf16 v[76:79], a[48:51], a[156:159], v[76:79]
ds_read_b64_tr_b16 a[22:23], v195 offset:2688
v_mfma_f32_16x16x32_bf16 v[80:83], a[52:55], a[144:147], v[80:83]
ds_read_b64_tr_b16 a[24:25], v194 offset:768
s_add_u32 m0, s35, 16384
v_mfma_f32_16x16x32_bf16 v[84:87], a[52:55], a[148:151], v[84:87]
buffer_load_dwordx4 v204, s[12:15], s29 offen offset:0 lds
v_mfma_f32_16x16x32_bf16 v[88:91], a[52:55], a[152:155], v[88:91]
ds_read_b64_tr_b16 a[26:27], v194 offset:2816
v_mfma_f32_16x16x32_bf16 v[92:95], a[52:55], a[156:159], v[92:95]
ds_read_b64_tr_b16 a[28:29], v195 offset:896
v_mfma_f32_16x16x32_bf16 v[96:99], a[56:59], a[144:147], v[96:99]
ds_read_b64_tr_b16 a[30:31], v195 offset:2944
v_mfma_f32_16x16x32_bf16 v[100:103], a[56:59], a[148:151], v[100:103]
v_mfma_f32_16x16x32_bf16 v[104:107], a[56:59], a[152:155], v[104:107]
buffer_load_dwordx4 v204, s[16:19], s29 offen offset:1024 lds
v_mfma_f32_16x16x32_bf16 v[108:111], a[56:59], a[156:159], v[108:111]
v_mfma_f32_16x16x32_bf16 v[112:115], a[60:63], a[144:147], v[112:115]
v_mfma_f32_16x16x32_bf16 v[116:119], a[60:63], a[148:151], v[116:119]
v_mfma_f32_16x16x32_bf16 v[120:123], a[60:63], a[152:155], v[120:123]
v_mfma_f32_16x16x32_bf16 v[124:127], a[60:63], a[156:159], v[124:127]
buffer_load_dwordx4 v205, s[12:15], s29 offen offset:2048 lds
s_waitcnt lgkmcnt(0)
v_mfma_f32_16x16x32_bf16 v[0:3], a[0:3], a[128:131], v[0:3]
ds_read_b64_tr_b16 a[32:33], v194 offset:16384
v_mfma_f32_16x16x32_bf16 v[4:7], a[0:3], a[132:135], v[4:7]
ds_read_b64_tr_b16 a[34:35], v194 offset:18432
v_mfma_f32_16x16x32_bf16 v[8:11], a[0:3], a[136:139], v[8:11]
ds_read_b128 a[144:147], v193 offset:0
v_mfma_f32_16x16x32_bf16 v[12:15], a[0:3], a[140:143], v[12:15]
ds_read_b128 a[148:151], v193 offset:2048
v_mfma_f32_16x16x32_bf16 v[16:19], a[4:7], a[128:131], v[16:19]
buffer_load_dwordx4 v205, s[16:19], s29 offen offset:3072 lds
v_mfma_f32_16x16x32_bf16 v[20:23], a[4:7], a[132:135], v[20:23]
ds_read_b128 a[152:155], v193 offset:4096
v_mfma_f32_16x16x32_bf16 v[24:27], a[4:7], a[136:139], v[24:27]
ds_read_b128 a[156:159], v193 offset:6144
v_mfma_f32_16x16x32_bf16 v[28:31], a[4:7], a[140:143], v[28:31]
ds_read_b64_tr_b16 a[36:37], v195 offset:16512
v_mfma_f32_16x16x32_bf16 v[32:35], a[8:11], a[128:131], v[32:35]
ds_read_b64_tr_b16 a[38:39], v195 offset:18560
s_add_u32 m0, s35, 20480
v_mfma_f32_16x16x32_bf16 v[36:39], a[8:11], a[132:135], v[36:39]
buffer_load_dwordx4 v206, s[12:15], s30 offen offset:0 lds
v_mfma_f32_16x16x32_bf16 v[40:43], a[8:11], a[136:139], v[40:43]
ds_read_b64_tr_b16 a[40:41], v194 offset:16640
v_mfma_f32_16x16x32_bf16 v[44:47], a[8:11], a[140:143], v[44:47]
ds_read_b64_tr_b16 a[42:43], v194 offset:18688
v_mfma_f32_16x16x32_bf16 v[48:51], a[12:15], a[128:131], v[48:51]
ds_read_b64_tr_b16 a[44:45], v195 offset:16768
v_mfma_f32_16x16x32_bf16 v[52:55], a[12:15], a[132:135], v[52:55]
ds_read_b64_tr_b16 a[46:47], v195 offset:18816
v_mfma_f32_16x16x32_bf16 v[56:59], a[12:15], a[136:139], v[56:59]
buffer_load_dwordx4 v206, s[16:19], s30 offen offset:1024 lds
v_mfma_f32_16x16x32_bf16 v[60:63], a[12:15], a[140:143], v[60:63]
ds_read_b64_tr_b16 a[48:49], v194 offset:16896
v_mfma_f32_16x16x32_bf16 v[64:67], a[16:19], a[128:131], v[64:67]
ds_read_b64_tr_b16 a[50:51], v194 offset:18944
v_mfma_f32_16x16x32_bf16 v[68:71], a[16:19], a[132:135], v[68:71]
ds_read_b64_tr_b16 a[52:53], v195 offset:17024
v_mfma_f32_16x16x32_bf16 v[72:75], a[16:19], a[136:139], v[72:75]
ds_read_b64_tr_b16 a[54:55], v195 offset:19072
v_mfma_f32_16x16x32_bf16 v[76:79], a[16:19], a[140:143], v[76:79]
buffer_load_dwordx4 v207, s[12:15], s30 offen offset:2048 lds
v_mfma_f32_16x16x32_bf16 v[80:83], a[20:23], a[128:131], v[80:83]
ds_read_b64_tr_b16 a[56:57], v194 offset:17152
v_mfma_f32_16x16x32_bf16 v[84:87], a[20:23], a[132:135], v[84:87]
ds_read_b64_tr_b16 a[58:59], v194 offset:19200
v_mfma_f32_16x16x32_bf16 v[88:91], a[20:23], a[136:139], v[88:91]
ds_read_b64_tr_b16 a[60:61], v195 offset:17280
v_mfma_f32_16x16x32_bf16 v[92:95], a[20:23], a[140:143], v[92:95]
ds_read_b64_tr_b16 a[62:63], v195 offset:19328
v_mfma_f32_16x16x32_bf16 v[96:99], a[24:27], a[128:131], v[96:99]
buffer_load_dwordx4 v207, s[16:19], s30 offen offset:3072 lds
v_mfma_f32_16x16x32_bf16 v[100:103], a[24:27], a[132:135], v[100:103]
s_add_u32 s24, s24, 1
s_cmp_eq_u32 s24, 8
v_mfma_f32_16x16x32_bf16 v[104:107], a[24:27], a[136:139], v[104:107]
s_cselect_b32 s24, 1, s24
s_lshl_b32 s28, s24, 7
v_mfma_f32_16x16x32_bf16 v[108:111], a[24:27], a[140:143], v[108:111]
s_add_u32 s28, s28, s25
s_lshl_b32 s29, s24, 19
v_mfma_f32_16x16x32_bf16 v[112:115], a[28:31], a[128:131], v[112:115]
s_add_u32 s29, s29, s58
s_add_u32 s30, s29, 65536
v_mfma_f32_16x16x32_bf16 v[116:119], a[28:31], a[132:135], v[116:119]
v_mfma_f32_16x16x32_bf16 v[120:123], a[28:31], a[136:139], v[120:123]
v_mfma_f32_16x16x32_bf16 v[124:127], a[28:31], a[140:143], v[124:127]
s_waitcnt vmcnt(12) lgkmcnt(0)
s_barrier
v_add_u32 v192, 0xc000, v192
v_add_u32 v193, 0xc000, v193
v_add_u32 v194, 0xc000, v194
v_add_u32 v195, 0xc000, v195
s_nop 0
v_mfma_f32_16x16x32_bf16 v[0:3], a[32:35], a[144:147], v[0:3]
ds_read_b64_tr_b16 a[0:1], v194 offset:0
s_add_u32 m0, s34, 65536
v_mfma_f32_16x16x32_bf16 v[4:7], a[32:35], a[148:151], v[4:7]
buffer_load_dwordx4 v200, s[8:11], s28 offen offset:0 lds
v_mfma_f32_16x16x32_bf16 v[8:11], a[32:35], a[152:155], v[8:11]
ds_read_b64_tr_b16 a[2:3], v194 offset:2048
v_mfma_f32_16x16x32_bf16 v[12:15], a[32:35], a[156:159], v[12:15]
ds_read_b128 a[128:131], v192 offset:0
v_mfma_f32_16x16x32_bf16 v[16:19], a[36:39], a[144:147], v[16:19]
ds_read_b128 a[132:135], v192 offset:2048
v_mfma_f32_16x16x32_bf16 v[20:23], a[36:39], a[148:151], v[20:23]
ds_read_b128 a[136:139], v192 offset:4096
v_mfma_f32_16x16x32_bf16 v[24:27], a[36:39], a[152:155], v[24:27]
buffer_load_dwordx4 v201, s[8:11], s28 offen offset:1024 lds
v_mfma_f32_16x16x32_bf16 v[28:31], a[36:39], a[156:159], v[28:31]
ds_read_b128 a[140:143], v192 offset:6144
v_mfma_f32_16x16x32_bf16 v[32:35], a[40:43], a[144:147], v[32:35]
ds_read_b64_tr_b16 a[4:5], v195 offset:128
v_mfma_f32_16x16x32_bf16 v[36:39], a[40:43], a[148:151], v[36:39]
ds_read_b64_tr_b16 a[6:7], v195 offset:2176
v_cvt_pk_bf16_f32 v128, v0, v1
v_cvt_pk_bf16_f32 v129, v2, v3
v_mfma_f32_16x16x32_bf16 v[40:43], a[40:43], a[152:155], v[40:43]
ds_read_b64_tr_b16 a[8:9], v194 offset:256
v_cvt_pk_bf16_f32 v130, v16, v17
v_cvt_pk_bf16_f32 v131, v18, v19
v_mfma_f32_16x16x32_bf16 v[44:47], a[40:43], a[156:159], v[44:47]
buffer_load_dwordx4 v202, s[8:11], s28 offen offset:2048 lds
v_cvt_pk_bf16_f32 v144, v4, v5
v_cvt_pk_bf16_f32 v145, v6, v7
v_mfma_f32_16x16x32_bf16 v[48:51], a[44:47], a[144:147], v[48:51]
ds_read_b64_tr_b16 a[10:11], v194 offset:2304
v_cvt_pk_bf16_f32 v146, v20, v21
v_cvt_pk_bf16_f32 v147, v22, v23
v_mfma_f32_16x16x32_bf16 v[52:55], a[44:47], a[148:151], v[52:55]
ds_read_b64_tr_b16 a[12:13], v195 offset:384
v_cvt_pk_bf16_f32 v160, v8, v9
v_cvt_pk_bf16_f32 v161, v10, v11
v_mfma_f32_16x16x32_bf16 v[56:59], a[44:47], a[152:155], v[56:59]
ds_read_b64_tr_b16 a[14:15], v195 offset:2432
v_cvt_pk_bf16_f32 v162, v24, v25
v_cvt_pk_bf16_f32 v163, v26, v27
v_mfma_f32_16x16x32_bf16 v[60:63], a[44:47], a[156:159], v[60:63]
ds_read_b64_tr_b16 a[16:17], v194 offset:512
v_cvt_pk_bf16_f32 v176, v12, v13
v_cvt_pk_bf16_f32 v177, v14, v15
v_mfma_f32_16x16x32_bf16 v[64:67], a[48:51], a[144:147], v[64:67]
buffer_load_dwordx4 v203, s[8:11], s28 offen offset:3072 lds
v_cvt_pk_bf16_f32 v178, v28, v29
v_cvt_pk_bf16_f32 v179, v30, v31
v_mfma_f32_16x16x32_bf16 v[68:71], a[48:51], a[148:151], v[68:71]
ds_read_b64_tr_b16 a[18:19], v194 offset:2560
v_cvt_pk_bf16_f32 v132, v32, v33
v_cvt_pk_bf16_f32 v133, v34, v35
v_mfma_f32_16x16x32_bf16 v[72:75], a[48:51], a[152:155], v[72:75]
ds_read_b64_tr_b16 a[20:21], v195 offset:640
v_cvt_pk_bf16_f32 v134, v48, v49
v_cvt_pk_bf16_f32 v135, v50, v51
v_mfma_f32_16x16x32_bf16 v[76:79], a[48:51], a[156:159], v[76:79]
ds_read_b64_tr_b16 a[22:23], v195 offset:2688
v_cvt_pk_bf16_f32 v148, v36, v37
v_cvt_pk_bf16_f32 v149, v38, v39
v_mfma_f32_16x16x32_bf16 v[80:83], a[52:55], a[144:147], v[80:83]
ds_read_b64_tr_b16 a[24:25], v194 offset:768
s_add_u32 m0, s35, 65536
v_cvt_pk_bf16_f32 v150, v52, v53
v_cvt_pk_bf16_f32 v151, v54, v55
v_mfma_f32_16x16x32_bf16 v[84:87], a[52:55], a[148:151], v[84:87]
buffer_load_dwordx4 v204, s[12:15], s29 offen offset:0 lds
v_cvt_pk_bf16_f32 v164, v40, v41
v_cvt_pk_bf16_f32 v165, v42, v43
v_mfma_f32_16x16x32_bf16 v[88:91], a[52:55], a[152:155], v[88:91]
ds_read_b64_tr_b16 a[26:27], v194 offset:2816
v_cvt_pk_bf16_f32 v166, v56, v57
v_cvt_pk_bf16_f32 v167, v58, v59
v_mfma_f32_16x16x32_bf16 v[92:95], a[52:55], a[156:159], v[92:95]
ds_read_b64_tr_b16 a[28:29], v195 offset:896
v_cvt_pk_bf16_f32 v180, v44, v45
v_cvt_pk_bf16_f32 v181, v46, v47
v_mfma_f32_16x16x32_bf16 v[96:99], a[56:59], a[144:147], v[96:99]
ds_read_b64_tr_b16 a[30:31], v195 offset:2944
v_cvt_pk_bf16_f32 v182, v60, v61
v_cvt_pk_bf16_f32 v183, v62, v63
v_mfma_f32_16x16x32_bf16 v[100:103], a[56:59], a[148:151], v[100:103]
v_cvt_pk_bf16_f32 v136, v64, v65
v_cvt_pk_bf16_f32 v137, v66, v67
v_mfma_f32_16x16x32_bf16 v[104:107], a[56:59], a[152:155], v[104:107]
buffer_load_dwordx4 v204, s[16:19], s29 offen offset:1024 lds
v_cvt_pk_bf16_f32 v138, v80, v81
v_cvt_pk_bf16_f32 v139, v82, v83
v_mfma_f32_16x16x32_bf16 v[108:111], a[56:59], a[156:159], v[108:111]
v_cvt_pk_bf16_f32 v152, v68, v69
v_cvt_pk_bf16_f32 v153, v70, v71
v_mfma_f32_16x16x32_bf16 v[112:115], a[60:63], a[144:147], v[112:115]
v_cvt_pk_bf16_f32 v154, v84, v85
v_cvt_pk_bf16_f32 v155, v86, v87
v_mfma_f32_16x16x32_bf16 v[116:119], a[60:63], a[148:151], v[116:119]
v_cvt_pk_bf16_f32 v168, v72, v73
v_cvt_pk_bf16_f32 v169, v74, v75
v_mfma_f32_16x16x32_bf16 v[120:123], a[60:63], a[152:155], v[120:123]
v_cvt_pk_bf16_f32 v170, v88, v89
v_cvt_pk_bf16_f32 v171, v90, v91
v_mfma_f32_16x16x32_bf16 v[124:127], a[60:63], a[156:159], v[124:127]
buffer_load_dwordx4 v205, s[12:15], s29 offen offset:2048 lds
v_cvt_pk_bf16_f32 v184, v76, v77
v_cvt_pk_bf16_f32 v185, v78, v79
s_waitcnt lgkmcnt(0)
v_mfma_f32_16x16x32_bf16 v[0:3], a[0:3], a[128:131], 0
ds_read_b64_tr_b16 a[32:33], v194 offset:16384
v_cvt_pk_bf16_f32 v186, v92, v93
v_cvt_pk_bf16_f32 v187, v94, v95
v_mfma_f32_16x16x32_bf16 v[4:7], a[0:3], a[132:135], 0
ds_read_b64_tr_b16 a[34:35], v194 offset:18432
v_cvt_pk_bf16_f32 v140, v96, v97
v_cvt_pk_bf16_f32 v141, v98, v99
v_mfma_f32_16x16x32_bf16 v[8:11], a[0:3], a[136:139], 0
ds_read_b128 a[144:147], v193 offset:0
v_cvt_pk_bf16_f32 v142, v112, v113
v_cvt_pk_bf16_f32 v143, v114, v115
v_mfma_f32_16x16x32_bf16 v[12:15], a[0:3], a[140:143], 0
ds_read_b128 a[148:151], v193 offset:2048
v_cvt_pk_bf16_f32 v156, v100, v101
v_cvt_pk_bf16_f32 v157, v102, v103
v_mfma_f32_16x16x32_bf16 v[16:19], a[4:7], a[128:131], 0
buffer_load_dwordx4 v205, s[16:19], s29 offen offset:3072 lds
v_cvt_pk_bf16_f32 v158, v116, v117
v_cvt_pk_bf16_f32 v159, v118, v119
v_mfma_f32_16x16x32_bf16 v[20:23], a[4:7], a[132:135], 0
ds_read_b128 a[152:155], v193 offset:4096
v_cvt_pk_bf16_f32 v172, v104, v105
v_cvt_pk_bf16_f32 v173, v106, v107
v_mfma_f32_16x16x32_bf16 v[24:27], a[4:7], a[136:139], 0
ds_read_b128 a[156:159], v193 offset:6144
v_cvt_pk_bf16_f32 v174, v120, v121
v_cvt_pk_bf16_f32 v175, v122, v123
v_mfma_f32_16x16x32_bf16 v[28:31], a[4:7], a[140:143], 0
ds_read_b64_tr_b16 a[36:37], v195 offset:16512
v_cvt_pk_bf16_f32 v188, v108, v109
v_cvt_pk_bf16_f32 v189, v110, v111
v_mfma_f32_16x16x32_bf16 v[32:35], a[8:11], a[128:131], 0
ds_read_b64_tr_b16 a[38:39], v195 offset:18560
s_add_u32 m0, s35, 69632
v_cvt_pk_bf16_f32 v190, v124, v125
v_cvt_pk_bf16_f32 v191, v126, v127
v_mfma_f32_16x16x32_bf16 v[36:39], a[8:11], a[132:135], 0
buffer_load_dwordx4 v206, s[12:15], s30 offen offset:0 lds
v_mfma_f32_16x16x32_bf16 v[40:43], a[8:11], a[136:139], 0
ds_read_b64_tr_b16 a[40:41], v194 offset:16640
v_mfma_f32_16x16x32_bf16 v[44:47], a[8:11], a[140:143], 0
ds_read_b64_tr_b16 a[42:43], v194 offset:18688
v_mfma_f32_16x16x32_bf16 v[48:51], a[12:15], a[128:131], 0
ds_read_b64_tr_b16 a[44:45], v195 offset:16768
v_mfma_f32_16x16x32_bf16 v[52:55], a[12:15], a[132:135], 0
ds_read_b64_tr_b16 a[46:47], v195 offset:18816
v_mfma_f32_16x16x32_bf16 v[56:59], a[12:15], a[136:139], 0
buffer_load_dwordx4 v206, s[16:19], s30 offen offset:1024 lds
v_mfma_f32_16x16x32_bf16 v[60:63], a[12:15], a[140:143], 0
ds_read_b64_tr_b16 a[48:49], v194 offset:16896
v_mfma_f32_16x16x32_bf16 v[64:67], a[16:19], a[128:131], 0
ds_read_b64_tr_b16 a[50:51], v194 offset:18944
v_mfma_f32_16x16x32_bf16 v[68:71], a[16:19], a[132:135], 0
ds_read_b64_tr_b16 a[52:53], v195 offset:17024
v_mfma_f32_16x16x32_bf16 v[72:75], a[16:19], a[136:139], 0
ds_read_b64_tr_b16 a[54:55], v195 offset:19072
v_mfma_f32_16x16x32_bf16 v[76:79], a[16:19], a[140:143], 0
buffer_load_dwordx4 v207, s[12:15], s30 offen offset:2048 lds
v_mfma_f32_16x16x32_bf16 v[80:83], a[20:23], a[128:131], 0
ds_read_b64_tr_b16 a[56:57], v194 offset:17152
v_mfma_f32_16x16x32_bf16 v[84:87], a[20:23], a[132:135], 0
ds_read_b64_tr_b16 a[58:59], v194 offset:19200
v_mfma_f32_16x16x32_bf16 v[88:91], a[20:23], a[136:139], 0
ds_read_b64_tr_b16 a[60:61], v195 offset:17280
v_mfma_f32_16x16x32_bf16 v[92:95], a[20:23], a[140:143], 0
ds_read_b64_tr_b16 a[62:63], v195 offset:19328
v_mfma_f32_16x16x32_bf16 v[96:99], a[24:27], a[128:131], 0
buffer_load_dwordx4 v207, s[16:19], s30 offen offset:3072 lds
v_mfma_f32_16x16x32_bf16 v[100:103], a[24:27], a[132:135], 0
s_add_u32 s24, s24, 1
s_cmp_eq_u32 s24, 8
v_mfma_f32_16x16x32_bf16 v[104:107], a[24:27], a[136:139], 0
s_cselect_b32 s24, 1, s24
s_lshl_b32 s28, s24, 7
v_mfma_f32_16x16x32_bf16 v[108:111], a[24:27], a[140:143], 0
s_add_u32 s28, s28, s25
s_lshl_b32 s29, s24, 19
v_mfma_f32_16x16x32_bf16 v[112:115], a[28:31], a[128:131], 0
s_add_u32 s29, s29, s58
s_add_u32 s30, s29, 65536
v_mfma_f32_16x16x32_bf16 v[116:119], a[28:31], a[132:135], 0
v_mfma_f32_16x16x32_bf16 v[120:123], a[28:31], a[136:139], 0
v_mfma_f32_16x16x32_bf16 v[124:127], a[28:31], a[140:143], 0
s_waitcnt vmcnt(12) lgkmcnt(0)
s_barrier
v_add_u32 v192, 0xfffe8000, v192
v_add_u32 v193, 0xfffe8000, v193
v_add_u32 v194, 0xfffe8000, v194
v_add_u32 v195, 0xfffe8000, v195
s_nop 0
v_mfma_f32_16x16x32_bf16 v[0:3], a[32:35], a[144:147], v[0:3]
ds_read_b64_tr_b16 a[0:1], v194 offset:0
s_add_u32 m0, s34, 114688
v_mfma_f32_16x16x32_bf16 v[4:7], a[32:35], a[148:151], v[4:7]
buffer_load_dwordx4 v200, s[8:11], s28 offen offset:0 lds
v_mfma_f32_16x16x32_bf16 v[8:11], a[32:35], a[152:155], v[8:11]
ds_read_b64_tr_b16 a[2:3], v194 offset:2048
v_mfma_f32_16x16x32_bf16 v[12:15], a[32:35], a[156:159], v[12:15]
ds_read_b128 a[128:131], v192 offset:0
v_mfma_f32_16x16x32_bf16 v[16:19], a[36:39], a[144:147], v[16:19]
ds_read_b128 a[132:135], v192 offset:2048
v_mfma_f32_16x16x32_bf16 v[20:23], a[36:39], a[148:151], v[20:23]
ds_read_b128 a[136:139], v192 offset:4096
v_mfma_f32_16x16x32_bf16 v[24:27], a[36:39], a[152:155], v[24:27]
buffer_load_dwordx4 v201, s[8:11], s28 offen offset:1024 lds
v_mfma_f32_16x16x32_bf16 v[28:31], a[36:39], a[156:159], v[28:31]
ds_read_b128 a[140:143], v192 offset:6144
v_mfma_f32_16x16x32_bf16 v[32:35], a[40:43], a[144:147], v[32:35]
ds_read_b64_tr_b16 a[4:5], v195 offset:128
v_mfma_f32_16x16x32_bf16 v[36:39], a[40:43], a[148:151], v[36:39]
ds_read_b64_tr_b16 a[6:7], v195 offset:2176
v_mfma_f32_16x16x32_bf16 v[40:43], a[40:43], a[152:155], v[40:43]
ds_read_b64_tr_b16 a[8:9], v194 offset:256
v_mfma_f32_16x16x32_bf16 v[44:47], a[40:43], a[156:159], v[44:47]
buffer_load_dwordx4 v202, s[8:11], s28 offen offset:2048 lds
v_mfma_f32_16x16x32_bf16 v[48:51], a[44:47], a[144:147], v[48:51]
ds_read_b64_tr_b16 a[10:11], v194 offset:2304
v_mfma_f32_16x16x32_bf16 v[52:55], a[44:47], a[148:151], v[52:55]
ds_read_b64_tr_b16 a[12:13], v195 offset:384
v_mfma_f32_16x16x32_bf16 v[56:59], a[44:47], a[152:155], v[56:59]
ds_read_b64_tr_b16 a[14:15], v195 offset:2432
v_mfma_f32_16x16x32_bf16 v[60:63], a[44:47], a[156:159], v[60:63]
ds_read_b64_tr_b16 a[16:17], v194 offset:512
v_mfma_f32_16x16x32_bf16 v[64:67], a[48:51], a[144:147], v[64:67]
buffer_load_dwordx4 v203, s[8:11], s28 offen offset:3072 lds
v_mfma_f32_16x16x32_bf16 v[68:71], a[48:51], a[148:151], v[68:71]
ds_read_b64_tr_b16 a[18:19], v194 offset:2560
v_mfma_f32_16x16x32_bf16 v[72:75], a[48:51], a[152:155], v[72:75]
ds_read_b64_tr_b16 a[20:21], v195 offset:640
v_mfma_f32_16x16x32_bf16 v[76:79], a[48:51], a[156:159], v[76:79]
ds_read_b64_tr_b16 a[22:23], v195 offset:2688
v_mfma_f32_16x16x32_bf16 v[80:83], a[52:55], a[144:147], v[80:83]
ds_read_b64_tr_b16 a[24:25], v194 offset:768
s_add_u32 m0, s35, 114688
v_mfma_f32_16x16x32_bf16 v[84:87], a[52:55], a[148:151], v[84:87]
buffer_load_dwordx4 v204, s[12:15], s29 offen offset:0 lds
v_mfma_f32_16x16x32_bf16 v[88:91], a[52:55], a[152:155], v[88:91]
ds_read_b64_tr_b16 a[26:27], v194 offset:2816
v_mfma_f32_16x16x32_bf16 v[92:95], a[52:55], a[156:159], v[92:95]
ds_read_b64_tr_b16 a[28:29], v195 offset:896
v_mfma_f32_16x16x32_bf16 v[96:99], a[56:59], a[144:147], v[96:99]
ds_read_b64_tr_b16 a[30:31], v195 offset:2944
v_mfma_f32_16x16x32_bf16 v[100:103], a[56:59], a[148:151], v[100:103]
v_mfma_f32_16x16x32_bf16 v[104:107], a[56:59], a[152:155], v[104:107]
buffer_load_dwordx4 v204, s[16:19], s29 offen offset:1024 lds
v_mfma_f32_16x16x32_bf16 v[108:111], a[56:59], a[156:159], v[108:111]
v_mfma_f32_16x16x32_bf16 v[112:115], a[60:63], a[144:147], v[112:115]
v_mfma_f32_16x16x32_bf16 v[116:119], a[60:63], a[148:151], v[116:119]
v_mfma_f32_16x16x32_bf16 v[120:123], a[60:63], a[152:155], v[120:123]
v_mfma_f32_16x16x32_bf16 v[124:127], a[60:63], a[156:159], v[124:127]
buffer_load_dwordx4 v205, s[12:15], s29 offen offset:2048 lds
s_waitcnt lgkmcnt(0)
v_mfma_f32_16x16x32_bf16 v[0:3], a[0:3], a[128:131], v[0:3]
ds_read_b64_tr_b16 a[32:33], v194 offset:16384
v_mfma_f32_16x16x32_bf16 v[4:7], a[0:3], a[132:135], v[4:7]
ds_read_b64_tr_b16 a[34:35], v194 offset:18432
v_mfma_f32_16x16x32_bf16 v[8:11], a[0:3], a[136:139], v[8:11]
ds_read_b128 a[144:147], v193 offset:0
v_mfma_f32_16x16x32_bf16 v[12:15], a[0:3], a[140:143], v[12:15]
ds_read_b128 a[148:151], v193 offset:2048
v_mfma_f32_16x16x32_bf16 v[16:19], a[4:7], a[128:131], v[16:19]
buffer_load_dwordx4 v205, s[16:19], s29 offen offset:3072 lds
v_mfma_f32_16x16x32_bf16 v[20:23], a[4:7], a[132:135], v[20:23]
ds_read_b128 a[152:155], v193 offset:4096
v_mfma_f32_16x16x32_bf16 v[24:27], a[4:7], a[136:139], v[24:27]
ds_read_b128 a[156:159], v193 offset:6144
v_mfma_f32_16x16x32_bf16 v[28:31], a[4:7], a[140:143], v[28:31]
ds_read_b64_tr_b16 a[36:37], v195 offset:16512
v_mfma_f32_16x16x32_bf16 v[32:35], a[8:11], a[128:131], v[32:35]
ds_read_b64_tr_b16 a[38:39], v195 offset:18560
s_add_u32 m0, s35, 118784
v_mfma_f32_16x16x32_bf16 v[36:39], a[8:11], a[132:135], v[36:39]
buffer_load_dwordx4 v206, s[12:15], s30 offen offset:0 lds
v_mfma_f32_16x16x32_bf16 v[40:43], a[8:11], a[136:139], v[40:43]
ds_read_b64_tr_b16 a[40:41], v194 offset:16640
v_mfma_f32_16x16x32_bf16 v[44:47], a[8:11], a[140:143], v[44:47]
ds_read_b64_tr_b16 a[42:43], v194 offset:18688
v_mfma_f32_16x16x32_bf16 v[48:51], a[12:15], a[128:131], v[48:51]
ds_read_b64_tr_b16 a[44:45], v195 offset:16768
v_mfma_f32_16x16x32_bf16 v[52:55], a[12:15], a[132:135], v[52:55]
ds_read_b64_tr_b16 a[46:47], v195 offset:18816
v_mfma_f32_16x16x32_bf16 v[56:59], a[12:15], a[136:139], v[56:59]
buffer_load_dwordx4 v206, s[16:19], s30 offen offset:1024 lds
v_mfma_f32_16x16x32_bf16 v[60:63], a[12:15], a[140:143], v[60:63]
ds_read_b64_tr_b16 a[48:49], v194 offset:16896
v_mfma_f32_16x16x32_bf16 v[64:67], a[16:19], a[128:131], v[64:67]
ds_read_b64_tr_b16 a[50:51], v194 offset:18944
v_mfma_f32_16x16x32_bf16 v[68:71], a[16:19], a[132:135], v[68:71]
ds_read_b64_tr_b16 a[52:53], v195 offset:17024
v_mfma_f32_16x16x32_bf16 v[72:75], a[16:19], a[136:139], v[72:75]
ds_read_b64_tr_b16 a[54:55], v195 offset:19072
v_mfma_f32_16x16x32_bf16 v[76:79], a[16:19], a[140:143], v[76:79]
buffer_load_dwordx4 v207, s[12:15], s30 offen offset:2048 lds
v_mfma_f32_16x16x32_bf16 v[80:83], a[20:23], a[128:131], v[80:83]
ds_read_b64_tr_b16 a[56:57], v194 offset:17152
v_mfma_f32_16x16x32_bf16 v[84:87], a[20:23], a[132:135], v[84:87]
ds_read_b64_tr_b16 a[58:59], v194 offset:19200
v_mfma_f32_16x16x32_bf16 v[88:91], a[20:23], a[136:139], v[88:91]
ds_read_b64_tr_b16 a[60:61], v195 offset:17280
v_mfma_f32_16x16x32_bf16 v[92:95], a[20:23], a[140:143], v[92:95]
ds_read_b64_tr_b16 a[62:63], v195 offset:19328
v_mfma_f32_16x16x32_bf16 v[96:99], a[24:27], a[128:131], v[96:99]
buffer_load_dwordx4 v207, s[16:19], s30 offen offset:3072 lds
v_mfma_f32_16x16x32_bf16 v[100:103], a[24:27], a[132:135], v[100:103]
s_add_u32 s24, s24, 1
s_cmp_eq_u32 s24, 8
v_mfma_f32_16x16x32_bf16 v[104:107], a[24:27], a[136:139], v[104:107]
s_cselect_b32 s24, 1, s24
s_lshl_b32 s28, s24, 7
v_mfma_f32_16x16x32_bf16 v[108:111], a[24:27], a[140:143], v[108:111]
s_add_u32 s28, s28, s25
s_lshl_b32 s29, s24, 19
v_mfma_f32_16x16x32_bf16 v[112:115], a[28:31], a[128:131], v[112:115]
s_add_u32 s29, s29, s58
s_add_u32 s30, s29, 65536
v_mfma_f32_16x16x32_bf16 v[116:119], a[28:31], a[132:135], v[116:119]
s_add_u32 s61, s62, 0
buffer_store_dwordx4 v[128:131], v252, s[40:43], s61 offen sc1
s_add_u32 s61, s62, 1024
buffer_store_dwordx4 v[132:135], v252, s[40:43], s61 offen sc1
v_mfma_f32_16x16x32_bf16 v[120:123], a[28:31], a[136:139], v[120:123]
v_mfma_f32_16x16x32_bf16 v[124:127], a[28:31], a[140:143], v[124:127]
s_waitcnt vmcnt(14) lgkmcnt(0)
s_barrier
v_add_u32 v192, 0xc000, v192
v_add_u32 v193, 0xc000, v193
v_add_u32 v194, 0xc000, v194
v_add_u32 v195, 0xc000, v195
s_nop 0
v_mfma_f32_16x16x32_bf16 v[0:3], a[32:35], a[144:147], v[0:3]
ds_read_b64_tr_b16 a[0:1], v194 offset:0
s_add_u32 m0, s34, 16384
v_mfma_f32_16x16x32_bf16 v[4:7], a[32:35], a[148:151], v[4:7]
buffer_load_dwordx4 v200, s[8:11], s28 offen offset:0 lds
v_mfma_f32_16x16x32_bf16 v[8:11], a[32:35], a[152:155], v[8:11]
ds_read_b64_tr_b16 a[2:3], v194 offset:2048
v_mfma_f32_16x16x32_bf16 v[12:15], a[32:35], a[156:159], v[12:15]
ds_read_b128 a[128:131], v192 offset:0
v_mfma_f32_16x16x32_bf16 v[16:19], a[36:39], a[144:147], v[16:19]
ds_read_b128 a[132:135], v192 offset:2048
v_mfma_f32_16x16x32_bf16 v[20:23], a[36:39], a[148:151], v[20:23]
ds_read_b128 a[136:139], v192 offset:4096
v_mfma_f32_16x16x32_bf16 v[24:27], a[36:39], a[152:155], v[24:27]
buffer_load_dwordx4 v201, s[8:11], s28 offen offset:1024 lds
v_mfma_f32_16x16x32_bf16 v[28:31], a[36:39], a[156:159], v[28:31]
ds_read_b128 a[140:143], v192 offset:6144
v_mfma_f32_16x16x32_bf16 v[32:35], a[40:43], a[144:147], v[32:35]
ds_read_b64_tr_b16 a[4:5], v195 offset:128
v_mfma_f32_16x16x32_bf16 v[36:39], a[40:43], a[148:151], v[36:39]
ds_read_b64_tr_b16 a[6:7], v195 offset:2176
v_mfma_f32_16x16x32_bf16 v[40:43], a[40:43], a[152:155], v[40:43]
ds_read_b64_tr_b16 a[8:9], v194 offset:256
v_mfma_f32_16x16x32_bf16 v[44:47], a[40:43], a[156:159], v[44:47]
buffer_load_dwordx4 v202, s[8:11], s28 offen offset:2048 lds
v_mfma_f32_16x16x32_bf16 v[48:51], a[44:47], a[144:147], v[48:51]
ds_read_b64_tr_b16 a[10:11], v194 offset:2304
v_mfma_f32_16x16x32_bf16 v[52:55], a[44:47], a[148:151], v[52:55]
ds_read_b64_tr_b16 a[12:13], v195 offset:384
v_mfma_f32_16x16x32_bf16 v[56:59], a[44:47], a[152:155], v[56:59]
ds_read_b64_tr_b16 a[14:15], v195 offset:2432
v_mfma_f32_16x16x32_bf16 v[60:63], a[44:47], a[156:159], v[60:63]
ds_read_b64_tr_b16 a[16:17], v194 offset:512
v_mfma_f32_16x16x32_bf16 v[64:67], a[48:51], a[144:147], v[64:67]
buffer_load_dwordx4 v203, s[8:11], s28 offen offset:3072 lds
v_mfma_f32_16x16x32_bf16 v[68:71], a[48:51], a[148:151], v[68:71]
ds_read_b64_tr_b16 a[18:19], v194 offset:2560
v_mfma_f32_16x16x32_bf16 v[72:75], a[48:51], a[152:155], v[72:75]
ds_read_b64_tr_b16 a[20:21], v195 offset:640
v_mfma_f32_16x16x32_bf16 v[76:79], a[48:51], a[156:159], v[76:79]
ds_read_b64_tr_b16 a[22:23], v195 offset:2688
v_mfma_f32_16x16x32_bf16 v[80:83], a[52:55], a[144:147], v[80:83]
ds_read_b64_tr_b16 a[24:25], v194 offset:768
s_add_u32 m0, s35, 16384
v_mfma_f32_16x16x32_bf16 v[84:87], a[52:55], a[148:151], v[84:87]
buffer_load_dwordx4 v204, s[12:15], s29 offen offset:0 lds
v_mfma_f32_16x16x32_bf16 v[88:91], a[52:55], a[152:155], v[88:91]
ds_read_b64_tr_b16 a[26:27], v194 offset:2816
v_mfma_f32_16x16x32_bf16 v[92:95], a[52:55], a[156:159], v[92:95]
ds_read_b64_tr_b16 a[28:29], v195 offset:896
v_mfma_f32_16x16x32_bf16 v[96:99], a[56:59], a[144:147], v[96:99]
ds_read_b64_tr_b16 a[30:31], v195 offset:2944
v_mfma_f32_16x16x32_bf16 v[100:103], a[56:59], a[148:151], v[100:103]
v_mfma_f32_16x16x32_bf16 v[104:107], a[56:59], a[152:155], v[104:107]
buffer_load_dwordx4 v204, s[16:19], s29 offen offset:1024 lds
v_mfma_f32_16x16x32_bf16 v[108:111], a[56:59], a[156:159], v[108:111]
v_mfma_f32_16x16x32_bf16 v[112:115], a[60:63], a[144:147], v[112:115]
v_mfma_f32_16x16x32_bf16 v[116:119], a[60:63], a[148:151], v[116:119]
v_mfma_f32_16x16x32_bf16 v[120:123], a[60:63], a[152:155], v[120:123]
v_mfma_f32_16x16x32_bf16 v[124:127], a[60:63], a[156:159], v[124:127]
buffer_load_dwordx4 v205, s[12:15], s29 offen offset:2048 lds
s_waitcnt lgkmcnt(0)
v_mfma_f32_16x16x32_bf16 v[0:3], a[0:3], a[128:131], v[0:3]
ds_read_b64_tr_b16 a[32:33], v194 offset:16384
v_mfma_f32_16x16x32_bf16 v[4:7], a[0:3], a[132:135], v[4:7]
ds_read_b64_tr_b16 a[34:35], v194 offset:18432
v_mfma_f32_16x16x32_bf16 v[8:11], a[0:3], a[136:139], v[8:11]
ds_read_b128 a[144:147], v193 offset:0
v_mfma_f32_16x16x32_bf16 v[12:15], a[0:3], a[140:143], v[12:15]
ds_read_b128 a[148:151], v193 offset:2048
v_mfma_f32_16x16x32_bf16 v[16:19], a[4:7], a[128:131], v[16:19]
buffer_load_dwordx4 v205, s[16:19], s29 offen offset:3072 lds
v_mfma_f32_16x16x32_bf16 v[20:23], a[4:7], a[132:135], v[20:23]
ds_read_b128 a[152:155], v193 offset:4096
v_mfma_f32_16x16x32_bf16 v[24:27], a[4:7], a[136:139], v[24:27]
ds_read_b128 a[156:159], v193 offset:6144
v_mfma_f32_16x16x32_bf16 v[28:31], a[4:7], a[140:143], v[28:31]
ds_read_b64_tr_b16 a[36:37], v195 offset:16512
v_mfma_f32_16x16x32_bf16 v[32:35], a[8:11], a[128:131], v[32:35]
ds_read_b64_tr_b16 a[38:39], v195 offset:18560
s_add_u32 m0, s35, 20480
v_mfma_f32_16x16x32_bf16 v[36:39], a[8:11], a[132:135], v[36:39]
buffer_load_dwordx4 v206, s[12:15], s30 offen offset:0 lds
v_mfma_f32_16x16x32_bf16 v[40:43], a[8:11], a[136:139], v[40:43]
ds_read_b64_tr_b16 a[40:41], v194 offset:16640
v_mfma_f32_16x16x32_bf16 v[44:47], a[8:11], a[140:143], v[44:47]
ds_read_b64_tr_b16 a[42:43], v194 offset:18688
v_mfma_f32_16x16x32_bf16 v[48:51], a[12:15], a[128:131], v[48:51]
ds_read_b64_tr_b16 a[44:45], v195 offset:16768
v_mfma_f32_16x16x32_bf16 v[52:55], a[12:15], a[132:135], v[52:55]
ds_read_b64_tr_b16 a[46:47], v195 offset:18816
v_mfma_f32_16x16x32_bf16 v[56:59], a[12:15], a[136:139], v[56:59]
buffer_load_dwordx4 v206, s[16:19], s30 offen offset:1024 lds
v_mfma_f32_16x16x32_bf16 v[60:63], a[12:15], a[140:143], v[60:63]
ds_read_b64_tr_b16 a[48:49], v194 offset:16896
v_mfma_f32_16x16x32_bf16 v[64:67], a[16:19], a[128:131], v[64:67]
ds_read_b64_tr_b16 a[50:51], v194 offset:18944
v_mfma_f32_16x16x32_bf16 v[68:71], a[16:19], a[132:135], v[68:71]
ds_read_b64_tr_b16 a[52:53], v195 offset:17024
v_mfma_f32_16x16x32_bf16 v[72:75], a[16:19], a[136:139], v[72:75]
ds_read_b64_tr_b16 a[54:55], v195 offset:19072
v_mfma_f32_16x16x32_bf16 v[76:79], a[16:19], a[140:143], v[76:79]
buffer_load_dwordx4 v207, s[12:15], s30 offen offset:2048 lds
v_mfma_f32_16x16x32_bf16 v[80:83], a[20:23], a[128:131], v[80:83]
ds_read_b64_tr_b16 a[56:57], v194 offset:17152
v_mfma_f32_16x16x32_bf16 v[84:87], a[20:23], a[132:135], v[84:87]
ds_read_b64_tr_b16 a[58:59], v194 offset:19200
v_mfma_f32_16x16x32_bf16 v[88:91], a[20:23], a[136:139], v[88:91]
ds_read_b64_tr_b16 a[60:61], v195 offset:17280
v_mfma_f32_16x16x32_bf16 v[92:95], a[20:23], a[140:143], v[92:95]
ds_read_b64_tr_b16 a[62:63], v195 offset:19328
v_mfma_f32_16x16x32_bf16 v[96:99], a[24:27], a[128:131], v[96:99]
buffer_load_dwordx4 v207, s[16:19], s30 offen offset:3072 lds
v_mfma_f32_16x16x32_bf16 v[100:103], a[24:27], a[132:135], v[100:103]
s_add_u32 s24, s24, 1
s_cmp_eq_u32 s24, 8
v_mfma_f32_16x16x32_bf16 v[104:107], a[24:27], a[136:139], v[104:107]
s_cselect_b32 s24, 1, s24
s_lshl_b32 s28, s24, 7
v_mfma_f32_16x16x32_bf16 v[108:111], a[24:27], a[140:143], v[108:111]
s_add_u32 s28, s28, s25
s_lshl_b32 s29, s24, 19
v_mfma_f32_16x16x32_bf16 v[112:115], a[28:31], a[128:131], v[112:115]
s_add_u32 s29, s29, s58
s_add_u32 s30, s29, 65536
v_mfma_f32_16x16x32_bf16 v[116:119], a[28:31], a[132:135], v[116:119]
s_add_u32 s61, s62, 2048
buffer_store_dwordx4 v[136:139], v252, s[40:43], s61 offen sc1
s_add_u32 s61, s62, 3072
buffer_store_dwordx4 v[140:143], v252, s[40:43], s61 offen sc1
v_mfma_f32_16x16x32_bf16 v[120:123], a[28:31], a[136:139], v[120:123]
v_mfma_f32_16x16x32_bf16 v[124:127], a[28:31], a[140:143], v[124:127]
s_waitcnt vmcnt(16) lgkmcnt(0)
s_barrier
v_add_u32 v192, 0xc000, v192
v_add_u32 v193, 0xc000, v193
v_add_u32 v194, 0xc000, v194
v_add_u32 v195, 0xc000, v195
s_nop 0
v_mfma_f32_16x16x32_bf16 v[0:3], a[32:35], a[144:147], v[0:3]
ds_read_b64_tr_b16 a[0:1], v194 offset:0
s_add_u32 m0, s34, 65536
v_mfma_f32_16x16x32_bf16 v[4:7], a[32:35], a[148:151], v[4:7]
buffer_load_dwordx4 v200, s[8:11], s28 offen offset:0 lds
v_mfma_f32_16x16x32_bf16 v[8:11], a[32:35], a[152:155], v[8:11]
ds_read_b64_tr_b16 a[2:3], v194 offset:2048
v_mfma_f32_16x16x32_bf16 v[12:15], a[32:35], a[156:159], v[12:15]
ds_read_b128 a[128:131], v192 offset:0
v_mfma_f32_16x16x32_bf16 v[16:19], a[36:39], a[144:147], v[16:19]
ds_read_b128 a[132:135], v192 offset:2048
v_mfma_f32_16x16x32_bf16 v[20:23], a[36:39], a[148:151], v[20:23]
ds_read_b128 a[136:139], v192 offset:4096
v_mfma_f32_16x16x32_bf16 v[24:27], a[36:39], a[152:155], v[24:27]
buffer_load_dwordx4 v201, s[8:11], s28 offen offset:1024 lds
v_mfma_f32_16x16x32_bf16 v[28:31], a[36:39], a[156:159], v[28:31]
ds_read_b128 a[140:143], v192 offset:6144
v_mfma_f32_16x16x32_bf16 v[32:35], a[40:43], a[144:147], v[32:35]
ds_read_b64_tr_b16 a[4:5], v195 offset:128
v_mfma_f32_16x16x32_bf16 v[36:39], a[40:43], a[148:151], v[36:39]
ds_read_b64_tr_b16 a[6:7], v195 offset:2176
v_mfma_f32_16x16x32_bf16 v[40:43], a[40:43], a[152:155], v[40:43]
ds_read_b64_tr_b16 a[8:9], v194 offset:256
v_mfma_f32_16x16x32_bf16 v[44:47], a[40:43], a[156:159], v[44:47]
buffer_load_dwordx4 v202, s[8:11], s28 offen offset:2048 lds
v_mfma_f32_16x16x32_bf16 v[48:51], a[44:47], a[144:147], v[48:51]
ds_read_b64_tr_b16 a[10:11], v194 offset:2304
v_mfma_f32_16x16x32_bf16 v[52:55], a[44:47], a[148:151], v[52:55]
ds_read_b64_tr_b16 a[12:13], v195 offset:384
v_mfma_f32_16x16x32_bf16 v[56:59], a[44:47], a[152:155], v[56:59]
ds_read_b64_tr_b16 a[14:15], v195 offset:2432
v_mfma_f32_16x16x32_bf16 v[60:63], a[44:47], a[156:159], v[60:63]
ds_read_b64_tr_b16 a[16:17], v194 offset:512
v_mfma_f32_16x16x32_bf16 v[64:67], a[48:51], a[144:147], v[64:67]
buffer_load_dwordx4 v203, s[8:11], s28 offen offset:3072 lds
v_mfma_f32_16x16x32_bf16 v[68:71], a[48:51], a[148:151], v[68:71]
ds_read_b64_tr_b16 a[18:19], v194 offset:2560
v_mfma_f32_16x16x32_bf16 v[72:75], a[48:51], a[152:155], v[72:75]
ds_read_b64_tr_b16 a[20:21], v195 offset:640
v_mfma_f32_16x16x32_bf16 v[76:79], a[48:51], a[156:159], v[76:79]
ds_read_b64_tr_b16 a[22:23], v195 offset:2688
v_mfma_f32_16x16x32_bf16 v[80:83], a[52:55], a[144:147], v[80:83]
ds_read_b64_tr_b16 a[24:25], v194 offset:768
s_add_u32 m0, s35, 65536
v_mfma_f32_16x16x32_bf16 v[84:87], a[52:55], a[148:151], v[84:87]
buffer_load_dwordx4 v204, s[12:15], s29 offen offset:0 lds
v_mfma_f32_16x16x32_bf16 v[88:91], a[52:55], a[152:155], v[88:91]
ds_read_b64_tr_b16 a[26:27], v194 offset:2816
v_mfma_f32_16x16x32_bf16 v[92:95], a[52:55], a[156:159], v[92:95]
ds_read_b64_tr_b16 a[28:29], v195 offset:896
v_mfma_f32_16x16x32_bf16 v[96:99], a[56:59], a[144:147], v[96:99]
ds_read_b64_tr_b16 a[30:31], v195 offset:2944
v_mfma_f32_16x16x32_bf16 v[100:103], a[56:59], a[148:151], v[100:103]
v_mfma_f32_16x16x32_bf16 v[104:107], a[56:59], a[152:155], v[104:107]
buffer_load_dwordx4 v204, s[16:19], s29 offen offset:1024 lds
v_mfma_f32_16x16x32_bf16 v[108:111], a[56:59], a[156:159], v[108:111]
v_mfma_f32_16x16x32_bf16 v[112:115], a[60:63], a[144:147], v[112:115]
v_mfma_f32_16x16x32_bf16 v[116:119], a[60:63], a[148:151], v[116:119]
v_mfma_f32_16x16x32_bf16 v[120:123], a[60:63], a[152:155], v[120:123]
v_mfma_f32_16x16x32_bf16 v[124:127], a[60:63], a[156:159], v[124:127]
buffer_load_dwordx4 v205, s[12:15], s29 offen offset:2048 lds
s_waitcnt lgkmcnt(0)
v_mfma_f32_16x16x32_bf16 v[0:3], a[0:3], a[128:131], v[0:3]
ds_read_b64_tr_b16 a[32:33], v194 offset:16384
v_mfma_f32_16x16x32_bf16 v[4:7], a[0:3], a[132:135], v[4:7]
ds_read_b64_tr_b16 a[34:35], v194 offset:18432
v_mfma_f32_16x16x32_bf16 v[8:11], a[0:3], a[136:139], v[8:11]
ds_read_b128 a[144:147], v193 offset:0
v_mfma_f32_16x16x32_bf16 v[12:15], a[0:3], a[140:143], v[12:15]
ds_read_b128 a[148:151], v193 offset:2048
v_mfma_f32_16x16x32_bf16 v[16:19], a[4:7], a[128:131], v[16:19]
buffer_load_dwordx4 v205, s[16:19], s29 offen offset:3072 lds
v_mfma_f32_16x16x32_bf16 v[20:23], a[4:7], a[132:135], v[20:23]
ds_read_b128 a[152:155], v193 offset:4096
v_mfma_f32_16x16x32_bf16 v[24:27], a[4:7], a[136:139], v[24:27]
ds_read_b128 a[156:159], v193 offset:6144
v_mfma_f32_16x16x32_bf16 v[28:31], a[4:7], a[140:143], v[28:31]
ds_read_b64_tr_b16 a[36:37], v195 offset:16512
v_mfma_f32_16x16x32_bf16 v[32:35], a[8:11], a[128:131], v[32:35]
ds_read_b64_tr_b16 a[38:39], v195 offset:18560
s_add_u32 m0, s35, 69632
v_mfma_f32_16x16x32_bf16 v[36:39], a[8:11], a[132:135], v[36:39]
buffer_load_dwordx4 v206, s[12:15], s30 offen offset:0 lds
v_mfma_f32_16x16x32_bf16 v[40:43], a[8:11], a[136:139], v[40:43]
ds_read_b64_tr_b16 a[40:41], v194 offset:16640
v_mfma_f32_16x16x32_bf16 v[44:47], a[8:11], a[140:143], v[44:47]
ds_read_b64_tr_b16 a[42:43], v194 offset:18688
v_mfma_f32_16x16x32_bf16 v[48:51], a[12:15], a[128:131], v[48:51]
ds_read_b64_tr_b16 a[44:45], v195 offset:16768
v_mfma_f32_16x16x32_bf16 v[52:55], a[12:15], a[132:135], v[52:55]
ds_read_b64_tr_b16 a[46:47], v195 offset:18816
v_mfma_f32_16x16x32_bf16 v[56:59], a[12:15], a[136:139], v[56:59]
buffer_load_dwordx4 v206, s[16:19], s30 offen offset:1024 lds
v_mfma_f32_16x16x32_bf16 v[60:63], a[12:15], a[140:143], v[60:63]
ds_read_b64_tr_b16 a[48:49], v194 offset:16896
v_mfma_f32_16x16x32_bf16 v[64:67], a[16:19], a[128:131], v[64:67]
ds_read_b64_tr_b16 a[50:51], v194 offset:18944
v_mfma_f32_16x16x32_bf16 v[68:71], a[16:19], a[132:135], v[68:71]
ds_read_b64_tr_b16 a[52:53], v195 offset:17024
v_mfma_f32_16x16x32_bf16 v[72:75], a[16:19], a[136:139], v[72:75]
ds_read_b64_tr_b16 a[54:55], v195 offset:19072
v_mfma_f32_16x16x32_bf16 v[76:79], a[16:19], a[140:143], v[76:79]
buffer_load_dwordx4 v207, s[12:15], s30 offen offset:2048 lds
v_mfma_f32_16x16x32_bf16 v[80:83], a[20:23], a[128:131], v[80:83]
ds_read_b64_tr_b16 a[56:57], v194 offset:17152
v_mfma_f32_16x16x32_bf16 v[84:87], a[20:23], a[132:135], v[84:87]
ds_read_b64_tr_b16 a[58:59], v194 offset:19200
v_mfma_f32_16x16x32_bf16 v[88:91], a[20:23], a[136:139], v[88:91]
ds_read_b64_tr_b16 a[60:61], v195 offset:17280
v_mfma_f32_16x16x32_bf16 v[92:95], a[20:23], a[140:143], v[92:95]
ds_read_b64_tr_b16 a[62:63], v195 offset:19328
v_mfma_f32_16x16x32_bf16 v[96:99], a[24:27], a[128:131], v[96:99]
buffer_load_dwordx4 v207, s[16:19], s30 offen offset:3072 lds
v_mfma_f32_16x16x32_bf16 v[100:103], a[24:27], a[132:135], v[100:103]
s_add_u32 s24, s24, 1
s_cmp_eq_u32 s24, 8
v_mfma_f32_16x16x32_bf16 v[104:107], a[24:27], a[136:139], v[104:107]
s_cselect_b32 s24, 1, s24
s_lshl_b32 s28, s24, 7
v_mfma_f32_16x16x32_bf16 v[108:111], a[24:27], a[140:143], v[108:111]
s_add_u32 s28, s28, s25
s_lshl_b32 s29, s24, 19
v_mfma_f32_16x16x32_bf16 v[112:115], a[28:31], a[128:131], v[112:115]
s_add_u32 s29, s29, s58
s_add_u32 s30, s29, 65536
v_mfma_f32_16x16x32_bf16 v[116:119], a[28:31], a[132:135], v[116:119]
s_add_u32 s61, s62, 4096
buffer_store_dwordx4 v[144:147], v252, s[40:43], s61 offen sc1
s_add_u32 s61, s62, 5120
buffer_store_dwordx4 v[148:151], v252, s[40:43], s61 offen sc1
v_mfma_f32_16x16x32_bf16 v[120:123], a[28:31], a[136:139], v[120:123]
v_mfma_f32_16x16x32_bf16 v[124:127], a[28:31], a[140:143], v[124:127]
s_waitcnt vmcnt(16) lgkmcnt(0)
s_barrier
s_lshl_b32 s46, s27, 7
s_sub_u32 s59, 15, s27
s_lshl_b32 s59, s59, 7
v_mov_b32 v249, s46
v_mov_b32 v254, s59
v_cmp_gt_u32 vcc, 16, v253
s_nop 1
v_cndmask_b32 v249, v249, v254, vcc
v_and_b32 v254, 15, v253
v_lshl_add_u32 v249, v254, 3, v249
s_mov_b32 s63, 4096
buffer_load_dwordx2 a[250:251], v249, s[88:91], s63 offen sc1
v_add_u32 v192, 0xfffe8000, v192
v_add_u32 v193, 0xfffe8000, v193
v_add_u32 v194, 0xfffe8000, v194
v_add_u32 v195, 0xfffe8000, v195
s_nop 0
v_mfma_f32_16x16x32_bf16 v[0:3], a[32:35], a[144:147], v[0:3]
ds_read_b64_tr_b16 a[0:1], v194 offset:0
s_add_u32 m0, s34, 114688
v_mfma_f32_16x16x32_bf16 v[4:7], a[32:35], a[148:151], v[4:7]
buffer_load_dwordx4 v200, s[8:11], s28 offen offset:0 lds
v_mfma_f32_16x16x32_bf16 v[8:11], a[32:35], a[152:155], v[8:11]
ds_read_b64_tr_b16 a[2:3], v194 offset:2048
v_mfma_f32_16x16x32_bf16 v[12:15], a[32:35], a[156:159], v[12:15]
ds_read_b128 a[128:131], v192 offset:0
v_mfma_f32_16x16x32_bf16 v[16:19], a[36:39], a[144:147], v[16:19]
ds_read_b128 a[132:135], v192 offset:2048
v_mfma_f32_16x16x32_bf16 v[20:23], a[36:39], a[148:151], v[20:23]
ds_read_b128 a[136:139], v192 offset:4096
v_mfma_f32_16x16x32_bf16 v[24:27], a[36:39], a[152:155], v[24:27]
buffer_load_dwordx4 v201, s[8:11], s28 offen offset:1024 lds
v_mfma_f32_16x16x32_bf16 v[28:31], a[36:39], a[156:159], v[28:31]
ds_read_b128 a[140:143], v192 offset:6144
v_mfma_f32_16x16x32_bf16 v[32:35], a[40:43], a[144:147], v[32:35]
ds_read_b64_tr_b16 a[4:5], v195 offset:128
v_mfma_f32_16x16x32_bf16 v[36:39], a[40:43], a[148:151], v[36:39]
)ASM"
R"ASM(ds_read_b64_tr_b16 a[6:7], v195 offset:2176
v_mfma_f32_16x16x32_bf16 v[40:43], a[40:43], a[152:155], v[40:43]
ds_read_b64_tr_b16 a[8:9], v194 offset:256
v_mfma_f32_16x16x32_bf16 v[44:47], a[40:43], a[156:159], v[44:47]
buffer_load_dwordx4 v202, s[8:11], s28 offen offset:2048 lds
v_mfma_f32_16x16x32_bf16 v[48:51], a[44:47], a[144:147], v[48:51]
ds_read_b64_tr_b16 a[10:11], v194 offset:2304
v_mfma_f32_16x16x32_bf16 v[52:55], a[44:47], a[148:151], v[52:55]
ds_read_b64_tr_b16 a[12:13], v195 offset:384
v_mfma_f32_16x16x32_bf16 v[56:59], a[44:47], a[152:155], v[56:59]
ds_read_b64_tr_b16 a[14:15], v195 offset:2432
v_mfma_f32_16x16x32_bf16 v[60:63], a[44:47], a[156:159], v[60:63]
ds_read_b64_tr_b16 a[16:17], v194 offset:512
v_mfma_f32_16x16x32_bf16 v[64:67], a[48:51], a[144:147], v[64:67]
buffer_load_dwordx4 v203, s[8:11], s28 offen offset:3072 lds
v_mfma_f32_16x16x32_bf16 v[68:71], a[48:51], a[148:151], v[68:71]
ds_read_b64_tr_b16 a[18:19], v194 offset:2560
v_mfma_f32_16x16x32_bf16 v[72:75], a[48:51], a[152:155], v[72:75]
ds_read_b64_tr_b16 a[20:21], v195 offset:640
v_mfma_f32_16x16x32_bf16 v[76:79], a[48:51], a[156:159], v[76:79]
ds_read_b64_tr_b16 a[22:23], v195 offset:2688
v_mfma_f32_16x16x32_bf16 v[80:83], a[52:55], a[144:147], v[80:83]
ds_read_b64_tr_b16 a[24:25], v194 offset:768
s_add_u32 m0, s35, 114688
v_mfma_f32_16x16x32_bf16 v[84:87], a[52:55], a[148:151], v[84:87]
buffer_load_dwordx4 v204, s[12:15], s29 offen offset:0 lds
v_mfma_f32_16x16x32_bf16 v[88:91], a[52:55], a[152:155], v[88:91]
ds_read_b64_tr_b16 a[26:27], v194 offset:2816
v_mfma_f32_16x16x32_bf16 v[92:95], a[52:55], a[156:159], v[92:95]
ds_read_b64_tr_b16 a[28:29], v195 offset:896
v_mfma_f32_16x16x32_bf16 v[96:99], a[56:59], a[144:147], v[96:99]
ds_read_b64_tr_b16 a[30:31], v195 offset:2944
v_mfma_f32_16x16x32_bf16 v[100:103], a[56:59], a[148:151], v[100:103]
v_mfma_f32_16x16x32_bf16 v[104:107], a[56:59], a[152:155], v[104:107]
buffer_load_dwordx4 v204, s[16:19], s29 offen offset:1024 lds
v_mfma_f32_16x16x32_bf16 v[108:111], a[56:59], a[156:159], v[108:111]
v_mfma_f32_16x16x32_bf16 v[112:115], a[60:63], a[144:147], v[112:115]
v_mfma_f32_16x16x32_bf16 v[116:119], a[60:63], a[148:151], v[116:119]
v_mfma_f32_16x16x32_bf16 v[120:123], a[60:63], a[152:155], v[120:123]
v_mfma_f32_16x16x32_bf16 v[124:127], a[60:63], a[156:159], v[124:127]
buffer_load_dwordx4 v205, s[12:15], s29 offen offset:2048 lds
s_waitcnt lgkmcnt(0)
v_mfma_f32_16x16x32_bf16 v[0:3], a[0:3], a[128:131], v[0:3]
ds_read_b64_tr_b16 a[32:33], v194 offset:16384
v_mfma_f32_16x16x32_bf16 v[4:7], a[0:3], a[132:135], v[4:7]
ds_read_b64_tr_b16 a[34:35], v194 offset:18432
v_mfma_f32_16x16x32_bf16 v[8:11], a[0:3], a[136:139], v[8:11]
ds_read_b128 a[144:147], v193 offset:0
v_mfma_f32_16x16x32_bf16 v[12:15], a[0:3], a[140:143], v[12:15]
ds_read_b128 a[148:151], v193 offset:2048
v_mfma_f32_16x16x32_bf16 v[16:19], a[4:7], a[128:131], v[16:19]
buffer_load_dwordx4 v205, s[16:19], s29 offen offset:3072 lds
v_mfma_f32_16x16x32_bf16 v[20:23], a[4:7], a[132:135], v[20:23]
ds_read_b128 a[152:155], v193 offset:4096
v_mfma_f32_16x16x32_bf16 v[24:27], a[4:7], a[136:139], v[24:27]
ds_read_b128 a[156:159], v193 offset:6144
v_mfma_f32_16x16x32_bf16 v[28:31], a[4:7], a[140:143], v[28:31]
ds_read_b64_tr_b16 a[36:37], v195 offset:16512
v_mfma_f32_16x16x32_bf16 v[32:35], a[8:11], a[128:131], v[32:35]
ds_read_b64_tr_b16 a[38:39], v195 offset:18560
s_add_u32 m0, s35, 118784
v_mfma_f32_16x16x32_bf16 v[36:39], a[8:11], a[132:135], v[36:39]
buffer_load_dwordx4 v206, s[12:15], s30 offen offset:0 lds
v_mfma_f32_16x16x32_bf16 v[40:43], a[8:11], a[136:139], v[40:43]
ds_read_b64_tr_b16 a[40:41], v194 offset:16640
v_mfma_f32_16x16x32_bf16 v[44:47], a[8:11], a[140:143], v[44:47]
ds_read_b64_tr_b16 a[42:43], v194 offset:18688
v_mfma_f32_16x16x32_bf16 v[48:51], a[12:15], a[128:131], v[48:51]
ds_read_b64_tr_b16 a[44:45], v195 offset:16768
v_mfma_f32_16x16x32_bf16 v[52:55], a[12:15], a[132:135], v[52:55]
ds_read_b64_tr_b16 a[46:47], v195 offset:18816
v_mfma_f32_16x16x32_bf16 v[56:59], a[12:15], a[136:139], v[56:59]
buffer_load_dwordx4 v206, s[16:19], s30 offen offset:1024 lds
v_mfma_f32_16x16x32_bf16 v[60:63], a[12:15], a[140:143], v[60:63]
ds_read_b64_tr_b16 a[48:49], v194 offset:16896
v_mfma_f32_16x16x32_bf16 v[64:67], a[16:19], a[128:131], v[64:67]
ds_read_b64_tr_b16 a[50:51], v194 offset:18944
v_mfma_f32_16x16x32_bf16 v[68:71], a[16:19], a[132:135], v[68:71]
ds_read_b64_tr_b16 a[52:53], v195 offset:17024
v_mfma_f32_16x16x32_bf16 v[72:75], a[16:19], a[136:139], v[72:75]
ds_read_b64_tr_b16 a[54:55], v195 offset:19072
v_mfma_f32_16x16x32_bf16 v[76:79], a[16:19], a[140:143], v[76:79]
buffer_load_dwordx4 v207, s[12:15], s30 offen offset:2048 lds
v_mfma_f32_16x16x32_bf16 v[80:83], a[20:23], a[128:131], v[80:83]
ds_read_b64_tr_b16 a[56:57], v194 offset:17152
v_mfma_f32_16x16x32_bf16 v[84:87], a[20:23], a[132:135], v[84:87]
ds_read_b64_tr_b16 a[58:59], v194 offset:19200
v_mfma_f32_16x16x32_bf16 v[88:91], a[20:23], a[136:139], v[88:91]
ds_read_b64_tr_b16 a[60:61], v195 offset:17280
v_mfma_f32_16x16x32_bf16 v[92:95], a[20:23], a[140:143], v[92:95]
ds_read_b64_tr_b16 a[62:63], v195 offset:19328
v_mfma_f32_16x16x32_bf16 v[96:99], a[24:27], a[128:131], v[96:99]
buffer_load_dwordx4 v207, s[16:19], s30 offen offset:3072 lds
v_mfma_f32_16x16x32_bf16 v[100:103], a[24:27], a[132:135], v[100:103]
s_add_u32 s24, s24, 1
s_cmp_eq_u32 s24, 8
v_mfma_f32_16x16x32_bf16 v[104:107], a[24:27], a[136:139], v[104:107]
s_cselect_b32 s24, 1, s24
s_lshl_b32 s28, s24, 7
v_mfma_f32_16x16x32_bf16 v[108:111], a[24:27], a[140:143], v[108:111]
s_add_u32 s28, s28, s25
s_lshl_b32 s29, s24, 19
v_mfma_f32_16x16x32_bf16 v[112:115], a[28:31], a[128:131], v[112:115]
s_add_u32 s29, s29, s58
s_add_u32 s30, s29, 65536
v_mfma_f32_16x16x32_bf16 v[116:119], a[28:31], a[132:135], v[116:119]
s_add_u32 s61, s62, 6144
buffer_store_dwordx4 v[152:155], v252, s[40:43], s61 offen sc1
s_add_u32 s61, s62, 7168
buffer_store_dwordx4 v[156:159], v252, s[40:43], s61 offen sc1
v_mfma_f32_16x16x32_bf16 v[120:123], a[28:31], a[136:139], v[120:123]
v_mfma_f32_16x16x32_bf16 v[124:127], a[28:31], a[140:143], v[124:127]
s_waitcnt vmcnt(17) lgkmcnt(0)
s_barrier
s_waitcnt vmcnt(14)
s_mov_b32 s86, 0
Llnc_2:
v_accvgpr_read_b32 v254, a250
s_nop 0
v_cmp_eq_u32 vcc, s84, v254
s_nop 1
s_mov_b64 s[46:47], vcc
v_accvgpr_read_b32 v254, a251
s_nop 0
v_cmp_eq_u32 vcc, s85, v254
s_nop 1
s_and_b64 vcc, vcc, s[46:47]
s_cmp_eq_u64 vcc, exec
s_cbranch_scc1 Llnd_3
s_sleep 1
buffer_load_dwordx2 a[250:251], v249, s[88:91], s63 offen sc1
s_waitcnt vmcnt(0)
s_add_u32 s86, s86, 1
s_cmp_lt_u32 s86, 0x40000
s_cbranch_scc1 Llnc_2
Llnd_3:
v_add_u32 v192, 0xc000, v192
v_add_u32 v193, 0xc000, v193
v_add_u32 v194, 0xc000, v194
v_add_u32 v195, 0xc000, v195
s_nop 0
v_mfma_f32_16x16x32_bf16 v[0:3], a[32:35], a[144:147], v[0:3]
ds_read_b64_tr_b16 a[0:1], v194 offset:0
s_add_u32 m0, s34, 16384
v_mfma_f32_16x16x32_bf16 v[4:7], a[32:35], a[148:151], v[4:7]
buffer_load_dwordx4 v200, s[8:11], s28 offen offset:0 lds
v_mfma_f32_16x16x32_bf16 v[8:11], a[32:35], a[152:155], v[8:11]
ds_read_b64_tr_b16 a[2:3], v194 offset:2048
v_mfma_f32_16x16x32_bf16 v[12:15], a[32:35], a[156:159], v[12:15]
ds_read_b128 a[128:131], v192 offset:0
v_mfma_f32_16x16x32_bf16 v[16:19], a[36:39], a[144:147], v[16:19]
ds_read_b128 a[132:135], v192 offset:2048
v_mfma_f32_16x16x32_bf16 v[20:23], a[36:39], a[148:151], v[20:23]
ds_read_b128 a[136:139], v192 offset:4096
v_mfma_f32_16x16x32_bf16 v[24:27], a[36:39], a[152:155], v[24:27]
buffer_load_dwordx4 v201, s[8:11], s28 offen offset:1024 lds
v_mfma_f32_16x16x32_bf16 v[28:31], a[36:39], a[156:159], v[28:31]
ds_read_b128 a[140:143], v192 offset:6144
v_mfma_f32_16x16x32_bf16 v[32:35], a[40:43], a[144:147], v[32:35]
ds_read_b64_tr_b16 a[4:5], v195 offset:128
v_mfma_f32_16x16x32_bf16 v[36:39], a[40:43], a[148:151], v[36:39]
ds_read_b64_tr_b16 a[6:7], v195 offset:2176
v_mfma_f32_16x16x32_bf16 v[40:43], a[40:43], a[152:155], v[40:43]
ds_read_b64_tr_b16 a[8:9], v194 offset:256
v_mfma_f32_16x16x32_bf16 v[44:47], a[40:43], a[156:159], v[44:47]
buffer_load_dwordx4 v202, s[8:11], s28 offen offset:2048 lds
v_mfma_f32_16x16x32_bf16 v[48:51], a[44:47], a[144:147], v[48:51]
ds_read_b64_tr_b16 a[10:11], v194 offset:2304
v_mfma_f32_16x16x32_bf16 v[52:55], a[44:47], a[148:151], v[52:55]
ds_read_b64_tr_b16 a[12:13], v195 offset:384
v_mfma_f32_16x16x32_bf16 v[56:59], a[44:47], a[152:155], v[56:59]
ds_read_b64_tr_b16 a[14:15], v195 offset:2432
v_mfma_f32_16x16x32_bf16 v[60:63], a[44:47], a[156:159], v[60:63]
ds_read_b64_tr_b16 a[16:17], v194 offset:512
v_mfma_f32_16x16x32_bf16 v[64:67], a[48:51], a[144:147], v[64:67]
buffer_load_dwordx4 v203, s[8:11], s28 offen offset:3072 lds
v_mfma_f32_16x16x32_bf16 v[68:71], a[48:51], a[148:151], v[68:71]
ds_read_b64_tr_b16 a[18:19], v194 offset:2560
v_mfma_f32_16x16x32_bf16 v[72:75], a[48:51], a[152:155], v[72:75]
ds_read_b64_tr_b16 a[20:21], v195 offset:640
v_mfma_f32_16x16x32_bf16 v[76:79], a[48:51], a[156:159], v[76:79]
ds_read_b64_tr_b16 a[22:23], v195 offset:2688
v_mfma_f32_16x16x32_bf16 v[80:83], a[52:55], a[144:147], v[80:83]
ds_read_b64_tr_b16 a[24:25], v194 offset:768
s_add_u32 m0, s35, 16384
v_mfma_f32_16x16x32_bf16 v[84:87], a[52:55], a[148:151], v[84:87]
buffer_load_dwordx4 v204, s[12:15], s29 offen offset:0 lds
v_mfma_f32_16x16x32_bf16 v[88:91], a[52:55], a[152:155], v[88:91]
ds_read_b64_tr_b16 a[26:27], v194 offset:2816
v_mfma_f32_16x16x32_bf16 v[92:95], a[52:55], a[156:159], v[92:95]
ds_read_b64_tr_b16 a[28:29], v195 offset:896
v_mfma_f32_16x16x32_bf16 v[96:99], a[56:59], a[144:147], v[96:99]
ds_read_b64_tr_b16 a[30:31], v195 offset:2944
v_mfma_f32_16x16x32_bf16 v[100:103], a[56:59], a[148:151], v[100:103]
v_mfma_f32_16x16x32_bf16 v[104:107], a[56:59], a[152:155], v[104:107]
buffer_load_dwordx4 v204, s[16:19], s29 offen offset:1024 lds
v_mfma_f32_16x16x32_bf16 v[108:111], a[56:59], a[156:159], v[108:111]
v_mfma_f32_16x16x32_bf16 v[112:115], a[60:63], a[144:147], v[112:115]
v_mfma_f32_16x16x32_bf16 v[116:119], a[60:63], a[148:151], v[116:119]
v_mfma_f32_16x16x32_bf16 v[120:123], a[60:63], a[152:155], v[120:123]
v_mfma_f32_16x16x32_bf16 v[124:127], a[60:63], a[156:159], v[124:127]
buffer_load_dwordx4 v205, s[12:15], s29 offen offset:2048 lds
s_waitcnt lgkmcnt(0)
v_mfma_f32_16x16x32_bf16 v[0:3], a[0:3], a[128:131], v[0:3]
ds_read_b64_tr_b16 a[32:33], v194 offset:16384
v_mfma_f32_16x16x32_bf16 v[4:7], a[0:3], a[132:135], v[4:7]
ds_read_b64_tr_b16 a[34:35], v194 offset:18432
v_mfma_f32_16x16x32_bf16 v[8:11], a[0:3], a[136:139], v[8:11]
ds_read_b128 a[144:147], v193 offset:0
v_mfma_f32_16x16x32_bf16 v[12:15], a[0:3], a[140:143], v[12:15]
ds_read_b128 a[148:151], v193 offset:2048
v_mfma_f32_16x16x32_bf16 v[16:19], a[4:7], a[128:131], v[16:19]
buffer_load_dwordx4 v205, s[16:19], s29 offen offset:3072 lds
v_mfma_f32_16x16x32_bf16 v[20:23], a[4:7], a[132:135], v[20:23]
ds_read_b128 a[152:155], v193 offset:4096
v_mfma_f32_16x16x32_bf16 v[24:27], a[4:7], a[136:139], v[24:27]
ds_read_b128 a[156:159], v193 offset:6144
v_mfma_f32_16x16x32_bf16 v[28:31], a[4:7], a[140:143], v[28:31]
ds_read_b64_tr_b16 a[36:37], v195 offset:16512
v_mfma_f32_16x16x32_bf16 v[32:35], a[8:11], a[128:131], v[32:35]
ds_read_b64_tr_b16 a[38:39], v195 offset:18560
s_add_u32 m0, s35, 20480
v_mfma_f32_16x16x32_bf16 v[36:39], a[8:11], a[132:135], v[36:39]
buffer_load_dwordx4 v206, s[12:15], s30 offen offset:0 lds
v_mfma_f32_16x16x32_bf16 v[40:43], a[8:11], a[136:139], v[40:43]
ds_read_b64_tr_b16 a[40:41], v194 offset:16640
v_mfma_f32_16x16x32_bf16 v[44:47], a[8:11], a[140:143], v[44:47]
ds_read_b64_tr_b16 a[42:43], v194 offset:18688
v_mfma_f32_16x16x32_bf16 v[48:51], a[12:15], a[128:131], v[48:51]
ds_read_b64_tr_b16 a[44:45], v195 offset:16768
v_mfma_f32_16x16x32_bf16 v[52:55], a[12:15], a[132:135], v[52:55]
ds_read_b64_tr_b16 a[46:47], v195 offset:18816
v_mfma_f32_16x16x32_bf16 v[56:59], a[12:15], a[136:139], v[56:59]
buffer_load_dwordx4 v206, s[16:19], s30 offen offset:1024 lds
v_mfma_f32_16x16x32_bf16 v[60:63], a[12:15], a[140:143], v[60:63]
ds_read_b64_tr_b16 a[48:49], v194 offset:16896
v_mfma_f32_16x16x32_bf16 v[64:67], a[16:19], a[128:131], v[64:67]
ds_read_b64_tr_b16 a[50:51], v194 offset:18944
v_mfma_f32_16x16x32_bf16 v[68:71], a[16:19], a[132:135], v[68:71]
ds_read_b64_tr_b16 a[52:53], v195 offset:17024
v_mfma_f32_16x16x32_bf16 v[72:75], a[16:19], a[136:139], v[72:75]
ds_read_b64_tr_b16 a[54:55], v195 offset:19072
v_mfma_f32_16x16x32_bf16 v[76:79], a[16:19], a[140:143], v[76:79]
buffer_load_dwordx4 v207, s[12:15], s30 offen offset:2048 lds
v_mfma_f32_16x16x32_bf16 v[80:83], a[20:23], a[128:131], v[80:83]
ds_read_b64_tr_b16 a[56:57], v194 offset:17152
v_mfma_f32_16x16x32_bf16 v[84:87], a[20:23], a[132:135], v[84:87]
ds_read_b64_tr_b16 a[58:59], v194 offset:19200
v_mfma_f32_16x16x32_bf16 v[88:91], a[20:23], a[136:139], v[88:91]
ds_read_b64_tr_b16 a[60:61], v195 offset:17280
v_mfma_f32_16x16x32_bf16 v[92:95], a[20:23], a[140:143], v[92:95]
ds_read_b64_tr_b16 a[62:63], v195 offset:19328
v_mfma_f32_16x16x32_bf16 v[96:99], a[24:27], a[128:131], v[96:99]
buffer_load_dwordx4 v207, s[16:19], s30 offen offset:3072 lds
v_mfma_f32_16x16x32_bf16 v[100:103], a[24:27], a[132:135], v[100:103]
s_add_u32 s24, s24, 1
s_and_b32 s24, s24, 7
v_mfma_f32_16x16x32_bf16 v[104:107], a[24:27], a[136:139], v[104:107]
s_mov_b32 s24, 0
s_lshl_b32 s32, s24, 7
v_mfma_f32_16x16x32_bf16 v[108:111], a[24:27], a[140:143], v[108:111]
s_add_u32 s31, s32, s60
s_add_u32 s32, s32, s96
v_mfma_f32_16x16x32_bf16 v[112:115], a[28:31], a[128:131], v[112:115]
s_lshl_b32 s33, s24, 19
s_add_u32 s33, s33, s97
v_mfma_f32_16x16x32_bf16 v[116:119], a[28:31], a[132:135], v[116:119]
s_add_u32 s61, s62, 8192
buffer_store_dwordx4 v[160:163], v252, s[40:43], s61 offen sc1
s_add_u32 s61, s62, 9216
buffer_store_dwordx4 v[164:167], v252, s[40:43], s61 offen sc1
v_mfma_f32_16x16x32_bf16 v[120:123], a[28:31], a[136:139], v[120:123]
v_mfma_f32_16x16x32_bf16 v[124:127], a[28:31], a[140:143], v[124:127]
s_waitcnt vmcnt(16) lgkmcnt(0)
s_barrier
v_add_u32 v192, 0xc000, v192
v_add_u32 v193, 0xc000, v193
v_add_u32 v194, 0xc000, v194
v_add_u32 v195, 0xc000, v195
s_nop 0
v_mfma_f32_16x16x32_bf16 v[0:3], a[32:35], a[144:147], v[0:3]
ds_read_b64_tr_b16 a[0:1], v194 offset:0
s_add_u32 m0, s56, 65536
v_mfma_f32_16x16x32_bf16 v[4:7], a[32:35], a[148:151], v[4:7]
buffer_load_dwordx4 v200, s[20:23], s31 offen offset:0 sc1 lds
v_mfma_f32_16x16x32_bf16 v[8:11], a[32:35], a[152:155], v[8:11]
ds_read_b64_tr_b16 a[2:3], v194 offset:2048
v_mfma_f32_16x16x32_bf16 v[12:15], a[32:35], a[156:159], v[12:15]
ds_read_b128 a[128:131], v192 offset:0
v_mfma_f32_16x16x32_bf16 v[16:19], a[36:39], a[144:147], v[16:19]
ds_read_b128 a[132:135], v192 offset:2048
v_mfma_f32_16x16x32_bf16 v[20:23], a[36:39], a[148:151], v[20:23]
ds_read_b128 a[136:139], v192 offset:4096
v_mfma_f32_16x16x32_bf16 v[24:27], a[36:39], a[152:155], v[24:27]
buffer_load_dwordx4 v201, s[20:23], s31 offen offset:1024 sc1 lds
v_mfma_f32_16x16x32_bf16 v[28:31], a[36:39], a[156:159], v[28:31]
ds_read_b128 a[140:143], v192 offset:6144
v_mfma_f32_16x16x32_bf16 v[32:35], a[40:43], a[144:147], v[32:35]
ds_read_b64_tr_b16 a[4:5], v195 offset:128
v_mfma_f32_16x16x32_bf16 v[36:39], a[40:43], a[148:151], v[36:39]
ds_read_b64_tr_b16 a[6:7], v195 offset:2176
v_mfma_f32_16x16x32_bf16 v[40:43], a[40:43], a[152:155], v[40:43]
ds_read_b64_tr_b16 a[8:9], v194 offset:256
v_mfma_f32_16x16x32_bf16 v[44:47], a[40:43], a[156:159], v[44:47]
buffer_load_dwordx4 v212, s[20:23], s31 offen offset:2048 sc1 lds
v_mfma_f32_16x16x32_bf16 v[48:51], a[44:47], a[144:147], v[48:51]
ds_read_b64_tr_b16 a[10:11], v194 offset:2304
v_mfma_f32_16x16x32_bf16 v[52:55], a[44:47], a[148:151], v[52:55]
ds_read_b64_tr_b16 a[12:13], v195 offset:384
v_mfma_f32_16x16x32_bf16 v[56:59], a[44:47], a[152:155], v[56:59]
ds_read_b64_tr_b16 a[14:15], v195 offset:2432
v_mfma_f32_16x16x32_bf16 v[60:63], a[44:47], a[156:159], v[60:63]
ds_read_b64_tr_b16 a[16:17], v194 offset:512
v_mfma_f32_16x16x32_bf16 v[64:67], a[48:51], a[144:147], v[64:67]
buffer_load_dwordx4 v213, s[20:23], s31 offen offset:3072 sc1 lds
v_mfma_f32_16x16x32_bf16 v[68:71], a[48:51], a[148:151], v[68:71]
ds_read_b64_tr_b16 a[18:19], v194 offset:2560
v_mfma_f32_16x16x32_bf16 v[72:75], a[48:51], a[152:155], v[72:75]
ds_read_b64_tr_b16 a[20:21], v195 offset:640
v_mfma_f32_16x16x32_bf16 v[76:79], a[48:51], a[156:159], v[76:79]
ds_read_b64_tr_b16 a[22:23], v195 offset:2688
v_mfma_f32_16x16x32_bf16 v[80:83], a[52:55], a[144:147], v[80:83]
ds_read_b64_tr_b16 a[24:25], v194 offset:768
s_add_u32 m0, s56, 69632
v_mfma_f32_16x16x32_bf16 v[84:87], a[52:55], a[148:151], v[84:87]
buffer_load_dwordx4 v200, s[20:23], s32 offen offset:0 sc1 lds
v_mfma_f32_16x16x32_bf16 v[88:91], a[52:55], a[152:155], v[88:91]
ds_read_b64_tr_b16 a[26:27], v194 offset:2816
v_mfma_f32_16x16x32_bf16 v[92:95], a[52:55], a[156:159], v[92:95]
ds_read_b64_tr_b16 a[28:29], v195 offset:896
v_mfma_f32_16x16x32_bf16 v[96:99], a[56:59], a[144:147], v[96:99]
ds_read_b64_tr_b16 a[30:31], v195 offset:2944
v_mfma_f32_16x16x32_bf16 v[100:103], a[56:59], a[148:151], v[100:103]
v_mfma_f32_16x16x32_bf16 v[104:107], a[56:59], a[152:155], v[104:107]
buffer_load_dwordx4 v201, s[20:23], s32 offen offset:1024 sc1 lds
v_mfma_f32_16x16x32_bf16 v[108:111], a[56:59], a[156:159], v[108:111]
v_mfma_f32_16x16x32_bf16 v[112:115], a[60:63], a[144:147], v[112:115]
v_mfma_f32_16x16x32_bf16 v[116:119], a[60:63], a[148:151], v[116:119]
v_mfma_f32_16x16x32_bf16 v[120:123], a[60:63], a[152:155], v[120:123]
v_mfma_f32_16x16x32_bf16 v[124:127], a[60:63], a[156:159], v[124:127]
buffer_load_dwordx4 v212, s[20:23], s32 offen offset:2048 sc1 lds
s_waitcnt lgkmcnt(0)
v_mfma_f32_16x16x32_bf16 v[0:3], a[0:3], a[128:131], v[0:3]
ds_read_b64_tr_b16 a[32:33], v194 offset:16384
v_mfma_f32_16x16x32_bf16 v[4:7], a[0:3], a[132:135], v[4:7]
ds_read_b64_tr_b16 a[34:35], v194 offset:18432
v_mfma_f32_16x16x32_bf16 v[8:11], a[0:3], a[136:139], v[8:11]
ds_read_b128 a[144:147], v193 offset:0
v_mfma_f32_16x16x32_bf16 v[12:15], a[0:3], a[140:143], v[12:15]
ds_read_b128 a[148:151], v193 offset:2048
v_mfma_f32_16x16x32_bf16 v[16:19], a[4:7], a[128:131], v[16:19]
buffer_load_dwordx4 v213, s[20:23], s32 offen offset:3072 sc1 lds
v_mfma_f32_16x16x32_bf16 v[20:23], a[4:7], a[132:135], v[20:23]
ds_read_b128 a[152:155], v193 offset:4096
v_mfma_f32_16x16x32_bf16 v[24:27], a[4:7], a[136:139], v[24:27]
ds_read_b128 a[156:159], v193 offset:6144
v_mfma_f32_16x16x32_bf16 v[28:31], a[4:7], a[140:143], v[28:31]
ds_read_b64_tr_b16 a[36:37], v195 offset:16512
v_mfma_f32_16x16x32_bf16 v[32:35], a[8:11], a[128:131], v[32:35]
ds_read_b64_tr_b16 a[38:39], v195 offset:18560
s_add_u32 m0, s57, 65536
v_mfma_f32_16x16x32_bf16 v[36:39], a[8:11], a[132:135], v[36:39]
buffer_load_dwordx4 v208, s[36:39], s33 offen offset:0 lds
v_mfma_f32_16x16x32_bf16 v[40:43], a[8:11], a[136:139], v[40:43]
ds_read_b64_tr_b16 a[40:41], v194 offset:16640
v_mfma_f32_16x16x32_bf16 v[44:47], a[8:11], a[140:143], v[44:47]
ds_read_b64_tr_b16 a[42:43], v194 offset:18688
v_mfma_f32_16x16x32_bf16 v[48:51], a[12:15], a[128:131], v[48:51]
ds_read_b64_tr_b16 a[44:45], v195 offset:16768
v_mfma_f32_16x16x32_bf16 v[52:55], a[12:15], a[132:135], v[52:55]
ds_read_b64_tr_b16 a[46:47], v195 offset:18816
v_mfma_f32_16x16x32_bf16 v[56:59], a[12:15], a[136:139], v[56:59]
buffer_load_dwordx4 v209, s[36:39], s33 offen offset:1024 lds
v_mfma_f32_16x16x32_bf16 v[60:63], a[12:15], a[140:143], v[60:63]
ds_read_b64_tr_b16 a[48:49], v194 offset:16896
v_mfma_f32_16x16x32_bf16 v[64:67], a[16:19], a[128:131], v[64:67]
ds_read_b64_tr_b16 a[50:51], v194 offset:18944
v_mfma_f32_16x16x32_bf16 v[68:71], a[16:19], a[132:135], v[68:71]
ds_read_b64_tr_b16 a[52:53], v195 offset:17024
v_mfma_f32_16x16x32_bf16 v[72:75], a[16:19], a[136:139], v[72:75]
ds_read_b64_tr_b16 a[54:55], v195 offset:19072
v_mfma_f32_16x16x32_bf16 v[76:79], a[16:19], a[140:143], v[76:79]
buffer_load_dwordx4 v210, s[36:39], s33 offen offset:2048 lds
v_mfma_f32_16x16x32_bf16 v[80:83], a[20:23], a[128:131], v[80:83]
ds_read_b64_tr_b16 a[56:57], v194 offset:17152
v_mfma_f32_16x16x32_bf16 v[84:87], a[20:23], a[132:135], v[84:87]
ds_read_b64_tr_b16 a[58:59], v194 offset:19200
v_mfma_f32_16x16x32_bf16 v[88:91], a[20:23], a[136:139], v[88:91]
ds_read_b64_tr_b16 a[60:61], v195 offset:17280
v_mfma_f32_16x16x32_bf16 v[92:95], a[20:23], a[140:143], v[92:95]
ds_read_b64_tr_b16 a[62:63], v195 offset:19328
v_mfma_f32_16x16x32_bf16 v[96:99], a[24:27], a[128:131], v[96:99]
buffer_load_dwordx4 v211, s[36:39], s33 offen offset:3072 lds
v_mfma_f32_16x16x32_bf16 v[100:103], a[24:27], a[132:135], v[100:103]
s_add_u32 s24, s24, 1
s_and_b32 s24, s24, 7
v_mfma_f32_16x16x32_bf16 v[104:107], a[24:27], a[136:139], v[104:107]
s_lshl_b32 s32, s24, 7
s_add_u32 s31, s32, s60
v_mfma_f32_16x16x32_bf16 v[108:111], a[24:27], a[140:143], v[108:111]
s_add_u32 s32, s32, s96
s_lshl_b32 s33, s24, 19
v_mfma_f32_16x16x32_bf16 v[112:115], a[28:31], a[128:131], v[112:115]
s_add_u32 s33, s33, s97
s_add_u32 s61, s62, 10240
buffer_store_dwordx4 v[168:171], v252, s[40:43], s61 offen sc1
v_mfma_f32_16x16x32_bf16 v[116:119], a[28:31], a[132:135], v[116:119]
s_add_u32 s61, s62, 11264
buffer_store_dwordx4 v[172:175], v252, s[40:43], s61 offen sc1
v_mfma_f32_16x16x32_bf16 v[120:123], a[28:31], a[136:139], v[120:123]
v_mfma_f32_16x16x32_bf16 v[124:127], a[28:31], a[140:143], v[124:127]
s_waitcnt vmcnt(16) lgkmcnt(0)
s_barrier
v_add_u32 v192, 0xfffe8000, v192
v_add_u32 v193, 0xfffe8000, v193
v_add_u32 v194, 0xfffe8000, v194
v_add_u32 v195, 0xfffe8000, v195
s_nop 0
v_mfma_f32_16x16x32_bf16 v[0:3], a[32:35], a[144:147], v[0:3]
ds_read_b64_tr_b16 a[0:1], v194 offset:0
s_add_u32 m0, s56, 114688
v_mfma_f32_16x16x32_bf16 v[4:7], a[32:35], a[148:151], v[4:7]
buffer_load_dwordx4 v200, s[20:23], s31 offen offset:0 sc1 lds
v_mfma_f32_16x16x32_bf16 v[8:11], a[32:35], a[152:155], v[8:11]
ds_read_b64_tr_b16 a[2:3], v194 offset:2048
v_mfma_f32_16x16x32_bf16 v[12:15], a[32:35], a[156:159], v[12:15]
ds_read_b128 a[128:131], v192 offset:0
v_mfma_f32_16x16x32_bf16 v[16:19], a[36:39], a[144:147], v[16:19]
ds_read_b128 a[132:135], v192 offset:2048
v_mfma_f32_16x16x32_bf16 v[20:23], a[36:39], a[148:151], v[20:23]
ds_read_b128 a[136:139], v192 offset:4096
v_mfma_f32_16x16x32_bf16 v[24:27], a[36:39], a[152:155], v[24:27]
buffer_load_dwordx4 v201, s[20:23], s31 offen offset:1024 sc1 lds
v_mfma_f32_16x16x32_bf16 v[28:31], a[36:39], a[156:159], v[28:31]
ds_read_b128 a[140:143], v192 offset:6144
v_mfma_f32_16x16x32_bf16 v[32:35], a[40:43], a[144:147], v[32:35]
ds_read_b64_tr_b16 a[4:5], v195 offset:128
v_mfma_f32_16x16x32_bf16 v[36:39], a[40:43], a[148:151], v[36:39]
ds_read_b64_tr_b16 a[6:7], v195 offset:2176
v_mfma_f32_16x16x32_bf16 v[40:43], a[40:43], a[152:155], v[40:43]
ds_read_b64_tr_b16 a[8:9], v194 offset:256
v_mfma_f32_16x16x32_bf16 v[44:47], a[40:43], a[156:159], v[44:47]
buffer_load_dwordx4 v212, s[20:23], s31 offen offset:2048 sc1 lds
v_mfma_f32_16x16x32_bf16 v[48:51], a[44:47], a[144:147], v[48:51]
ds_read_b64_tr_b16 a[10:11], v194 offset:2304
v_mfma_f32_16x16x32_bf16 v[52:55], a[44:47], a[148:151], v[52:55]
ds_read_b64_tr_b16 a[12:13], v195 offset:384
v_mfma_f32_16x16x32_bf16 v[56:59], a[44:47], a[152:155], v[56:59]
ds_read_b64_tr_b16 a[14:15], v195 offset:2432
v_mfma_f32_16x16x32_bf16 v[60:63], a[44:47], a[156:159], v[60:63]
ds_read_b64_tr_b16 a[16:17], v194 offset:512
v_mfma_f32_16x16x32_bf16 v[64:67], a[48:51], a[144:147], v[64:67]
buffer_load_dwordx4 v213, s[20:23], s31 offen offset:3072 sc1 lds
v_mfma_f32_16x16x32_bf16 v[68:71], a[48:51], a[148:151], v[68:71]
ds_read_b64_tr_b16 a[18:19], v194 offset:2560
v_mfma_f32_16x16x32_bf16 v[72:75], a[48:51], a[152:155], v[72:75]
ds_read_b64_tr_b16 a[20:21], v195 offset:640
v_mfma_f32_16x16x32_bf16 v[76:79], a[48:51], a[156:159], v[76:79]
ds_read_b64_tr_b16 a[22:23], v195 offset:2688
v_mfma_f32_16x16x32_bf16 v[80:83], a[52:55], a[144:147], v[80:83]
ds_read_b64_tr_b16 a[24:25], v194 offset:768
s_add_u32 m0, s56, 118784
v_mfma_f32_16x16x32_bf16 v[84:87], a[52:55], a[148:151], v[84:87]
buffer_load_dwordx4 v200, s[20:23], s32 offen offset:0 sc1 lds
v_mfma_f32_16x16x32_bf16 v[88:91], a[52:55], a[152:155], v[88:91]
ds_read_b64_tr_b16 a[26:27], v194 offset:2816
v_mfma_f32_16x16x32_bf16 v[92:95], a[52:55], a[156:159], v[92:95]
ds_read_b64_tr_b16 a[28:29], v195 offset:896
v_mfma_f32_16x16x32_bf16 v[96:99], a[56:59], a[144:147], v[96:99]
ds_read_b64_tr_b16 a[30:31], v195 offset:2944
v_mfma_f32_16x16x32_bf16 v[100:103], a[56:59], a[148:151], v[100:103]
v_mfma_f32_16x16x32_bf16 v[104:107], a[56:59], a[152:155], v[104:107]
buffer_load_dwordx4 v201, s[20:23], s32 offen offset:1024 sc1 lds
v_mfma_f32_16x16x32_bf16 v[108:111], a[56:59], a[156:159], v[108:111]
v_mfma_f32_16x16x32_bf16 v[112:115], a[60:63], a[144:147], v[112:115]
v_mfma_f32_16x16x32_bf16 v[116:119], a[60:63], a[148:151], v[116:119]
v_mfma_f32_16x16x32_bf16 v[120:123], a[60:63], a[152:155], v[120:123]
v_mfma_f32_16x16x32_bf16 v[124:127], a[60:63], a[156:159], v[124:127]
buffer_load_dwordx4 v212, s[20:23], s32 offen offset:2048 sc1 lds
s_waitcnt lgkmcnt(0)
v_mfma_f32_16x16x32_bf16 v[0:3], a[0:3], a[128:131], v[0:3]
ds_read_b64_tr_b16 a[32:33], v194 offset:16384
v_mfma_f32_16x16x32_bf16 v[4:7], a[0:3], a[132:135], v[4:7]
ds_read_b64_tr_b16 a[34:35], v194 offset:18432
v_mfma_f32_16x16x32_bf16 v[8:11], a[0:3], a[136:139], v[8:11]
ds_read_b128 a[144:147], v193 offset:0
v_mfma_f32_16x16x32_bf16 v[12:15], a[0:3], a[140:143], v[12:15]
ds_read_b128 a[148:151], v193 offset:2048
v_mfma_f32_16x16x32_bf16 v[16:19], a[4:7], a[128:131], v[16:19]
buffer_load_dwordx4 v213, s[20:23], s32 offen offset:3072 sc1 lds
v_mfma_f32_16x16x32_bf16 v[20:23], a[4:7], a[132:135], v[20:23]
ds_read_b128 a[152:155], v193 offset:4096
v_mfma_f32_16x16x32_bf16 v[24:27], a[4:7], a[136:139], v[24:27]
ds_read_b128 a[156:159], v193 offset:6144
v_mfma_f32_16x16x32_bf16 v[28:31], a[4:7], a[140:143], v[28:31]
ds_read_b64_tr_b16 a[36:37], v195 offset:16512
v_mfma_f32_16x16x32_bf16 v[32:35], a[8:11], a[128:131], v[32:35]
ds_read_b64_tr_b16 a[38:39], v195 offset:18560
s_add_u32 m0, s57, 114688
v_mfma_f32_16x16x32_bf16 v[36:39], a[8:11], a[132:135], v[36:39]
buffer_load_dwordx4 v208, s[36:39], s33 offen offset:0 lds
v_mfma_f32_16x16x32_bf16 v[40:43], a[8:11], a[136:139], v[40:43]
ds_read_b64_tr_b16 a[40:41], v194 offset:16640
v_mfma_f32_16x16x32_bf16 v[44:47], a[8:11], a[140:143], v[44:47]
ds_read_b64_tr_b16 a[42:43], v194 offset:18688
v_mfma_f32_16x16x32_bf16 v[48:51], a[12:15], a[128:131], v[48:51]
ds_read_b64_tr_b16 a[44:45], v195 offset:16768
v_mfma_f32_16x16x32_bf16 v[52:55], a[12:15], a[132:135], v[52:55]
ds_read_b64_tr_b16 a[46:47], v195 offset:18816
v_mfma_f32_16x16x32_bf16 v[56:59], a[12:15], a[136:139], v[56:59]
buffer_load_dwordx4 v209, s[36:39], s33 offen offset:1024 lds
v_mfma_f32_16x16x32_bf16 v[60:63], a[12:15], a[140:143], v[60:63]
ds_read_b64_tr_b16 a[48:49], v194 offset:16896
v_mfma_f32_16x16x32_bf16 v[64:67], a[16:19], a[128:131], v[64:67]
ds_read_b64_tr_b16 a[50:51], v194 offset:18944
v_mfma_f32_16x16x32_bf16 v[68:71], a[16:19], a[132:135], v[68:71]
ds_read_b64_tr_b16 a[52:53], v195 offset:17024
v_mfma_f32_16x16x32_bf16 v[72:75], a[16:19], a[136:139], v[72:75]
ds_read_b64_tr_b16 a[54:55], v195 offset:19072
v_mfma_f32_16x16x32_bf16 v[76:79], a[16:19], a[140:143], v[76:79]
buffer_load_dwordx4 v210, s[36:39], s33 offen offset:2048 lds
v_mfma_f32_16x16x32_bf16 v[80:83], a[20:23], a[128:131], v[80:83]
ds_read_b64_tr_b16 a[56:57], v194 offset:17152
v_mfma_f32_16x16x32_bf16 v[84:87], a[20:23], a[132:135], v[84:87]
ds_read_b64_tr_b16 a[58:59], v194 offset:19200
v_mfma_f32_16x16x32_bf16 v[88:91], a[20:23], a[136:139], v[88:91]
ds_read_b64_tr_b16 a[60:61], v195 offset:17280
v_mfma_f32_16x16x32_bf16 v[92:95], a[20:23], a[140:143], v[92:95]
ds_read_b64_tr_b16 a[62:63], v195 offset:19328
v_mfma_f32_16x16x32_bf16 v[96:99], a[24:27], a[128:131], v[96:99]
buffer_load_dwordx4 v211, s[36:39], s33 offen offset:3072 lds
v_mfma_f32_16x16x32_bf16 v[100:103], a[24:27], a[132:135], v[100:103]
s_add_u32 s24, s24, 1
s_and_b32 s24, s24, 7
v_mfma_f32_16x16x32_bf16 v[104:107], a[24:27], a[136:139], v[104:107]
s_lshl_b32 s32, s24, 7
s_add_u32 s31, s32, s60
v_mfma_f32_16x16x32_bf16 v[108:111], a[24:27], a[140:143], v[108:111]
s_add_u32 s32, s32, s96
s_lshl_b32 s33, s24, 19
v_mfma_f32_16x16x32_bf16 v[112:115], a[28:31], a[128:131], v[112:115]
s_add_u32 s33, s33, s97
s_add_u32 s61, s62, 12288
buffer_store_dwordx4 v[176:179], v252, s[40:43], s61 offen sc1
v_mfma_f32_16x16x32_bf16 v[116:119], a[28:31], a[132:135], v[116:119]
s_add_u32 s61, s62, 13312
buffer_store_dwordx4 v[180:183], v252, s[40:43], s61 offen sc1
v_mfma_f32_16x16x32_bf16 v[120:123], a[28:31], a[136:139], v[120:123]
v_mfma_f32_16x16x32_bf16 v[124:127], a[28:31], a[140:143], v[124:127]
s_waitcnt vmcnt(16) lgkmcnt(0)
s_barrier
s_nop 0
v_mfma_f32_16x16x32_bf16 v[0:3], a[32:35], a[144:147], v[0:3]
ds_read_b64_tr_b16 a[0:1], v198 offset:0
s_add_u32 m0, s56, 16384
v_mfma_f32_16x16x32_bf16 v[4:7], a[32:35], a[148:151], v[4:7]
buffer_load_dwordx4 v200, s[20:23], s31 offen offset:0 sc1 lds
v_mfma_f32_16x16x32_bf16 v[8:11], a[32:35], a[152:155], v[8:11]
ds_read_b64_tr_b16 a[2:3], v198 offset:1024
v_mfma_f32_16x16x32_bf16 v[12:15], a[32:35], a[156:159], v[12:15]
ds_read_b128 a[128:131], v196 offset:0
v_mfma_f32_16x16x32_bf16 v[16:19], a[36:39], a[144:147], v[16:19]
ds_read_b128 a[132:135], v196 offset:2048
v_mfma_f32_16x16x32_bf16 v[20:23], a[36:39], a[148:151], v[20:23]
ds_read_b128 a[136:139], v196 offset:4096
v_mfma_f32_16x16x32_bf16 v[24:27], a[36:39], a[152:155], v[24:27]
buffer_load_dwordx4 v201, s[20:23], s31 offen offset:1024 sc1 lds
v_mfma_f32_16x16x32_bf16 v[28:31], a[36:39], a[156:159], v[28:31]
ds_read_b128 a[140:143], v196 offset:6144
v_mfma_f32_16x16x32_bf16 v[32:35], a[40:43], a[144:147], v[32:35]
ds_read_b64_tr_b16 a[4:5], v199 offset:128
v_mfma_f32_16x16x32_bf16 v[36:39], a[40:43], a[148:151], v[36:39]
ds_read_b64_tr_b16 a[6:7], v199 offset:1152
v_cvt_pk_bf16_f32 v214, v0, v1
v_cvt_pk_bf16_f32 v215, v2, v3
v_cvt_pk_bf16_f32 v216, v16, v17
v_cvt_pk_bf16_f32 v217, v18, v19
v_mfma_f32_16x16x32_bf16 v[40:43], a[40:43], a[152:155], v[40:43]
ds_read_b64_tr_b16 a[8:9], v198 offset:256
v_accvgpr_write_b32 a164, v214
v_accvgpr_write_b32 a165, v215
v_accvgpr_write_b32 a166, v216
v_accvgpr_write_b32 a167, v217
v_mfma_f32_16x16x32_bf16 v[44:47], a[40:43], a[156:159], v[44:47]
buffer_load_dwordx4 v212, s[20:23], s31 offen offset:2048 sc1 lds
v_cvt_pk_bf16_f32 v218, v4, v5
v_cvt_pk_bf16_f32 v219, v6, v7
v_cvt_pk_bf16_f32 v220, v20, v21
v_cvt_pk_bf16_f32 v221, v22, v23
v_mfma_f32_16x16x32_bf16 v[48:51], a[44:47], a[144:147], v[48:51]
ds_read_b64_tr_b16 a[10:11], v198 offset:1280
v_accvgpr_write_b32 a180, v218
v_accvgpr_write_b32 a181, v219
v_accvgpr_write_b32 a182, v220
v_accvgpr_write_b32 a183, v221
v_mfma_f32_16x16x32_bf16 v[52:55], a[44:47], a[148:151], v[52:55]
ds_read_b64_tr_b16 a[12:13], v199 offset:384
v_cvt_pk_bf16_f32 v214, v8, v9
v_cvt_pk_bf16_f32 v215, v10, v11
v_cvt_pk_bf16_f32 v216, v24, v25
v_cvt_pk_bf16_f32 v217, v26, v27
v_mfma_f32_16x16x32_bf16 v[56:59], a[44:47], a[152:155], v[56:59]
ds_read_b64_tr_b16 a[14:15], v199 offset:1408
v_accvgpr_write_b32 a196, v214
v_accvgpr_write_b32 a197, v215
v_accvgpr_write_b32 a198, v216
v_accvgpr_write_b32 a199, v217
v_mfma_f32_16x16x32_bf16 v[60:63], a[44:47], a[156:159], v[60:63]
ds_read_b64_tr_b16 a[16:17], v198 offset:512
v_cvt_pk_bf16_f32 v218, v12, v13
v_cvt_pk_bf16_f32 v219, v14, v15
v_cvt_pk_bf16_f32 v220, v28, v29
v_cvt_pk_bf16_f32 v221, v30, v31
v_mfma_f32_16x16x32_bf16 v[64:67], a[48:51], a[144:147], v[64:67]
buffer_load_dwordx4 v213, s[20:23], s31 offen offset:3072 sc1 lds
v_accvgpr_write_b32 a212, v218
v_accvgpr_write_b32 a213, v219
v_accvgpr_write_b32 a214, v220
v_accvgpr_write_b32 a215, v221
v_mfma_f32_16x16x32_bf16 v[68:71], a[48:51], a[148:151], v[68:71]
ds_read_b64_tr_b16 a[18:19], v198 offset:1536
v_cvt_pk_bf16_f32 v214, v32, v33
v_cvt_pk_bf16_f32 v215, v34, v35
v_cvt_pk_bf16_f32 v216, v48, v49
v_cvt_pk_bf16_f32 v217, v50, v51
v_mfma_f32_16x16x32_bf16 v[72:75], a[48:51], a[152:155], v[72:75]
ds_read_b64_tr_b16 a[20:21], v199 offset:640
v_accvgpr_write_b32 a168, v214
v_accvgpr_write_b32 a169, v215
v_accvgpr_write_b32 a170, v216
v_accvgpr_write_b32 a171, v217
v_mfma_f32_16x16x32_bf16 v[76:79], a[48:51], a[156:159], v[76:79]
ds_read_b64_tr_b16 a[22:23], v199 offset:1664
v_cvt_pk_bf16_f32 v218, v36, v37
v_cvt_pk_bf16_f32 v219, v38, v39
v_cvt_pk_bf16_f32 v220, v52, v53
v_cvt_pk_bf16_f32 v221, v54, v55
v_mfma_f32_16x16x32_bf16 v[80:83], a[52:55], a[144:147], v[80:83]
ds_read_b64_tr_b16 a[24:25], v198 offset:768
s_add_u32 m0, s56, 20480
v_accvgpr_write_b32 a184, v218
v_accvgpr_write_b32 a185, v219
v_accvgpr_write_b32 a186, v220
v_accvgpr_write_b32 a187, v221
v_mfma_f32_16x16x32_bf16 v[84:87], a[52:55], a[148:151], v[84:87]
buffer_load_dwordx4 v200, s[20:23], s32 offen offset:0 sc1 lds
v_cvt_pk_bf16_f32 v214, v40, v41
v_cvt_pk_bf16_f32 v215, v42, v43
v_cvt_pk_bf16_f32 v216, v56, v57
v_cvt_pk_bf16_f32 v217, v58, v59
v_mfma_f32_16x16x32_bf16 v[88:91], a[52:55], a[152:155], v[88:91]
ds_read_b64_tr_b16 a[26:27], v198 offset:1792
v_accvgpr_write_b32 a200, v214
v_accvgpr_write_b32 a201, v215
v_accvgpr_write_b32 a202, v216
v_accvgpr_write_b32 a203, v217
v_mfma_f32_16x16x32_bf16 v[92:95], a[52:55], a[156:159], v[92:95]
ds_read_b64_tr_b16 a[28:29], v199 offset:896
v_cvt_pk_bf16_f32 v218, v44, v45
v_cvt_pk_bf16_f32 v219, v46, v47
v_cvt_pk_bf16_f32 v220, v60, v61
v_cvt_pk_bf16_f32 v221, v62, v63
v_mfma_f32_16x16x32_bf16 v[96:99], a[56:59], a[144:147], v[96:99]
ds_read_b64_tr_b16 a[30:31], v199 offset:1920
v_accvgpr_write_b32 a216, v218
v_accvgpr_write_b32 a217, v219
v_accvgpr_write_b32 a218, v220
v_accvgpr_write_b32 a219, v221
v_mfma_f32_16x16x32_bf16 v[100:103], a[56:59], a[148:151], v[100:103]
v_cvt_pk_bf16_f32 v214, v64, v65
v_cvt_pk_bf16_f32 v215, v66, v67
v_cvt_pk_bf16_f32 v216, v80, v81
v_cvt_pk_bf16_f32 v217, v82, v83
v_mfma_f32_16x16x32_bf16 v[104:107], a[56:59], a[152:155], v[104:107]
buffer_load_dwordx4 v201, s[20:23], s32 offen offset:1024 sc1 lds
v_accvgpr_write_b32 a172, v214
v_accvgpr_write_b32 a173, v215
v_accvgpr_write_b32 a174, v216
v_accvgpr_write_b32 a175, v217
v_mfma_f32_16x16x32_bf16 v[108:111], a[56:59], a[156:159], v[108:111]
v_cvt_pk_bf16_f32 v218, v68, v69
v_cvt_pk_bf16_f32 v219, v70, v71
v_cvt_pk_bf16_f32 v220, v84, v85
v_cvt_pk_bf16_f32 v221, v86, v87
v_mfma_f32_16x16x32_bf16 v[112:115], a[60:63], a[144:147], v[112:115]
v_accvgpr_write_b32 a188, v218
v_accvgpr_write_b32 a189, v219
v_accvgpr_write_b32 a190, v220
v_accvgpr_write_b32 a191, v221
v_mfma_f32_16x16x32_bf16 v[116:119], a[60:63], a[148:151], v[116:119]
v_cvt_pk_bf16_f32 v214, v72, v73
v_cvt_pk_bf16_f32 v215, v74, v75
v_cvt_pk_bf16_f32 v216, v88, v89
v_cvt_pk_bf16_f32 v217, v90, v91
v_mfma_f32_16x16x32_bf16 v[120:123], a[60:63], a[152:155], v[120:123]
v_accvgpr_write_b32 a204, v214
v_accvgpr_write_b32 a205, v215
v_accvgpr_write_b32 a206, v216
v_accvgpr_write_b32 a207, v217
v_mfma_f32_16x16x32_bf16 v[124:127], a[60:63], a[156:159], v[124:127]
buffer_load_dwordx4 v212, s[20:23], s32 offen offset:2048 sc1 lds
v_cvt_pk_bf16_f32 v218, v76, v77
v_cvt_pk_bf16_f32 v219, v78, v79
v_cvt_pk_bf16_f32 v220, v92, v93
v_cvt_pk_bf16_f32 v221, v94, v95
s_waitcnt lgkmcnt(0)
v_mfma_f32_16x16x32_bf16 v[0:3], a[0:3], a[128:131], 0
ds_read_b64_tr_b16 a[32:33], v198 offset:8192
v_accvgpr_write_b32 a220, v218
v_accvgpr_write_b32 a221, v219
v_accvgpr_write_b32 a222, v220
v_accvgpr_write_b32 a223, v221
v_mfma_f32_16x16x32_bf16 v[4:7], a[0:3], a[132:135], 0
ds_read_b64_tr_b16 a[34:35], v198 offset:9216
v_cvt_pk_bf16_f32 v214, v96, v97
v_cvt_pk_bf16_f32 v215, v98, v99
v_cvt_pk_bf16_f32 v216, v112, v113
v_cvt_pk_bf16_f32 v217, v114, v115
v_mfma_f32_16x16x32_bf16 v[8:11], a[0:3], a[136:139], 0
ds_read_b128 a[144:147], v197 offset:0
v_accvgpr_write_b32 a176, v214
v_accvgpr_write_b32 a177, v215
v_accvgpr_write_b32 a178, v216
v_accvgpr_write_b32 a179, v217
v_mfma_f32_16x16x32_bf16 v[12:15], a[0:3], a[140:143], 0
ds_read_b128 a[148:151], v197 offset:2048
v_cvt_pk_bf16_f32 v218, v100, v101
v_cvt_pk_bf16_f32 v219, v102, v103
v_cvt_pk_bf16_f32 v220, v116, v117
v_cvt_pk_bf16_f32 v221, v118, v119
v_mfma_f32_16x16x32_bf16 v[16:19], a[4:7], a[128:131], 0
buffer_load_dwordx4 v213, s[20:23], s32 offen offset:3072 sc1 lds
v_accvgpr_write_b32 a192, v218
v_accvgpr_write_b32 a193, v219
v_accvgpr_write_b32 a194, v220
v_accvgpr_write_b32 a195, v221
v_mfma_f32_16x16x32_bf16 v[20:23], a[4:7], a[132:135], 0
ds_read_b128 a[152:155], v197 offset:4096
v_cvt_pk_bf16_f32 v214, v104, v105
v_cvt_pk_bf16_f32 v215, v106, v107
v_cvt_pk_bf16_f32 v216, v120, v121
v_cvt_pk_bf16_f32 v217, v122, v123
v_mfma_f32_16x16x32_bf16 v[24:27], a[4:7], a[136:139], 0
ds_read_b128 a[156:159], v197 offset:6144
v_accvgpr_write_b32 a208, v214
v_accvgpr_write_b32 a209, v215
v_accvgpr_write_b32 a210, v216
v_accvgpr_write_b32 a211, v217
v_mfma_f32_16x16x32_bf16 v[28:31], a[4:7], a[140:143], 0
ds_read_b64_tr_b16 a[36:37], v199 offset:8320
v_cvt_pk_bf16_f32 v218, v108, v109
v_cvt_pk_bf16_f32 v219, v110, v111
v_cvt_pk_bf16_f32 v220, v124, v125
v_cvt_pk_bf16_f32 v221, v126, v127
v_mfma_f32_16x16x32_bf16 v[32:35], a[8:11], a[128:131], 0
ds_read_b64_tr_b16 a[38:39], v199 offset:9344
s_add_u32 m0, s57, 16384
v_accvgpr_write_b32 a224, v218
v_accvgpr_write_b32 a225, v219
v_accvgpr_write_b32 a226, v220
v_accvgpr_write_b32 a227, v221
v_mfma_f32_16x16x32_bf16 v[36:39], a[8:11], a[132:135], 0
buffer_load_dwordx4 v208, s[36:39], s33 offen offset:0 lds
v_mfma_f32_16x16x32_bf16 v[40:43], a[8:11], a[136:139], 0
ds_read_b64_tr_b16 a[40:41], v198 offset:8448
v_mfma_f32_16x16x32_bf16 v[44:47], a[8:11], a[140:143], 0
ds_read_b64_tr_b16 a[42:43], v198 offset:9472
v_mfma_f32_16x16x32_bf16 v[48:51], a[12:15], a[128:131], 0
ds_read_b64_tr_b16 a[44:45], v199 offset:8576
v_mfma_f32_16x16x32_bf16 v[52:55], a[12:15], a[132:135], 0
ds_read_b64_tr_b16 a[46:47], v199 offset:9600
v_mfma_f32_16x16x32_bf16 v[56:59], a[12:15], a[136:139], 0
buffer_load_dwordx4 v209, s[36:39], s33 offen offset:1024 lds
v_mfma_f32_16x16x32_bf16 v[60:63], a[12:15], a[140:143], 0
ds_read_b64_tr_b16 a[48:49], v198 offset:8704
v_mfma_f32_16x16x32_bf16 v[64:67], a[16:19], a[128:131], 0
ds_read_b64_tr_b16 a[50:51], v198 offset:9728
v_mfma_f32_16x16x32_bf16 v[68:71], a[16:19], a[132:135], 0
ds_read_b64_tr_b16 a[52:53], v199 offset:8832
v_mfma_f32_16x16x32_bf16 v[72:75], a[16:19], a[136:139], 0
ds_read_b64_tr_b16 a[54:55], v199 offset:9856
v_mfma_f32_16x16x32_bf16 v[76:79], a[16:19], a[140:143], 0
buffer_load_dwordx4 v210, s[36:39], s33 offen offset:2048 lds
v_mfma_f32_16x16x32_bf16 v[80:83], a[20:23], a[128:131], 0
ds_read_b64_tr_b16 a[56:57], v198 offset:8960
v_mfma_f32_16x16x32_bf16 v[84:87], a[20:23], a[132:135], 0
ds_read_b64_tr_b16 a[58:59], v198 offset:9984
v_mfma_f32_16x16x32_bf16 v[88:91], a[20:23], a[136:139], 0
ds_read_b64_tr_b16 a[60:61], v199 offset:9088
v_mfma_f32_16x16x32_bf16 v[92:95], a[20:23], a[140:143], 0
ds_read_b64_tr_b16 a[62:63], v199 offset:10112
v_mfma_f32_16x16x32_bf16 v[96:99], a[24:27], a[128:131], 0
buffer_load_dwordx4 v211, s[36:39], s33 offen offset:3072 lds
v_mfma_f32_16x16x32_bf16 v[100:103], a[24:27], a[132:135], 0
s_add_u32 s24, s24, 1
s_and_b32 s24, s24, 7
v_mfma_f32_16x16x32_bf16 v[104:107], a[24:27], a[136:139], 0
s_lshl_b32 s32, s24, 7
s_add_u32 s31, s32, s60
v_mfma_f32_16x16x32_bf16 v[108:111], a[24:27], a[140:143], 0
s_add_u32 s32, s32, s96
s_lshl_b32 s33, s24, 19
v_mfma_f32_16x16x32_bf16 v[112:115], a[28:31], a[128:131], 0
s_add_u32 s33, s33, s97
s_add_u32 s61, s62, 14336
buffer_store_dwordx4 v[184:187], v252, s[40:43], s61 offen sc1
v_mfma_f32_16x16x32_bf16 v[116:119], a[28:31], a[132:135], 0
s_add_u32 s61, s62, 15360
buffer_store_dwordx4 v[188:191], v252, s[40:43], s61 offen sc1
v_mfma_f32_16x16x32_bf16 v[120:123], a[28:31], a[136:139], 0
v_mfma_f32_16x16x32_bf16 v[124:127], a[28:31], a[140:143], 0
s_branch G3_END
G3_V:
ds_read_b64_tr_b16 a[0:1], v194 offset:0
ds_read_b64_tr_b16 a[2:3], v194 offset:2048
ds_read_b128 a[128:131], v192 offset:0
ds_read_b128 a[132:135], v192 offset:2048
ds_read_b128 a[136:139], v192 offset:4096
ds_read_b128 a[140:143], v192 offset:6144
ds_read_b64_tr_b16 a[4:5], v195 offset:128
ds_read_b64_tr_b16 a[6:7], v195 offset:2176
ds_read_b64_tr_b16 a[8:9], v194 offset:256
ds_read_b64_tr_b16 a[10:11], v194 offset:2304
ds_read_b64_tr_b16 a[12:13], v195 offset:384
ds_read_b64_tr_b16 a[14:15], v195 offset:2432
ds_read_b64_tr_b16 a[16:17], v194 offset:512
ds_read_b64_tr_b16 a[18:19], v194 offset:2560
ds_read_b64_tr_b16 a[20:21], v195 offset:640
ds_read_b64_tr_b16 a[22:23], v195 offset:2688
ds_read_b64_tr_b16 a[24:25], v194 offset:768
ds_read_b64_tr_b16 a[26:27], v194 offset:2816
ds_read_b64_tr_b16 a[28:29], v195 offset:896
ds_read_b64_tr_b16 a[30:31], v195 offset:2944
s_waitcnt lgkmcnt(0)
v_mfma_f32_16x16x32_bf16 v[0:3], a[128:131], a[0:3], 0
ds_read_b64_tr_b16 a[32:33], v194 offset:16384
v_mfma_f32_16x16x32_bf16 v[4:7], a[132:135], a[0:3], 0
ds_read_b64_tr_b16 a[34:35], v194 offset:18432
v_mfma_f32_16x16x32_bf16 v[8:11], a[136:139], a[0:3], 0
ds_read_b128 a[144:147], v193 offset:0
v_mfma_f32_16x16x32_bf16 v[12:15], a[140:143], a[0:3], 0
ds_read_b128 a[148:151], v193 offset:2048
v_mfma_f32_16x16x32_bf16 v[16:19], a[128:131], a[4:7], 0
ds_read_b128 a[152:155], v193 offset:4096
v_mfma_f32_16x16x32_bf16 v[20:23], a[132:135], a[4:7], 0
ds_read_b128 a[156:159], v193 offset:6144
v_mfma_f32_16x16x32_bf16 v[24:27], a[136:139], a[4:7], 0
ds_read_b64_tr_b16 a[36:37], v195 offset:16512
v_mfma_f32_16x16x32_bf16 v[28:31], a[140:143], a[4:7], 0
ds_read_b64_tr_b16 a[38:39], v195 offset:18560
v_mfma_f32_16x16x32_bf16 v[32:35], a[128:131], a[8:11], 0
ds_read_b64_tr_b16 a[40:41], v194 offset:16640
v_mfma_f32_16x16x32_bf16 v[36:39], a[132:135], a[8:11], 0
ds_read_b64_tr_b16 a[42:43], v194 offset:18688
v_mfma_f32_16x16x32_bf16 v[40:43], a[136:139], a[8:11], 0
ds_read_b64_tr_b16 a[44:45], v195 offset:16768
v_mfma_f32_16x16x32_bf16 v[44:47], a[140:143], a[8:11], 0
ds_read_b64_tr_b16 a[46:47], v195 offset:18816
v_mfma_f32_16x16x32_bf16 v[48:51], a[128:131], a[12:15], 0
ds_read_b64_tr_b16 a[48:49], v194 offset:16896
v_mfma_f32_16x16x32_bf16 v[52:55], a[132:135], a[12:15], 0
ds_read_b64_tr_b16 a[50:51], v194 offset:18944
v_mfma_f32_16x16x32_bf16 v[56:59], a[136:139], a[12:15], 0
ds_read_b64_tr_b16 a[52:53], v195 offset:17024
v_mfma_f32_16x16x32_bf16 v[60:63], a[140:143], a[12:15], 0
ds_read_b64_tr_b16 a[54:55], v195 offset:19072
v_mfma_f32_16x16x32_bf16 v[64:67], a[128:131], a[16:19], 0
ds_read_b64_tr_b16 a[56:57], v194 offset:17152
v_mfma_f32_16x16x32_bf16 v[68:71], a[132:135], a[16:19], 0
ds_read_b64_tr_b16 a[58:59], v194 offset:19200
v_mfma_f32_16x16x32_bf16 v[72:75], a[136:139], a[16:19], 0
ds_read_b64_tr_b16 a[60:61], v195 offset:17280
v_mfma_f32_16x16x32_bf16 v[76:79], a[140:143], a[16:19], 0
ds_read_b64_tr_b16 a[62:63], v195 offset:19328
v_mfma_f32_16x16x32_bf16 v[80:83], a[128:131], a[20:23], 0
v_mfma_f32_16x16x32_bf16 v[84:87], a[132:135], a[20:23], 0
v_mfma_f32_16x16x32_bf16 v[88:91], a[136:139], a[20:23], 0
v_mfma_f32_16x16x32_bf16 v[92:95], a[140:143], a[20:23], 0
v_mfma_f32_16x16x32_bf16 v[96:99], a[128:131], a[24:27], 0
v_mfma_f32_16x16x32_bf16 v[100:103], a[132:135], a[24:27], 0
v_mfma_f32_16x16x32_bf16 v[104:107], a[136:139], a[24:27], 0
v_mfma_f32_16x16x32_bf16 v[108:111], a[140:143], a[24:27], 0
v_mfma_f32_16x16x32_bf16 v[112:115], a[128:131], a[28:31], 0
v_mfma_f32_16x16x32_bf16 v[116:119], a[132:135], a[28:31], 0
v_mfma_f32_16x16x32_bf16 v[120:123], a[136:139], a[28:31], 0
v_mfma_f32_16x16x32_bf16 v[124:127], a[140:143], a[28:31], 0
s_waitcnt vmcnt(14) lgkmcnt(0)
s_barrier
v_add_u32 v192, 0xc000, v192
v_add_u32 v193, 0xc000, v193
v_add_u32 v194, 0xc000, v194
v_add_u32 v195, 0xc000, v195
s_nop 0
v_mfma_f32_16x16x32_bf16 v[0:3], a[144:147], a[32:35], v[0:3]
ds_read_b64_tr_b16 a[0:1], v194 offset:0
s_add_u32 m0, s34, 16384
v_mfma_f32_16x16x32_bf16 v[4:7], a[148:151], a[32:35], v[4:7]
buffer_load_dwordx4 v200, s[8:11], s28 offen offset:0 lds
v_mfma_f32_16x16x32_bf16 v[8:11], a[152:155], a[32:35], v[8:11]
ds_read_b64_tr_b16 a[2:3], v194 offset:2048
v_mfma_f32_16x16x32_bf16 v[12:15], a[156:159], a[32:35], v[12:15]
ds_read_b128 a[128:131], v192 offset:0
v_mfma_f32_16x16x32_bf16 v[16:19], a[144:147], a[36:39], v[16:19]
ds_read_b128 a[132:135], v192 offset:2048
v_mfma_f32_16x16x32_bf16 v[20:23], a[148:151], a[36:39], v[20:23]
ds_read_b128 a[136:139], v192 offset:4096
v_mfma_f32_16x16x32_bf16 v[24:27], a[152:155], a[36:39], v[24:27]
buffer_load_dwordx4 v201, s[8:11], s28 offen offset:1024 lds
v_mfma_f32_16x16x32_bf16 v[28:31], a[156:159], a[36:39], v[28:31]
ds_read_b128 a[140:143], v192 offset:6144
v_mfma_f32_16x16x32_bf16 v[32:35], a[144:147], a[40:43], v[32:35]
ds_read_b64_tr_b16 a[4:5], v195 offset:128
v_mfma_f32_16x16x32_bf16 v[36:39], a[148:151], a[40:43], v[36:39]
ds_read_b64_tr_b16 a[6:7], v195 offset:2176
v_mfma_f32_16x16x32_bf16 v[40:43], a[152:155], a[40:43], v[40:43]
ds_read_b64_tr_b16 a[8:9], v194 offset:256
v_mfma_f32_16x16x32_bf16 v[44:47], a[156:159], a[40:43], v[44:47]
buffer_load_dwordx4 v202, s[8:11], s28 offen offset:2048 lds
v_mfma_f32_16x16x32_bf16 v[48:51], a[144:147], a[44:47], v[48:51]
ds_read_b64_tr_b16 a[10:11], v194 offset:2304
v_mfma_f32_16x16x32_bf16 v[52:55], a[148:151], a[44:47], v[52:55]
ds_read_b64_tr_b16 a[12:13], v195 offset:384
v_mfma_f32_16x16x32_bf16 v[56:59], a[152:155], a[44:47], v[56:59]
ds_read_b64_tr_b16 a[14:15], v195 offset:2432
v_mfma_f32_16x16x32_bf16 v[60:63], a[156:159], a[44:47], v[60:63]
ds_read_b64_tr_b16 a[16:17], v194 offset:512
v_mfma_f32_16x16x32_bf16 v[64:67], a[144:147], a[48:51], v[64:67]
buffer_load_dwordx4 v203, s[8:11], s28 offen offset:3072 lds
v_mfma_f32_16x16x32_bf16 v[68:71], a[148:151], a[48:51], v[68:71]
ds_read_b64_tr_b16 a[18:19], v194 offset:2560
v_mfma_f32_16x16x32_bf16 v[72:75], a[152:155], a[48:51], v[72:75]
ds_read_b64_tr_b16 a[20:21], v195 offset:640
v_mfma_f32_16x16x32_bf16 v[76:79], a[156:159], a[48:51], v[76:79]
ds_read_b64_tr_b16 a[22:23], v195 offset:2688
v_mfma_f32_16x16x32_bf16 v[80:83], a[144:147], a[52:55], v[80:83]
ds_read_b64_tr_b16 a[24:25], v194 offset:768
s_add_u32 m0, s35, 16384
v_mfma_f32_16x16x32_bf16 v[84:87], a[148:151], a[52:55], v[84:87]
buffer_load_dwordx4 v204, s[12:15], s29 offen offset:0 lds
v_mfma_f32_16x16x32_bf16 v[88:91], a[152:155], a[52:55], v[88:91]
ds_read_b64_tr_b16 a[26:27], v194 offset:2816
v_mfma_f32_16x16x32_bf16 v[92:95], a[156:159], a[52:55], v[92:95]
ds_read_b64_tr_b16 a[28:29], v195 offset:896
v_mfma_f32_16x16x32_bf16 v[96:99], a[144:147], a[56:59], v[96:99]
ds_read_b64_tr_b16 a[30:31], v195 offset:2944
v_mfma_f32_16x16x32_bf16 v[100:103], a[148:151], a[56:59], v[100:103]
v_mfma_f32_16x16x32_bf16 v[104:107], a[152:155], a[56:59], v[104:107]
buffer_load_dwordx4 v204, s[16:19], s29 offen offset:1024 lds
v_mfma_f32_16x16x32_bf16 v[108:111], a[156:159], a[56:59], v[108:111]
v_mfma_f32_16x16x32_bf16 v[112:115], a[144:147], a[60:63], v[112:115]
v_mfma_f32_16x16x32_bf16 v[116:119], a[148:151], a[60:63], v[116:119]
v_mfma_f32_16x16x32_bf16 v[120:123], a[152:155], a[60:63], v[120:123]
v_mfma_f32_16x16x32_bf16 v[124:127], a[156:159], a[60:63], v[124:127]
buffer_load_dwordx4 v205, s[12:15], s29 offen offset:2048 lds
s_waitcnt lgkmcnt(0)
v_mfma_f32_16x16x32_bf16 v[0:3], a[128:131], a[0:3], v[0:3]
ds_read_b64_tr_b16 a[32:33], v194 offset:16384
v_mfma_f32_16x16x32_bf16 v[4:7], a[132:135], a[0:3], v[4:7]
ds_read_b64_tr_b16 a[34:35], v194 offset:18432
v_mfma_f32_16x16x32_bf16 v[8:11], a[136:139], a[0:3], v[8:11]
ds_read_b128 a[144:147], v193 offset:0
v_mfma_f32_16x16x32_bf16 v[12:15], a[140:143], a[0:3], v[12:15]
ds_read_b128 a[148:151], v193 offset:2048
v_mfma_f32_16x16x32_bf16 v[16:19], a[128:131], a[4:7], v[16:19]
buffer_load_dwordx4 v205, s[16:19], s29 offen offset:3072 lds
v_mfma_f32_16x16x32_bf16 v[20:23], a[132:135], a[4:7], v[20:23]
ds_read_b128 a[152:155], v193 offset:4096
v_mfma_f32_16x16x32_bf16 v[24:27], a[136:139], a[4:7], v[24:27]
ds_read_b128 a[156:159], v193 offset:6144
v_mfma_f32_16x16x32_bf16 v[28:31], a[140:143], a[4:7], v[28:31]
ds_read_b64_tr_b16 a[36:37], v195 offset:16512
v_mfma_f32_16x16x32_bf16 v[32:35], a[128:131], a[8:11], v[32:35]
ds_read_b64_tr_b16 a[38:39], v195 offset:18560
s_add_u32 m0, s35, 20480
v_mfma_f32_16x16x32_bf16 v[36:39], a[132:135], a[8:11], v[36:39]
buffer_load_dwordx4 v206, s[12:15], s30 offen offset:0 lds
v_mfma_f32_16x16x32_bf16 v[40:43], a[136:139], a[8:11], v[40:43]
ds_read_b64_tr_b16 a[40:41], v194 offset:16640
v_mfma_f32_16x16x32_bf16 v[44:47], a[140:143], a[8:11], v[44:47]
ds_read_b64_tr_b16 a[42:43], v194 offset:18688
v_mfma_f32_16x16x32_bf16 v[48:51], a[128:131], a[12:15], v[48:51]
ds_read_b64_tr_b16 a[44:45], v195 offset:16768
v_mfma_f32_16x16x32_bf16 v[52:55], a[132:135], a[12:15], v[52:55]
ds_read_b64_tr_b16 a[46:47], v195 offset:18816
v_mfma_f32_16x16x32_bf16 v[56:59], a[136:139], a[12:15], v[56:59]
buffer_load_dwordx4 v206, s[16:19], s30 offen offset:1024 lds
v_mfma_f32_16x16x32_bf16 v[60:63], a[140:143], a[12:15], v[60:63]
ds_read_b64_tr_b16 a[48:49], v194 offset:16896
v_mfma_f32_16x16x32_bf16 v[64:67], a[128:131], a[16:19], v[64:67]
ds_read_b64_tr_b16 a[50:51], v194 offset:18944
v_mfma_f32_16x16x32_bf16 v[68:71], a[132:135], a[16:19], v[68:71]
ds_read_b64_tr_b16 a[52:53], v195 offset:17024
v_mfma_f32_16x16x32_bf16 v[72:75], a[136:139], a[16:19], v[72:75]
ds_read_b64_tr_b16 a[54:55], v195 offset:19072
v_mfma_f32_16x16x32_bf16 v[76:79], a[140:143], a[16:19], v[76:79]
buffer_load_dwordx4 v207, s[12:15], s30 offen offset:2048 lds
v_mfma_f32_16x16x32_bf16 v[80:83], a[128:131], a[20:23], v[80:83]
ds_read_b64_tr_b16 a[56:57], v194 offset:17152
v_mfma_f32_16x16x32_bf16 v[84:87], a[132:135], a[20:23], v[84:87]
ds_read_b64_tr_b16 a[58:59], v194 offset:19200
v_mfma_f32_16x16x32_bf16 v[88:91], a[136:139], a[20:23], v[88:91]
ds_read_b64_tr_b16 a[60:61], v195 offset:17280
v_mfma_f32_16x16x32_bf16 v[92:95], a[140:143], a[20:23], v[92:95]
)ASM"
R"ASM(ds_read_b64_tr_b16 a[62:63], v195 offset:19328
v_mfma_f32_16x16x32_bf16 v[96:99], a[128:131], a[24:27], v[96:99]
buffer_load_dwordx4 v207, s[16:19], s30 offen offset:3072 lds
v_mfma_f32_16x16x32_bf16 v[100:103], a[132:135], a[24:27], v[100:103]
s_add_u32 s24, s24, 1
s_cmp_eq_u32 s24, 8
v_mfma_f32_16x16x32_bf16 v[104:107], a[136:139], a[24:27], v[104:107]
s_cselect_b32 s24, 1, s24
s_lshl_b32 s28, s24, 7
v_mfma_f32_16x16x32_bf16 v[108:111], a[140:143], a[24:27], v[108:111]
s_add_u32 s28, s28, s25
s_lshl_b32 s29, s24, 19
v_mfma_f32_16x16x32_bf16 v[112:115], a[128:131], a[28:31], v[112:115]
s_add_u32 s29, s29, s58
s_add_u32 s30, s29, 65536
v_mfma_f32_16x16x32_bf16 v[116:119], a[132:135], a[28:31], v[116:119]
v_mfma_f32_16x16x32_bf16 v[120:123], a[136:139], a[28:31], v[120:123]
v_mfma_f32_16x16x32_bf16 v[124:127], a[140:143], a[28:31], v[124:127]
s_waitcnt vmcnt(14) lgkmcnt(0)
s_barrier
v_add_u32 v192, 0xc000, v192
v_add_u32 v193, 0xc000, v193
v_add_u32 v194, 0xc000, v194
v_add_u32 v195, 0xc000, v195
s_nop 0
v_mfma_f32_16x16x32_bf16 v[0:3], a[144:147], a[32:35], v[0:3]
ds_read_b64_tr_b16 a[0:1], v194 offset:0
s_add_u32 m0, s34, 65536
v_mfma_f32_16x16x32_bf16 v[4:7], a[148:151], a[32:35], v[4:7]
buffer_load_dwordx4 v200, s[8:11], s28 offen offset:0 lds
v_mfma_f32_16x16x32_bf16 v[8:11], a[152:155], a[32:35], v[8:11]
ds_read_b64_tr_b16 a[2:3], v194 offset:2048
v_mfma_f32_16x16x32_bf16 v[12:15], a[156:159], a[32:35], v[12:15]
ds_read_b128 a[128:131], v192 offset:0
v_mfma_f32_16x16x32_bf16 v[16:19], a[144:147], a[36:39], v[16:19]
ds_read_b128 a[132:135], v192 offset:2048
v_mfma_f32_16x16x32_bf16 v[20:23], a[148:151], a[36:39], v[20:23]
ds_read_b128 a[136:139], v192 offset:4096
v_mfma_f32_16x16x32_bf16 v[24:27], a[152:155], a[36:39], v[24:27]
buffer_load_dwordx4 v201, s[8:11], s28 offen offset:1024 lds
v_mfma_f32_16x16x32_bf16 v[28:31], a[156:159], a[36:39], v[28:31]
ds_read_b128 a[140:143], v192 offset:6144
v_mfma_f32_16x16x32_bf16 v[32:35], a[144:147], a[40:43], v[32:35]
ds_read_b64_tr_b16 a[4:5], v195 offset:128
v_mfma_f32_16x16x32_bf16 v[36:39], a[148:151], a[40:43], v[36:39]
ds_read_b64_tr_b16 a[6:7], v195 offset:2176
v_mfma_f32_16x16x32_bf16 v[40:43], a[152:155], a[40:43], v[40:43]
ds_read_b64_tr_b16 a[8:9], v194 offset:256
v_mfma_f32_16x16x32_bf16 v[44:47], a[156:159], a[40:43], v[44:47]
buffer_load_dwordx4 v202, s[8:11], s28 offen offset:2048 lds
v_mfma_f32_16x16x32_bf16 v[48:51], a[144:147], a[44:47], v[48:51]
ds_read_b64_tr_b16 a[10:11], v194 offset:2304
v_mfma_f32_16x16x32_bf16 v[52:55], a[148:151], a[44:47], v[52:55]
ds_read_b64_tr_b16 a[12:13], v195 offset:384
v_mfma_f32_16x16x32_bf16 v[56:59], a[152:155], a[44:47], v[56:59]
ds_read_b64_tr_b16 a[14:15], v195 offset:2432
v_mfma_f32_16x16x32_bf16 v[60:63], a[156:159], a[44:47], v[60:63]
ds_read_b64_tr_b16 a[16:17], v194 offset:512
v_mfma_f32_16x16x32_bf16 v[64:67], a[144:147], a[48:51], v[64:67]
buffer_load_dwordx4 v203, s[8:11], s28 offen offset:3072 lds
v_mfma_f32_16x16x32_bf16 v[68:71], a[148:151], a[48:51], v[68:71]
ds_read_b64_tr_b16 a[18:19], v194 offset:2560
v_mfma_f32_16x16x32_bf16 v[72:75], a[152:155], a[48:51], v[72:75]
ds_read_b64_tr_b16 a[20:21], v195 offset:640
v_mfma_f32_16x16x32_bf16 v[76:79], a[156:159], a[48:51], v[76:79]
ds_read_b64_tr_b16 a[22:23], v195 offset:2688
v_mfma_f32_16x16x32_bf16 v[80:83], a[144:147], a[52:55], v[80:83]
ds_read_b64_tr_b16 a[24:25], v194 offset:768
s_add_u32 m0, s35, 65536
v_mfma_f32_16x16x32_bf16 v[84:87], a[148:151], a[52:55], v[84:87]
buffer_load_dwordx4 v204, s[12:15], s29 offen offset:0 lds
v_mfma_f32_16x16x32_bf16 v[88:91], a[152:155], a[52:55], v[88:91]
ds_read_b64_tr_b16 a[26:27], v194 offset:2816
v_mfma_f32_16x16x32_bf16 v[92:95], a[156:159], a[52:55], v[92:95]
ds_read_b64_tr_b16 a[28:29], v195 offset:896
v_mfma_f32_16x16x32_bf16 v[96:99], a[144:147], a[56:59], v[96:99]
ds_read_b64_tr_b16 a[30:31], v195 offset:2944
v_mfma_f32_16x16x32_bf16 v[100:103], a[148:151], a[56:59], v[100:103]
v_mfma_f32_16x16x32_bf16 v[104:107], a[152:155], a[56:59], v[104:107]
buffer_load_dwordx4 v204, s[16:19], s29 offen offset:1024 lds
v_mfma_f32_16x16x32_bf16 v[108:111], a[156:159], a[56:59], v[108:111]
v_mfma_f32_16x16x32_bf16 v[112:115], a[144:147], a[60:63], v[112:115]
v_mfma_f32_16x16x32_bf16 v[116:119], a[148:151], a[60:63], v[116:119]
v_mfma_f32_16x16x32_bf16 v[120:123], a[152:155], a[60:63], v[120:123]
v_mfma_f32_16x16x32_bf16 v[124:127], a[156:159], a[60:63], v[124:127]
buffer_load_dwordx4 v205, s[12:15], s29 offen offset:2048 lds
s_waitcnt lgkmcnt(0)
v_mfma_f32_16x16x32_bf16 v[0:3], a[128:131], a[0:3], v[0:3]
ds_read_b64_tr_b16 a[32:33], v194 offset:16384
v_mfma_f32_16x16x32_bf16 v[4:7], a[132:135], a[0:3], v[4:7]
ds_read_b64_tr_b16 a[34:35], v194 offset:18432
v_mfma_f32_16x16x32_bf16 v[8:11], a[136:139], a[0:3], v[8:11]
ds_read_b128 a[144:147], v193 offset:0
v_mfma_f32_16x16x32_bf16 v[12:15], a[140:143], a[0:3], v[12:15]
ds_read_b128 a[148:151], v193 offset:2048
v_mfma_f32_16x16x32_bf16 v[16:19], a[128:131], a[4:7], v[16:19]
buffer_load_dwordx4 v205, s[16:19], s29 offen offset:3072 lds
v_mfma_f32_16x16x32_bf16 v[20:23], a[132:135], a[4:7], v[20:23]
ds_read_b128 a[152:155], v193 offset:4096
v_mfma_f32_16x16x32_bf16 v[24:27], a[136:139], a[4:7], v[24:27]
ds_read_b128 a[156:159], v193 offset:6144
v_mfma_f32_16x16x32_bf16 v[28:31], a[140:143], a[4:7], v[28:31]
ds_read_b64_tr_b16 a[36:37], v195 offset:16512
v_mfma_f32_16x16x32_bf16 v[32:35], a[128:131], a[8:11], v[32:35]
ds_read_b64_tr_b16 a[38:39], v195 offset:18560
s_add_u32 m0, s35, 69632
v_mfma_f32_16x16x32_bf16 v[36:39], a[132:135], a[8:11], v[36:39]
buffer_load_dwordx4 v206, s[12:15], s30 offen offset:0 lds
v_mfma_f32_16x16x32_bf16 v[40:43], a[136:139], a[8:11], v[40:43]
ds_read_b64_tr_b16 a[40:41], v194 offset:16640
v_mfma_f32_16x16x32_bf16 v[44:47], a[140:143], a[8:11], v[44:47]
ds_read_b64_tr_b16 a[42:43], v194 offset:18688
v_mfma_f32_16x16x32_bf16 v[48:51], a[128:131], a[12:15], v[48:51]
ds_read_b64_tr_b16 a[44:45], v195 offset:16768
v_mfma_f32_16x16x32_bf16 v[52:55], a[132:135], a[12:15], v[52:55]
ds_read_b64_tr_b16 a[46:47], v195 offset:18816
v_mfma_f32_16x16x32_bf16 v[56:59], a[136:139], a[12:15], v[56:59]
buffer_load_dwordx4 v206, s[16:19], s30 offen offset:1024 lds
v_mfma_f32_16x16x32_bf16 v[60:63], a[140:143], a[12:15], v[60:63]
ds_read_b64_tr_b16 a[48:49], v194 offset:16896
v_mfma_f32_16x16x32_bf16 v[64:67], a[128:131], a[16:19], v[64:67]
ds_read_b64_tr_b16 a[50:51], v194 offset:18944
v_mfma_f32_16x16x32_bf16 v[68:71], a[132:135], a[16:19], v[68:71]
ds_read_b64_tr_b16 a[52:53], v195 offset:17024
v_mfma_f32_16x16x32_bf16 v[72:75], a[136:139], a[16:19], v[72:75]
ds_read_b64_tr_b16 a[54:55], v195 offset:19072
v_mfma_f32_16x16x32_bf16 v[76:79], a[140:143], a[16:19], v[76:79]
buffer_load_dwordx4 v207, s[12:15], s30 offen offset:2048 lds
v_mfma_f32_16x16x32_bf16 v[80:83], a[128:131], a[20:23], v[80:83]
ds_read_b64_tr_b16 a[56:57], v194 offset:17152
v_mfma_f32_16x16x32_bf16 v[84:87], a[132:135], a[20:23], v[84:87]
ds_read_b64_tr_b16 a[58:59], v194 offset:19200
v_mfma_f32_16x16x32_bf16 v[88:91], a[136:139], a[20:23], v[88:91]
ds_read_b64_tr_b16 a[60:61], v195 offset:17280
v_mfma_f32_16x16x32_bf16 v[92:95], a[140:143], a[20:23], v[92:95]
ds_read_b64_tr_b16 a[62:63], v195 offset:19328
v_mfma_f32_16x16x32_bf16 v[96:99], a[128:131], a[24:27], v[96:99]
buffer_load_dwordx4 v207, s[16:19], s30 offen offset:3072 lds
v_mfma_f32_16x16x32_bf16 v[100:103], a[132:135], a[24:27], v[100:103]
s_add_u32 s24, s24, 1
s_cmp_eq_u32 s24, 8
v_mfma_f32_16x16x32_bf16 v[104:107], a[136:139], a[24:27], v[104:107]
s_cselect_b32 s24, 1, s24
s_lshl_b32 s28, s24, 7
v_mfma_f32_16x16x32_bf16 v[108:111], a[140:143], a[24:27], v[108:111]
s_add_u32 s28, s28, s25
s_lshl_b32 s29, s24, 19
v_mfma_f32_16x16x32_bf16 v[112:115], a[128:131], a[28:31], v[112:115]
s_add_u32 s29, s29, s58
s_add_u32 s30, s29, 65536
v_mfma_f32_16x16x32_bf16 v[116:119], a[132:135], a[28:31], v[116:119]
v_mfma_f32_16x16x32_bf16 v[120:123], a[136:139], a[28:31], v[120:123]
v_mfma_f32_16x16x32_bf16 v[124:127], a[140:143], a[28:31], v[124:127]
s_waitcnt vmcnt(12) lgkmcnt(0)
s_barrier
v_add_u32 v192, 0xfffe8000, v192
v_add_u32 v193, 0xfffe8000, v193
v_add_u32 v194, 0xfffe8000, v194
v_add_u32 v195, 0xfffe8000, v195
s_nop 0
v_mfma_f32_16x16x32_bf16 v[0:3], a[144:147], a[32:35], v[0:3]
ds_read_b64_tr_b16 a[0:1], v194 offset:0
s_add_u32 m0, s34, 114688
v_mfma_f32_16x16x32_bf16 v[4:7], a[148:151], a[32:35], v[4:7]
buffer_load_dwordx4 v200, s[8:11], s28 offen offset:0 lds
v_mfma_f32_16x16x32_bf16 v[8:11], a[152:155], a[32:35], v[8:11]
ds_read_b64_tr_b16 a[2:3], v194 offset:2048
v_mfma_f32_16x16x32_bf16 v[12:15], a[156:159], a[32:35], v[12:15]
ds_read_b128 a[128:131], v192 offset:0
v_mfma_f32_16x16x32_bf16 v[16:19], a[144:147], a[36:39], v[16:19]
ds_read_b128 a[132:135], v192 offset:2048
v_mfma_f32_16x16x32_bf16 v[20:23], a[148:151], a[36:39], v[20:23]
ds_read_b128 a[136:139], v192 offset:4096
v_mfma_f32_16x16x32_bf16 v[24:27], a[152:155], a[36:39], v[24:27]
buffer_load_dwordx4 v201, s[8:11], s28 offen offset:1024 lds
v_mfma_f32_16x16x32_bf16 v[28:31], a[156:159], a[36:39], v[28:31]
ds_read_b128 a[140:143], v192 offset:6144
v_mfma_f32_16x16x32_bf16 v[32:35], a[144:147], a[40:43], v[32:35]
ds_read_b64_tr_b16 a[4:5], v195 offset:128
v_mfma_f32_16x16x32_bf16 v[36:39], a[148:151], a[40:43], v[36:39]
ds_read_b64_tr_b16 a[6:7], v195 offset:2176
v_mfma_f32_16x16x32_bf16 v[40:43], a[152:155], a[40:43], v[40:43]
ds_read_b64_tr_b16 a[8:9], v194 offset:256
v_mfma_f32_16x16x32_bf16 v[44:47], a[156:159], a[40:43], v[44:47]
buffer_load_dwordx4 v202, s[8:11], s28 offen offset:2048 lds
v_mfma_f32_16x16x32_bf16 v[48:51], a[144:147], a[44:47], v[48:51]
ds_read_b64_tr_b16 a[10:11], v194 offset:2304
v_mfma_f32_16x16x32_bf16 v[52:55], a[148:151], a[44:47], v[52:55]
ds_read_b64_tr_b16 a[12:13], v195 offset:384
v_mfma_f32_16x16x32_bf16 v[56:59], a[152:155], a[44:47], v[56:59]
ds_read_b64_tr_b16 a[14:15], v195 offset:2432
v_mfma_f32_16x16x32_bf16 v[60:63], a[156:159], a[44:47], v[60:63]
ds_read_b64_tr_b16 a[16:17], v194 offset:512
v_mfma_f32_16x16x32_bf16 v[64:67], a[144:147], a[48:51], v[64:67]
buffer_load_dwordx4 v203, s[8:11], s28 offen offset:3072 lds
v_mfma_f32_16x16x32_bf16 v[68:71], a[148:151], a[48:51], v[68:71]
ds_read_b64_tr_b16 a[18:19], v194 offset:2560
v_mfma_f32_16x16x32_bf16 v[72:75], a[152:155], a[48:51], v[72:75]
ds_read_b64_tr_b16 a[20:21], v195 offset:640
v_mfma_f32_16x16x32_bf16 v[76:79], a[156:159], a[48:51], v[76:79]
ds_read_b64_tr_b16 a[22:23], v195 offset:2688
v_mfma_f32_16x16x32_bf16 v[80:83], a[144:147], a[52:55], v[80:83]
ds_read_b64_tr_b16 a[24:25], v194 offset:768
s_add_u32 m0, s35, 114688
v_mfma_f32_16x16x32_bf16 v[84:87], a[148:151], a[52:55], v[84:87]
buffer_load_dwordx4 v204, s[12:15], s29 offen offset:0 lds
v_mfma_f32_16x16x32_bf16 v[88:91], a[152:155], a[52:55], v[88:91]
ds_read_b64_tr_b16 a[26:27], v194 offset:2816
v_mfma_f32_16x16x32_bf16 v[92:95], a[156:159], a[52:55], v[92:95]
ds_read_b64_tr_b16 a[28:29], v195 offset:896
v_mfma_f32_16x16x32_bf16 v[96:99], a[144:147], a[56:59], v[96:99]
ds_read_b64_tr_b16 a[30:31], v195 offset:2944
v_mfma_f32_16x16x32_bf16 v[100:103], a[148:151], a[56:59], v[100:103]
v_mfma_f32_16x16x32_bf16 v[104:107], a[152:155], a[56:59], v[104:107]
buffer_load_dwordx4 v204, s[16:19], s29 offen offset:1024 lds
v_mfma_f32_16x16x32_bf16 v[108:111], a[156:159], a[56:59], v[108:111]
v_mfma_f32_16x16x32_bf16 v[112:115], a[144:147], a[60:63], v[112:115]
v_mfma_f32_16x16x32_bf16 v[116:119], a[148:151], a[60:63], v[116:119]
v_mfma_f32_16x16x32_bf16 v[120:123], a[152:155], a[60:63], v[120:123]
v_mfma_f32_16x16x32_bf16 v[124:127], a[156:159], a[60:63], v[124:127]
buffer_load_dwordx4 v205, s[12:15], s29 offen offset:2048 lds
s_waitcnt lgkmcnt(0)
v_mfma_f32_16x16x32_bf16 v[0:3], a[128:131], a[0:3], v[0:3]
ds_read_b64_tr_b16 a[32:33], v194 offset:16384
v_mfma_f32_16x16x32_bf16 v[4:7], a[132:135], a[0:3], v[4:7]
ds_read_b64_tr_b16 a[34:35], v194 offset:18432
v_mfma_f32_16x16x32_bf16 v[8:11], a[136:139], a[0:3], v[8:11]
ds_read_b128 a[144:147], v193 offset:0
v_mfma_f32_16x16x32_bf16 v[12:15], a[140:143], a[0:3], v[12:15]
ds_read_b128 a[148:151], v193 offset:2048
v_mfma_f32_16x16x32_bf16 v[16:19], a[128:131], a[4:7], v[16:19]
buffer_load_dwordx4 v205, s[16:19], s29 offen offset:3072 lds
v_mfma_f32_16x16x32_bf16 v[20:23], a[132:135], a[4:7], v[20:23]
ds_read_b128 a[152:155], v193 offset:4096
v_mfma_f32_16x16x32_bf16 v[24:27], a[136:139], a[4:7], v[24:27]
ds_read_b128 a[156:159], v193 offset:6144
v_mfma_f32_16x16x32_bf16 v[28:31], a[140:143], a[4:7], v[28:31]
ds_read_b64_tr_b16 a[36:37], v195 offset:16512
v_mfma_f32_16x16x32_bf16 v[32:35], a[128:131], a[8:11], v[32:35]
ds_read_b64_tr_b16 a[38:39], v195 offset:18560
s_add_u32 m0, s35, 118784
v_mfma_f32_16x16x32_bf16 v[36:39], a[132:135], a[8:11], v[36:39]
buffer_load_dwordx4 v206, s[12:15], s30 offen offset:0 lds
v_mfma_f32_16x16x32_bf16 v[40:43], a[136:139], a[8:11], v[40:43]
ds_read_b64_tr_b16 a[40:41], v194 offset:16640
v_mfma_f32_16x16x32_bf16 v[44:47], a[140:143], a[8:11], v[44:47]
ds_read_b64_tr_b16 a[42:43], v194 offset:18688
v_mfma_f32_16x16x32_bf16 v[48:51], a[128:131], a[12:15], v[48:51]
ds_read_b64_tr_b16 a[44:45], v195 offset:16768
v_mfma_f32_16x16x32_bf16 v[52:55], a[132:135], a[12:15], v[52:55]
ds_read_b64_tr_b16 a[46:47], v195 offset:18816
v_mfma_f32_16x16x32_bf16 v[56:59], a[136:139], a[12:15], v[56:59]
buffer_load_dwordx4 v206, s[16:19], s30 offen offset:1024 lds
v_mfma_f32_16x16x32_bf16 v[60:63], a[140:143], a[12:15], v[60:63]
ds_read_b64_tr_b16 a[48:49], v194 offset:16896
v_mfma_f32_16x16x32_bf16 v[64:67], a[128:131], a[16:19], v[64:67]
ds_read_b64_tr_b16 a[50:51], v194 offset:18944
v_mfma_f32_16x16x32_bf16 v[68:71], a[132:135], a[16:19], v[68:71]
ds_read_b64_tr_b16 a[52:53], v195 offset:17024
v_mfma_f32_16x16x32_bf16 v[72:75], a[136:139], a[16:19], v[72:75]
ds_read_b64_tr_b16 a[54:55], v195 offset:19072
v_mfma_f32_16x16x32_bf16 v[76:79], a[140:143], a[16:19], v[76:79]
buffer_load_dwordx4 v207, s[12:15], s30 offen offset:2048 lds
v_mfma_f32_16x16x32_bf16 v[80:83], a[128:131], a[20:23], v[80:83]
ds_read_b64_tr_b16 a[56:57], v194 offset:17152
v_mfma_f32_16x16x32_bf16 v[84:87], a[132:135], a[20:23], v[84:87]
ds_read_b64_tr_b16 a[58:59], v194 offset:19200
v_mfma_f32_16x16x32_bf16 v[88:91], a[136:139], a[20:23], v[88:91]
ds_read_b64_tr_b16 a[60:61], v195 offset:17280
v_mfma_f32_16x16x32_bf16 v[92:95], a[140:143], a[20:23], v[92:95]
ds_read_b64_tr_b16 a[62:63], v195 offset:19328
v_mfma_f32_16x16x32_bf16 v[96:99], a[128:131], a[24:27], v[96:99]
buffer_load_dwordx4 v207, s[16:19], s30 offen offset:3072 lds
v_mfma_f32_16x16x32_bf16 v[100:103], a[132:135], a[24:27], v[100:103]
s_add_u32 s24, s24, 1
s_cmp_eq_u32 s24, 8
v_mfma_f32_16x16x32_bf16 v[104:107], a[136:139], a[24:27], v[104:107]
s_cselect_b32 s24, 1, s24
s_lshl_b32 s28, s24, 7
v_mfma_f32_16x16x32_bf16 v[108:111], a[140:143], a[24:27], v[108:111]
s_add_u32 s28, s28, s25
s_lshl_b32 s29, s24, 19
v_mfma_f32_16x16x32_bf16 v[112:115], a[128:131], a[28:31], v[112:115]
s_add_u32 s29, s29, s58
s_add_u32 s30, s29, 65536
v_mfma_f32_16x16x32_bf16 v[116:119], a[132:135], a[28:31], v[116:119]
v_mfma_f32_16x16x32_bf16 v[120:123], a[136:139], a[28:31], v[120:123]
v_mfma_f32_16x16x32_bf16 v[124:127], a[140:143], a[28:31], v[124:127]
s_waitcnt vmcnt(12) lgkmcnt(0)
s_barrier
s_mov_b32 s48, s68
s_and_b32 s49, s69, 0xffff
s_mov_b32 s50, 0x400000
s_mov_b32 s51, 0x20000
v_lshrrev_b32 v254, 4, v253
v_lshlrev_b32 v254, 6, v254
v_and_or_b32 v254, v253, 7, v254
v_lshlrev_b32 v254, 7, v254
s_lshr_b32 s46, s6, 3
s_lshl_b32 s46, s46, 2
s_add_u32 s46, s46, s7
s_lshl_b32 s46, s46, 15
s_and_b32 s47, s6, 7
s_lshl_b32 s47, s47, 10
s_add_u32 s46, s46, s47
buffer_load_dword a252, v254, s[48:51], s46 offen
v_add_u32 v192, 0xc000, v192
v_add_u32 v193, 0xc000, v193
v_add_u32 v194, 0xc000, v194
v_add_u32 v195, 0xc000, v195
s_nop 0
v_mfma_f32_16x16x32_bf16 v[0:3], a[144:147], a[32:35], v[0:3]
ds_read_b64_tr_b16 a[0:1], v194 offset:0
s_add_u32 m0, s34, 16384
v_mfma_f32_16x16x32_bf16 v[4:7], a[148:151], a[32:35], v[4:7]
buffer_load_dwordx4 v200, s[8:11], s28 offen offset:0 lds
v_mfma_f32_16x16x32_bf16 v[8:11], a[152:155], a[32:35], v[8:11]
ds_read_b64_tr_b16 a[2:3], v194 offset:2048
v_mfma_f32_16x16x32_bf16 v[12:15], a[156:159], a[32:35], v[12:15]
ds_read_b128 a[128:131], v192 offset:0
v_mfma_f32_16x16x32_bf16 v[16:19], a[144:147], a[36:39], v[16:19]
ds_read_b128 a[132:135], v192 offset:2048
v_mfma_f32_16x16x32_bf16 v[20:23], a[148:151], a[36:39], v[20:23]
ds_read_b128 a[136:139], v192 offset:4096
v_mfma_f32_16x16x32_bf16 v[24:27], a[152:155], a[36:39], v[24:27]
buffer_load_dwordx4 v201, s[8:11], s28 offen offset:1024 lds
v_mfma_f32_16x16x32_bf16 v[28:31], a[156:159], a[36:39], v[28:31]
ds_read_b128 a[140:143], v192 offset:6144
v_mfma_f32_16x16x32_bf16 v[32:35], a[144:147], a[40:43], v[32:35]
ds_read_b64_tr_b16 a[4:5], v195 offset:128
v_mfma_f32_16x16x32_bf16 v[36:39], a[148:151], a[40:43], v[36:39]
ds_read_b64_tr_b16 a[6:7], v195 offset:2176
v_mfma_f32_16x16x32_bf16 v[40:43], a[152:155], a[40:43], v[40:43]
ds_read_b64_tr_b16 a[8:9], v194 offset:256
v_mfma_f32_16x16x32_bf16 v[44:47], a[156:159], a[40:43], v[44:47]
buffer_load_dwordx4 v202, s[8:11], s28 offen offset:2048 lds
v_mfma_f32_16x16x32_bf16 v[48:51], a[144:147], a[44:47], v[48:51]
ds_read_b64_tr_b16 a[10:11], v194 offset:2304
v_mfma_f32_16x16x32_bf16 v[52:55], a[148:151], a[44:47], v[52:55]
ds_read_b64_tr_b16 a[12:13], v195 offset:384
v_mfma_f32_16x16x32_bf16 v[56:59], a[152:155], a[44:47], v[56:59]
ds_read_b64_tr_b16 a[14:15], v195 offset:2432
v_mfma_f32_16x16x32_bf16 v[60:63], a[156:159], a[44:47], v[60:63]
ds_read_b64_tr_b16 a[16:17], v194 offset:512
v_mfma_f32_16x16x32_bf16 v[64:67], a[144:147], a[48:51], v[64:67]
buffer_load_dwordx4 v203, s[8:11], s28 offen offset:3072 lds
v_mfma_f32_16x16x32_bf16 v[68:71], a[148:151], a[48:51], v[68:71]
ds_read_b64_tr_b16 a[18:19], v194 offset:2560
v_mfma_f32_16x16x32_bf16 v[72:75], a[152:155], a[48:51], v[72:75]
ds_read_b64_tr_b16 a[20:21], v195 offset:640
v_mfma_f32_16x16x32_bf16 v[76:79], a[156:159], a[48:51], v[76:79]
ds_read_b64_tr_b16 a[22:23], v195 offset:2688
v_mfma_f32_16x16x32_bf16 v[80:83], a[144:147], a[52:55], v[80:83]
ds_read_b64_tr_b16 a[24:25], v194 offset:768
s_add_u32 m0, s35, 16384
v_mfma_f32_16x16x32_bf16 v[84:87], a[148:151], a[52:55], v[84:87]
buffer_load_dwordx4 v204, s[12:15], s29 offen offset:0 lds
v_mfma_f32_16x16x32_bf16 v[88:91], a[152:155], a[52:55], v[88:91]
ds_read_b64_tr_b16 a[26:27], v194 offset:2816
v_mfma_f32_16x16x32_bf16 v[92:95], a[156:159], a[52:55], v[92:95]
ds_read_b64_tr_b16 a[28:29], v195 offset:896
v_mfma_f32_16x16x32_bf16 v[96:99], a[144:147], a[56:59], v[96:99]
ds_read_b64_tr_b16 a[30:31], v195 offset:2944
v_mfma_f32_16x16x32_bf16 v[100:103], a[148:151], a[56:59], v[100:103]
v_mfma_f32_16x16x32_bf16 v[104:107], a[152:155], a[56:59], v[104:107]
buffer_load_dwordx4 v204, s[16:19], s29 offen offset:1024 lds
v_mfma_f32_16x16x32_bf16 v[108:111], a[156:159], a[56:59], v[108:111]
v_mfma_f32_16x16x32_bf16 v[112:115], a[144:147], a[60:63], v[112:115]
v_mfma_f32_16x16x32_bf16 v[116:119], a[148:151], a[60:63], v[116:119]
v_mfma_f32_16x16x32_bf16 v[120:123], a[152:155], a[60:63], v[120:123]
v_mfma_f32_16x16x32_bf16 v[124:127], a[156:159], a[60:63], v[124:127]
buffer_load_dwordx4 v205, s[12:15], s29 offen offset:2048 lds
s_waitcnt lgkmcnt(0)
v_mfma_f32_16x16x32_bf16 v[0:3], a[128:131], a[0:3], v[0:3]
ds_read_b64_tr_b16 a[32:33], v194 offset:16384
v_mfma_f32_16x16x32_bf16 v[4:7], a[132:135], a[0:3], v[4:7]
ds_read_b64_tr_b16 a[34:35], v194 offset:18432
v_mfma_f32_16x16x32_bf16 v[8:11], a[136:139], a[0:3], v[8:11]
ds_read_b128 a[144:147], v193 offset:0
v_mfma_f32_16x16x32_bf16 v[12:15], a[140:143], a[0:3], v[12:15]
ds_read_b128 a[148:151], v193 offset:2048
v_mfma_f32_16x16x32_bf16 v[16:19], a[128:131], a[4:7], v[16:19]
buffer_load_dwordx4 v205, s[16:19], s29 offen offset:3072 lds
v_mfma_f32_16x16x32_bf16 v[20:23], a[132:135], a[4:7], v[20:23]
ds_read_b128 a[152:155], v193 offset:4096
v_mfma_f32_16x16x32_bf16 v[24:27], a[136:139], a[4:7], v[24:27]
ds_read_b128 a[156:159], v193 offset:6144
v_mfma_f32_16x16x32_bf16 v[28:31], a[140:143], a[4:7], v[28:31]
ds_read_b64_tr_b16 a[36:37], v195 offset:16512
v_mfma_f32_16x16x32_bf16 v[32:35], a[128:131], a[8:11], v[32:35]
ds_read_b64_tr_b16 a[38:39], v195 offset:18560
s_add_u32 m0, s35, 20480
v_mfma_f32_16x16x32_bf16 v[36:39], a[132:135], a[8:11], v[36:39]
buffer_load_dwordx4 v206, s[12:15], s30 offen offset:0 lds
v_mfma_f32_16x16x32_bf16 v[40:43], a[136:139], a[8:11], v[40:43]
ds_read_b64_tr_b16 a[40:41], v194 offset:16640
v_mfma_f32_16x16x32_bf16 v[44:47], a[140:143], a[8:11], v[44:47]
ds_read_b64_tr_b16 a[42:43], v194 offset:18688
v_mfma_f32_16x16x32_bf16 v[48:51], a[128:131], a[12:15], v[48:51]
ds_read_b64_tr_b16 a[44:45], v195 offset:16768
v_mfma_f32_16x16x32_bf16 v[52:55], a[132:135], a[12:15], v[52:55]
ds_read_b64_tr_b16 a[46:47], v195 offset:18816
v_mfma_f32_16x16x32_bf16 v[56:59], a[136:139], a[12:15], v[56:59]
buffer_load_dwordx4 v206, s[16:19], s30 offen offset:1024 lds
v_mfma_f32_16x16x32_bf16 v[60:63], a[140:143], a[12:15], v[60:63]
ds_read_b64_tr_b16 a[48:49], v194 offset:16896
v_mfma_f32_16x16x32_bf16 v[64:67], a[128:131], a[16:19], v[64:67]
ds_read_b64_tr_b16 a[50:51], v194 offset:18944
v_mfma_f32_16x16x32_bf16 v[68:71], a[132:135], a[16:19], v[68:71]
ds_read_b64_tr_b16 a[52:53], v195 offset:17024
v_mfma_f32_16x16x32_bf16 v[72:75], a[136:139], a[16:19], v[72:75]
ds_read_b64_tr_b16 a[54:55], v195 offset:19072
v_mfma_f32_16x16x32_bf16 v[76:79], a[140:143], a[16:19], v[76:79]
buffer_load_dwordx4 v207, s[12:15], s30 offen offset:2048 lds
v_mfma_f32_16x16x32_bf16 v[80:83], a[128:131], a[20:23], v[80:83]
ds_read_b64_tr_b16 a[56:57], v194 offset:17152
v_mfma_f32_16x16x32_bf16 v[84:87], a[132:135], a[20:23], v[84:87]
ds_read_b64_tr_b16 a[58:59], v194 offset:19200
v_mfma_f32_16x16x32_bf16 v[88:91], a[136:139], a[20:23], v[88:91]
ds_read_b64_tr_b16 a[60:61], v195 offset:17280
v_mfma_f32_16x16x32_bf16 v[92:95], a[140:143], a[20:23], v[92:95]
ds_read_b64_tr_b16 a[62:63], v195 offset:19328
v_mfma_f32_16x16x32_bf16 v[96:99], a[128:131], a[24:27], v[96:99]
buffer_load_dwordx4 v207, s[16:19], s30 offen offset:3072 lds
v_mfma_f32_16x16x32_bf16 v[100:103], a[132:135], a[24:27], v[100:103]
s_add_u32 s24, s24, 1
s_cmp_eq_u32 s24, 8
v_mfma_f32_16x16x32_bf16 v[104:107], a[136:139], a[24:27], v[104:107]
s_cselect_b32 s24, 1, s24
s_lshl_b32 s28, s24, 7
v_mfma_f32_16x16x32_bf16 v[108:111], a[140:143], a[24:27], v[108:111]
s_add_u32 s28, s28, s25
s_lshl_b32 s29, s24, 19
v_mfma_f32_16x16x32_bf16 v[112:115], a[128:131], a[28:31], v[112:115]
s_add_u32 s29, s29, s58
s_add_u32 s30, s29, 65536
v_mfma_f32_16x16x32_bf16 v[116:119], a[132:135], a[28:31], v[116:119]
v_mfma_f32_16x16x32_bf16 v[120:123], a[136:139], a[28:31], v[120:123]
v_mfma_f32_16x16x32_bf16 v[124:127], a[140:143], a[28:31], v[124:127]
s_waitcnt vmcnt(12) lgkmcnt(0)
s_barrier
v_add_u32 v192, 0xc000, v192
v_add_u32 v193, 0xc000, v193
v_add_u32 v194, 0xc000, v194
v_add_u32 v195, 0xc000, v195
s_nop 0
v_mfma_f32_16x16x32_bf16 v[0:3], a[144:147], a[32:35], v[0:3]
ds_read_b64_tr_b16 a[0:1], v194 offset:0
s_add_u32 m0, s34, 65536
v_mfma_f32_16x16x32_bf16 v[4:7], a[148:151], a[32:35], v[4:7]
buffer_load_dwordx4 v200, s[8:11], s28 offen offset:0 lds
v_mfma_f32_16x16x32_bf16 v[8:11], a[152:155], a[32:35], v[8:11]
ds_read_b64_tr_b16 a[2:3], v194 offset:2048
v_mfma_f32_16x16x32_bf16 v[12:15], a[156:159], a[32:35], v[12:15]
ds_read_b128 a[128:131], v192 offset:0
v_mfma_f32_16x16x32_bf16 v[16:19], a[144:147], a[36:39], v[16:19]
ds_read_b128 a[132:135], v192 offset:2048
v_mfma_f32_16x16x32_bf16 v[20:23], a[148:151], a[36:39], v[20:23]
ds_read_b128 a[136:139], v192 offset:4096
v_mfma_f32_16x16x32_bf16 v[24:27], a[152:155], a[36:39], v[24:27]
buffer_load_dwordx4 v201, s[8:11], s28 offen offset:1024 lds
v_mfma_f32_16x16x32_bf16 v[28:31], a[156:159], a[36:39], v[28:31]
ds_read_b128 a[140:143], v192 offset:6144
v_mfma_f32_16x16x32_bf16 v[32:35], a[144:147], a[40:43], v[32:35]
ds_read_b64_tr_b16 a[4:5], v195 offset:128
v_mfma_f32_16x16x32_bf16 v[36:39], a[148:151], a[40:43], v[36:39]
ds_read_b64_tr_b16 a[6:7], v195 offset:2176
v_mfma_f32_16x16x32_bf16 v[40:43], a[152:155], a[40:43], v[40:43]
ds_read_b64_tr_b16 a[8:9], v194 offset:256
v_mfma_f32_16x16x32_bf16 v[44:47], a[156:159], a[40:43], v[44:47]
buffer_load_dwordx4 v202, s[8:11], s28 offen offset:2048 lds
v_mfma_f32_16x16x32_bf16 v[48:51], a[144:147], a[44:47], v[48:51]
ds_read_b64_tr_b16 a[10:11], v194 offset:2304
v_mfma_f32_16x16x32_bf16 v[52:55], a[148:151], a[44:47], v[52:55]
ds_read_b64_tr_b16 a[12:13], v195 offset:384
v_mfma_f32_16x16x32_bf16 v[56:59], a[152:155], a[44:47], v[56:59]
ds_read_b64_tr_b16 a[14:15], v195 offset:2432
v_mfma_f32_16x16x32_bf16 v[60:63], a[156:159], a[44:47], v[60:63]
ds_read_b64_tr_b16 a[16:17], v194 offset:512
v_mfma_f32_16x16x32_bf16 v[64:67], a[144:147], a[48:51], v[64:67]
buffer_load_dwordx4 v203, s[8:11], s28 offen offset:3072 lds
v_mfma_f32_16x16x32_bf16 v[68:71], a[148:151], a[48:51], v[68:71]
ds_read_b64_tr_b16 a[18:19], v194 offset:2560
v_mfma_f32_16x16x32_bf16 v[72:75], a[152:155], a[48:51], v[72:75]
ds_read_b64_tr_b16 a[20:21], v195 offset:640
v_mfma_f32_16x16x32_bf16 v[76:79], a[156:159], a[48:51], v[76:79]
ds_read_b64_tr_b16 a[22:23], v195 offset:2688
v_mfma_f32_16x16x32_bf16 v[80:83], a[144:147], a[52:55], v[80:83]
ds_read_b64_tr_b16 a[24:25], v194 offset:768
s_add_u32 m0, s35, 65536
v_mfma_f32_16x16x32_bf16 v[84:87], a[148:151], a[52:55], v[84:87]
buffer_load_dwordx4 v204, s[12:15], s29 offen offset:0 lds
v_mfma_f32_16x16x32_bf16 v[88:91], a[152:155], a[52:55], v[88:91]
ds_read_b64_tr_b16 a[26:27], v194 offset:2816
v_mfma_f32_16x16x32_bf16 v[92:95], a[156:159], a[52:55], v[92:95]
ds_read_b64_tr_b16 a[28:29], v195 offset:896
v_mfma_f32_16x16x32_bf16 v[96:99], a[144:147], a[56:59], v[96:99]
ds_read_b64_tr_b16 a[30:31], v195 offset:2944
v_mfma_f32_16x16x32_bf16 v[100:103], a[148:151], a[56:59], v[100:103]
v_mfma_f32_16x16x32_bf16 v[104:107], a[152:155], a[56:59], v[104:107]
buffer_load_dwordx4 v204, s[16:19], s29 offen offset:1024 lds
v_mfma_f32_16x16x32_bf16 v[108:111], a[156:159], a[56:59], v[108:111]
v_mfma_f32_16x16x32_bf16 v[112:115], a[144:147], a[60:63], v[112:115]
v_mfma_f32_16x16x32_bf16 v[116:119], a[148:151], a[60:63], v[116:119]
v_mfma_f32_16x16x32_bf16 v[120:123], a[152:155], a[60:63], v[120:123]
v_mfma_f32_16x16x32_bf16 v[124:127], a[156:159], a[60:63], v[124:127]
buffer_load_dwordx4 v205, s[12:15], s29 offen offset:2048 lds
s_waitcnt lgkmcnt(0)
v_mfma_f32_16x16x32_bf16 v[0:3], a[128:131], a[0:3], v[0:3]
ds_read_b64_tr_b16 a[32:33], v194 offset:16384
v_mfma_f32_16x16x32_bf16 v[4:7], a[132:135], a[0:3], v[4:7]
ds_read_b64_tr_b16 a[34:35], v194 offset:18432
v_mfma_f32_16x16x32_bf16 v[8:11], a[136:139], a[0:3], v[8:11]
ds_read_b128 a[144:147], v193 offset:0
v_mfma_f32_16x16x32_bf16 v[12:15], a[140:143], a[0:3], v[12:15]
ds_read_b128 a[148:151], v193 offset:2048
v_mfma_f32_16x16x32_bf16 v[16:19], a[128:131], a[4:7], v[16:19]
buffer_load_dwordx4 v205, s[16:19], s29 offen offset:3072 lds
v_mfma_f32_16x16x32_bf16 v[20:23], a[132:135], a[4:7], v[20:23]
ds_read_b128 a[152:155], v193 offset:4096
v_mfma_f32_16x16x32_bf16 v[24:27], a[136:139], a[4:7], v[24:27]
ds_read_b128 a[156:159], v193 offset:6144
v_mfma_f32_16x16x32_bf16 v[28:31], a[140:143], a[4:7], v[28:31]
ds_read_b64_tr_b16 a[36:37], v195 offset:16512
v_mfma_f32_16x16x32_bf16 v[32:35], a[128:131], a[8:11], v[32:35]
ds_read_b64_tr_b16 a[38:39], v195 offset:18560
s_add_u32 m0, s35, 69632
v_mfma_f32_16x16x32_bf16 v[36:39], a[132:135], a[8:11], v[36:39]
buffer_load_dwordx4 v206, s[12:15], s30 offen offset:0 lds
v_mfma_f32_16x16x32_bf16 v[40:43], a[136:139], a[8:11], v[40:43]
ds_read_b64_tr_b16 a[40:41], v194 offset:16640
v_mfma_f32_16x16x32_bf16 v[44:47], a[140:143], a[8:11], v[44:47]
ds_read_b64_tr_b16 a[42:43], v194 offset:18688
v_mfma_f32_16x16x32_bf16 v[48:51], a[128:131], a[12:15], v[48:51]
ds_read_b64_tr_b16 a[44:45], v195 offset:16768
v_mfma_f32_16x16x32_bf16 v[52:55], a[132:135], a[12:15], v[52:55]
ds_read_b64_tr_b16 a[46:47], v195 offset:18816
v_mfma_f32_16x16x32_bf16 v[56:59], a[136:139], a[12:15], v[56:59]
buffer_load_dwordx4 v206, s[16:19], s30 offen offset:1024 lds
v_mfma_f32_16x16x32_bf16 v[60:63], a[140:143], a[12:15], v[60:63]
ds_read_b64_tr_b16 a[48:49], v194 offset:16896
v_mfma_f32_16x16x32_bf16 v[64:67], a[128:131], a[16:19], v[64:67]
ds_read_b64_tr_b16 a[50:51], v194 offset:18944
v_mfma_f32_16x16x32_bf16 v[68:71], a[132:135], a[16:19], v[68:71]
ds_read_b64_tr_b16 a[52:53], v195 offset:17024
v_mfma_f32_16x16x32_bf16 v[72:75], a[136:139], a[16:19], v[72:75]
ds_read_b64_tr_b16 a[54:55], v195 offset:19072
v_mfma_f32_16x16x32_bf16 v[76:79], a[140:143], a[16:19], v[76:79]
buffer_load_dwordx4 v207, s[12:15], s30 offen offset:2048 lds
v_mfma_f32_16x16x32_bf16 v[80:83], a[128:131], a[20:23], v[80:83]
ds_read_b64_tr_b16 a[56:57], v194 offset:17152
v_mfma_f32_16x16x32_bf16 v[84:87], a[132:135], a[20:23], v[84:87]
ds_read_b64_tr_b16 a[58:59], v194 offset:19200
v_mfma_f32_16x16x32_bf16 v[88:91], a[136:139], a[20:23], v[88:91]
ds_read_b64_tr_b16 a[60:61], v195 offset:17280
v_mfma_f32_16x16x32_bf16 v[92:95], a[140:143], a[20:23], v[92:95]
ds_read_b64_tr_b16 a[62:63], v195 offset:19328
v_mfma_f32_16x16x32_bf16 v[96:99], a[128:131], a[24:27], v[96:99]
buffer_load_dwordx4 v207, s[16:19], s30 offen offset:3072 lds
v_mfma_f32_16x16x32_bf16 v[100:103], a[132:135], a[24:27], v[100:103]
s_mov_b32 s24, 0
s_add_u32 s25, s25, 1048576
v_mfma_f32_16x16x32_bf16 v[104:107], a[136:139], a[24:27], v[104:107]
s_lshl_b32 s28, s24, 7
s_add_u32 s28, s28, s25
v_mfma_f32_16x16x32_bf16 v[108:111], a[140:143], a[24:27], v[108:111]
s_lshl_b32 s29, s24, 19
s_add_u32 s29, s29, s58
v_mfma_f32_16x16x32_bf16 v[112:115], a[128:131], a[28:31], v[112:115]
s_add_u32 s30, s29, 65536
v_mfma_f32_16x16x32_bf16 v[116:119], a[132:135], a[28:31], v[116:119]
v_mfma_f32_16x16x32_bf16 v[120:123], a[136:139], a[28:31], v[120:123]
v_mfma_f32_16x16x32_bf16 v[124:127], a[140:143], a[28:31], v[124:127]
s_waitcnt vmcnt(12) lgkmcnt(0)
s_barrier
v_add_u32 v192, 0xfffe8000, v192
v_add_u32 v193, 0xfffe8000, v193
v_add_u32 v194, 0xfffe8000, v194
v_add_u32 v195, 0xfffe8000, v195
s_nop 0
v_mfma_f32_16x16x32_bf16 v[0:3], a[144:147], a[32:35], v[0:3]
ds_read_b64_tr_b16 a[0:1], v194 offset:0
s_add_u32 m0, s34, 114688
v_mfma_f32_16x16x32_bf16 v[4:7], a[148:151], a[32:35], v[4:7]
buffer_load_dwordx4 v200, s[8:11], s28 offen offset:0 lds
v_mfma_f32_16x16x32_bf16 v[8:11], a[152:155], a[32:35], v[8:11]
ds_read_b64_tr_b16 a[2:3], v194 offset:2048
v_mfma_f32_16x16x32_bf16 v[12:15], a[156:159], a[32:35], v[12:15]
ds_read_b128 a[128:131], v192 offset:0
v_mfma_f32_16x16x32_bf16 v[16:19], a[144:147], a[36:39], v[16:19]
ds_read_b128 a[132:135], v192 offset:2048
v_mfma_f32_16x16x32_bf16 v[20:23], a[148:151], a[36:39], v[20:23]
ds_read_b128 a[136:139], v192 offset:4096
v_mfma_f32_16x16x32_bf16 v[24:27], a[152:155], a[36:39], v[24:27]
buffer_load_dwordx4 v201, s[8:11], s28 offen offset:1024 lds
v_mfma_f32_16x16x32_bf16 v[28:31], a[156:159], a[36:39], v[28:31]
ds_read_b128 a[140:143], v192 offset:6144
v_mfma_f32_16x16x32_bf16 v[32:35], a[144:147], a[40:43], v[32:35]
ds_read_b64_tr_b16 a[4:5], v195 offset:128
v_mfma_f32_16x16x32_bf16 v[36:39], a[148:151], a[40:43], v[36:39]
ds_read_b64_tr_b16 a[6:7], v195 offset:2176
v_mfma_f32_16x16x32_bf16 v[40:43], a[152:155], a[40:43], v[40:43]
ds_read_b64_tr_b16 a[8:9], v194 offset:256
v_mfma_f32_16x16x32_bf16 v[44:47], a[156:159], a[40:43], v[44:47]
buffer_load_dwordx4 v202, s[8:11], s28 offen offset:2048 lds
v_mfma_f32_16x16x32_bf16 v[48:51], a[144:147], a[44:47], v[48:51]
ds_read_b64_tr_b16 a[10:11], v194 offset:2304
v_mfma_f32_16x16x32_bf16 v[52:55], a[148:151], a[44:47], v[52:55]
ds_read_b64_tr_b16 a[12:13], v195 offset:384
v_mfma_f32_16x16x32_bf16 v[56:59], a[152:155], a[44:47], v[56:59]
ds_read_b64_tr_b16 a[14:15], v195 offset:2432
v_mfma_f32_16x16x32_bf16 v[60:63], a[156:159], a[44:47], v[60:63]
ds_read_b64_tr_b16 a[16:17], v194 offset:512
v_mfma_f32_16x16x32_bf16 v[64:67], a[144:147], a[48:51], v[64:67]
buffer_load_dwordx4 v203, s[8:11], s28 offen offset:3072 lds
v_mfma_f32_16x16x32_bf16 v[68:71], a[148:151], a[48:51], v[68:71]
ds_read_b64_tr_b16 a[18:19], v194 offset:2560
v_mfma_f32_16x16x32_bf16 v[72:75], a[152:155], a[48:51], v[72:75]
ds_read_b64_tr_b16 a[20:21], v195 offset:640
v_mfma_f32_16x16x32_bf16 v[76:79], a[156:159], a[48:51], v[76:79]
ds_read_b64_tr_b16 a[22:23], v195 offset:2688
v_mfma_f32_16x16x32_bf16 v[80:83], a[144:147], a[52:55], v[80:83]
ds_read_b64_tr_b16 a[24:25], v194 offset:768
s_add_u32 m0, s35, 114688
v_mfma_f32_16x16x32_bf16 v[84:87], a[148:151], a[52:55], v[84:87]
buffer_load_dwordx4 v204, s[12:15], s29 offen offset:0 lds
v_mfma_f32_16x16x32_bf16 v[88:91], a[152:155], a[52:55], v[88:91]
ds_read_b64_tr_b16 a[26:27], v194 offset:2816
v_mfma_f32_16x16x32_bf16 v[92:95], a[156:159], a[52:55], v[92:95]
ds_read_b64_tr_b16 a[28:29], v195 offset:896
v_mfma_f32_16x16x32_bf16 v[96:99], a[144:147], a[56:59], v[96:99]
ds_read_b64_tr_b16 a[30:31], v195 offset:2944
v_mfma_f32_16x16x32_bf16 v[100:103], a[148:151], a[56:59], v[100:103]
v_mfma_f32_16x16x32_bf16 v[104:107], a[152:155], a[56:59], v[104:107]
buffer_load_dwordx4 v204, s[16:19], s29 offen offset:1024 lds
v_mfma_f32_16x16x32_bf16 v[108:111], a[156:159], a[56:59], v[108:111]
v_mfma_f32_16x16x32_bf16 v[112:115], a[144:147], a[60:63], v[112:115]
v_mfma_f32_16x16x32_bf16 v[116:119], a[148:151], a[60:63], v[116:119]
v_mfma_f32_16x16x32_bf16 v[120:123], a[152:155], a[60:63], v[120:123]
v_mfma_f32_16x16x32_bf16 v[124:127], a[156:159], a[60:63], v[124:127]
buffer_load_dwordx4 v205, s[12:15], s29 offen offset:2048 lds
s_waitcnt lgkmcnt(0)
v_mfma_f32_16x16x32_bf16 v[0:3], a[128:131], a[0:3], v[0:3]
ds_read_b64_tr_b16 a[32:33], v194 offset:16384
v_mfma_f32_16x16x32_bf16 v[4:7], a[132:135], a[0:3], v[4:7]
ds_read_b64_tr_b16 a[34:35], v194 offset:18432
v_mfma_f32_16x16x32_bf16 v[8:11], a[136:139], a[0:3], v[8:11]
ds_read_b128 a[144:147], v193 offset:0
v_mfma_f32_16x16x32_bf16 v[12:15], a[140:143], a[0:3], v[12:15]
ds_read_b128 a[148:151], v193 offset:2048
v_mfma_f32_16x16x32_bf16 v[16:19], a[128:131], a[4:7], v[16:19]
buffer_load_dwordx4 v205, s[16:19], s29 offen offset:3072 lds
v_mfma_f32_16x16x32_bf16 v[20:23], a[132:135], a[4:7], v[20:23]
ds_read_b128 a[152:155], v193 offset:4096
v_mfma_f32_16x16x32_bf16 v[24:27], a[136:139], a[4:7], v[24:27]
ds_read_b128 a[156:159], v193 offset:6144
v_mfma_f32_16x16x32_bf16 v[28:31], a[140:143], a[4:7], v[28:31]
ds_read_b64_tr_b16 a[36:37], v195 offset:16512
v_mfma_f32_16x16x32_bf16 v[32:35], a[128:131], a[8:11], v[32:35]
ds_read_b64_tr_b16 a[38:39], v195 offset:18560
s_add_u32 m0, s35, 118784
v_mfma_f32_16x16x32_bf16 v[36:39], a[132:135], a[8:11], v[36:39]
buffer_load_dwordx4 v206, s[12:15], s30 offen offset:0 lds
v_mfma_f32_16x16x32_bf16 v[40:43], a[136:139], a[8:11], v[40:43]
ds_read_b64_tr_b16 a[40:41], v194 offset:16640
v_mfma_f32_16x16x32_bf16 v[44:47], a[140:143], a[8:11], v[44:47]
ds_read_b64_tr_b16 a[42:43], v194 offset:18688
v_mfma_f32_16x16x32_bf16 v[48:51], a[128:131], a[12:15], v[48:51]
ds_read_b64_tr_b16 a[44:45], v195 offset:16768
v_mfma_f32_16x16x32_bf16 v[52:55], a[132:135], a[12:15], v[52:55]
ds_read_b64_tr_b16 a[46:47], v195 offset:18816
v_mfma_f32_16x16x32_bf16 v[56:59], a[136:139], a[12:15], v[56:59]
buffer_load_dwordx4 v206, s[16:19], s30 offen offset:1024 lds
v_mfma_f32_16x16x32_bf16 v[60:63], a[140:143], a[12:15], v[60:63]
ds_read_b64_tr_b16 a[48:49], v194 offset:16896
v_mfma_f32_16x16x32_bf16 v[64:67], a[128:131], a[16:19], v[64:67]
ds_read_b64_tr_b16 a[50:51], v194 offset:18944
v_mfma_f32_16x16x32_bf16 v[68:71], a[132:135], a[16:19], v[68:71]
ds_read_b64_tr_b16 a[52:53], v195 offset:17024
v_mfma_f32_16x16x32_bf16 v[72:75], a[136:139], a[16:19], v[72:75]
ds_read_b64_tr_b16 a[54:55], v195 offset:19072
v_mfma_f32_16x16x32_bf16 v[76:79], a[140:143], a[16:19], v[76:79]
buffer_load_dwordx4 v207, s[12:15], s30 offen offset:2048 lds
v_mfma_f32_16x16x32_bf16 v[80:83], a[128:131], a[20:23], v[80:83]
ds_read_b64_tr_b16 a[56:57], v194 offset:17152
v_mfma_f32_16x16x32_bf16 v[84:87], a[132:135], a[20:23], v[84:87]
ds_read_b64_tr_b16 a[58:59], v194 offset:19200
v_mfma_f32_16x16x32_bf16 v[88:91], a[136:139], a[20:23], v[88:91]
ds_read_b64_tr_b16 a[60:61], v195 offset:17280
v_mfma_f32_16x16x32_bf16 v[92:95], a[140:143], a[20:23], v[92:95]
ds_read_b64_tr_b16 a[62:63], v195 offset:19328
v_mfma_f32_16x16x32_bf16 v[96:99], a[128:131], a[24:27], v[96:99]
buffer_load_dwordx4 v207, s[16:19], s30 offen offset:3072 lds
v_mfma_f32_16x16x32_bf16 v[100:103], a[132:135], a[24:27], v[100:103]
s_mov_b32 s24, s98
s_lshl_b32 s28, s24, 7
v_mfma_f32_16x16x32_bf16 v[104:107], a[136:139], a[24:27], v[104:107]
s_add_u32 s28, s28, s25
s_lshl_b32 s29, s24, 19
v_mfma_f32_16x16x32_bf16 v[108:111], a[140:143], a[24:27], v[108:111]
s_add_u32 s29, s29, s58
s_add_u32 s30, s29, 65536
v_mfma_f32_16x16x32_bf16 v[112:115], a[128:131], a[28:31], v[112:115]
v_mfma_f32_16x16x32_bf16 v[116:119], a[132:135], a[28:31], v[116:119]
v_mfma_f32_16x16x32_bf16 v[120:123], a[136:139], a[28:31], v[120:123]
v_mfma_f32_16x16x32_bf16 v[124:127], a[140:143], a[28:31], v[124:127]
s_waitcnt vmcnt(12) lgkmcnt(0)
s_barrier
v_add_u32 v192, 0xc000, v192
v_add_u32 v193, 0xc000, v193
v_add_u32 v194, 0xc000, v194
v_add_u32 v195, 0xc000, v195
s_nop 0
v_mfma_f32_16x16x32_bf16 v[0:3], a[144:147], a[32:35], v[0:3]
ds_read_b64_tr_b16 a[0:1], v194 offset:0
s_add_u32 m0, s34, 16384
v_mfma_f32_16x16x32_bf16 v[4:7], a[148:151], a[32:35], v[4:7]
buffer_load_dwordx4 v200, s[8:11], s28 offen offset:0 lds
v_mfma_f32_16x16x32_bf16 v[8:11], a[152:155], a[32:35], v[8:11]
ds_read_b64_tr_b16 a[2:3], v194 offset:2048
v_mfma_f32_16x16x32_bf16 v[12:15], a[156:159], a[32:35], v[12:15]
ds_read_b128 a[128:131], v192 offset:0
v_mfma_f32_16x16x32_bf16 v[16:19], a[144:147], a[36:39], v[16:19]
ds_read_b128 a[132:135], v192 offset:2048
v_mfma_f32_16x16x32_bf16 v[20:23], a[148:151], a[36:39], v[20:23]
ds_read_b128 a[136:139], v192 offset:4096
v_mfma_f32_16x16x32_bf16 v[24:27], a[152:155], a[36:39], v[24:27]
buffer_load_dwordx4 v201, s[8:11], s28 offen offset:1024 lds
v_mfma_f32_16x16x32_bf16 v[28:31], a[156:159], a[36:39], v[28:31]
ds_read_b128 a[140:143], v192 offset:6144
v_mfma_f32_16x16x32_bf16 v[32:35], a[144:147], a[40:43], v[32:35]
ds_read_b64_tr_b16 a[4:5], v195 offset:128
v_mfma_f32_16x16x32_bf16 v[36:39], a[148:151], a[40:43], v[36:39]
ds_read_b64_tr_b16 a[6:7], v195 offset:2176
v_mfma_f32_16x16x32_bf16 v[40:43], a[152:155], a[40:43], v[40:43]
ds_read_b64_tr_b16 a[8:9], v194 offset:256
v_mfma_f32_16x16x32_bf16 v[44:47], a[156:159], a[40:43], v[44:47]
buffer_load_dwordx4 v202, s[8:11], s28 offen offset:2048 lds
v_mfma_f32_16x16x32_bf16 v[48:51], a[144:147], a[44:47], v[48:51]
ds_read_b64_tr_b16 a[10:11], v194 offset:2304
v_mfma_f32_16x16x32_bf16 v[52:55], a[148:151], a[44:47], v[52:55]
ds_read_b64_tr_b16 a[12:13], v195 offset:384
v_mfma_f32_16x16x32_bf16 v[56:59], a[152:155], a[44:47], v[56:59]
ds_read_b64_tr_b16 a[14:15], v195 offset:2432
v_mfma_f32_16x16x32_bf16 v[60:63], a[156:159], a[44:47], v[60:63]
ds_read_b64_tr_b16 a[16:17], v194 offset:512
v_mfma_f32_16x16x32_bf16 v[64:67], a[144:147], a[48:51], v[64:67]
buffer_load_dwordx4 v203, s[8:11], s28 offen offset:3072 lds
v_mfma_f32_16x16x32_bf16 v[68:71], a[148:151], a[48:51], v[68:71]
ds_read_b64_tr_b16 a[18:19], v194 offset:2560
v_mfma_f32_16x16x32_bf16 v[72:75], a[152:155], a[48:51], v[72:75]
ds_read_b64_tr_b16 a[20:21], v195 offset:640
v_mfma_f32_16x16x32_bf16 v[76:79], a[156:159], a[48:51], v[76:79]
ds_read_b64_tr_b16 a[22:23], v195 offset:2688
v_mfma_f32_16x16x32_bf16 v[80:83], a[144:147], a[52:55], v[80:83]
ds_read_b64_tr_b16 a[24:25], v194 offset:768
s_add_u32 m0, s35, 16384
v_mfma_f32_16x16x32_bf16 v[84:87], a[148:151], a[52:55], v[84:87]
buffer_load_dwordx4 v204, s[12:15], s29 offen offset:0 lds
v_mfma_f32_16x16x32_bf16 v[88:91], a[152:155], a[52:55], v[88:91]
ds_read_b64_tr_b16 a[26:27], v194 offset:2816
v_mfma_f32_16x16x32_bf16 v[92:95], a[156:159], a[52:55], v[92:95]
ds_read_b64_tr_b16 a[28:29], v195 offset:896
v_mfma_f32_16x16x32_bf16 v[96:99], a[144:147], a[56:59], v[96:99]
ds_read_b64_tr_b16 a[30:31], v195 offset:2944
v_mfma_f32_16x16x32_bf16 v[100:103], a[148:151], a[56:59], v[100:103]
v_mfma_f32_16x16x32_bf16 v[104:107], a[152:155], a[56:59], v[104:107]
buffer_load_dwordx4 v204, s[16:19], s29 offen offset:1024 lds
v_mfma_f32_16x16x32_bf16 v[108:111], a[156:159], a[56:59], v[108:111]
v_mfma_f32_16x16x32_bf16 v[112:115], a[144:147], a[60:63], v[112:115]
v_mfma_f32_16x16x32_bf16 v[116:119], a[148:151], a[60:63], v[116:119]
v_mfma_f32_16x16x32_bf16 v[120:123], a[152:155], a[60:63], v[120:123]
v_mfma_f32_16x16x32_bf16 v[124:127], a[156:159], a[60:63], v[124:127]
buffer_load_dwordx4 v205, s[12:15], s29 offen offset:2048 lds
s_waitcnt lgkmcnt(0)
v_mfma_f32_16x16x32_bf16 v[0:3], a[128:131], a[0:3], v[0:3]
ds_read_b64_tr_b16 a[32:33], v194 offset:16384
v_mfma_f32_16x16x32_bf16 v[4:7], a[132:135], a[0:3], v[4:7]
ds_read_b64_tr_b16 a[34:35], v194 offset:18432
v_mfma_f32_16x16x32_bf16 v[8:11], a[136:139], a[0:3], v[8:11]
ds_read_b128 a[144:147], v193 offset:0
v_mfma_f32_16x16x32_bf16 v[12:15], a[140:143], a[0:3], v[12:15]
ds_read_b128 a[148:151], v193 offset:2048
v_mfma_f32_16x16x32_bf16 v[16:19], a[128:131], a[4:7], v[16:19]
buffer_load_dwordx4 v205, s[16:19], s29 offen offset:3072 lds
v_mfma_f32_16x16x32_bf16 v[20:23], a[132:135], a[4:7], v[20:23]
ds_read_b128 a[152:155], v193 offset:4096
v_mfma_f32_16x16x32_bf16 v[24:27], a[136:139], a[4:7], v[24:27]
ds_read_b128 a[156:159], v193 offset:6144
v_mfma_f32_16x16x32_bf16 v[28:31], a[140:143], a[4:7], v[28:31]
ds_read_b64_tr_b16 a[36:37], v195 offset:16512
v_mfma_f32_16x16x32_bf16 v[32:35], a[128:131], a[8:11], v[32:35]
ds_read_b64_tr_b16 a[38:39], v195 offset:18560
s_add_u32 m0, s35, 20480
v_mfma_f32_16x16x32_bf16 v[36:39], a[132:135], a[8:11], v[36:39]
buffer_load_dwordx4 v206, s[12:15], s30 offen offset:0 lds
v_mfma_f32_16x16x32_bf16 v[40:43], a[136:139], a[8:11], v[40:43]
ds_read_b64_tr_b16 a[40:41], v194 offset:16640
v_mfma_f32_16x16x32_bf16 v[44:47], a[140:143], a[8:11], v[44:47]
ds_read_b64_tr_b16 a[42:43], v194 offset:18688
v_mfma_f32_16x16x32_bf16 v[48:51], a[128:131], a[12:15], v[48:51]
ds_read_b64_tr_b16 a[44:45], v195 offset:16768
v_mfma_f32_16x16x32_bf16 v[52:55], a[132:135], a[12:15], v[52:55]
ds_read_b64_tr_b16 a[46:47], v195 offset:18816
v_mfma_f32_16x16x32_bf16 v[56:59], a[136:139], a[12:15], v[56:59]
buffer_load_dwordx4 v206, s[16:19], s30 offen offset:1024 lds
v_mfma_f32_16x16x32_bf16 v[60:63], a[140:143], a[12:15], v[60:63]
ds_read_b64_tr_b16 a[48:49], v194 offset:16896
v_mfma_f32_16x16x32_bf16 v[64:67], a[128:131], a[16:19], v[64:67]
ds_read_b64_tr_b16 a[50:51], v194 offset:18944
v_mfma_f32_16x16x32_bf16 v[68:71], a[132:135], a[16:19], v[68:71]
ds_read_b64_tr_b16 a[52:53], v195 offset:17024
v_mfma_f32_16x16x32_bf16 v[72:75], a[136:139], a[16:19], v[72:75]
ds_read_b64_tr_b16 a[54:55], v195 offset:19072
v_mfma_f32_16x16x32_bf16 v[76:79], a[140:143], a[16:19], v[76:79]
buffer_load_dwordx4 v207, s[12:15], s30 offen offset:2048 lds
v_mfma_f32_16x16x32_bf16 v[80:83], a[128:131], a[20:23], v[80:83]
ds_read_b64_tr_b16 a[56:57], v194 offset:17152
v_mfma_f32_16x16x32_bf16 v[84:87], a[132:135], a[20:23], v[84:87]
ds_read_b64_tr_b16 a[58:59], v194 offset:19200
v_mfma_f32_16x16x32_bf16 v[88:91], a[136:139], a[20:23], v[88:91]
ds_read_b64_tr_b16 a[60:61], v195 offset:17280
v_mfma_f32_16x16x32_bf16 v[92:95], a[140:143], a[20:23], v[92:95]
ds_read_b64_tr_b16 a[62:63], v195 offset:19328
v_mfma_f32_16x16x32_bf16 v[96:99], a[128:131], a[24:27], v[96:99]
buffer_load_dwordx4 v207, s[16:19], s30 offen offset:3072 lds
v_mfma_f32_16x16x32_bf16 v[100:103], a[132:135], a[24:27], v[100:103]
s_add_u32 s24, s24, 1
s_cmp_eq_u32 s24, 8
v_mfma_f32_16x16x32_bf16 v[104:107], a[136:139], a[24:27], v[104:107]
s_cselect_b32 s24, 1, s24
s_lshl_b32 s28, s24, 7
v_mfma_f32_16x16x32_bf16 v[108:111], a[140:143], a[24:27], v[108:111]
s_add_u32 s28, s28, s25
s_lshl_b32 s29, s24, 19
v_mfma_f32_16x16x32_bf16 v[112:115], a[128:131], a[28:31], v[112:115]
s_add_u32 s29, s29, s58
s_add_u32 s30, s29, 65536
v_mfma_f32_16x16x32_bf16 v[116:119], a[132:135], a[28:31], v[116:119]
v_mfma_f32_16x16x32_bf16 v[120:123], a[136:139], a[28:31], v[120:123]
v_mfma_f32_16x16x32_bf16 v[124:127], a[140:143], a[28:31], v[124:127]
s_waitcnt vmcnt(12) lgkmcnt(0)
s_barrier
v_add_u32 v192, 0xc000, v192
v_add_u32 v193, 0xc000, v193
v_add_u32 v194, 0xc000, v194
v_add_u32 v195, 0xc000, v195
s_nop 0
v_mfma_f32_16x16x32_bf16 v[0:3], a[144:147], a[32:35], v[0:3]
ds_read_b64_tr_b16 a[0:1], v194 offset:0
s_add_u32 m0, s34, 65536
v_mfma_f32_16x16x32_bf16 v[4:7], a[148:151], a[32:35], v[4:7]
buffer_load_dwordx4 v200, s[8:11], s28 offen offset:0 lds
v_mfma_f32_16x16x32_bf16 v[8:11], a[152:155], a[32:35], v[8:11]
ds_read_b64_tr_b16 a[2:3], v194 offset:2048
v_mfma_f32_16x16x32_bf16 v[12:15], a[156:159], a[32:35], v[12:15]
ds_read_b128 a[128:131], v192 offset:0
v_mfma_f32_16x16x32_bf16 v[16:19], a[144:147], a[36:39], v[16:19]
ds_read_b128 a[132:135], v192 offset:2048
v_mfma_f32_16x16x32_bf16 v[20:23], a[148:151], a[36:39], v[20:23]
ds_read_b128 a[136:139], v192 offset:4096
v_mfma_f32_16x16x32_bf16 v[24:27], a[152:155], a[36:39], v[24:27]
buffer_load_dwordx4 v201, s[8:11], s28 offen offset:1024 lds
v_cvt_pk_bf16_f32 v128, v0, v1
v_cvt_pk_bf16_f32 v129, v2, v3
v_mfma_f32_16x16x32_bf16 v[28:31], a[156:159], a[36:39], v[28:31]
ds_read_b128 a[140:143], v192 offset:6144
v_cvt_pk_bf16_f32 v130, v4, v5
v_cvt_pk_bf16_f32 v131, v6, v7
v_mfma_f32_16x16x32_bf16 v[32:35], a[144:147], a[40:43], v[32:35]
ds_read_b64_tr_b16 a[4:5], v195 offset:128
v_cvt_pk_bf16_f32 v160, v8, v9
v_cvt_pk_bf16_f32 v161, v10, v11
v_mfma_f32_16x16x32_bf16 v[36:39], a[148:151], a[40:43], v[36:39]
ds_read_b64_tr_b16 a[6:7], v195 offset:2176
v_cvt_pk_bf16_f32 v162, v12, v13
v_cvt_pk_bf16_f32 v163, v14, v15
v_mfma_f32_16x16x32_bf16 v[40:43], a[152:155], a[40:43], v[40:43]
ds_read_b64_tr_b16 a[8:9], v194 offset:256
v_cvt_pk_bf16_f32 v132, v16, v17
v_cvt_pk_bf16_f32 v133, v18, v19
v_mfma_f32_16x16x32_bf16 v[44:47], a[156:159], a[40:43], v[44:47]
buffer_load_dwordx4 v202, s[8:11], s28 offen offset:2048 lds
v_cvt_pk_bf16_f32 v134, v20, v21
v_cvt_pk_bf16_f32 v135, v22, v23
v_mfma_f32_16x16x32_bf16 v[48:51], a[144:147], a[44:47], v[48:51]
ds_read_b64_tr_b16 a[10:11], v194 offset:2304
v_cvt_pk_bf16_f32 v164, v24, v25
v_cvt_pk_bf16_f32 v165, v26, v27
v_mfma_f32_16x16x32_bf16 v[52:55], a[148:151], a[44:47], v[52:55]
ds_read_b64_tr_b16 a[12:13], v195 offset:384
v_cvt_pk_bf16_f32 v166, v28, v29
v_cvt_pk_bf16_f32 v167, v30, v31
v_mfma_f32_16x16x32_bf16 v[56:59], a[152:155], a[44:47], v[56:59]
ds_read_b64_tr_b16 a[14:15], v195 offset:2432
v_cvt_pk_bf16_f32 v136, v32, v33
v_cvt_pk_bf16_f32 v137, v34, v35
v_mfma_f32_16x16x32_bf16 v[60:63], a[156:159], a[44:47], v[60:63]
ds_read_b64_tr_b16 a[16:17], v194 offset:512
v_cvt_pk_bf16_f32 v138, v36, v37
v_cvt_pk_bf16_f32 v139, v38, v39
v_mfma_f32_16x16x32_bf16 v[64:67], a[144:147], a[48:51], v[64:67]
buffer_load_dwordx4 v203, s[8:11], s28 offen offset:3072 lds
v_cvt_pk_bf16_f32 v168, v40, v41
v_cvt_pk_bf16_f32 v169, v42, v43
v_mfma_f32_16x16x32_bf16 v[68:71], a[148:151], a[48:51], v[68:71]
ds_read_b64_tr_b16 a[18:19], v194 offset:2560
v_cvt_pk_bf16_f32 v170, v44, v45
v_cvt_pk_bf16_f32 v171, v46, v47
v_mfma_f32_16x16x32_bf16 v[72:75], a[152:155], a[48:51], v[72:75]
ds_read_b64_tr_b16 a[20:21], v195 offset:640
v_cvt_pk_bf16_f32 v140, v48, v49
v_cvt_pk_bf16_f32 v141, v50, v51
v_mfma_f32_16x16x32_bf16 v[76:79], a[156:159], a[48:51], v[76:79]
ds_read_b64_tr_b16 a[22:23], v195 offset:2688
v_cvt_pk_bf16_f32 v142, v52, v53
v_cvt_pk_bf16_f32 v143, v54, v55
v_mfma_f32_16x16x32_bf16 v[80:83], a[144:147], a[52:55], v[80:83]
ds_read_b64_tr_b16 a[24:25], v194 offset:768
s_add_u32 m0, s35, 65536
v_cvt_pk_bf16_f32 v172, v56, v57
v_cvt_pk_bf16_f32 v173, v58, v59
v_mfma_f32_16x16x32_bf16 v[84:87], a[148:151], a[52:55], v[84:87]
buffer_load_dwordx4 v204, s[12:15], s29 offen offset:0 lds
v_cvt_pk_bf16_f32 v174, v60, v61
v_cvt_pk_bf16_f32 v175, v62, v63
v_mfma_f32_16x16x32_bf16 v[88:91], a[152:155], a[52:55], v[88:91]
ds_read_b64_tr_b16 a[26:27], v194 offset:2816
v_cvt_pk_bf16_f32 v144, v64, v65
v_cvt_pk_bf16_f32 v145, v66, v67
v_mfma_f32_16x16x32_bf16 v[92:95], a[156:159], a[52:55], v[92:95]
ds_read_b64_tr_b16 a[28:29], v195 offset:896
v_cvt_pk_bf16_f32 v146, v68, v69
v_cvt_pk_bf16_f32 v147, v70, v71
v_mfma_f32_16x16x32_bf16 v[96:99], a[144:147], a[56:59], v[96:99]
ds_read_b64_tr_b16 a[30:31], v195 offset:2944
v_cvt_pk_bf16_f32 v176, v72, v73
v_cvt_pk_bf16_f32 v177, v74, v75
v_mfma_f32_16x16x32_bf16 v[100:103], a[148:151], a[56:59], v[100:103]
v_cvt_pk_bf16_f32 v178, v76, v77
v_cvt_pk_bf16_f32 v179, v78, v79
v_mfma_f32_16x16x32_bf16 v[104:107], a[152:155], a[56:59], v[104:107]
buffer_load_dwordx4 v204, s[16:19], s29 offen offset:1024 lds
v_cvt_pk_bf16_f32 v148, v80, v81
v_cvt_pk_bf16_f32 v149, v82, v83
v_mfma_f32_16x16x32_bf16 v[108:111], a[156:159], a[56:59], v[108:111]
v_cvt_pk_bf16_f32 v150, v84, v85
v_cvt_pk_bf16_f32 v151, v86, v87
v_mfma_f32_16x16x32_bf16 v[112:115], a[144:147], a[60:63], v[112:115]
v_cvt_pk_bf16_f32 v180, v88, v89
v_cvt_pk_bf16_f32 v181, v90, v91
v_mfma_f32_16x16x32_bf16 v[116:119], a[148:151], a[60:63], v[116:119]
v_cvt_pk_bf16_f32 v182, v92, v93
v_cvt_pk_bf16_f32 v183, v94, v95
v_mfma_f32_16x16x32_bf16 v[120:123], a[152:155], a[60:63], v[120:123]
v_cvt_pk_bf16_f32 v152, v96, v97
v_cvt_pk_bf16_f32 v153, v98, v99
v_mfma_f32_16x16x32_bf16 v[124:127], a[156:159], a[60:63], v[124:127]
buffer_load_dwordx4 v205, s[12:15], s29 offen offset:2048 lds
v_cvt_pk_bf16_f32 v154, v100, v101
v_cvt_pk_bf16_f32 v155, v102, v103
s_waitcnt lgkmcnt(0)
v_mfma_f32_16x16x32_bf16 v[0:3], a[128:131], a[0:3], 0
ds_read_b64_tr_b16 a[32:33], v194 offset:16384
)ASM"
R"ASM(v_cvt_pk_bf16_f32 v184, v104, v105
v_cvt_pk_bf16_f32 v185, v106, v107
v_mfma_f32_16x16x32_bf16 v[4:7], a[132:135], a[0:3], 0
ds_read_b64_tr_b16 a[34:35], v194 offset:18432
v_cvt_pk_bf16_f32 v186, v108, v109
v_cvt_pk_bf16_f32 v187, v110, v111
v_mfma_f32_16x16x32_bf16 v[8:11], a[136:139], a[0:3], 0
ds_read_b128 a[144:147], v193 offset:0
v_cvt_pk_bf16_f32 v156, v112, v113
v_cvt_pk_bf16_f32 v157, v114, v115
v_mfma_f32_16x16x32_bf16 v[12:15], a[140:143], a[0:3], 0
ds_read_b128 a[148:151], v193 offset:2048
v_cvt_pk_bf16_f32 v158, v116, v117
v_cvt_pk_bf16_f32 v159, v118, v119
v_mfma_f32_16x16x32_bf16 v[16:19], a[128:131], a[4:7], 0
buffer_load_dwordx4 v205, s[16:19], s29 offen offset:3072 lds
v_cvt_pk_bf16_f32 v188, v120, v121
v_cvt_pk_bf16_f32 v189, v122, v123
v_mfma_f32_16x16x32_bf16 v[20:23], a[132:135], a[4:7], 0
ds_read_b128 a[152:155], v193 offset:4096
v_cvt_pk_bf16_f32 v190, v124, v125
v_cvt_pk_bf16_f32 v191, v126, v127
v_mfma_f32_16x16x32_bf16 v[24:27], a[136:139], a[4:7], 0
ds_read_b128 a[156:159], v193 offset:6144
v_mfma_f32_16x16x32_bf16 v[28:31], a[140:143], a[4:7], 0
ds_read_b64_tr_b16 a[36:37], v195 offset:16512
v_mfma_f32_16x16x32_bf16 v[32:35], a[128:131], a[8:11], 0
ds_read_b64_tr_b16 a[38:39], v195 offset:18560
s_add_u32 m0, s35, 69632
v_mfma_f32_16x16x32_bf16 v[36:39], a[132:135], a[8:11], 0
buffer_load_dwordx4 v206, s[12:15], s30 offen offset:0 lds
v_mfma_f32_16x16x32_bf16 v[40:43], a[136:139], a[8:11], 0
ds_read_b64_tr_b16 a[40:41], v194 offset:16640
v_mfma_f32_16x16x32_bf16 v[44:47], a[140:143], a[8:11], 0
ds_read_b64_tr_b16 a[42:43], v194 offset:18688
v_mfma_f32_16x16x32_bf16 v[48:51], a[128:131], a[12:15], 0
ds_read_b64_tr_b16 a[44:45], v195 offset:16768
v_mfma_f32_16x16x32_bf16 v[52:55], a[132:135], a[12:15], 0
ds_read_b64_tr_b16 a[46:47], v195 offset:18816
v_mfma_f32_16x16x32_bf16 v[56:59], a[136:139], a[12:15], 0
buffer_load_dwordx4 v206, s[16:19], s30 offen offset:1024 lds
v_mfma_f32_16x16x32_bf16 v[60:63], a[140:143], a[12:15], 0
ds_read_b64_tr_b16 a[48:49], v194 offset:16896
v_mfma_f32_16x16x32_bf16 v[64:67], a[128:131], a[16:19], 0
ds_read_b64_tr_b16 a[50:51], v194 offset:18944
v_mfma_f32_16x16x32_bf16 v[68:71], a[132:135], a[16:19], 0
ds_read_b64_tr_b16 a[52:53], v195 offset:17024
v_mfma_f32_16x16x32_bf16 v[72:75], a[136:139], a[16:19], 0
ds_read_b64_tr_b16 a[54:55], v195 offset:19072
v_mfma_f32_16x16x32_bf16 v[76:79], a[140:143], a[16:19], 0
buffer_load_dwordx4 v207, s[12:15], s30 offen offset:2048 lds
v_mfma_f32_16x16x32_bf16 v[80:83], a[128:131], a[20:23], 0
ds_read_b64_tr_b16 a[56:57], v194 offset:17152
v_mfma_f32_16x16x32_bf16 v[84:87], a[132:135], a[20:23], 0
ds_read_b64_tr_b16 a[58:59], v194 offset:19200
v_mfma_f32_16x16x32_bf16 v[88:91], a[136:139], a[20:23], 0
ds_read_b64_tr_b16 a[60:61], v195 offset:17280
v_mfma_f32_16x16x32_bf16 v[92:95], a[140:143], a[20:23], 0
ds_read_b64_tr_b16 a[62:63], v195 offset:19328
v_mfma_f32_16x16x32_bf16 v[96:99], a[128:131], a[24:27], 0
buffer_load_dwordx4 v207, s[16:19], s30 offen offset:3072 lds
v_mfma_f32_16x16x32_bf16 v[100:103], a[132:135], a[24:27], 0
s_add_u32 s24, s24, 1
s_cmp_eq_u32 s24, 8
v_mfma_f32_16x16x32_bf16 v[104:107], a[136:139], a[24:27], 0
s_cselect_b32 s24, 1, s24
s_lshl_b32 s28, s24, 7
v_mfma_f32_16x16x32_bf16 v[108:111], a[140:143], a[24:27], 0
s_add_u32 s28, s28, s25
s_lshl_b32 s29, s24, 19
v_mfma_f32_16x16x32_bf16 v[112:115], a[128:131], a[28:31], 0
s_add_u32 s29, s29, s58
s_add_u32 s30, s29, 65536
v_mfma_f32_16x16x32_bf16 v[116:119], a[132:135], a[28:31], 0
v_mfma_f32_16x16x32_bf16 v[120:123], a[136:139], a[28:31], 0
v_mfma_f32_16x16x32_bf16 v[124:127], a[140:143], a[28:31], 0
s_waitcnt vmcnt(12) lgkmcnt(0)
s_barrier
v_add_u32 v192, 0xfffe8000, v192
v_add_u32 v193, 0xfffe8000, v193
v_add_u32 v194, 0xfffe8000, v194
v_add_u32 v195, 0xfffe8000, v195
s_nop 0
v_mfma_f32_16x16x32_bf16 v[0:3], a[144:147], a[32:35], v[0:3]
ds_read_b64_tr_b16 a[0:1], v194 offset:0
s_add_u32 m0, s34, 114688
v_mfma_f32_16x16x32_bf16 v[4:7], a[148:151], a[32:35], v[4:7]
buffer_load_dwordx4 v200, s[8:11], s28 offen offset:0 lds
v_mfma_f32_16x16x32_bf16 v[8:11], a[152:155], a[32:35], v[8:11]
ds_read_b64_tr_b16 a[2:3], v194 offset:2048
v_mfma_f32_16x16x32_bf16 v[12:15], a[156:159], a[32:35], v[12:15]
ds_read_b128 a[128:131], v192 offset:0
v_mfma_f32_16x16x32_bf16 v[16:19], a[144:147], a[36:39], v[16:19]
ds_read_b128 a[132:135], v192 offset:2048
v_mfma_f32_16x16x32_bf16 v[20:23], a[148:151], a[36:39], v[20:23]
ds_read_b128 a[136:139], v192 offset:4096
v_mfma_f32_16x16x32_bf16 v[24:27], a[152:155], a[36:39], v[24:27]
buffer_load_dwordx4 v201, s[8:11], s28 offen offset:1024 lds
v_mfma_f32_16x16x32_bf16 v[28:31], a[156:159], a[36:39], v[28:31]
ds_read_b128 a[140:143], v192 offset:6144
v_mfma_f32_16x16x32_bf16 v[32:35], a[144:147], a[40:43], v[32:35]
ds_read_b64_tr_b16 a[4:5], v195 offset:128
v_mfma_f32_16x16x32_bf16 v[36:39], a[148:151], a[40:43], v[36:39]
ds_read_b64_tr_b16 a[6:7], v195 offset:2176
v_mfma_f32_16x16x32_bf16 v[40:43], a[152:155], a[40:43], v[40:43]
ds_read_b64_tr_b16 a[8:9], v194 offset:256
v_mfma_f32_16x16x32_bf16 v[44:47], a[156:159], a[40:43], v[44:47]
buffer_load_dwordx4 v202, s[8:11], s28 offen offset:2048 lds
v_mfma_f32_16x16x32_bf16 v[48:51], a[144:147], a[44:47], v[48:51]
ds_read_b64_tr_b16 a[10:11], v194 offset:2304
v_mfma_f32_16x16x32_bf16 v[52:55], a[148:151], a[44:47], v[52:55]
ds_read_b64_tr_b16 a[12:13], v195 offset:384
v_mfma_f32_16x16x32_bf16 v[56:59], a[152:155], a[44:47], v[56:59]
ds_read_b64_tr_b16 a[14:15], v195 offset:2432
v_mfma_f32_16x16x32_bf16 v[60:63], a[156:159], a[44:47], v[60:63]
ds_read_b64_tr_b16 a[16:17], v194 offset:512
v_mfma_f32_16x16x32_bf16 v[64:67], a[144:147], a[48:51], v[64:67]
buffer_load_dwordx4 v203, s[8:11], s28 offen offset:3072 lds
v_mfma_f32_16x16x32_bf16 v[68:71], a[148:151], a[48:51], v[68:71]
ds_read_b64_tr_b16 a[18:19], v194 offset:2560
v_mfma_f32_16x16x32_bf16 v[72:75], a[152:155], a[48:51], v[72:75]
ds_read_b64_tr_b16 a[20:21], v195 offset:640
v_mfma_f32_16x16x32_bf16 v[76:79], a[156:159], a[48:51], v[76:79]
ds_read_b64_tr_b16 a[22:23], v195 offset:2688
v_mfma_f32_16x16x32_bf16 v[80:83], a[144:147], a[52:55], v[80:83]
ds_read_b64_tr_b16 a[24:25], v194 offset:768
s_add_u32 m0, s35, 114688
v_mfma_f32_16x16x32_bf16 v[84:87], a[148:151], a[52:55], v[84:87]
buffer_load_dwordx4 v204, s[12:15], s29 offen offset:0 lds
v_mfma_f32_16x16x32_bf16 v[88:91], a[152:155], a[52:55], v[88:91]
ds_read_b64_tr_b16 a[26:27], v194 offset:2816
v_mfma_f32_16x16x32_bf16 v[92:95], a[156:159], a[52:55], v[92:95]
ds_read_b64_tr_b16 a[28:29], v195 offset:896
v_mfma_f32_16x16x32_bf16 v[96:99], a[144:147], a[56:59], v[96:99]
ds_read_b64_tr_b16 a[30:31], v195 offset:2944
v_mfma_f32_16x16x32_bf16 v[100:103], a[148:151], a[56:59], v[100:103]
v_mfma_f32_16x16x32_bf16 v[104:107], a[152:155], a[56:59], v[104:107]
buffer_load_dwordx4 v204, s[16:19], s29 offen offset:1024 lds
v_mfma_f32_16x16x32_bf16 v[108:111], a[156:159], a[56:59], v[108:111]
v_mfma_f32_16x16x32_bf16 v[112:115], a[144:147], a[60:63], v[112:115]
v_mfma_f32_16x16x32_bf16 v[116:119], a[148:151], a[60:63], v[116:119]
v_mfma_f32_16x16x32_bf16 v[120:123], a[152:155], a[60:63], v[120:123]
v_mfma_f32_16x16x32_bf16 v[124:127], a[156:159], a[60:63], v[124:127]
buffer_load_dwordx4 v205, s[12:15], s29 offen offset:2048 lds
s_waitcnt lgkmcnt(0)
v_mfma_f32_16x16x32_bf16 v[0:3], a[128:131], a[0:3], v[0:3]
ds_read_b64_tr_b16 a[32:33], v194 offset:16384
v_mfma_f32_16x16x32_bf16 v[4:7], a[132:135], a[0:3], v[4:7]
ds_read_b64_tr_b16 a[34:35], v194 offset:18432
v_mfma_f32_16x16x32_bf16 v[8:11], a[136:139], a[0:3], v[8:11]
ds_read_b128 a[144:147], v193 offset:0
v_mfma_f32_16x16x32_bf16 v[12:15], a[140:143], a[0:3], v[12:15]
ds_read_b128 a[148:151], v193 offset:2048
v_mfma_f32_16x16x32_bf16 v[16:19], a[128:131], a[4:7], v[16:19]
buffer_load_dwordx4 v205, s[16:19], s29 offen offset:3072 lds
v_mfma_f32_16x16x32_bf16 v[20:23], a[132:135], a[4:7], v[20:23]
ds_read_b128 a[152:155], v193 offset:4096
v_mfma_f32_16x16x32_bf16 v[24:27], a[136:139], a[4:7], v[24:27]
ds_read_b128 a[156:159], v193 offset:6144
v_mfma_f32_16x16x32_bf16 v[28:31], a[140:143], a[4:7], v[28:31]
ds_read_b64_tr_b16 a[36:37], v195 offset:16512
v_mfma_f32_16x16x32_bf16 v[32:35], a[128:131], a[8:11], v[32:35]
ds_read_b64_tr_b16 a[38:39], v195 offset:18560
s_add_u32 m0, s35, 118784
v_mfma_f32_16x16x32_bf16 v[36:39], a[132:135], a[8:11], v[36:39]
buffer_load_dwordx4 v206, s[12:15], s30 offen offset:0 lds
v_mfma_f32_16x16x32_bf16 v[40:43], a[136:139], a[8:11], v[40:43]
ds_read_b64_tr_b16 a[40:41], v194 offset:16640
v_mfma_f32_16x16x32_bf16 v[44:47], a[140:143], a[8:11], v[44:47]
ds_read_b64_tr_b16 a[42:43], v194 offset:18688
v_mfma_f32_16x16x32_bf16 v[48:51], a[128:131], a[12:15], v[48:51]
ds_read_b64_tr_b16 a[44:45], v195 offset:16768
v_mfma_f32_16x16x32_bf16 v[52:55], a[132:135], a[12:15], v[52:55]
ds_read_b64_tr_b16 a[46:47], v195 offset:18816
v_mfma_f32_16x16x32_bf16 v[56:59], a[136:139], a[12:15], v[56:59]
buffer_load_dwordx4 v206, s[16:19], s30 offen offset:1024 lds
v_mfma_f32_16x16x32_bf16 v[60:63], a[140:143], a[12:15], v[60:63]
ds_read_b64_tr_b16 a[48:49], v194 offset:16896
v_mfma_f32_16x16x32_bf16 v[64:67], a[128:131], a[16:19], v[64:67]
ds_read_b64_tr_b16 a[50:51], v194 offset:18944
v_mfma_f32_16x16x32_bf16 v[68:71], a[132:135], a[16:19], v[68:71]
ds_read_b64_tr_b16 a[52:53], v195 offset:17024
v_mfma_f32_16x16x32_bf16 v[72:75], a[136:139], a[16:19], v[72:75]
ds_read_b64_tr_b16 a[54:55], v195 offset:19072
v_mfma_f32_16x16x32_bf16 v[76:79], a[140:143], a[16:19], v[76:79]
buffer_load_dwordx4 v207, s[12:15], s30 offen offset:2048 lds
v_mfma_f32_16x16x32_bf16 v[80:83], a[128:131], a[20:23], v[80:83]
ds_read_b64_tr_b16 a[56:57], v194 offset:17152
v_mfma_f32_16x16x32_bf16 v[84:87], a[132:135], a[20:23], v[84:87]
ds_read_b64_tr_b16 a[58:59], v194 offset:19200
v_mfma_f32_16x16x32_bf16 v[88:91], a[136:139], a[20:23], v[88:91]
ds_read_b64_tr_b16 a[60:61], v195 offset:17280
v_mfma_f32_16x16x32_bf16 v[92:95], a[140:143], a[20:23], v[92:95]
ds_read_b64_tr_b16 a[62:63], v195 offset:19328
v_mfma_f32_16x16x32_bf16 v[96:99], a[128:131], a[24:27], v[96:99]
buffer_load_dwordx4 v207, s[16:19], s30 offen offset:3072 lds
v_mfma_f32_16x16x32_bf16 v[100:103], a[132:135], a[24:27], v[100:103]
s_add_u32 s24, s24, 1
s_cmp_eq_u32 s24, 8
v_mfma_f32_16x16x32_bf16 v[104:107], a[136:139], a[24:27], v[104:107]
s_cselect_b32 s24, 1, s24
s_lshl_b32 s28, s24, 7
v_mfma_f32_16x16x32_bf16 v[108:111], a[140:143], a[24:27], v[108:111]
s_add_u32 s28, s28, s25
s_lshl_b32 s29, s24, 19
v_mfma_f32_16x16x32_bf16 v[112:115], a[128:131], a[28:31], v[112:115]
s_add_u32 s29, s29, s58
s_add_u32 s30, s29, 65536
v_mfma_f32_16x16x32_bf16 v[116:119], a[132:135], a[28:31], v[116:119]
s_add_u32 s61, s62, 0
buffer_store_dwordx4 v[128:131], v252, s[40:43], s61 offen sc1
s_add_u32 s61, s62, 1024
buffer_store_dwordx4 v[132:135], v252, s[40:43], s61 offen sc1
v_mfma_f32_16x16x32_bf16 v[120:123], a[136:139], a[28:31], v[120:123]
v_mfma_f32_16x16x32_bf16 v[124:127], a[140:143], a[28:31], v[124:127]
s_waitcnt vmcnt(14) lgkmcnt(0)
s_barrier
v_add_u32 v192, 0xc000, v192
v_add_u32 v193, 0xc000, v193
v_add_u32 v194, 0xc000, v194
v_add_u32 v195, 0xc000, v195
s_nop 0
v_mfma_f32_16x16x32_bf16 v[0:3], a[144:147], a[32:35], v[0:3]
ds_read_b64_tr_b16 a[0:1], v194 offset:0
s_add_u32 m0, s34, 16384
v_mfma_f32_16x16x32_bf16 v[4:7], a[148:151], a[32:35], v[4:7]
buffer_load_dwordx4 v200, s[8:11], s28 offen offset:0 lds
v_mfma_f32_16x16x32_bf16 v[8:11], a[152:155], a[32:35], v[8:11]
ds_read_b64_tr_b16 a[2:3], v194 offset:2048
v_mfma_f32_16x16x32_bf16 v[12:15], a[156:159], a[32:35], v[12:15]
ds_read_b128 a[128:131], v192 offset:0
v_mfma_f32_16x16x32_bf16 v[16:19], a[144:147], a[36:39], v[16:19]
ds_read_b128 a[132:135], v192 offset:2048
v_mfma_f32_16x16x32_bf16 v[20:23], a[148:151], a[36:39], v[20:23]
ds_read_b128 a[136:139], v192 offset:4096
v_mfma_f32_16x16x32_bf16 v[24:27], a[152:155], a[36:39], v[24:27]
buffer_load_dwordx4 v201, s[8:11], s28 offen offset:1024 lds
v_mfma_f32_16x16x32_bf16 v[28:31], a[156:159], a[36:39], v[28:31]
ds_read_b128 a[140:143], v192 offset:6144
v_mfma_f32_16x16x32_bf16 v[32:35], a[144:147], a[40:43], v[32:35]
ds_read_b64_tr_b16 a[4:5], v195 offset:128
v_mfma_f32_16x16x32_bf16 v[36:39], a[148:151], a[40:43], v[36:39]
ds_read_b64_tr_b16 a[6:7], v195 offset:2176
v_mfma_f32_16x16x32_bf16 v[40:43], a[152:155], a[40:43], v[40:43]
ds_read_b64_tr_b16 a[8:9], v194 offset:256
v_mfma_f32_16x16x32_bf16 v[44:47], a[156:159], a[40:43], v[44:47]
buffer_load_dwordx4 v202, s[8:11], s28 offen offset:2048 lds
v_mfma_f32_16x16x32_bf16 v[48:51], a[144:147], a[44:47], v[48:51]
ds_read_b64_tr_b16 a[10:11], v194 offset:2304
v_mfma_f32_16x16x32_bf16 v[52:55], a[148:151], a[44:47], v[52:55]
ds_read_b64_tr_b16 a[12:13], v195 offset:384
v_mfma_f32_16x16x32_bf16 v[56:59], a[152:155], a[44:47], v[56:59]
ds_read_b64_tr_b16 a[14:15], v195 offset:2432
v_mfma_f32_16x16x32_bf16 v[60:63], a[156:159], a[44:47], v[60:63]
ds_read_b64_tr_b16 a[16:17], v194 offset:512
v_mfma_f32_16x16x32_bf16 v[64:67], a[144:147], a[48:51], v[64:67]
buffer_load_dwordx4 v203, s[8:11], s28 offen offset:3072 lds
v_mfma_f32_16x16x32_bf16 v[68:71], a[148:151], a[48:51], v[68:71]
ds_read_b64_tr_b16 a[18:19], v194 offset:2560
v_mfma_f32_16x16x32_bf16 v[72:75], a[152:155], a[48:51], v[72:75]
ds_read_b64_tr_b16 a[20:21], v195 offset:640
v_mfma_f32_16x16x32_bf16 v[76:79], a[156:159], a[48:51], v[76:79]
ds_read_b64_tr_b16 a[22:23], v195 offset:2688
v_mfma_f32_16x16x32_bf16 v[80:83], a[144:147], a[52:55], v[80:83]
ds_read_b64_tr_b16 a[24:25], v194 offset:768
s_add_u32 m0, s35, 16384
v_mfma_f32_16x16x32_bf16 v[84:87], a[148:151], a[52:55], v[84:87]
buffer_load_dwordx4 v204, s[12:15], s29 offen offset:0 lds
v_mfma_f32_16x16x32_bf16 v[88:91], a[152:155], a[52:55], v[88:91]
ds_read_b64_tr_b16 a[26:27], v194 offset:2816
v_mfma_f32_16x16x32_bf16 v[92:95], a[156:159], a[52:55], v[92:95]
ds_read_b64_tr_b16 a[28:29], v195 offset:896
v_mfma_f32_16x16x32_bf16 v[96:99], a[144:147], a[56:59], v[96:99]
ds_read_b64_tr_b16 a[30:31], v195 offset:2944
v_mfma_f32_16x16x32_bf16 v[100:103], a[148:151], a[56:59], v[100:103]
v_mfma_f32_16x16x32_bf16 v[104:107], a[152:155], a[56:59], v[104:107]
buffer_load_dwordx4 v204, s[16:19], s29 offen offset:1024 lds
v_mfma_f32_16x16x32_bf16 v[108:111], a[156:159], a[56:59], v[108:111]
v_mfma_f32_16x16x32_bf16 v[112:115], a[144:147], a[60:63], v[112:115]
v_mfma_f32_16x16x32_bf16 v[116:119], a[148:151], a[60:63], v[116:119]
v_mfma_f32_16x16x32_bf16 v[120:123], a[152:155], a[60:63], v[120:123]
v_mfma_f32_16x16x32_bf16 v[124:127], a[156:159], a[60:63], v[124:127]
buffer_load_dwordx4 v205, s[12:15], s29 offen offset:2048 lds
s_waitcnt lgkmcnt(0)
v_mfma_f32_16x16x32_bf16 v[0:3], a[128:131], a[0:3], v[0:3]
ds_read_b64_tr_b16 a[32:33], v194 offset:16384
v_mfma_f32_16x16x32_bf16 v[4:7], a[132:135], a[0:3], v[4:7]
ds_read_b64_tr_b16 a[34:35], v194 offset:18432
v_mfma_f32_16x16x32_bf16 v[8:11], a[136:139], a[0:3], v[8:11]
ds_read_b128 a[144:147], v193 offset:0
v_mfma_f32_16x16x32_bf16 v[12:15], a[140:143], a[0:3], v[12:15]
ds_read_b128 a[148:151], v193 offset:2048
v_mfma_f32_16x16x32_bf16 v[16:19], a[128:131], a[4:7], v[16:19]
buffer_load_dwordx4 v205, s[16:19], s29 offen offset:3072 lds
v_mfma_f32_16x16x32_bf16 v[20:23], a[132:135], a[4:7], v[20:23]
ds_read_b128 a[152:155], v193 offset:4096
v_mfma_f32_16x16x32_bf16 v[24:27], a[136:139], a[4:7], v[24:27]
ds_read_b128 a[156:159], v193 offset:6144
v_mfma_f32_16x16x32_bf16 v[28:31], a[140:143], a[4:7], v[28:31]
ds_read_b64_tr_b16 a[36:37], v195 offset:16512
v_mfma_f32_16x16x32_bf16 v[32:35], a[128:131], a[8:11], v[32:35]
ds_read_b64_tr_b16 a[38:39], v195 offset:18560
s_add_u32 m0, s35, 20480
v_mfma_f32_16x16x32_bf16 v[36:39], a[132:135], a[8:11], v[36:39]
buffer_load_dwordx4 v206, s[12:15], s30 offen offset:0 lds
v_mfma_f32_16x16x32_bf16 v[40:43], a[136:139], a[8:11], v[40:43]
ds_read_b64_tr_b16 a[40:41], v194 offset:16640
v_mfma_f32_16x16x32_bf16 v[44:47], a[140:143], a[8:11], v[44:47]
ds_read_b64_tr_b16 a[42:43], v194 offset:18688
v_mfma_f32_16x16x32_bf16 v[48:51], a[128:131], a[12:15], v[48:51]
ds_read_b64_tr_b16 a[44:45], v195 offset:16768
v_mfma_f32_16x16x32_bf16 v[52:55], a[132:135], a[12:15], v[52:55]
ds_read_b64_tr_b16 a[46:47], v195 offset:18816
v_mfma_f32_16x16x32_bf16 v[56:59], a[136:139], a[12:15], v[56:59]
buffer_load_dwordx4 v206, s[16:19], s30 offen offset:1024 lds
v_mfma_f32_16x16x32_bf16 v[60:63], a[140:143], a[12:15], v[60:63]
ds_read_b64_tr_b16 a[48:49], v194 offset:16896
v_mfma_f32_16x16x32_bf16 v[64:67], a[128:131], a[16:19], v[64:67]
ds_read_b64_tr_b16 a[50:51], v194 offset:18944
v_mfma_f32_16x16x32_bf16 v[68:71], a[132:135], a[16:19], v[68:71]
ds_read_b64_tr_b16 a[52:53], v195 offset:17024
v_mfma_f32_16x16x32_bf16 v[72:75], a[136:139], a[16:19], v[72:75]
ds_read_b64_tr_b16 a[54:55], v195 offset:19072
v_mfma_f32_16x16x32_bf16 v[76:79], a[140:143], a[16:19], v[76:79]
buffer_load_dwordx4 v207, s[12:15], s30 offen offset:2048 lds
v_mfma_f32_16x16x32_bf16 v[80:83], a[128:131], a[20:23], v[80:83]
ds_read_b64_tr_b16 a[56:57], v194 offset:17152
v_mfma_f32_16x16x32_bf16 v[84:87], a[132:135], a[20:23], v[84:87]
ds_read_b64_tr_b16 a[58:59], v194 offset:19200
v_mfma_f32_16x16x32_bf16 v[88:91], a[136:139], a[20:23], v[88:91]
ds_read_b64_tr_b16 a[60:61], v195 offset:17280
v_mfma_f32_16x16x32_bf16 v[92:95], a[140:143], a[20:23], v[92:95]
ds_read_b64_tr_b16 a[62:63], v195 offset:19328
v_mfma_f32_16x16x32_bf16 v[96:99], a[128:131], a[24:27], v[96:99]
buffer_load_dwordx4 v207, s[16:19], s30 offen offset:3072 lds
v_mfma_f32_16x16x32_bf16 v[100:103], a[132:135], a[24:27], v[100:103]
s_add_u32 s24, s24, 1
s_cmp_eq_u32 s24, 8
v_mfma_f32_16x16x32_bf16 v[104:107], a[136:139], a[24:27], v[104:107]
s_cselect_b32 s24, 1, s24
s_lshl_b32 s28, s24, 7
v_mfma_f32_16x16x32_bf16 v[108:111], a[140:143], a[24:27], v[108:111]
s_add_u32 s28, s28, s25
s_lshl_b32 s29, s24, 19
v_mfma_f32_16x16x32_bf16 v[112:115], a[128:131], a[28:31], v[112:115]
s_add_u32 s29, s29, s58
s_add_u32 s30, s29, 65536
v_mfma_f32_16x16x32_bf16 v[116:119], a[132:135], a[28:31], v[116:119]
s_add_u32 s61, s62, 2048
buffer_store_dwordx4 v[136:139], v252, s[40:43], s61 offen sc1
s_add_u32 s61, s62, 3072
buffer_store_dwordx4 v[140:143], v252, s[40:43], s61 offen sc1
v_mfma_f32_16x16x32_bf16 v[120:123], a[136:139], a[28:31], v[120:123]
v_mfma_f32_16x16x32_bf16 v[124:127], a[140:143], a[28:31], v[124:127]
s_waitcnt vmcnt(16) lgkmcnt(0)
s_barrier
v_add_u32 v192, 0xc000, v192
v_add_u32 v193, 0xc000, v193
v_add_u32 v194, 0xc000, v194
v_add_u32 v195, 0xc000, v195
s_nop 0
v_mfma_f32_16x16x32_bf16 v[0:3], a[144:147], a[32:35], v[0:3]
ds_read_b64_tr_b16 a[0:1], v194 offset:0
s_add_u32 m0, s34, 65536
v_mfma_f32_16x16x32_bf16 v[4:7], a[148:151], a[32:35], v[4:7]
buffer_load_dwordx4 v200, s[8:11], s28 offen offset:0 lds
v_mfma_f32_16x16x32_bf16 v[8:11], a[152:155], a[32:35], v[8:11]
ds_read_b64_tr_b16 a[2:3], v194 offset:2048
v_mfma_f32_16x16x32_bf16 v[12:15], a[156:159], a[32:35], v[12:15]
ds_read_b128 a[128:131], v192 offset:0
v_mfma_f32_16x16x32_bf16 v[16:19], a[144:147], a[36:39], v[16:19]
ds_read_b128 a[132:135], v192 offset:2048
v_mfma_f32_16x16x32_bf16 v[20:23], a[148:151], a[36:39], v[20:23]
ds_read_b128 a[136:139], v192 offset:4096
v_mfma_f32_16x16x32_bf16 v[24:27], a[152:155], a[36:39], v[24:27]
buffer_load_dwordx4 v201, s[8:11], s28 offen offset:1024 lds
v_mfma_f32_16x16x32_bf16 v[28:31], a[156:159], a[36:39], v[28:31]
ds_read_b128 a[140:143], v192 offset:6144
v_mfma_f32_16x16x32_bf16 v[32:35], a[144:147], a[40:43], v[32:35]
ds_read_b64_tr_b16 a[4:5], v195 offset:128
v_mfma_f32_16x16x32_bf16 v[36:39], a[148:151], a[40:43], v[36:39]
ds_read_b64_tr_b16 a[6:7], v195 offset:2176
v_mfma_f32_16x16x32_bf16 v[40:43], a[152:155], a[40:43], v[40:43]
ds_read_b64_tr_b16 a[8:9], v194 offset:256
v_mfma_f32_16x16x32_bf16 v[44:47], a[156:159], a[40:43], v[44:47]
buffer_load_dwordx4 v202, s[8:11], s28 offen offset:2048 lds
v_mfma_f32_16x16x32_bf16 v[48:51], a[144:147], a[44:47], v[48:51]
ds_read_b64_tr_b16 a[10:11], v194 offset:2304
v_mfma_f32_16x16x32_bf16 v[52:55], a[148:151], a[44:47], v[52:55]
ds_read_b64_tr_b16 a[12:13], v195 offset:384
v_mfma_f32_16x16x32_bf16 v[56:59], a[152:155], a[44:47], v[56:59]
ds_read_b64_tr_b16 a[14:15], v195 offset:2432
v_mfma_f32_16x16x32_bf16 v[60:63], a[156:159], a[44:47], v[60:63]
ds_read_b64_tr_b16 a[16:17], v194 offset:512
v_mfma_f32_16x16x32_bf16 v[64:67], a[144:147], a[48:51], v[64:67]
buffer_load_dwordx4 v203, s[8:11], s28 offen offset:3072 lds
v_mfma_f32_16x16x32_bf16 v[68:71], a[148:151], a[48:51], v[68:71]
ds_read_b64_tr_b16 a[18:19], v194 offset:2560
v_mfma_f32_16x16x32_bf16 v[72:75], a[152:155], a[48:51], v[72:75]
ds_read_b64_tr_b16 a[20:21], v195 offset:640
v_mfma_f32_16x16x32_bf16 v[76:79], a[156:159], a[48:51], v[76:79]
ds_read_b64_tr_b16 a[22:23], v195 offset:2688
v_mfma_f32_16x16x32_bf16 v[80:83], a[144:147], a[52:55], v[80:83]
ds_read_b64_tr_b16 a[24:25], v194 offset:768
s_add_u32 m0, s35, 65536
v_mfma_f32_16x16x32_bf16 v[84:87], a[148:151], a[52:55], v[84:87]
buffer_load_dwordx4 v204, s[12:15], s29 offen offset:0 lds
v_mfma_f32_16x16x32_bf16 v[88:91], a[152:155], a[52:55], v[88:91]
ds_read_b64_tr_b16 a[26:27], v194 offset:2816
v_mfma_f32_16x16x32_bf16 v[92:95], a[156:159], a[52:55], v[92:95]
ds_read_b64_tr_b16 a[28:29], v195 offset:896
v_mfma_f32_16x16x32_bf16 v[96:99], a[144:147], a[56:59], v[96:99]
ds_read_b64_tr_b16 a[30:31], v195 offset:2944
v_mfma_f32_16x16x32_bf16 v[100:103], a[148:151], a[56:59], v[100:103]
v_mfma_f32_16x16x32_bf16 v[104:107], a[152:155], a[56:59], v[104:107]
buffer_load_dwordx4 v204, s[16:19], s29 offen offset:1024 lds
v_mfma_f32_16x16x32_bf16 v[108:111], a[156:159], a[56:59], v[108:111]
v_mfma_f32_16x16x32_bf16 v[112:115], a[144:147], a[60:63], v[112:115]
v_mfma_f32_16x16x32_bf16 v[116:119], a[148:151], a[60:63], v[116:119]
v_mfma_f32_16x16x32_bf16 v[120:123], a[152:155], a[60:63], v[120:123]
v_mfma_f32_16x16x32_bf16 v[124:127], a[156:159], a[60:63], v[124:127]
buffer_load_dwordx4 v205, s[12:15], s29 offen offset:2048 lds
s_waitcnt lgkmcnt(0)
v_mfma_f32_16x16x32_bf16 v[0:3], a[128:131], a[0:3], v[0:3]
ds_read_b64_tr_b16 a[32:33], v194 offset:16384
v_mfma_f32_16x16x32_bf16 v[4:7], a[132:135], a[0:3], v[4:7]
ds_read_b64_tr_b16 a[34:35], v194 offset:18432
v_mfma_f32_16x16x32_bf16 v[8:11], a[136:139], a[0:3], v[8:11]
ds_read_b128 a[144:147], v193 offset:0
v_mfma_f32_16x16x32_bf16 v[12:15], a[140:143], a[0:3], v[12:15]
ds_read_b128 a[148:151], v193 offset:2048
v_mfma_f32_16x16x32_bf16 v[16:19], a[128:131], a[4:7], v[16:19]
buffer_load_dwordx4 v205, s[16:19], s29 offen offset:3072 lds
v_mfma_f32_16x16x32_bf16 v[20:23], a[132:135], a[4:7], v[20:23]
ds_read_b128 a[152:155], v193 offset:4096
v_mfma_f32_16x16x32_bf16 v[24:27], a[136:139], a[4:7], v[24:27]
ds_read_b128 a[156:159], v193 offset:6144
v_mfma_f32_16x16x32_bf16 v[28:31], a[140:143], a[4:7], v[28:31]
ds_read_b64_tr_b16 a[36:37], v195 offset:16512
v_mfma_f32_16x16x32_bf16 v[32:35], a[128:131], a[8:11], v[32:35]
ds_read_b64_tr_b16 a[38:39], v195 offset:18560
s_add_u32 m0, s35, 69632
v_mfma_f32_16x16x32_bf16 v[36:39], a[132:135], a[8:11], v[36:39]
buffer_load_dwordx4 v206, s[12:15], s30 offen offset:0 lds
v_mfma_f32_16x16x32_bf16 v[40:43], a[136:139], a[8:11], v[40:43]
ds_read_b64_tr_b16 a[40:41], v194 offset:16640
v_mfma_f32_16x16x32_bf16 v[44:47], a[140:143], a[8:11], v[44:47]
ds_read_b64_tr_b16 a[42:43], v194 offset:18688
v_mfma_f32_16x16x32_bf16 v[48:51], a[128:131], a[12:15], v[48:51]
ds_read_b64_tr_b16 a[44:45], v195 offset:16768
v_mfma_f32_16x16x32_bf16 v[52:55], a[132:135], a[12:15], v[52:55]
ds_read_b64_tr_b16 a[46:47], v195 offset:18816
v_mfma_f32_16x16x32_bf16 v[56:59], a[136:139], a[12:15], v[56:59]
buffer_load_dwordx4 v206, s[16:19], s30 offen offset:1024 lds
v_mfma_f32_16x16x32_bf16 v[60:63], a[140:143], a[12:15], v[60:63]
ds_read_b64_tr_b16 a[48:49], v194 offset:16896
v_mfma_f32_16x16x32_bf16 v[64:67], a[128:131], a[16:19], v[64:67]
ds_read_b64_tr_b16 a[50:51], v194 offset:18944
v_mfma_f32_16x16x32_bf16 v[68:71], a[132:135], a[16:19], v[68:71]
ds_read_b64_tr_b16 a[52:53], v195 offset:17024
v_mfma_f32_16x16x32_bf16 v[72:75], a[136:139], a[16:19], v[72:75]
ds_read_b64_tr_b16 a[54:55], v195 offset:19072
v_mfma_f32_16x16x32_bf16 v[76:79], a[140:143], a[16:19], v[76:79]
buffer_load_dwordx4 v207, s[12:15], s30 offen offset:2048 lds
v_mfma_f32_16x16x32_bf16 v[80:83], a[128:131], a[20:23], v[80:83]
ds_read_b64_tr_b16 a[56:57], v194 offset:17152
v_mfma_f32_16x16x32_bf16 v[84:87], a[132:135], a[20:23], v[84:87]
ds_read_b64_tr_b16 a[58:59], v194 offset:19200
v_mfma_f32_16x16x32_bf16 v[88:91], a[136:139], a[20:23], v[88:91]
ds_read_b64_tr_b16 a[60:61], v195 offset:17280
v_mfma_f32_16x16x32_bf16 v[92:95], a[140:143], a[20:23], v[92:95]
ds_read_b64_tr_b16 a[62:63], v195 offset:19328
v_mfma_f32_16x16x32_bf16 v[96:99], a[128:131], a[24:27], v[96:99]
buffer_load_dwordx4 v207, s[16:19], s30 offen offset:3072 lds
v_mfma_f32_16x16x32_bf16 v[100:103], a[132:135], a[24:27], v[100:103]
s_add_u32 s24, s24, 1
s_cmp_eq_u32 s24, 8
v_mfma_f32_16x16x32_bf16 v[104:107], a[136:139], a[24:27], v[104:107]
s_cselect_b32 s24, 1, s24
s_lshl_b32 s28, s24, 7
v_mfma_f32_16x16x32_bf16 v[108:111], a[140:143], a[24:27], v[108:111]
s_add_u32 s28, s28, s25
s_lshl_b32 s29, s24, 19
v_mfma_f32_16x16x32_bf16 v[112:115], a[128:131], a[28:31], v[112:115]
s_add_u32 s29, s29, s58
s_add_u32 s30, s29, 65536
v_mfma_f32_16x16x32_bf16 v[116:119], a[132:135], a[28:31], v[116:119]
s_add_u32 s61, s62, 4096
buffer_store_dwordx4 v[144:147], v252, s[40:43], s61 offen sc1
s_add_u32 s61, s62, 5120
buffer_store_dwordx4 v[148:151], v252, s[40:43], s61 offen sc1
v_mfma_f32_16x16x32_bf16 v[120:123], a[136:139], a[28:31], v[120:123]
v_mfma_f32_16x16x32_bf16 v[124:127], a[140:143], a[28:31], v[124:127]
s_waitcnt vmcnt(16) lgkmcnt(0)
s_barrier
s_lshl_b32 s46, s27, 7
s_sub_u32 s59, 15, s27
s_lshl_b32 s59, s59, 7
v_mov_b32 v249, s46
v_mov_b32 v254, s59
v_cmp_gt_u32 vcc, 16, v253
s_nop 1
v_cndmask_b32 v249, v249, v254, vcc
v_and_b32 v254, 15, v253
v_lshl_add_u32 v249, v254, 3, v249
s_mov_b32 s63, 4096
buffer_load_dwordx2 a[250:251], v249, s[88:91], s63 offen sc1
v_add_u32 v192, 0xfffe8000, v192
v_add_u32 v193, 0xfffe8000, v193
v_add_u32 v194, 0xfffe8000, v194
v_add_u32 v195, 0xfffe8000, v195
s_nop 0
v_mfma_f32_16x16x32_bf16 v[0:3], a[144:147], a[32:35], v[0:3]
ds_read_b64_tr_b16 a[0:1], v194 offset:0
s_add_u32 m0, s34, 114688
v_mfma_f32_16x16x32_bf16 v[4:7], a[148:151], a[32:35], v[4:7]
buffer_load_dwordx4 v200, s[8:11], s28 offen offset:0 lds
v_mfma_f32_16x16x32_bf16 v[8:11], a[152:155], a[32:35], v[8:11]
ds_read_b64_tr_b16 a[2:3], v194 offset:2048
v_mfma_f32_16x16x32_bf16 v[12:15], a[156:159], a[32:35], v[12:15]
ds_read_b128 a[128:131], v192 offset:0
v_mfma_f32_16x16x32_bf16 v[16:19], a[144:147], a[36:39], v[16:19]
ds_read_b128 a[132:135], v192 offset:2048
v_mfma_f32_16x16x32_bf16 v[20:23], a[148:151], a[36:39], v[20:23]
ds_read_b128 a[136:139], v192 offset:4096
v_mfma_f32_16x16x32_bf16 v[24:27], a[152:155], a[36:39], v[24:27]
buffer_load_dwordx4 v201, s[8:11], s28 offen offset:1024 lds
v_mfma_f32_16x16x32_bf16 v[28:31], a[156:159], a[36:39], v[28:31]
ds_read_b128 a[140:143], v192 offset:6144
v_mfma_f32_16x16x32_bf16 v[32:35], a[144:147], a[40:43], v[32:35]
ds_read_b64_tr_b16 a[4:5], v195 offset:128
v_mfma_f32_16x16x32_bf16 v[36:39], a[148:151], a[40:43], v[36:39]
ds_read_b64_tr_b16 a[6:7], v195 offset:2176
v_mfma_f32_16x16x32_bf16 v[40:43], a[152:155], a[40:43], v[40:43]
ds_read_b64_tr_b16 a[8:9], v194 offset:256
v_mfma_f32_16x16x32_bf16 v[44:47], a[156:159], a[40:43], v[44:47]
buffer_load_dwordx4 v202, s[8:11], s28 offen offset:2048 lds
v_mfma_f32_16x16x32_bf16 v[48:51], a[144:147], a[44:47], v[48:51]
ds_read_b64_tr_b16 a[10:11], v194 offset:2304
v_mfma_f32_16x16x32_bf16 v[52:55], a[148:151], a[44:47], v[52:55]
ds_read_b64_tr_b16 a[12:13], v195 offset:384
v_mfma_f32_16x16x32_bf16 v[56:59], a[152:155], a[44:47], v[56:59]
ds_read_b64_tr_b16 a[14:15], v195 offset:2432
v_mfma_f32_16x16x32_bf16 v[60:63], a[156:159], a[44:47], v[60:63]
ds_read_b64_tr_b16 a[16:17], v194 offset:512
v_mfma_f32_16x16x32_bf16 v[64:67], a[144:147], a[48:51], v[64:67]
buffer_load_dwordx4 v203, s[8:11], s28 offen offset:3072 lds
v_mfma_f32_16x16x32_bf16 v[68:71], a[148:151], a[48:51], v[68:71]
ds_read_b64_tr_b16 a[18:19], v194 offset:2560
v_mfma_f32_16x16x32_bf16 v[72:75], a[152:155], a[48:51], v[72:75]
ds_read_b64_tr_b16 a[20:21], v195 offset:640
v_mfma_f32_16x16x32_bf16 v[76:79], a[156:159], a[48:51], v[76:79]
ds_read_b64_tr_b16 a[22:23], v195 offset:2688
v_mfma_f32_16x16x32_bf16 v[80:83], a[144:147], a[52:55], v[80:83]
ds_read_b64_tr_b16 a[24:25], v194 offset:768
s_add_u32 m0, s35, 114688
v_mfma_f32_16x16x32_bf16 v[84:87], a[148:151], a[52:55], v[84:87]
buffer_load_dwordx4 v204, s[12:15], s29 offen offset:0 lds
v_mfma_f32_16x16x32_bf16 v[88:91], a[152:155], a[52:55], v[88:91]
ds_read_b64_tr_b16 a[26:27], v194 offset:2816
v_mfma_f32_16x16x32_bf16 v[92:95], a[156:159], a[52:55], v[92:95]
ds_read_b64_tr_b16 a[28:29], v195 offset:896
v_mfma_f32_16x16x32_bf16 v[96:99], a[144:147], a[56:59], v[96:99]
ds_read_b64_tr_b16 a[30:31], v195 offset:2944
v_mfma_f32_16x16x32_bf16 v[100:103], a[148:151], a[56:59], v[100:103]
v_mfma_f32_16x16x32_bf16 v[104:107], a[152:155], a[56:59], v[104:107]
buffer_load_dwordx4 v204, s[16:19], s29 offen offset:1024 lds
v_mfma_f32_16x16x32_bf16 v[108:111], a[156:159], a[56:59], v[108:111]
v_mfma_f32_16x16x32_bf16 v[112:115], a[144:147], a[60:63], v[112:115]
v_mfma_f32_16x16x32_bf16 v[116:119], a[148:151], a[60:63], v[116:119]
v_mfma_f32_16x16x32_bf16 v[120:123], a[152:155], a[60:63], v[120:123]
v_mfma_f32_16x16x32_bf16 v[124:127], a[156:159], a[60:63], v[124:127]
buffer_load_dwordx4 v205, s[12:15], s29 offen offset:2048 lds
s_waitcnt lgkmcnt(0)
v_mfma_f32_16x16x32_bf16 v[0:3], a[128:131], a[0:3], v[0:3]
ds_read_b64_tr_b16 a[32:33], v194 offset:16384
v_mfma_f32_16x16x32_bf16 v[4:7], a[132:135], a[0:3], v[4:7]
ds_read_b64_tr_b16 a[34:35], v194 offset:18432
v_mfma_f32_16x16x32_bf16 v[8:11], a[136:139], a[0:3], v[8:11]
ds_read_b128 a[144:147], v193 offset:0
v_mfma_f32_16x16x32_bf16 v[12:15], a[140:143], a[0:3], v[12:15]
ds_read_b128 a[148:151], v193 offset:2048
v_mfma_f32_16x16x32_bf16 v[16:19], a[128:131], a[4:7], v[16:19]
buffer_load_dwordx4 v205, s[16:19], s29 offen offset:3072 lds
v_mfma_f32_16x16x32_bf16 v[20:23], a[132:135], a[4:7], v[20:23]
ds_read_b128 a[152:155], v193 offset:4096
v_mfma_f32_16x16x32_bf16 v[24:27], a[136:139], a[4:7], v[24:27]
ds_read_b128 a[156:159], v193 offset:6144
v_mfma_f32_16x16x32_bf16 v[28:31], a[140:143], a[4:7], v[28:31]
ds_read_b64_tr_b16 a[36:37], v195 offset:16512
v_mfma_f32_16x16x32_bf16 v[32:35], a[128:131], a[8:11], v[32:35]
ds_read_b64_tr_b16 a[38:39], v195 offset:18560
s_add_u32 m0, s35, 118784
v_mfma_f32_16x16x32_bf16 v[36:39], a[132:135], a[8:11], v[36:39]
buffer_load_dwordx4 v206, s[12:15], s30 offen offset:0 lds
v_mfma_f32_16x16x32_bf16 v[40:43], a[136:139], a[8:11], v[40:43]
ds_read_b64_tr_b16 a[40:41], v194 offset:16640
v_mfma_f32_16x16x32_bf16 v[44:47], a[140:143], a[8:11], v[44:47]
ds_read_b64_tr_b16 a[42:43], v194 offset:18688
v_mfma_f32_16x16x32_bf16 v[48:51], a[128:131], a[12:15], v[48:51]
ds_read_b64_tr_b16 a[44:45], v195 offset:16768
v_mfma_f32_16x16x32_bf16 v[52:55], a[132:135], a[12:15], v[52:55]
ds_read_b64_tr_b16 a[46:47], v195 offset:18816
v_mfma_f32_16x16x32_bf16 v[56:59], a[136:139], a[12:15], v[56:59]
buffer_load_dwordx4 v206, s[16:19], s30 offen offset:1024 lds
v_mfma_f32_16x16x32_bf16 v[60:63], a[140:143], a[12:15], v[60:63]
ds_read_b64_tr_b16 a[48:49], v194 offset:16896
v_mfma_f32_16x16x32_bf16 v[64:67], a[128:131], a[16:19], v[64:67]
ds_read_b64_tr_b16 a[50:51], v194 offset:18944
v_mfma_f32_16x16x32_bf16 v[68:71], a[132:135], a[16:19], v[68:71]
ds_read_b64_tr_b16 a[52:53], v195 offset:17024
v_mfma_f32_16x16x32_bf16 v[72:75], a[136:139], a[16:19], v[72:75]
ds_read_b64_tr_b16 a[54:55], v195 offset:19072
v_mfma_f32_16x16x32_bf16 v[76:79], a[140:143], a[16:19], v[76:79]
buffer_load_dwordx4 v207, s[12:15], s30 offen offset:2048 lds
v_mfma_f32_16x16x32_bf16 v[80:83], a[128:131], a[20:23], v[80:83]
ds_read_b64_tr_b16 a[56:57], v194 offset:17152
v_mfma_f32_16x16x32_bf16 v[84:87], a[132:135], a[20:23], v[84:87]
ds_read_b64_tr_b16 a[58:59], v194 offset:19200
v_mfma_f32_16x16x32_bf16 v[88:91], a[136:139], a[20:23], v[88:91]
ds_read_b64_tr_b16 a[60:61], v195 offset:17280
v_mfma_f32_16x16x32_bf16 v[92:95], a[140:143], a[20:23], v[92:95]
ds_read_b64_tr_b16 a[62:63], v195 offset:19328
v_mfma_f32_16x16x32_bf16 v[96:99], a[128:131], a[24:27], v[96:99]
buffer_load_dwordx4 v207, s[16:19], s30 offen offset:3072 lds
v_mfma_f32_16x16x32_bf16 v[100:103], a[132:135], a[24:27], v[100:103]
s_add_u32 s24, s24, 1
s_cmp_eq_u32 s24, 8
v_mfma_f32_16x16x32_bf16 v[104:107], a[136:139], a[24:27], v[104:107]
s_cselect_b32 s24, 1, s24
s_lshl_b32 s28, s24, 7
v_mfma_f32_16x16x32_bf16 v[108:111], a[140:143], a[24:27], v[108:111]
s_add_u32 s28, s28, s25
s_lshl_b32 s29, s24, 19
v_mfma_f32_16x16x32_bf16 v[112:115], a[128:131], a[28:31], v[112:115]
s_add_u32 s29, s29, s58
s_add_u32 s30, s29, 65536
v_mfma_f32_16x16x32_bf16 v[116:119], a[132:135], a[28:31], v[116:119]
s_add_u32 s61, s62, 6144
buffer_store_dwordx4 v[152:155], v252, s[40:43], s61 offen sc1
s_add_u32 s61, s62, 7168
buffer_store_dwordx4 v[156:159], v252, s[40:43], s61 offen sc1
v_mfma_f32_16x16x32_bf16 v[120:123], a[136:139], a[28:31], v[120:123]
v_mfma_f32_16x16x32_bf16 v[124:127], a[140:143], a[28:31], v[124:127]
s_waitcnt vmcnt(17) lgkmcnt(0)
s_barrier
s_waitcnt vmcnt(14)
s_mov_b32 s86, 0
Llnc_4:
v_accvgpr_read_b32 v254, a250
s_nop 0
v_cmp_eq_u32 vcc, s84, v254
s_nop 1
s_mov_b64 s[46:47], vcc
v_accvgpr_read_b32 v254, a251
s_nop 0
v_cmp_eq_u32 vcc, s85, v254
s_nop 1
s_and_b64 vcc, vcc, s[46:47]
s_cmp_eq_u64 vcc, exec
s_cbranch_scc1 Llnd_5
s_sleep 1
buffer_load_dwordx2 a[250:251], v249, s[88:91], s63 offen sc1
s_waitcnt vmcnt(0)
s_add_u32 s86, s86, 1
s_cmp_lt_u32 s86, 0x40000
s_cbranch_scc1 Llnc_4
Llnd_5:
v_add_u32 v192, 0xc000, v192
v_add_u32 v193, 0xc000, v193
v_add_u32 v194, 0xc000, v194
v_add_u32 v195, 0xc000, v195
s_nop 0
v_mfma_f32_16x16x32_bf16 v[0:3], a[144:147], a[32:35], v[0:3]
ds_read_b64_tr_b16 a[0:1], v194 offset:0
s_add_u32 m0, s34, 16384
v_mfma_f32_16x16x32_bf16 v[4:7], a[148:151], a[32:35], v[4:7]
buffer_load_dwordx4 v200, s[8:11], s28 offen offset:0 lds
v_mfma_f32_16x16x32_bf16 v[8:11], a[152:155], a[32:35], v[8:11]
ds_read_b64_tr_b16 a[2:3], v194 offset:2048
v_mfma_f32_16x16x32_bf16 v[12:15], a[156:159], a[32:35], v[12:15]
ds_read_b128 a[128:131], v192 offset:0
v_mfma_f32_16x16x32_bf16 v[16:19], a[144:147], a[36:39], v[16:19]
ds_read_b128 a[132:135], v192 offset:2048
v_mfma_f32_16x16x32_bf16 v[20:23], a[148:151], a[36:39], v[20:23]
ds_read_b128 a[136:139], v192 offset:4096
v_mfma_f32_16x16x32_bf16 v[24:27], a[152:155], a[36:39], v[24:27]
buffer_load_dwordx4 v201, s[8:11], s28 offen offset:1024 lds
v_mfma_f32_16x16x32_bf16 v[28:31], a[156:159], a[36:39], v[28:31]
ds_read_b128 a[140:143], v192 offset:6144
v_mfma_f32_16x16x32_bf16 v[32:35], a[144:147], a[40:43], v[32:35]
ds_read_b64_tr_b16 a[4:5], v195 offset:128
v_mfma_f32_16x16x32_bf16 v[36:39], a[148:151], a[40:43], v[36:39]
ds_read_b64_tr_b16 a[6:7], v195 offset:2176
v_mfma_f32_16x16x32_bf16 v[40:43], a[152:155], a[40:43], v[40:43]
ds_read_b64_tr_b16 a[8:9], v194 offset:256
v_mfma_f32_16x16x32_bf16 v[44:47], a[156:159], a[40:43], v[44:47]
buffer_load_dwordx4 v202, s[8:11], s28 offen offset:2048 lds
v_mfma_f32_16x16x32_bf16 v[48:51], a[144:147], a[44:47], v[48:51]
ds_read_b64_tr_b16 a[10:11], v194 offset:2304
v_mfma_f32_16x16x32_bf16 v[52:55], a[148:151], a[44:47], v[52:55]
ds_read_b64_tr_b16 a[12:13], v195 offset:384
v_mfma_f32_16x16x32_bf16 v[56:59], a[152:155], a[44:47], v[56:59]
ds_read_b64_tr_b16 a[14:15], v195 offset:2432
v_mfma_f32_16x16x32_bf16 v[60:63], a[156:159], a[44:47], v[60:63]
ds_read_b64_tr_b16 a[16:17], v194 offset:512
v_mfma_f32_16x16x32_bf16 v[64:67], a[144:147], a[48:51], v[64:67]
buffer_load_dwordx4 v203, s[8:11], s28 offen offset:3072 lds
v_mfma_f32_16x16x32_bf16 v[68:71], a[148:151], a[48:51], v[68:71]
ds_read_b64_tr_b16 a[18:19], v194 offset:2560
v_mfma_f32_16x16x32_bf16 v[72:75], a[152:155], a[48:51], v[72:75]
ds_read_b64_tr_b16 a[20:21], v195 offset:640
v_mfma_f32_16x16x32_bf16 v[76:79], a[156:159], a[48:51], v[76:79]
ds_read_b64_tr_b16 a[22:23], v195 offset:2688
v_mfma_f32_16x16x32_bf16 v[80:83], a[144:147], a[52:55], v[80:83]
ds_read_b64_tr_b16 a[24:25], v194 offset:768
s_add_u32 m0, s35, 16384
v_mfma_f32_16x16x32_bf16 v[84:87], a[148:151], a[52:55], v[84:87]
buffer_load_dwordx4 v204, s[12:15], s29 offen offset:0 lds
v_mfma_f32_16x16x32_bf16 v[88:91], a[152:155], a[52:55], v[88:91]
ds_read_b64_tr_b16 a[26:27], v194 offset:2816
v_mfma_f32_16x16x32_bf16 v[92:95], a[156:159], a[52:55], v[92:95]
ds_read_b64_tr_b16 a[28:29], v195 offset:896
v_mfma_f32_16x16x32_bf16 v[96:99], a[144:147], a[56:59], v[96:99]
ds_read_b64_tr_b16 a[30:31], v195 offset:2944
v_mfma_f32_16x16x32_bf16 v[100:103], a[148:151], a[56:59], v[100:103]
v_mfma_f32_16x16x32_bf16 v[104:107], a[152:155], a[56:59], v[104:107]
buffer_load_dwordx4 v204, s[16:19], s29 offen offset:1024 lds
v_mfma_f32_16x16x32_bf16 v[108:111], a[156:159], a[56:59], v[108:111]
v_mfma_f32_16x16x32_bf16 v[112:115], a[144:147], a[60:63], v[112:115]
v_mfma_f32_16x16x32_bf16 v[116:119], a[148:151], a[60:63], v[116:119]
v_mfma_f32_16x16x32_bf16 v[120:123], a[152:155], a[60:63], v[120:123]
v_mfma_f32_16x16x32_bf16 v[124:127], a[156:159], a[60:63], v[124:127]
buffer_load_dwordx4 v205, s[12:15], s29 offen offset:2048 lds
s_waitcnt lgkmcnt(0)
v_mfma_f32_16x16x32_bf16 v[0:3], a[128:131], a[0:3], v[0:3]
ds_read_b64_tr_b16 a[32:33], v194 offset:16384
v_mfma_f32_16x16x32_bf16 v[4:7], a[132:135], a[0:3], v[4:7]
ds_read_b64_tr_b16 a[34:35], v194 offset:18432
v_mfma_f32_16x16x32_bf16 v[8:11], a[136:139], a[0:3], v[8:11]
ds_read_b128 a[144:147], v193 offset:0
v_mfma_f32_16x16x32_bf16 v[12:15], a[140:143], a[0:3], v[12:15]
ds_read_b128 a[148:151], v193 offset:2048
v_mfma_f32_16x16x32_bf16 v[16:19], a[128:131], a[4:7], v[16:19]
buffer_load_dwordx4 v205, s[16:19], s29 offen offset:3072 lds
v_mfma_f32_16x16x32_bf16 v[20:23], a[132:135], a[4:7], v[20:23]
ds_read_b128 a[152:155], v193 offset:4096
v_mfma_f32_16x16x32_bf16 v[24:27], a[136:139], a[4:7], v[24:27]
ds_read_b128 a[156:159], v193 offset:6144
v_mfma_f32_16x16x32_bf16 v[28:31], a[140:143], a[4:7], v[28:31]
ds_read_b64_tr_b16 a[36:37], v195 offset:16512
v_mfma_f32_16x16x32_bf16 v[32:35], a[128:131], a[8:11], v[32:35]
ds_read_b64_tr_b16 a[38:39], v195 offset:18560
s_add_u32 m0, s35, 20480
v_mfma_f32_16x16x32_bf16 v[36:39], a[132:135], a[8:11], v[36:39]
buffer_load_dwordx4 v206, s[12:15], s30 offen offset:0 lds
v_mfma_f32_16x16x32_bf16 v[40:43], a[136:139], a[8:11], v[40:43]
ds_read_b64_tr_b16 a[40:41], v194 offset:16640
v_mfma_f32_16x16x32_bf16 v[44:47], a[140:143], a[8:11], v[44:47]
ds_read_b64_tr_b16 a[42:43], v194 offset:18688
v_mfma_f32_16x16x32_bf16 v[48:51], a[128:131], a[12:15], v[48:51]
ds_read_b64_tr_b16 a[44:45], v195 offset:16768
v_mfma_f32_16x16x32_bf16 v[52:55], a[132:135], a[12:15], v[52:55]
ds_read_b64_tr_b16 a[46:47], v195 offset:18816
v_mfma_f32_16x16x32_bf16 v[56:59], a[136:139], a[12:15], v[56:59]
buffer_load_dwordx4 v206, s[16:19], s30 offen offset:1024 lds
v_mfma_f32_16x16x32_bf16 v[60:63], a[140:143], a[12:15], v[60:63]
ds_read_b64_tr_b16 a[48:49], v194 offset:16896
v_mfma_f32_16x16x32_bf16 v[64:67], a[128:131], a[16:19], v[64:67]
ds_read_b64_tr_b16 a[50:51], v194 offset:18944
v_mfma_f32_16x16x32_bf16 v[68:71], a[132:135], a[16:19], v[68:71]
ds_read_b64_tr_b16 a[52:53], v195 offset:17024
v_mfma_f32_16x16x32_bf16 v[72:75], a[136:139], a[16:19], v[72:75]
ds_read_b64_tr_b16 a[54:55], v195 offset:19072
v_mfma_f32_16x16x32_bf16 v[76:79], a[140:143], a[16:19], v[76:79]
buffer_load_dwordx4 v207, s[12:15], s30 offen offset:2048 lds
v_mfma_f32_16x16x32_bf16 v[80:83], a[128:131], a[20:23], v[80:83]
ds_read_b64_tr_b16 a[56:57], v194 offset:17152
v_mfma_f32_16x16x32_bf16 v[84:87], a[132:135], a[20:23], v[84:87]
ds_read_b64_tr_b16 a[58:59], v194 offset:19200
v_mfma_f32_16x16x32_bf16 v[88:91], a[136:139], a[20:23], v[88:91]
ds_read_b64_tr_b16 a[60:61], v195 offset:17280
v_mfma_f32_16x16x32_bf16 v[92:95], a[140:143], a[20:23], v[92:95]
ds_read_b64_tr_b16 a[62:63], v195 offset:19328
v_mfma_f32_16x16x32_bf16 v[96:99], a[128:131], a[24:27], v[96:99]
buffer_load_dwordx4 v207, s[16:19], s30 offen offset:3072 lds
v_mfma_f32_16x16x32_bf16 v[100:103], a[132:135], a[24:27], v[100:103]
s_add_u32 s24, s24, 1
s_and_b32 s24, s24, 7
v_mfma_f32_16x16x32_bf16 v[104:107], a[136:139], a[24:27], v[104:107]
s_mov_b32 s24, 0
s_lshl_b32 s32, s24, 7
v_mfma_f32_16x16x32_bf16 v[108:111], a[140:143], a[24:27], v[108:111]
s_add_u32 s31, s32, s60
s_add_u32 s32, s32, s96
v_mfma_f32_16x16x32_bf16 v[112:115], a[128:131], a[28:31], v[112:115]
s_lshl_b32 s33, s24, 19
s_add_u32 s33, s33, s97
v_mfma_f32_16x16x32_bf16 v[116:119], a[132:135], a[28:31], v[116:119]
s_add_u32 s61, s62, 8192
buffer_store_dwordx4 v[160:163], v252, s[40:43], s61 offen sc1
s_add_u32 s61, s62, 9216
buffer_store_dwordx4 v[164:167], v252, s[40:43], s61 offen sc1
v_mfma_f32_16x16x32_bf16 v[120:123], a[136:139], a[28:31], v[120:123]
v_mfma_f32_16x16x32_bf16 v[124:127], a[140:143], a[28:31], v[124:127]
s_waitcnt vmcnt(16) lgkmcnt(0)
s_barrier
v_add_u32 v192, 0xc000, v192
v_add_u32 v193, 0xc000, v193
v_add_u32 v194, 0xc000, v194
v_add_u32 v195, 0xc000, v195
s_nop 0
v_mfma_f32_16x16x32_bf16 v[0:3], a[144:147], a[32:35], v[0:3]
ds_read_b64_tr_b16 a[0:1], v194 offset:0
s_add_u32 m0, s56, 65536
v_mfma_f32_16x16x32_bf16 v[4:7], a[148:151], a[32:35], v[4:7]
buffer_load_dwordx4 v200, s[20:23], s31 offen offset:0 sc1 lds
v_mfma_f32_16x16x32_bf16 v[8:11], a[152:155], a[32:35], v[8:11]
ds_read_b64_tr_b16 a[2:3], v194 offset:2048
v_mfma_f32_16x16x32_bf16 v[12:15], a[156:159], a[32:35], v[12:15]
ds_read_b128 a[128:131], v192 offset:0
v_mfma_f32_16x16x32_bf16 v[16:19], a[144:147], a[36:39], v[16:19]
ds_read_b128 a[132:135], v192 offset:2048
v_mfma_f32_16x16x32_bf16 v[20:23], a[148:151], a[36:39], v[20:23]
ds_read_b128 a[136:139], v192 offset:4096
v_mfma_f32_16x16x32_bf16 v[24:27], a[152:155], a[36:39], v[24:27]
buffer_load_dwordx4 v201, s[20:23], s31 offen offset:1024 sc1 lds
v_mfma_f32_16x16x32_bf16 v[28:31], a[156:159], a[36:39], v[28:31]
ds_read_b128 a[140:143], v192 offset:6144
v_mfma_f32_16x16x32_bf16 v[32:35], a[144:147], a[40:43], v[32:35]
ds_read_b64_tr_b16 a[4:5], v195 offset:128
v_mfma_f32_16x16x32_bf16 v[36:39], a[148:151], a[40:43], v[36:39]
ds_read_b64_tr_b16 a[6:7], v195 offset:2176
v_mfma_f32_16x16x32_bf16 v[40:43], a[152:155], a[40:43], v[40:43]
ds_read_b64_tr_b16 a[8:9], v194 offset:256
v_mfma_f32_16x16x32_bf16 v[44:47], a[156:159], a[40:43], v[44:47]
buffer_load_dwordx4 v212, s[20:23], s31 offen offset:2048 sc1 lds
v_mfma_f32_16x16x32_bf16 v[48:51], a[144:147], a[44:47], v[48:51]
ds_read_b64_tr_b16 a[10:11], v194 offset:2304
v_mfma_f32_16x16x32_bf16 v[52:55], a[148:151], a[44:47], v[52:55]
ds_read_b64_tr_b16 a[12:13], v195 offset:384
v_mfma_f32_16x16x32_bf16 v[56:59], a[152:155], a[44:47], v[56:59]
ds_read_b64_tr_b16 a[14:15], v195 offset:2432
v_mfma_f32_16x16x32_bf16 v[60:63], a[156:159], a[44:47], v[60:63]
ds_read_b64_tr_b16 a[16:17], v194 offset:512
v_mfma_f32_16x16x32_bf16 v[64:67], a[144:147], a[48:51], v[64:67]
buffer_load_dwordx4 v213, s[20:23], s31 offen offset:3072 sc1 lds
v_mfma_f32_16x16x32_bf16 v[68:71], a[148:151], a[48:51], v[68:71]
ds_read_b64_tr_b16 a[18:19], v194 offset:2560
v_mfma_f32_16x16x32_bf16 v[72:75], a[152:155], a[48:51], v[72:75]
ds_read_b64_tr_b16 a[20:21], v195 offset:640
v_mfma_f32_16x16x32_bf16 v[76:79], a[156:159], a[48:51], v[76:79]
ds_read_b64_tr_b16 a[22:23], v195 offset:2688
v_mfma_f32_16x16x32_bf16 v[80:83], a[144:147], a[52:55], v[80:83]
ds_read_b64_tr_b16 a[24:25], v194 offset:768
s_add_u32 m0, s56, 69632
v_mfma_f32_16x16x32_bf16 v[84:87], a[148:151], a[52:55], v[84:87]
buffer_load_dwordx4 v200, s[20:23], s32 offen offset:0 sc1 lds
v_mfma_f32_16x16x32_bf16 v[88:91], a[152:155], a[52:55], v[88:91]
ds_read_b64_tr_b16 a[26:27], v194 offset:2816
v_mfma_f32_16x16x32_bf16 v[92:95], a[156:159], a[52:55], v[92:95]
ds_read_b64_tr_b16 a[28:29], v195 offset:896
v_mfma_f32_16x16x32_bf16 v[96:99], a[144:147], a[56:59], v[96:99]
ds_read_b64_tr_b16 a[30:31], v195 offset:2944
v_mfma_f32_16x16x32_bf16 v[100:103], a[148:151], a[56:59], v[100:103]
v_mfma_f32_16x16x32_bf16 v[104:107], a[152:155], a[56:59], v[104:107]
buffer_load_dwordx4 v201, s[20:23], s32 offen offset:1024 sc1 lds
v_mfma_f32_16x16x32_bf16 v[108:111], a[156:159], a[56:59], v[108:111]
v_mfma_f32_16x16x32_bf16 v[112:115], a[144:147], a[60:63], v[112:115]
v_mfma_f32_16x16x32_bf16 v[116:119], a[148:151], a[60:63], v[116:119]
v_mfma_f32_16x16x32_bf16 v[120:123], a[152:155], a[60:63], v[120:123]
v_mfma_f32_16x16x32_bf16 v[124:127], a[156:159], a[60:63], v[124:127]
buffer_load_dwordx4 v212, s[20:23], s32 offen offset:2048 sc1 lds
s_waitcnt lgkmcnt(0)
v_mfma_f32_16x16x32_bf16 v[0:3], a[128:131], a[0:3], v[0:3]
ds_read_b64_tr_b16 a[32:33], v194 offset:16384
v_mfma_f32_16x16x32_bf16 v[4:7], a[132:135], a[0:3], v[4:7]
ds_read_b64_tr_b16 a[34:35], v194 offset:18432
v_mfma_f32_16x16x32_bf16 v[8:11], a[136:139], a[0:3], v[8:11]
ds_read_b128 a[144:147], v193 offset:0
v_mfma_f32_16x16x32_bf16 v[12:15], a[140:143], a[0:3], v[12:15]
ds_read_b128 a[148:151], v193 offset:2048
v_mfma_f32_16x16x32_bf16 v[16:19], a[128:131], a[4:7], v[16:19]
buffer_load_dwordx4 v213, s[20:23], s32 offen offset:3072 sc1 lds
v_mfma_f32_16x16x32_bf16 v[20:23], a[132:135], a[4:7], v[20:23]
ds_read_b128 a[152:155], v193 offset:4096
v_mfma_f32_16x16x32_bf16 v[24:27], a[136:139], a[4:7], v[24:27]
ds_read_b128 a[156:159], v193 offset:6144
v_mfma_f32_16x16x32_bf16 v[28:31], a[140:143], a[4:7], v[28:31]
ds_read_b64_tr_b16 a[36:37], v195 offset:16512
v_mfma_f32_16x16x32_bf16 v[32:35], a[128:131], a[8:11], v[32:35]
ds_read_b64_tr_b16 a[38:39], v195 offset:18560
s_add_u32 m0, s57, 65536
v_mfma_f32_16x16x32_bf16 v[36:39], a[132:135], a[8:11], v[36:39]
buffer_load_dwordx4 v208, s[36:39], s33 offen offset:0 lds
v_mfma_f32_16x16x32_bf16 v[40:43], a[136:139], a[8:11], v[40:43]
ds_read_b64_tr_b16 a[40:41], v194 offset:16640
v_mfma_f32_16x16x32_bf16 v[44:47], a[140:143], a[8:11], v[44:47]
ds_read_b64_tr_b16 a[42:43], v194 offset:18688
v_mfma_f32_16x16x32_bf16 v[48:51], a[128:131], a[12:15], v[48:51]
ds_read_b64_tr_b16 a[44:45], v195 offset:16768
v_mfma_f32_16x16x32_bf16 v[52:55], a[132:135], a[12:15], v[52:55]
ds_read_b64_tr_b16 a[46:47], v195 offset:18816
v_mfma_f32_16x16x32_bf16 v[56:59], a[136:139], a[12:15], v[56:59]
buffer_load_dwordx4 v209, s[36:39], s33 offen offset:1024 lds
v_mfma_f32_16x16x32_bf16 v[60:63], a[140:143], a[12:15], v[60:63]
ds_read_b64_tr_b16 a[48:49], v194 offset:16896
v_mfma_f32_16x16x32_bf16 v[64:67], a[128:131], a[16:19], v[64:67]
ds_read_b64_tr_b16 a[50:51], v194 offset:18944
v_mfma_f32_16x16x32_bf16 v[68:71], a[132:135], a[16:19], v[68:71]
ds_read_b64_tr_b16 a[52:53], v195 offset:17024
v_mfma_f32_16x16x32_bf16 v[72:75], a[136:139], a[16:19], v[72:75]
ds_read_b64_tr_b16 a[54:55], v195 offset:19072
v_mfma_f32_16x16x32_bf16 v[76:79], a[140:143], a[16:19], v[76:79]
buffer_load_dwordx4 v210, s[36:39], s33 offen offset:2048 lds
v_mfma_f32_16x16x32_bf16 v[80:83], a[128:131], a[20:23], v[80:83]
ds_read_b64_tr_b16 a[56:57], v194 offset:17152
v_mfma_f32_16x16x32_bf16 v[84:87], a[132:135], a[20:23], v[84:87]
ds_read_b64_tr_b16 a[58:59], v194 offset:19200
v_mfma_f32_16x16x32_bf16 v[88:91], a[136:139], a[20:23], v[88:91]
ds_read_b64_tr_b16 a[60:61], v195 offset:17280
v_mfma_f32_16x16x32_bf16 v[92:95], a[140:143], a[20:23], v[92:95]
ds_read_b64_tr_b16 a[62:63], v195 offset:19328
v_mfma_f32_16x16x32_bf16 v[96:99], a[128:131], a[24:27], v[96:99]
buffer_load_dwordx4 v211, s[36:39], s33 offen offset:3072 lds
v_mfma_f32_16x16x32_bf16 v[100:103], a[132:135], a[24:27], v[100:103]
s_add_u32 s24, s24, 1
s_and_b32 s24, s24, 7
v_mfma_f32_16x16x32_bf16 v[104:107], a[136:139], a[24:27], v[104:107]
s_lshl_b32 s32, s24, 7
s_add_u32 s31, s32, s60
v_mfma_f32_16x16x32_bf16 v[108:111], a[140:143], a[24:27], v[108:111]
s_add_u32 s32, s32, s96
s_lshl_b32 s33, s24, 19
v_mfma_f32_16x16x32_bf16 v[112:115], a[128:131], a[28:31], v[112:115]
s_add_u32 s33, s33, s97
s_add_u32 s61, s62, 10240
buffer_store_dwordx4 v[168:171], v252, s[40:43], s61 offen sc1
v_mfma_f32_16x16x32_bf16 v[116:119], a[132:135], a[28:31], v[116:119]
s_add_u32 s61, s62, 11264
buffer_store_dwordx4 v[172:175], v252, s[40:43], s61 offen sc1
v_mfma_f32_16x16x32_bf16 v[120:123], a[136:139], a[28:31], v[120:123]
v_mfma_f32_16x16x32_bf16 v[124:127], a[140:143], a[28:31], v[124:127]
s_waitcnt vmcnt(16) lgkmcnt(0)
s_barrier
v_add_u32 v192, 0xfffe8000, v192
v_add_u32 v193, 0xfffe8000, v193
v_add_u32 v194, 0xfffe8000, v194
v_add_u32 v195, 0xfffe8000, v195
s_nop 0
v_mfma_f32_16x16x32_bf16 v[0:3], a[144:147], a[32:35], v[0:3]
ds_read_b64_tr_b16 a[0:1], v194 offset:0
s_add_u32 m0, s56, 114688
v_mfma_f32_16x16x32_bf16 v[4:7], a[148:151], a[32:35], v[4:7]
buffer_load_dwordx4 v200, s[20:23], s31 offen offset:0 sc1 lds
v_mfma_f32_16x16x32_bf16 v[8:11], a[152:155], a[32:35], v[8:11]
ds_read_b64_tr_b16 a[2:3], v194 offset:2048
v_mfma_f32_16x16x32_bf16 v[12:15], a[156:159], a[32:35], v[12:15]
ds_read_b128 a[128:131], v192 offset:0
v_mfma_f32_16x16x32_bf16 v[16:19], a[144:147], a[36:39], v[16:19]
ds_read_b128 a[132:135], v192 offset:2048
v_mfma_f32_16x16x32_bf16 v[20:23], a[148:151], a[36:39], v[20:23]
ds_read_b128 a[136:139], v192 offset:4096
v_mfma_f32_16x16x32_bf16 v[24:27], a[152:155], a[36:39], v[24:27]
buffer_load_dwordx4 v201, s[20:23], s31 offen offset:1024 sc1 lds
v_mfma_f32_16x16x32_bf16 v[28:31], a[156:159], a[36:39], v[28:31]
ds_read_b128 a[140:143], v192 offset:6144
v_mfma_f32_16x16x32_bf16 v[32:35], a[144:147], a[40:43], v[32:35]
ds_read_b64_tr_b16 a[4:5], v195 offset:128
)ASM"
R"ASM(v_mfma_f32_16x16x32_bf16 v[36:39], a[148:151], a[40:43], v[36:39]
ds_read_b64_tr_b16 a[6:7], v195 offset:2176
v_mfma_f32_16x16x32_bf16 v[40:43], a[152:155], a[40:43], v[40:43]
ds_read_b64_tr_b16 a[8:9], v194 offset:256
v_mfma_f32_16x16x32_bf16 v[44:47], a[156:159], a[40:43], v[44:47]
buffer_load_dwordx4 v212, s[20:23], s31 offen offset:2048 sc1 lds
v_mfma_f32_16x16x32_bf16 v[48:51], a[144:147], a[44:47], v[48:51]
ds_read_b64_tr_b16 a[10:11], v194 offset:2304
v_mfma_f32_16x16x32_bf16 v[52:55], a[148:151], a[44:47], v[52:55]
ds_read_b64_tr_b16 a[12:13], v195 offset:384
v_mfma_f32_16x16x32_bf16 v[56:59], a[152:155], a[44:47], v[56:59]
ds_read_b64_tr_b16 a[14:15], v195 offset:2432
v_mfma_f32_16x16x32_bf16 v[60:63], a[156:159], a[44:47], v[60:63]
ds_read_b64_tr_b16 a[16:17], v194 offset:512
v_mfma_f32_16x16x32_bf16 v[64:67], a[144:147], a[48:51], v[64:67]
buffer_load_dwordx4 v213, s[20:23], s31 offen offset:3072 sc1 lds
v_mfma_f32_16x16x32_bf16 v[68:71], a[148:151], a[48:51], v[68:71]
ds_read_b64_tr_b16 a[18:19], v194 offset:2560
v_mfma_f32_16x16x32_bf16 v[72:75], a[152:155], a[48:51], v[72:75]
ds_read_b64_tr_b16 a[20:21], v195 offset:640
v_mfma_f32_16x16x32_bf16 v[76:79], a[156:159], a[48:51], v[76:79]
ds_read_b64_tr_b16 a[22:23], v195 offset:2688
v_mfma_f32_16x16x32_bf16 v[80:83], a[144:147], a[52:55], v[80:83]
ds_read_b64_tr_b16 a[24:25], v194 offset:768
s_add_u32 m0, s56, 118784
v_mfma_f32_16x16x32_bf16 v[84:87], a[148:151], a[52:55], v[84:87]
buffer_load_dwordx4 v200, s[20:23], s32 offen offset:0 sc1 lds
v_mfma_f32_16x16x32_bf16 v[88:91], a[152:155], a[52:55], v[88:91]
ds_read_b64_tr_b16 a[26:27], v194 offset:2816
v_mfma_f32_16x16x32_bf16 v[92:95], a[156:159], a[52:55], v[92:95]
ds_read_b64_tr_b16 a[28:29], v195 offset:896
v_mfma_f32_16x16x32_bf16 v[96:99], a[144:147], a[56:59], v[96:99]
ds_read_b64_tr_b16 a[30:31], v195 offset:2944
v_mfma_f32_16x16x32_bf16 v[100:103], a[148:151], a[56:59], v[100:103]
v_mfma_f32_16x16x32_bf16 v[104:107], a[152:155], a[56:59], v[104:107]
buffer_load_dwordx4 v201, s[20:23], s32 offen offset:1024 sc1 lds
v_mfma_f32_16x16x32_bf16 v[108:111], a[156:159], a[56:59], v[108:111]
v_mfma_f32_16x16x32_bf16 v[112:115], a[144:147], a[60:63], v[112:115]
v_mfma_f32_16x16x32_bf16 v[116:119], a[148:151], a[60:63], v[116:119]
v_mfma_f32_16x16x32_bf16 v[120:123], a[152:155], a[60:63], v[120:123]
v_mfma_f32_16x16x32_bf16 v[124:127], a[156:159], a[60:63], v[124:127]
buffer_load_dwordx4 v212, s[20:23], s32 offen offset:2048 sc1 lds
s_waitcnt lgkmcnt(0)
v_mfma_f32_16x16x32_bf16 v[0:3], a[128:131], a[0:3], v[0:3]
ds_read_b64_tr_b16 a[32:33], v194 offset:16384
v_mfma_f32_16x16x32_bf16 v[4:7], a[132:135], a[0:3], v[4:7]
ds_read_b64_tr_b16 a[34:35], v194 offset:18432
v_mfma_f32_16x16x32_bf16 v[8:11], a[136:139], a[0:3], v[8:11]
ds_read_b128 a[144:147], v193 offset:0
v_mfma_f32_16x16x32_bf16 v[12:15], a[140:143], a[0:3], v[12:15]
ds_read_b128 a[148:151], v193 offset:2048
v_mfma_f32_16x16x32_bf16 v[16:19], a[128:131], a[4:7], v[16:19]
buffer_load_dwordx4 v213, s[20:23], s32 offen offset:3072 sc1 lds
v_mfma_f32_16x16x32_bf16 v[20:23], a[132:135], a[4:7], v[20:23]
ds_read_b128 a[152:155], v193 offset:4096
v_mfma_f32_16x16x32_bf16 v[24:27], a[136:139], a[4:7], v[24:27]
ds_read_b128 a[156:159], v193 offset:6144
v_mfma_f32_16x16x32_bf16 v[28:31], a[140:143], a[4:7], v[28:31]
ds_read_b64_tr_b16 a[36:37], v195 offset:16512
v_mfma_f32_16x16x32_bf16 v[32:35], a[128:131], a[8:11], v[32:35]
ds_read_b64_tr_b16 a[38:39], v195 offset:18560
s_add_u32 m0, s57, 114688
v_mfma_f32_16x16x32_bf16 v[36:39], a[132:135], a[8:11], v[36:39]
buffer_load_dwordx4 v208, s[36:39], s33 offen offset:0 lds
v_mfma_f32_16x16x32_bf16 v[40:43], a[136:139], a[8:11], v[40:43]
ds_read_b64_tr_b16 a[40:41], v194 offset:16640
v_mfma_f32_16x16x32_bf16 v[44:47], a[140:143], a[8:11], v[44:47]
ds_read_b64_tr_b16 a[42:43], v194 offset:18688
v_mfma_f32_16x16x32_bf16 v[48:51], a[128:131], a[12:15], v[48:51]
ds_read_b64_tr_b16 a[44:45], v195 offset:16768
v_mfma_f32_16x16x32_bf16 v[52:55], a[132:135], a[12:15], v[52:55]
ds_read_b64_tr_b16 a[46:47], v195 offset:18816
v_mfma_f32_16x16x32_bf16 v[56:59], a[136:139], a[12:15], v[56:59]
buffer_load_dwordx4 v209, s[36:39], s33 offen offset:1024 lds
v_mfma_f32_16x16x32_bf16 v[60:63], a[140:143], a[12:15], v[60:63]
ds_read_b64_tr_b16 a[48:49], v194 offset:16896
v_mfma_f32_16x16x32_bf16 v[64:67], a[128:131], a[16:19], v[64:67]
ds_read_b64_tr_b16 a[50:51], v194 offset:18944
v_mfma_f32_16x16x32_bf16 v[68:71], a[132:135], a[16:19], v[68:71]
ds_read_b64_tr_b16 a[52:53], v195 offset:17024
v_mfma_f32_16x16x32_bf16 v[72:75], a[136:139], a[16:19], v[72:75]
ds_read_b64_tr_b16 a[54:55], v195 offset:19072
v_mfma_f32_16x16x32_bf16 v[76:79], a[140:143], a[16:19], v[76:79]
buffer_load_dwordx4 v210, s[36:39], s33 offen offset:2048 lds
v_mfma_f32_16x16x32_bf16 v[80:83], a[128:131], a[20:23], v[80:83]
ds_read_b64_tr_b16 a[56:57], v194 offset:17152
v_mfma_f32_16x16x32_bf16 v[84:87], a[132:135], a[20:23], v[84:87]
ds_read_b64_tr_b16 a[58:59], v194 offset:19200
v_mfma_f32_16x16x32_bf16 v[88:91], a[136:139], a[20:23], v[88:91]
ds_read_b64_tr_b16 a[60:61], v195 offset:17280
v_mfma_f32_16x16x32_bf16 v[92:95], a[140:143], a[20:23], v[92:95]
ds_read_b64_tr_b16 a[62:63], v195 offset:19328
v_mfma_f32_16x16x32_bf16 v[96:99], a[128:131], a[24:27], v[96:99]
buffer_load_dwordx4 v211, s[36:39], s33 offen offset:3072 lds
v_mfma_f32_16x16x32_bf16 v[100:103], a[132:135], a[24:27], v[100:103]
s_add_u32 s24, s24, 1
s_and_b32 s24, s24, 7
v_mfma_f32_16x16x32_bf16 v[104:107], a[136:139], a[24:27], v[104:107]
s_lshl_b32 s32, s24, 7
s_add_u32 s31, s32, s60
v_mfma_f32_16x16x32_bf16 v[108:111], a[140:143], a[24:27], v[108:111]
s_add_u32 s32, s32, s96
s_lshl_b32 s33, s24, 19
v_mfma_f32_16x16x32_bf16 v[112:115], a[128:131], a[28:31], v[112:115]
s_add_u32 s33, s33, s97
s_add_u32 s61, s62, 12288
buffer_store_dwordx4 v[176:179], v252, s[40:43], s61 offen sc1
v_mfma_f32_16x16x32_bf16 v[116:119], a[132:135], a[28:31], v[116:119]
s_add_u32 s61, s62, 13312
buffer_store_dwordx4 v[180:183], v252, s[40:43], s61 offen sc1
v_mfma_f32_16x16x32_bf16 v[120:123], a[136:139], a[28:31], v[120:123]
v_mfma_f32_16x16x32_bf16 v[124:127], a[140:143], a[28:31], v[124:127]
s_waitcnt vmcnt(16) lgkmcnt(0)
s_barrier
s_nop 0
v_mfma_f32_16x16x32_bf16 v[0:3], a[144:147], a[32:35], v[0:3]
ds_read_b64_tr_b16 a[0:1], v198 offset:0
s_add_u32 m0, s56, 16384
v_mfma_f32_16x16x32_bf16 v[4:7], a[148:151], a[32:35], v[4:7]
buffer_load_dwordx4 v200, s[20:23], s31 offen offset:0 sc1 lds
v_mfma_f32_16x16x32_bf16 v[8:11], a[152:155], a[32:35], v[8:11]
ds_read_b64_tr_b16 a[2:3], v198 offset:1024
v_mfma_f32_16x16x32_bf16 v[12:15], a[156:159], a[32:35], v[12:15]
ds_read_b128 a[128:131], v196 offset:0
v_mfma_f32_16x16x32_bf16 v[16:19], a[144:147], a[36:39], v[16:19]
ds_read_b128 a[132:135], v196 offset:2048
v_mfma_f32_16x16x32_bf16 v[20:23], a[148:151], a[36:39], v[20:23]
ds_read_b128 a[136:139], v196 offset:4096
v_mfma_f32_16x16x32_bf16 v[24:27], a[152:155], a[36:39], v[24:27]
buffer_load_dwordx4 v201, s[20:23], s31 offen offset:1024 sc1 lds
v_cvt_pk_bf16_f32 v214, v0, v1
v_cvt_pk_bf16_f32 v215, v2, v3
v_cvt_pk_bf16_f32 v216, v4, v5
v_cvt_pk_bf16_f32 v217, v6, v7
v_mfma_f32_16x16x32_bf16 v[28:31], a[156:159], a[36:39], v[28:31]
ds_read_b128 a[140:143], v196 offset:6144
v_accvgpr_write_b32 a164, v214
v_accvgpr_write_b32 a165, v215
v_accvgpr_write_b32 a166, v216
v_accvgpr_write_b32 a167, v217
v_mfma_f32_16x16x32_bf16 v[32:35], a[144:147], a[40:43], v[32:35]
ds_read_b64_tr_b16 a[4:5], v199 offset:128
v_cvt_pk_bf16_f32 v218, v8, v9
v_cvt_pk_bf16_f32 v219, v10, v11
v_cvt_pk_bf16_f32 v220, v12, v13
v_cvt_pk_bf16_f32 v221, v14, v15
v_mfma_f32_16x16x32_bf16 v[36:39], a[148:151], a[40:43], v[36:39]
ds_read_b64_tr_b16 a[6:7], v199 offset:1152
v_accvgpr_write_b32 a196, v218
v_accvgpr_write_b32 a197, v219
v_accvgpr_write_b32 a198, v220
v_accvgpr_write_b32 a199, v221
v_mfma_f32_16x16x32_bf16 v[40:43], a[152:155], a[40:43], v[40:43]
ds_read_b64_tr_b16 a[8:9], v198 offset:256
v_cvt_pk_bf16_f32 v214, v16, v17
v_cvt_pk_bf16_f32 v215, v18, v19
v_cvt_pk_bf16_f32 v216, v20, v21
v_cvt_pk_bf16_f32 v217, v22, v23
v_mfma_f32_16x16x32_bf16 v[44:47], a[156:159], a[40:43], v[44:47]
buffer_load_dwordx4 v212, s[20:23], s31 offen offset:2048 sc1 lds
v_accvgpr_write_b32 a168, v214
v_accvgpr_write_b32 a169, v215
v_accvgpr_write_b32 a170, v216
v_accvgpr_write_b32 a171, v217
v_mfma_f32_16x16x32_bf16 v[48:51], a[144:147], a[44:47], v[48:51]
ds_read_b64_tr_b16 a[10:11], v198 offset:1280
v_cvt_pk_bf16_f32 v218, v24, v25
v_cvt_pk_bf16_f32 v219, v26, v27
v_cvt_pk_bf16_f32 v220, v28, v29
v_cvt_pk_bf16_f32 v221, v30, v31
v_mfma_f32_16x16x32_bf16 v[52:55], a[148:151], a[44:47], v[52:55]
ds_read_b64_tr_b16 a[12:13], v199 offset:384
v_accvgpr_write_b32 a200, v218
v_accvgpr_write_b32 a201, v219
v_accvgpr_write_b32 a202, v220
v_accvgpr_write_b32 a203, v221
v_mfma_f32_16x16x32_bf16 v[56:59], a[152:155], a[44:47], v[56:59]
ds_read_b64_tr_b16 a[14:15], v199 offset:1408
v_cvt_pk_bf16_f32 v214, v32, v33
v_cvt_pk_bf16_f32 v215, v34, v35
v_cvt_pk_bf16_f32 v216, v36, v37
v_cvt_pk_bf16_f32 v217, v38, v39
v_mfma_f32_16x16x32_bf16 v[60:63], a[156:159], a[44:47], v[60:63]
ds_read_b64_tr_b16 a[16:17], v198 offset:512
v_accvgpr_write_b32 a172, v214
v_accvgpr_write_b32 a173, v215
v_accvgpr_write_b32 a174, v216
v_accvgpr_write_b32 a175, v217
v_mfma_f32_16x16x32_bf16 v[64:67], a[144:147], a[48:51], v[64:67]
buffer_load_dwordx4 v213, s[20:23], s31 offen offset:3072 sc1 lds
v_cvt_pk_bf16_f32 v218, v40, v41
v_cvt_pk_bf16_f32 v219, v42, v43
v_cvt_pk_bf16_f32 v220, v44, v45
v_cvt_pk_bf16_f32 v221, v46, v47
v_mfma_f32_16x16x32_bf16 v[68:71], a[148:151], a[48:51], v[68:71]
ds_read_b64_tr_b16 a[18:19], v198 offset:1536
v_accvgpr_write_b32 a204, v218
v_accvgpr_write_b32 a205, v219
v_accvgpr_write_b32 a206, v220
v_accvgpr_write_b32 a207, v221
v_mfma_f32_16x16x32_bf16 v[72:75], a[152:155], a[48:51], v[72:75]
ds_read_b64_tr_b16 a[20:21], v199 offset:640
v_cvt_pk_bf16_f32 v214, v48, v49
v_cvt_pk_bf16_f32 v215, v50, v51
v_cvt_pk_bf16_f32 v216, v52, v53
v_cvt_pk_bf16_f32 v217, v54, v55
v_mfma_f32_16x16x32_bf16 v[76:79], a[156:159], a[48:51], v[76:79]
ds_read_b64_tr_b16 a[22:23], v199 offset:1664
v_accvgpr_write_b32 a176, v214
v_accvgpr_write_b32 a177, v215
v_accvgpr_write_b32 a178, v216
v_accvgpr_write_b32 a179, v217
v_mfma_f32_16x16x32_bf16 v[80:83], a[144:147], a[52:55], v[80:83]
ds_read_b64_tr_b16 a[24:25], v198 offset:768
s_add_u32 m0, s56, 20480
v_cvt_pk_bf16_f32 v218, v56, v57
v_cvt_pk_bf16_f32 v219, v58, v59
v_cvt_pk_bf16_f32 v220, v60, v61
v_cvt_pk_bf16_f32 v221, v62, v63
v_mfma_f32_16x16x32_bf16 v[84:87], a[148:151], a[52:55], v[84:87]
buffer_load_dwordx4 v200, s[20:23], s32 offen offset:0 sc1 lds
v_accvgpr_write_b32 a208, v218
v_accvgpr_write_b32 a209, v219
v_accvgpr_write_b32 a210, v220
v_accvgpr_write_b32 a211, v221
v_mfma_f32_16x16x32_bf16 v[88:91], a[152:155], a[52:55], v[88:91]
ds_read_b64_tr_b16 a[26:27], v198 offset:1792
v_cvt_pk_bf16_f32 v214, v64, v65
v_cvt_pk_bf16_f32 v215, v66, v67
v_cvt_pk_bf16_f32 v216, v68, v69
v_cvt_pk_bf16_f32 v217, v70, v71
v_mfma_f32_16x16x32_bf16 v[92:95], a[156:159], a[52:55], v[92:95]
ds_read_b64_tr_b16 a[28:29], v199 offset:896
v_accvgpr_write_b32 a180, v214
v_accvgpr_write_b32 a181, v215
v_accvgpr_write_b32 a182, v216
v_accvgpr_write_b32 a183, v217
v_mfma_f32_16x16x32_bf16 v[96:99], a[144:147], a[56:59], v[96:99]
ds_read_b64_tr_b16 a[30:31], v199 offset:1920
v_cvt_pk_bf16_f32 v218, v72, v73
v_cvt_pk_bf16_f32 v219, v74, v75
v_cvt_pk_bf16_f32 v220, v76, v77
v_cvt_pk_bf16_f32 v221, v78, v79
v_mfma_f32_16x16x32_bf16 v[100:103], a[148:151], a[56:59], v[100:103]
v_accvgpr_write_b32 a212, v218
v_accvgpr_write_b32 a213, v219
v_accvgpr_write_b32 a214, v220
v_accvgpr_write_b32 a215, v221
v_mfma_f32_16x16x32_bf16 v[104:107], a[152:155], a[56:59], v[104:107]
buffer_load_dwordx4 v201, s[20:23], s32 offen offset:1024 sc1 lds
v_cvt_pk_bf16_f32 v214, v80, v81
v_cvt_pk_bf16_f32 v215, v82, v83
v_cvt_pk_bf16_f32 v216, v84, v85
v_cvt_pk_bf16_f32 v217, v86, v87
v_mfma_f32_16x16x32_bf16 v[108:111], a[156:159], a[56:59], v[108:111]
v_accvgpr_write_b32 a184, v214
v_accvgpr_write_b32 a185, v215
v_accvgpr_write_b32 a186, v216
v_accvgpr_write_b32 a187, v217
v_mfma_f32_16x16x32_bf16 v[112:115], a[144:147], a[60:63], v[112:115]
v_cvt_pk_bf16_f32 v218, v88, v89
v_cvt_pk_bf16_f32 v219, v90, v91
v_cvt_pk_bf16_f32 v220, v92, v93
v_cvt_pk_bf16_f32 v221, v94, v95
v_mfma_f32_16x16x32_bf16 v[116:119], a[148:151], a[60:63], v[116:119]
v_accvgpr_write_b32 a216, v218
v_accvgpr_write_b32 a217, v219
v_accvgpr_write_b32 a218, v220
v_accvgpr_write_b32 a219, v221
v_mfma_f32_16x16x32_bf16 v[120:123], a[152:155], a[60:63], v[120:123]
v_cvt_pk_bf16_f32 v214, v96, v97
v_cvt_pk_bf16_f32 v215, v98, v99
v_cvt_pk_bf16_f32 v216, v100, v101
v_cvt_pk_bf16_f32 v217, v102, v103
v_mfma_f32_16x16x32_bf16 v[124:127], a[156:159], a[60:63], v[124:127]
buffer_load_dwordx4 v212, s[20:23], s32 offen offset:2048 sc1 lds
v_accvgpr_write_b32 a188, v214
v_accvgpr_write_b32 a189, v215
v_accvgpr_write_b32 a190, v216
v_accvgpr_write_b32 a191, v217
s_waitcnt lgkmcnt(0)
v_mfma_f32_16x16x32_bf16 v[0:3], a[0:3], a[128:131], 0
ds_read_b64_tr_b16 a[32:33], v198 offset:8192
v_cvt_pk_bf16_f32 v218, v104, v105
v_cvt_pk_bf16_f32 v219, v106, v107
v_cvt_pk_bf16_f32 v220, v108, v109
v_cvt_pk_bf16_f32 v221, v110, v111
v_mfma_f32_16x16x32_bf16 v[4:7], a[0:3], a[132:135], 0
ds_read_b64_tr_b16 a[34:35], v198 offset:9216
v_accvgpr_write_b32 a220, v218
v_accvgpr_write_b32 a221, v219
v_accvgpr_write_b32 a222, v220
v_accvgpr_write_b32 a223, v221
v_mfma_f32_16x16x32_bf16 v[8:11], a[0:3], a[136:139], 0
ds_read_b128 a[144:147], v197 offset:0
v_cvt_pk_bf16_f32 v214, v112, v113
v_cvt_pk_bf16_f32 v215, v114, v115
v_cvt_pk_bf16_f32 v216, v116, v117
v_cvt_pk_bf16_f32 v217, v118, v119
v_mfma_f32_16x16x32_bf16 v[12:15], a[0:3], a[140:143], 0
ds_read_b128 a[148:151], v197 offset:2048
v_accvgpr_write_b32 a192, v214
v_accvgpr_write_b32 a193, v215
v_accvgpr_write_b32 a194, v216
v_accvgpr_write_b32 a195, v217
v_mfma_f32_16x16x32_bf16 v[16:19], a[4:7], a[128:131], 0
buffer_load_dwordx4 v213, s[20:23], s32 offen offset:3072 sc1 lds
v_cvt_pk_bf16_f32 v218, v120, v121
v_cvt_pk_bf16_f32 v219, v122, v123
v_cvt_pk_bf16_f32 v220, v124, v125
v_cvt_pk_bf16_f32 v221, v126, v127
v_mfma_f32_16x16x32_bf16 v[20:23], a[4:7], a[132:135], 0
ds_read_b128 a[152:155], v197 offset:4096
v_accvgpr_write_b32 a224, v218
v_accvgpr_write_b32 a225, v219
v_accvgpr_write_b32 a226, v220
v_accvgpr_write_b32 a227, v221
v_mfma_f32_16x16x32_bf16 v[24:27], a[4:7], a[136:139], 0
ds_read_b128 a[156:159], v197 offset:6144
v_mfma_f32_16x16x32_bf16 v[28:31], a[4:7], a[140:143], 0
ds_read_b64_tr_b16 a[36:37], v199 offset:8320
v_mfma_f32_16x16x32_bf16 v[32:35], a[8:11], a[128:131], 0
ds_read_b64_tr_b16 a[38:39], v199 offset:9344
s_add_u32 m0, s57, 16384
v_mfma_f32_16x16x32_bf16 v[36:39], a[8:11], a[132:135], 0
buffer_load_dwordx4 v208, s[36:39], s33 offen offset:0 lds
v_mfma_f32_16x16x32_bf16 v[40:43], a[8:11], a[136:139], 0
ds_read_b64_tr_b16 a[40:41], v198 offset:8448
v_mfma_f32_16x16x32_bf16 v[44:47], a[8:11], a[140:143], 0
ds_read_b64_tr_b16 a[42:43], v198 offset:9472
v_mfma_f32_16x16x32_bf16 v[48:51], a[12:15], a[128:131], 0
ds_read_b64_tr_b16 a[44:45], v199 offset:8576
v_mfma_f32_16x16x32_bf16 v[52:55], a[12:15], a[132:135], 0
ds_read_b64_tr_b16 a[46:47], v199 offset:9600
v_mfma_f32_16x16x32_bf16 v[56:59], a[12:15], a[136:139], 0
buffer_load_dwordx4 v209, s[36:39], s33 offen offset:1024 lds
v_mfma_f32_16x16x32_bf16 v[60:63], a[12:15], a[140:143], 0
ds_read_b64_tr_b16 a[48:49], v198 offset:8704
v_mfma_f32_16x16x32_bf16 v[64:67], a[16:19], a[128:131], 0
ds_read_b64_tr_b16 a[50:51], v198 offset:9728
v_mfma_f32_16x16x32_bf16 v[68:71], a[16:19], a[132:135], 0
ds_read_b64_tr_b16 a[52:53], v199 offset:8832
v_mfma_f32_16x16x32_bf16 v[72:75], a[16:19], a[136:139], 0
ds_read_b64_tr_b16 a[54:55], v199 offset:9856
v_mfma_f32_16x16x32_bf16 v[76:79], a[16:19], a[140:143], 0
buffer_load_dwordx4 v210, s[36:39], s33 offen offset:2048 lds
v_mfma_f32_16x16x32_bf16 v[80:83], a[20:23], a[128:131], 0
ds_read_b64_tr_b16 a[56:57], v198 offset:8960
v_mfma_f32_16x16x32_bf16 v[84:87], a[20:23], a[132:135], 0
ds_read_b64_tr_b16 a[58:59], v198 offset:9984
v_mfma_f32_16x16x32_bf16 v[88:91], a[20:23], a[136:139], 0
ds_read_b64_tr_b16 a[60:61], v199 offset:9088
v_mfma_f32_16x16x32_bf16 v[92:95], a[20:23], a[140:143], 0
ds_read_b64_tr_b16 a[62:63], v199 offset:10112
v_mfma_f32_16x16x32_bf16 v[96:99], a[24:27], a[128:131], 0
buffer_load_dwordx4 v211, s[36:39], s33 offen offset:3072 lds
v_mfma_f32_16x16x32_bf16 v[100:103], a[24:27], a[132:135], 0
s_add_u32 s24, s24, 1
s_and_b32 s24, s24, 7
v_mfma_f32_16x16x32_bf16 v[104:107], a[24:27], a[136:139], 0
s_lshl_b32 s32, s24, 7
s_add_u32 s31, s32, s60
v_mfma_f32_16x16x32_bf16 v[108:111], a[24:27], a[140:143], 0
s_add_u32 s32, s32, s96
s_lshl_b32 s33, s24, 19
v_mfma_f32_16x16x32_bf16 v[112:115], a[28:31], a[128:131], 0
s_add_u32 s33, s33, s97
s_add_u32 s61, s62, 14336
buffer_store_dwordx4 v[184:187], v252, s[40:43], s61 offen sc1
v_mfma_f32_16x16x32_bf16 v[116:119], a[28:31], a[132:135], 0
s_add_u32 s61, s62, 15360
buffer_store_dwordx4 v[188:191], v252, s[40:43], s61 offen sc1
v_mfma_f32_16x16x32_bf16 v[120:123], a[28:31], a[136:139], 0
v_mfma_f32_16x16x32_bf16 v[124:127], a[28:31], a[140:143], 0
G3_END:
s_waitcnt vmcnt(16) lgkmcnt(0)
s_barrier
s_add_u32 s8, s82, 0x400000
s_addc_u32 s9, s83, 0
s_and_b32 s9, s9, 0xffff
s_mov_b32 s10, 0x2000000
s_mov_b32 s11, 0x20000
s_add_u32 s63, s62, 524288
v_add_u32 v196, 0xc000, v196
v_add_u32 v197, 0xc000, v197
v_add_u32 v198, 0xc000, v198
v_add_u32 v199, 0xc000, v199
s_nop 0
v_mfma_f32_16x16x32_bf16 v[0:3], a[32:35], a[144:147], v[0:3]
ds_read_b64_tr_b16 a[0:1], v198 offset:0
s_add_u32 m0, s56, 65536
v_mfma_f32_16x16x32_bf16 v[4:7], a[32:35], a[148:151], v[4:7]
buffer_load_dwordx4 v200, s[20:23], s31 offen offset:0 sc1 lds
v_mfma_f32_16x16x32_bf16 v[8:11], a[32:35], a[152:155], v[8:11]
ds_read_b64_tr_b16 a[2:3], v198 offset:1024
v_mfma_f32_16x16x32_bf16 v[12:15], a[32:35], a[156:159], v[12:15]
ds_read_b128 a[128:131], v196 offset:0
v_mfma_f32_16x16x32_bf16 v[16:19], a[36:39], a[144:147], v[16:19]
ds_read_b128 a[132:135], v196 offset:2048
v_mfma_f32_16x16x32_bf16 v[20:23], a[36:39], a[148:151], v[20:23]
ds_read_b128 a[136:139], v196 offset:4096
v_mfma_f32_16x16x32_bf16 v[24:27], a[36:39], a[152:155], v[24:27]
buffer_load_dwordx4 v201, s[20:23], s31 offen offset:1024 sc1 lds
v_mfma_f32_16x16x32_bf16 v[28:31], a[36:39], a[156:159], v[28:31]
ds_read_b128 a[140:143], v196 offset:6144
v_mfma_f32_16x16x32_bf16 v[32:35], a[40:43], a[144:147], v[32:35]
ds_read_b64_tr_b16 a[4:5], v199 offset:128
v_mfma_f32_16x16x32_bf16 v[36:39], a[40:43], a[148:151], v[36:39]
ds_read_b64_tr_b16 a[6:7], v199 offset:1152
v_mfma_f32_16x16x32_bf16 v[40:43], a[40:43], a[152:155], v[40:43]
ds_read_b64_tr_b16 a[8:9], v198 offset:256
v_mfma_f32_16x16x32_bf16 v[44:47], a[40:43], a[156:159], v[44:47]
buffer_load_dwordx4 v212, s[20:23], s31 offen offset:2048 sc1 lds
v_mfma_f32_16x16x32_bf16 v[48:51], a[44:47], a[144:147], v[48:51]
ds_read_b64_tr_b16 a[10:11], v198 offset:1280
v_mfma_f32_16x16x32_bf16 v[52:55], a[44:47], a[148:151], v[52:55]
ds_read_b64_tr_b16 a[12:13], v199 offset:384
v_mfma_f32_16x16x32_bf16 v[56:59], a[44:47], a[152:155], v[56:59]
ds_read_b64_tr_b16 a[14:15], v199 offset:1408
v_mfma_f32_16x16x32_bf16 v[60:63], a[44:47], a[156:159], v[60:63]
ds_read_b64_tr_b16 a[16:17], v198 offset:512
v_mfma_f32_16x16x32_bf16 v[64:67], a[48:51], a[144:147], v[64:67]
buffer_load_dwordx4 v213, s[20:23], s31 offen offset:3072 sc1 lds
v_mfma_f32_16x16x32_bf16 v[68:71], a[48:51], a[148:151], v[68:71]
ds_read_b64_tr_b16 a[18:19], v198 offset:1536
v_mfma_f32_16x16x32_bf16 v[72:75], a[48:51], a[152:155], v[72:75]
ds_read_b64_tr_b16 a[20:21], v199 offset:640
v_mfma_f32_16x16x32_bf16 v[76:79], a[48:51], a[156:159], v[76:79]
ds_read_b64_tr_b16 a[22:23], v199 offset:1664
v_mfma_f32_16x16x32_bf16 v[80:83], a[52:55], a[144:147], v[80:83]
ds_read_b64_tr_b16 a[24:25], v198 offset:768
s_add_u32 m0, s56, 69632
v_mfma_f32_16x16x32_bf16 v[84:87], a[52:55], a[148:151], v[84:87]
buffer_load_dwordx4 v200, s[20:23], s32 offen offset:0 sc1 lds
v_mfma_f32_16x16x32_bf16 v[88:91], a[52:55], a[152:155], v[88:91]
ds_read_b64_tr_b16 a[26:27], v198 offset:1792
v_mfma_f32_16x16x32_bf16 v[92:95], a[52:55], a[156:159], v[92:95]
ds_read_b64_tr_b16 a[28:29], v199 offset:896
v_mfma_f32_16x16x32_bf16 v[96:99], a[56:59], a[144:147], v[96:99]
ds_read_b64_tr_b16 a[30:31], v199 offset:1920
v_mfma_f32_16x16x32_bf16 v[100:103], a[56:59], a[148:151], v[100:103]
v_mfma_f32_16x16x32_bf16 v[104:107], a[56:59], a[152:155], v[104:107]
buffer_load_dwordx4 v201, s[20:23], s32 offen offset:1024 sc1 lds
v_mfma_f32_16x16x32_bf16 v[108:111], a[56:59], a[156:159], v[108:111]
v_mfma_f32_16x16x32_bf16 v[112:115], a[60:63], a[144:147], v[112:115]
v_mfma_f32_16x16x32_bf16 v[116:119], a[60:63], a[148:151], v[116:119]
v_mfma_f32_16x16x32_bf16 v[120:123], a[60:63], a[152:155], v[120:123]
v_mfma_f32_16x16x32_bf16 v[124:127], a[60:63], a[156:159], v[124:127]
buffer_load_dwordx4 v212, s[20:23], s32 offen offset:2048 sc1 lds
s_waitcnt lgkmcnt(0)
v_mfma_f32_16x16x32_bf16 v[0:3], a[0:3], a[128:131], v[0:3]
ds_read_b64_tr_b16 a[32:33], v198 offset:8192
v_mfma_f32_16x16x32_bf16 v[4:7], a[0:3], a[132:135], v[4:7]
ds_read_b64_tr_b16 a[34:35], v198 offset:9216
v_mfma_f32_16x16x32_bf16 v[8:11], a[0:3], a[136:139], v[8:11]
ds_read_b128 a[144:147], v197 offset:0
v_mfma_f32_16x16x32_bf16 v[12:15], a[0:3], a[140:143], v[12:15]
ds_read_b128 a[148:151], v197 offset:2048
v_mfma_f32_16x16x32_bf16 v[16:19], a[4:7], a[128:131], v[16:19]
buffer_load_dwordx4 v213, s[20:23], s32 offen offset:3072 sc1 lds
v_mfma_f32_16x16x32_bf16 v[20:23], a[4:7], a[132:135], v[20:23]
ds_read_b128 a[152:155], v197 offset:4096
v_mfma_f32_16x16x32_bf16 v[24:27], a[4:7], a[136:139], v[24:27]
ds_read_b128 a[156:159], v197 offset:6144
v_mfma_f32_16x16x32_bf16 v[28:31], a[4:7], a[140:143], v[28:31]
ds_read_b64_tr_b16 a[36:37], v199 offset:8320
v_mfma_f32_16x16x32_bf16 v[32:35], a[8:11], a[128:131], v[32:35]
ds_read_b64_tr_b16 a[38:39], v199 offset:9344
s_add_u32 m0, s57, 65536
v_mfma_f32_16x16x32_bf16 v[36:39], a[8:11], a[132:135], v[36:39]
buffer_load_dwordx4 v208, s[36:39], s33 offen offset:0 lds
v_mfma_f32_16x16x32_bf16 v[40:43], a[8:11], a[136:139], v[40:43]
ds_read_b64_tr_b16 a[40:41], v198 offset:8448
v_mfma_f32_16x16x32_bf16 v[44:47], a[8:11], a[140:143], v[44:47]
ds_read_b64_tr_b16 a[42:43], v198 offset:9472
v_mfma_f32_16x16x32_bf16 v[48:51], a[12:15], a[128:131], v[48:51]
ds_read_b64_tr_b16 a[44:45], v199 offset:8576
v_mfma_f32_16x16x32_bf16 v[52:55], a[12:15], a[132:135], v[52:55]
ds_read_b64_tr_b16 a[46:47], v199 offset:9600
v_mfma_f32_16x16x32_bf16 v[56:59], a[12:15], a[136:139], v[56:59]
buffer_load_dwordx4 v209, s[36:39], s33 offen offset:1024 lds
v_mfma_f32_16x16x32_bf16 v[60:63], a[12:15], a[140:143], v[60:63]
ds_read_b64_tr_b16 a[48:49], v198 offset:8704
v_mfma_f32_16x16x32_bf16 v[64:67], a[16:19], a[128:131], v[64:67]
ds_read_b64_tr_b16 a[50:51], v198 offset:9728
v_mfma_f32_16x16x32_bf16 v[68:71], a[16:19], a[132:135], v[68:71]
ds_read_b64_tr_b16 a[52:53], v199 offset:8832
v_mfma_f32_16x16x32_bf16 v[72:75], a[16:19], a[136:139], v[72:75]
ds_read_b64_tr_b16 a[54:55], v199 offset:9856
v_mfma_f32_16x16x32_bf16 v[76:79], a[16:19], a[140:143], v[76:79]
buffer_load_dwordx4 v210, s[36:39], s33 offen offset:2048 lds
v_mfma_f32_16x16x32_bf16 v[80:83], a[20:23], a[128:131], v[80:83]
ds_read_b64_tr_b16 a[56:57], v198 offset:8960
v_mfma_f32_16x16x32_bf16 v[84:87], a[20:23], a[132:135], v[84:87]
ds_read_b64_tr_b16 a[58:59], v198 offset:9984
v_mfma_f32_16x16x32_bf16 v[88:91], a[20:23], a[136:139], v[88:91]
ds_read_b64_tr_b16 a[60:61], v199 offset:9088
v_mfma_f32_16x16x32_bf16 v[92:95], a[20:23], a[140:143], v[92:95]
ds_read_b64_tr_b16 a[62:63], v199 offset:10112
v_mfma_f32_16x16x32_bf16 v[96:99], a[24:27], a[128:131], v[96:99]
buffer_load_dwordx4 v211, s[36:39], s33 offen offset:3072 lds
v_mfma_f32_16x16x32_bf16 v[100:103], a[24:27], a[132:135], v[100:103]
s_add_u32 s24, s24, 1
s_and_b32 s24, s24, 7
v_mfma_f32_16x16x32_bf16 v[104:107], a[24:27], a[136:139], v[104:107]
s_lshl_b32 s32, s24, 7
s_add_u32 s31, s32, s60
v_mfma_f32_16x16x32_bf16 v[108:111], a[24:27], a[140:143], v[108:111]
s_add_u32 s32, s32, s96
s_lshl_b32 s33, s24, 19
v_mfma_f32_16x16x32_bf16 v[112:115], a[28:31], a[128:131], v[112:115]
s_add_u32 s33, s33, s97
s_add_u32 s61, s63, 0
buffer_store_dwordx4 a[164:167], v252, s[8:11], s61 offen sc1
v_mfma_f32_16x16x32_bf16 v[116:119], a[28:31], a[132:135], v[116:119]
s_add_u32 s61, s63, 1024
buffer_store_dwordx4 a[168:171], v252, s[8:11], s61 offen sc1
v_mfma_f32_16x16x32_bf16 v[120:123], a[28:31], a[136:139], v[120:123]
v_mfma_f32_16x16x32_bf16 v[124:127], a[28:31], a[140:143], v[124:127]
s_waitcnt vmcnt(16) lgkmcnt(0)
s_barrier
v_add_u32 v196, 0xfffe8000, v196
v_add_u32 v197, 0xfffe8000, v197
v_add_u32 v198, 0xfffe8000, v198
v_add_u32 v199, 0xfffe8000, v199
s_nop 0
v_mfma_f32_16x16x32_bf16 v[0:3], a[32:35], a[144:147], v[0:3]
ds_read_b64_tr_b16 a[0:1], v198 offset:0
s_add_u32 m0, s56, 114688
v_mfma_f32_16x16x32_bf16 v[4:7], a[32:35], a[148:151], v[4:7]
buffer_load_dwordx4 v200, s[20:23], s31 offen offset:0 sc1 lds
v_mfma_f32_16x16x32_bf16 v[8:11], a[32:35], a[152:155], v[8:11]
ds_read_b64_tr_b16 a[2:3], v198 offset:1024
v_mfma_f32_16x16x32_bf16 v[12:15], a[32:35], a[156:159], v[12:15]
ds_read_b128 a[128:131], v196 offset:0
v_mfma_f32_16x16x32_bf16 v[16:19], a[36:39], a[144:147], v[16:19]
ds_read_b128 a[132:135], v196 offset:2048
v_mfma_f32_16x16x32_bf16 v[20:23], a[36:39], a[148:151], v[20:23]
ds_read_b128 a[136:139], v196 offset:4096
v_mfma_f32_16x16x32_bf16 v[24:27], a[36:39], a[152:155], v[24:27]
buffer_load_dwordx4 v201, s[20:23], s31 offen offset:1024 sc1 lds
v_mfma_f32_16x16x32_bf16 v[28:31], a[36:39], a[156:159], v[28:31]
ds_read_b128 a[140:143], v196 offset:6144
v_mfma_f32_16x16x32_bf16 v[32:35], a[40:43], a[144:147], v[32:35]
ds_read_b64_tr_b16 a[4:5], v199 offset:128
v_mfma_f32_16x16x32_bf16 v[36:39], a[40:43], a[148:151], v[36:39]
ds_read_b64_tr_b16 a[6:7], v199 offset:1152
v_mfma_f32_16x16x32_bf16 v[40:43], a[40:43], a[152:155], v[40:43]
ds_read_b64_tr_b16 a[8:9], v198 offset:256
v_mfma_f32_16x16x32_bf16 v[44:47], a[40:43], a[156:159], v[44:47]
buffer_load_dwordx4 v212, s[20:23], s31 offen offset:2048 sc1 lds
v_mfma_f32_16x16x32_bf16 v[48:51], a[44:47], a[144:147], v[48:51]
ds_read_b64_tr_b16 a[10:11], v198 offset:1280
v_mfma_f32_16x16x32_bf16 v[52:55], a[44:47], a[148:151], v[52:55]
ds_read_b64_tr_b16 a[12:13], v199 offset:384
v_mfma_f32_16x16x32_bf16 v[56:59], a[44:47], a[152:155], v[56:59]
ds_read_b64_tr_b16 a[14:15], v199 offset:1408
v_mfma_f32_16x16x32_bf16 v[60:63], a[44:47], a[156:159], v[60:63]
ds_read_b64_tr_b16 a[16:17], v198 offset:512
v_mfma_f32_16x16x32_bf16 v[64:67], a[48:51], a[144:147], v[64:67]
buffer_load_dwordx4 v213, s[20:23], s31 offen offset:3072 sc1 lds
v_mfma_f32_16x16x32_bf16 v[68:71], a[48:51], a[148:151], v[68:71]
ds_read_b64_tr_b16 a[18:19], v198 offset:1536
v_mfma_f32_16x16x32_bf16 v[72:75], a[48:51], a[152:155], v[72:75]
ds_read_b64_tr_b16 a[20:21], v199 offset:640
v_mfma_f32_16x16x32_bf16 v[76:79], a[48:51], a[156:159], v[76:79]
ds_read_b64_tr_b16 a[22:23], v199 offset:1664
v_mfma_f32_16x16x32_bf16 v[80:83], a[52:55], a[144:147], v[80:83]
ds_read_b64_tr_b16 a[24:25], v198 offset:768
s_add_u32 m0, s56, 118784
v_mfma_f32_16x16x32_bf16 v[84:87], a[52:55], a[148:151], v[84:87]
buffer_load_dwordx4 v200, s[20:23], s32 offen offset:0 sc1 lds
v_mfma_f32_16x16x32_bf16 v[88:91], a[52:55], a[152:155], v[88:91]
ds_read_b64_tr_b16 a[26:27], v198 offset:1792
v_mfma_f32_16x16x32_bf16 v[92:95], a[52:55], a[156:159], v[92:95]
ds_read_b64_tr_b16 a[28:29], v199 offset:896
v_mfma_f32_16x16x32_bf16 v[96:99], a[56:59], a[144:147], v[96:99]
ds_read_b64_tr_b16 a[30:31], v199 offset:1920
v_mfma_f32_16x16x32_bf16 v[100:103], a[56:59], a[148:151], v[100:103]
v_mfma_f32_16x16x32_bf16 v[104:107], a[56:59], a[152:155], v[104:107]
buffer_load_dwordx4 v201, s[20:23], s32 offen offset:1024 sc1 lds
v_mfma_f32_16x16x32_bf16 v[108:111], a[56:59], a[156:159], v[108:111]
v_mfma_f32_16x16x32_bf16 v[112:115], a[60:63], a[144:147], v[112:115]
v_mfma_f32_16x16x32_bf16 v[116:119], a[60:63], a[148:151], v[116:119]
v_mfma_f32_16x16x32_bf16 v[120:123], a[60:63], a[152:155], v[120:123]
v_mfma_f32_16x16x32_bf16 v[124:127], a[60:63], a[156:159], v[124:127]
buffer_load_dwordx4 v212, s[20:23], s32 offen offset:2048 sc1 lds
s_waitcnt lgkmcnt(0)
v_mfma_f32_16x16x32_bf16 v[0:3], a[0:3], a[128:131], v[0:3]
ds_read_b64_tr_b16 a[32:33], v198 offset:8192
v_mfma_f32_16x16x32_bf16 v[4:7], a[0:3], a[132:135], v[4:7]
ds_read_b64_tr_b16 a[34:35], v198 offset:9216
v_mfma_f32_16x16x32_bf16 v[8:11], a[0:3], a[136:139], v[8:11]
ds_read_b128 a[144:147], v197 offset:0
v_mfma_f32_16x16x32_bf16 v[12:15], a[0:3], a[140:143], v[12:15]
ds_read_b128 a[148:151], v197 offset:2048
v_mfma_f32_16x16x32_bf16 v[16:19], a[4:7], a[128:131], v[16:19]
buffer_load_dwordx4 v213, s[20:23], s32 offen offset:3072 sc1 lds
v_mfma_f32_16x16x32_bf16 v[20:23], a[4:7], a[132:135], v[20:23]
ds_read_b128 a[152:155], v197 offset:4096
v_mfma_f32_16x16x32_bf16 v[24:27], a[4:7], a[136:139], v[24:27]
ds_read_b128 a[156:159], v197 offset:6144
v_mfma_f32_16x16x32_bf16 v[28:31], a[4:7], a[140:143], v[28:31]
ds_read_b64_tr_b16 a[36:37], v199 offset:8320
v_mfma_f32_16x16x32_bf16 v[32:35], a[8:11], a[128:131], v[32:35]
ds_read_b64_tr_b16 a[38:39], v199 offset:9344
s_add_u32 m0, s57, 114688
v_mfma_f32_16x16x32_bf16 v[36:39], a[8:11], a[132:135], v[36:39]
buffer_load_dwordx4 v208, s[36:39], s33 offen offset:0 lds
v_mfma_f32_16x16x32_bf16 v[40:43], a[8:11], a[136:139], v[40:43]
ds_read_b64_tr_b16 a[40:41], v198 offset:8448
v_mfma_f32_16x16x32_bf16 v[44:47], a[8:11], a[140:143], v[44:47]
ds_read_b64_tr_b16 a[42:43], v198 offset:9472
v_mfma_f32_16x16x32_bf16 v[48:51], a[12:15], a[128:131], v[48:51]
ds_read_b64_tr_b16 a[44:45], v199 offset:8576
v_mfma_f32_16x16x32_bf16 v[52:55], a[12:15], a[132:135], v[52:55]
ds_read_b64_tr_b16 a[46:47], v199 offset:9600
v_mfma_f32_16x16x32_bf16 v[56:59], a[12:15], a[136:139], v[56:59]
buffer_load_dwordx4 v209, s[36:39], s33 offen offset:1024 lds
v_mfma_f32_16x16x32_bf16 v[60:63], a[12:15], a[140:143], v[60:63]
ds_read_b64_tr_b16 a[48:49], v198 offset:8704
v_mfma_f32_16x16x32_bf16 v[64:67], a[16:19], a[128:131], v[64:67]
ds_read_b64_tr_b16 a[50:51], v198 offset:9728
v_mfma_f32_16x16x32_bf16 v[68:71], a[16:19], a[132:135], v[68:71]
ds_read_b64_tr_b16 a[52:53], v199 offset:8832
v_mfma_f32_16x16x32_bf16 v[72:75], a[16:19], a[136:139], v[72:75]
ds_read_b64_tr_b16 a[54:55], v199 offset:9856
v_mfma_f32_16x16x32_bf16 v[76:79], a[16:19], a[140:143], v[76:79]
buffer_load_dwordx4 v210, s[36:39], s33 offen offset:2048 lds
v_mfma_f32_16x16x32_bf16 v[80:83], a[20:23], a[128:131], v[80:83]
ds_read_b64_tr_b16 a[56:57], v198 offset:8960
v_mfma_f32_16x16x32_bf16 v[84:87], a[20:23], a[132:135], v[84:87]
ds_read_b64_tr_b16 a[58:59], v198 offset:9984
v_mfma_f32_16x16x32_bf16 v[88:91], a[20:23], a[136:139], v[88:91]
ds_read_b64_tr_b16 a[60:61], v199 offset:9088
v_mfma_f32_16x16x32_bf16 v[92:95], a[20:23], a[140:143], v[92:95]
ds_read_b64_tr_b16 a[62:63], v199 offset:10112
v_mfma_f32_16x16x32_bf16 v[96:99], a[24:27], a[128:131], v[96:99]
buffer_load_dwordx4 v211, s[36:39], s33 offen offset:3072 lds
v_mfma_f32_16x16x32_bf16 v[100:103], a[24:27], a[132:135], v[100:103]
s_add_u32 s24, s24, 1
s_and_b32 s24, s24, 7
v_mfma_f32_16x16x32_bf16 v[104:107], a[24:27], a[136:139], v[104:107]
s_lshl_b32 s32, s24, 7
s_add_u32 s31, s32, s60
v_mfma_f32_16x16x32_bf16 v[108:111], a[24:27], a[140:143], v[108:111]
s_add_u32 s32, s32, s96
s_lshl_b32 s33, s24, 19
v_mfma_f32_16x16x32_bf16 v[112:115], a[28:31], a[128:131], v[112:115]
s_add_u32 s33, s33, s97
s_add_u32 s61, s63, 2048
buffer_store_dwordx4 a[172:175], v252, s[8:11], s61 offen sc1
v_mfma_f32_16x16x32_bf16 v[116:119], a[28:31], a[132:135], v[116:119]
s_add_u32 s61, s63, 3072
buffer_store_dwordx4 a[176:179], v252, s[8:11], s61 offen sc1
v_mfma_f32_16x16x32_bf16 v[120:123], a[28:31], a[136:139], v[120:123]
v_mfma_f32_16x16x32_bf16 v[124:127], a[28:31], a[140:143], v[124:127]
s_waitcnt vmcnt(16) lgkmcnt(0)
s_barrier
s_nop 0
s_lshl_b32 s46, s26, 6
s_lshl_b32 s59, s27, 3
s_add_u32 s46, s46, s59
s_add_u32 s46, s46, 8192
s_cmp_eq_u32 s7, 0
s_cbranch_scc0 Lpkv_6
v_mov_b32 v254, s84
v_accvgpr_write_b32 a250, v254
v_mov_b32 v254, s85
v_accvgpr_write_b32 a251, v254
v_mov_b32 v254, 0
s_mov_b64 exec, 1
s_nop 1
buffer_store_dwordx2 a[250:251], v254, s[88:91], s46 offen sc1
s_nop 0
s_mov_b64 exec, -1
Lpkv_6:
s_getpc_b64 s[48:49]
Lpc_7:
s_and_b32 s49, s49, 0xffff
s_mov_b32 s50, KEND-Lpc_7
s_mov_b32 s51, 0x20000
v_lshlrev_b32 v254, 7, v253
s_lshl_b32 s59, s7, 13
s_add_u32 s59, s59, K2_BLOCK-Lpc_7
buffer_load_dword a252, v254, s[48:51], s59 offen
v_add_u32 v196, 0xc000, v196
v_add_u32 v197, 0xc000, v197
v_add_u32 v198, 0xc000, v198
v_add_u32 v199, 0xc000, v199
s_nop 0
v_mfma_f32_16x16x32_bf16 v[0:3], a[32:35], a[144:147], v[0:3]
ds_read_b64_tr_b16 a[0:1], v198 offset:0
s_add_u32 m0, s56, 16384
v_mfma_f32_16x16x32_bf16 v[4:7], a[32:35], a[148:151], v[4:7]
buffer_load_dwordx4 v200, s[20:23], s31 offen offset:0 sc1 lds
v_mfma_f32_16x16x32_bf16 v[8:11], a[32:35], a[152:155], v[8:11]
ds_read_b64_tr_b16 a[2:3], v198 offset:1024
v_mfma_f32_16x16x32_bf16 v[12:15], a[32:35], a[156:159], v[12:15]
ds_read_b128 a[128:131], v196 offset:0
v_mfma_f32_16x16x32_bf16 v[16:19], a[36:39], a[144:147], v[16:19]
ds_read_b128 a[132:135], v196 offset:2048
v_mfma_f32_16x16x32_bf16 v[20:23], a[36:39], a[148:151], v[20:23]
ds_read_b128 a[136:139], v196 offset:4096
v_mfma_f32_16x16x32_bf16 v[24:27], a[36:39], a[152:155], v[24:27]
buffer_load_dwordx4 v201, s[20:23], s31 offen offset:1024 sc1 lds
v_mfma_f32_16x16x32_bf16 v[28:31], a[36:39], a[156:159], v[28:31]
ds_read_b128 a[140:143], v196 offset:6144
v_mfma_f32_16x16x32_bf16 v[32:35], a[40:43], a[144:147], v[32:35]
ds_read_b64_tr_b16 a[4:5], v199 offset:128
v_mfma_f32_16x16x32_bf16 v[36:39], a[40:43], a[148:151], v[36:39]
ds_read_b64_tr_b16 a[6:7], v199 offset:1152
v_mfma_f32_16x16x32_bf16 v[40:43], a[40:43], a[152:155], v[40:43]
ds_read_b64_tr_b16 a[8:9], v198 offset:256
v_mfma_f32_16x16x32_bf16 v[44:47], a[40:43], a[156:159], v[44:47]
buffer_load_dwordx4 v212, s[20:23], s31 offen offset:2048 sc1 lds
v_mfma_f32_16x16x32_bf16 v[48:51], a[44:47], a[144:147], v[48:51]
ds_read_b64_tr_b16 a[10:11], v198 offset:1280
v_mfma_f32_16x16x32_bf16 v[52:55], a[44:47], a[148:151], v[52:55]
ds_read_b64_tr_b16 a[12:13], v199 offset:384
v_mfma_f32_16x16x32_bf16 v[56:59], a[44:47], a[152:155], v[56:59]
ds_read_b64_tr_b16 a[14:15], v199 offset:1408
v_mfma_f32_16x16x32_bf16 v[60:63], a[44:47], a[156:159], v[60:63]
ds_read_b64_tr_b16 a[16:17], v198 offset:512
v_mfma_f32_16x16x32_bf16 v[64:67], a[48:51], a[144:147], v[64:67]
buffer_load_dwordx4 v213, s[20:23], s31 offen offset:3072 sc1 lds
v_mfma_f32_16x16x32_bf16 v[68:71], a[48:51], a[148:151], v[68:71]
ds_read_b64_tr_b16 a[18:19], v198 offset:1536
v_mfma_f32_16x16x32_bf16 v[72:75], a[48:51], a[152:155], v[72:75]
ds_read_b64_tr_b16 a[20:21], v199 offset:640
v_mfma_f32_16x16x32_bf16 v[76:79], a[48:51], a[156:159], v[76:79]
ds_read_b64_tr_b16 a[22:23], v199 offset:1664
v_mfma_f32_16x16x32_bf16 v[80:83], a[52:55], a[144:147], v[80:83]
ds_read_b64_tr_b16 a[24:25], v198 offset:768
s_add_u32 m0, s56, 20480
v_mfma_f32_16x16x32_bf16 v[84:87], a[52:55], a[148:151], v[84:87]
buffer_load_dwordx4 v200, s[20:23], s32 offen offset:0 sc1 lds
v_mfma_f32_16x16x32_bf16 v[88:91], a[52:55], a[152:155], v[88:91]
ds_read_b64_tr_b16 a[26:27], v198 offset:1792
v_mfma_f32_16x16x32_bf16 v[92:95], a[52:55], a[156:159], v[92:95]
ds_read_b64_tr_b16 a[28:29], v199 offset:896
v_mfma_f32_16x16x32_bf16 v[96:99], a[56:59], a[144:147], v[96:99]
ds_read_b64_tr_b16 a[30:31], v199 offset:1920
v_mfma_f32_16x16x32_bf16 v[100:103], a[56:59], a[148:151], v[100:103]
v_mfma_f32_16x16x32_bf16 v[104:107], a[56:59], a[152:155], v[104:107]
buffer_load_dwordx4 v201, s[20:23], s32 offen offset:1024 sc1 lds
v_mfma_f32_16x16x32_bf16 v[108:111], a[56:59], a[156:159], v[108:111]
v_mfma_f32_16x16x32_bf16 v[112:115], a[60:63], a[144:147], v[112:115]
v_mfma_f32_16x16x32_bf16 v[116:119], a[60:63], a[148:151], v[116:119]
v_mfma_f32_16x16x32_bf16 v[120:123], a[60:63], a[152:155], v[120:123]
v_mfma_f32_16x16x32_bf16 v[124:127], a[60:63], a[156:159], v[124:127]
buffer_load_dwordx4 v212, s[20:23], s32 offen offset:2048 sc1 lds
s_waitcnt lgkmcnt(0)
v_mfma_f32_16x16x32_bf16 v[0:3], a[0:3], a[128:131], v[0:3]
ds_read_b64_tr_b16 a[32:33], v198 offset:8192
v_mfma_f32_16x16x32_bf16 v[4:7], a[0:3], a[132:135], v[4:7]
ds_read_b64_tr_b16 a[34:35], v198 offset:9216
v_mfma_f32_16x16x32_bf16 v[8:11], a[0:3], a[136:139], v[8:11]
ds_read_b128 a[144:147], v197 offset:0
v_mfma_f32_16x16x32_bf16 v[12:15], a[0:3], a[140:143], v[12:15]
ds_read_b128 a[148:151], v197 offset:2048
v_mfma_f32_16x16x32_bf16 v[16:19], a[4:7], a[128:131], v[16:19]
buffer_load_dwordx4 v213, s[20:23], s32 offen offset:3072 sc1 lds
v_mfma_f32_16x16x32_bf16 v[20:23], a[4:7], a[132:135], v[20:23]
ds_read_b128 a[152:155], v197 offset:4096
v_mfma_f32_16x16x32_bf16 v[24:27], a[4:7], a[136:139], v[24:27]
ds_read_b128 a[156:159], v197 offset:6144
v_mfma_f32_16x16x32_bf16 v[28:31], a[4:7], a[140:143], v[28:31]
ds_read_b64_tr_b16 a[36:37], v199 offset:8320
v_mfma_f32_16x16x32_bf16 v[32:35], a[8:11], a[128:131], v[32:35]
ds_read_b64_tr_b16 a[38:39], v199 offset:9344
s_add_u32 m0, s57, 16384
v_mfma_f32_16x16x32_bf16 v[36:39], a[8:11], a[132:135], v[36:39]
buffer_load_dwordx4 v208, s[36:39], s33 offen offset:0 lds
v_mfma_f32_16x16x32_bf16 v[40:43], a[8:11], a[136:139], v[40:43]
ds_read_b64_tr_b16 a[40:41], v198 offset:8448
v_mfma_f32_16x16x32_bf16 v[44:47], a[8:11], a[140:143], v[44:47]
ds_read_b64_tr_b16 a[42:43], v198 offset:9472
v_mfma_f32_16x16x32_bf16 v[48:51], a[12:15], a[128:131], v[48:51]
ds_read_b64_tr_b16 a[44:45], v199 offset:8576
v_mfma_f32_16x16x32_bf16 v[52:55], a[12:15], a[132:135], v[52:55]
ds_read_b64_tr_b16 a[46:47], v199 offset:9600
v_mfma_f32_16x16x32_bf16 v[56:59], a[12:15], a[136:139], v[56:59]
buffer_load_dwordx4 v209, s[36:39], s33 offen offset:1024 lds
v_mfma_f32_16x16x32_bf16 v[60:63], a[12:15], a[140:143], v[60:63]
ds_read_b64_tr_b16 a[48:49], v198 offset:8704
v_mfma_f32_16x16x32_bf16 v[64:67], a[16:19], a[128:131], v[64:67]
ds_read_b64_tr_b16 a[50:51], v198 offset:9728
v_mfma_f32_16x16x32_bf16 v[68:71], a[16:19], a[132:135], v[68:71]
ds_read_b64_tr_b16 a[52:53], v199 offset:8832
v_mfma_f32_16x16x32_bf16 v[72:75], a[16:19], a[136:139], v[72:75]
ds_read_b64_tr_b16 a[54:55], v199 offset:9856
v_mfma_f32_16x16x32_bf16 v[76:79], a[16:19], a[140:143], v[76:79]
buffer_load_dwordx4 v210, s[36:39], s33 offen offset:2048 lds
v_mfma_f32_16x16x32_bf16 v[80:83], a[20:23], a[128:131], v[80:83]
ds_read_b64_tr_b16 a[56:57], v198 offset:8960
v_mfma_f32_16x16x32_bf16 v[84:87], a[20:23], a[132:135], v[84:87]
ds_read_b64_tr_b16 a[58:59], v198 offset:9984
v_mfma_f32_16x16x32_bf16 v[88:91], a[20:23], a[136:139], v[88:91]
ds_read_b64_tr_b16 a[60:61], v199 offset:9088
v_mfma_f32_16x16x32_bf16 v[92:95], a[20:23], a[140:143], v[92:95]
ds_read_b64_tr_b16 a[62:63], v199 offset:10112
v_mfma_f32_16x16x32_bf16 v[96:99], a[24:27], a[128:131], v[96:99]
buffer_load_dwordx4 v211, s[36:39], s33 offen offset:3072 lds
v_mfma_f32_16x16x32_bf16 v[100:103], a[24:27], a[132:135], v[100:103]
s_add_u32 s24, s24, 1
s_and_b32 s24, s24, 7
v_mfma_f32_16x16x32_bf16 v[104:107], a[24:27], a[136:139], v[104:107]
s_lshl_b32 s32, s24, 7
s_add_u32 s31, s32, s60
v_mfma_f32_16x16x32_bf16 v[108:111], a[24:27], a[140:143], v[108:111]
s_add_u32 s32, s32, s96
s_lshl_b32 s33, s24, 19
v_mfma_f32_16x16x32_bf16 v[112:115], a[28:31], a[128:131], v[112:115]
s_add_u32 s33, s33, s97
s_add_u32 s61, s63, 4096
buffer_store_dwordx4 a[180:183], v252, s[8:11], s61 offen sc1
v_mfma_f32_16x16x32_bf16 v[116:119], a[28:31], a[132:135], v[116:119]
s_add_u32 s61, s63, 5120
buffer_store_dwordx4 a[184:187], v252, s[8:11], s61 offen sc1
v_mfma_f32_16x16x32_bf16 v[120:123], a[28:31], a[136:139], v[120:123]
v_mfma_f32_16x16x32_bf16 v[124:127], a[28:31], a[140:143], v[124:127]
s_waitcnt vmcnt(17) lgkmcnt(0)
s_barrier
v_add_u32 v196, 0xc000, v196
v_add_u32 v197, 0xc000, v197
v_add_u32 v198, 0xc000, v198
v_add_u32 v199, 0xc000, v199
s_nop 0
v_mfma_f32_16x16x32_bf16 v[0:3], a[32:35], a[144:147], v[0:3]
ds_read_b64_tr_b16 a[0:1], v198 offset:0
s_add_u32 m0, s56, 65536
v_mfma_f32_16x16x32_bf16 v[4:7], a[32:35], a[148:151], v[4:7]
buffer_load_dwordx4 v200, s[20:23], s31 offen offset:0 sc1 lds
v_mfma_f32_16x16x32_bf16 v[8:11], a[32:35], a[152:155], v[8:11]
ds_read_b64_tr_b16 a[2:3], v198 offset:1024
v_mfma_f32_16x16x32_bf16 v[12:15], a[32:35], a[156:159], v[12:15]
ds_read_b128 a[128:131], v196 offset:0
v_mfma_f32_16x16x32_bf16 v[16:19], a[36:39], a[144:147], v[16:19]
ds_read_b128 a[132:135], v196 offset:2048
v_mfma_f32_16x16x32_bf16 v[20:23], a[36:39], a[148:151], v[20:23]
ds_read_b128 a[136:139], v196 offset:4096
v_mfma_f32_16x16x32_bf16 v[24:27], a[36:39], a[152:155], v[24:27]
buffer_load_dwordx4 v201, s[20:23], s31 offen offset:1024 sc1 lds
v_mfma_f32_16x16x32_bf16 v[28:31], a[36:39], a[156:159], v[28:31]
ds_read_b128 a[140:143], v196 offset:6144
v_mfma_f32_16x16x32_bf16 v[32:35], a[40:43], a[144:147], v[32:35]
ds_read_b64_tr_b16 a[4:5], v199 offset:128
v_mfma_f32_16x16x32_bf16 v[36:39], a[40:43], a[148:151], v[36:39]
ds_read_b64_tr_b16 a[6:7], v199 offset:1152
v_mfma_f32_16x16x32_bf16 v[40:43], a[40:43], a[152:155], v[40:43]
ds_read_b64_tr_b16 a[8:9], v198 offset:256
v_mfma_f32_16x16x32_bf16 v[44:47], a[40:43], a[156:159], v[44:47]
buffer_load_dwordx4 v212, s[20:23], s31 offen offset:2048 sc1 lds
v_mfma_f32_16x16x32_bf16 v[48:51], a[44:47], a[144:147], v[48:51]
ds_read_b64_tr_b16 a[10:11], v198 offset:1280
v_mfma_f32_16x16x32_bf16 v[52:55], a[44:47], a[148:151], v[52:55]
ds_read_b64_tr_b16 a[12:13], v199 offset:384
v_mfma_f32_16x16x32_bf16 v[56:59], a[44:47], a[152:155], v[56:59]
ds_read_b64_tr_b16 a[14:15], v199 offset:1408
v_mfma_f32_16x16x32_bf16 v[60:63], a[44:47], a[156:159], v[60:63]
ds_read_b64_tr_b16 a[16:17], v198 offset:512
v_mfma_f32_16x16x32_bf16 v[64:67], a[48:51], a[144:147], v[64:67]
buffer_load_dwordx4 v213, s[20:23], s31 offen offset:3072 sc1 lds
v_mfma_f32_16x16x32_bf16 v[68:71], a[48:51], a[148:151], v[68:71]
ds_read_b64_tr_b16 a[18:19], v198 offset:1536
v_mfma_f32_16x16x32_bf16 v[72:75], a[48:51], a[152:155], v[72:75]
ds_read_b64_tr_b16 a[20:21], v199 offset:640
v_mfma_f32_16x16x32_bf16 v[76:79], a[48:51], a[156:159], v[76:79]
ds_read_b64_tr_b16 a[22:23], v199 offset:1664
v_mfma_f32_16x16x32_bf16 v[80:83], a[52:55], a[144:147], v[80:83]
ds_read_b64_tr_b16 a[24:25], v198 offset:768
s_add_u32 m0, s56, 69632
v_mfma_f32_16x16x32_bf16 v[84:87], a[52:55], a[148:151], v[84:87]
buffer_load_dwordx4 v200, s[20:23], s32 offen offset:0 sc1 lds
v_mfma_f32_16x16x32_bf16 v[88:91], a[52:55], a[152:155], v[88:91]
ds_read_b64_tr_b16 a[26:27], v198 offset:1792
v_mfma_f32_16x16x32_bf16 v[92:95], a[52:55], a[156:159], v[92:95]
ds_read_b64_tr_b16 a[28:29], v199 offset:896
v_mfma_f32_16x16x32_bf16 v[96:99], a[56:59], a[144:147], v[96:99]
ds_read_b64_tr_b16 a[30:31], v199 offset:1920
v_mfma_f32_16x16x32_bf16 v[100:103], a[56:59], a[148:151], v[100:103]
v_mfma_f32_16x16x32_bf16 v[104:107], a[56:59], a[152:155], v[104:107]
buffer_load_dwordx4 v201, s[20:23], s32 offen offset:1024 sc1 lds
v_mfma_f32_16x16x32_bf16 v[108:111], a[56:59], a[156:159], v[108:111]
v_mfma_f32_16x16x32_bf16 v[112:115], a[60:63], a[144:147], v[112:115]
v_mfma_f32_16x16x32_bf16 v[116:119], a[60:63], a[148:151], v[116:119]
v_mfma_f32_16x16x32_bf16 v[120:123], a[60:63], a[152:155], v[120:123]
v_mfma_f32_16x16x32_bf16 v[124:127], a[60:63], a[156:159], v[124:127]
buffer_load_dwordx4 v212, s[20:23], s32 offen offset:2048 sc1 lds
s_waitcnt lgkmcnt(0)
v_mfma_f32_16x16x32_bf16 v[0:3], a[0:3], a[128:131], v[0:3]
ds_read_b64_tr_b16 a[32:33], v198 offset:8192
v_mfma_f32_16x16x32_bf16 v[4:7], a[0:3], a[132:135], v[4:7]
ds_read_b64_tr_b16 a[34:35], v198 offset:9216
v_mfma_f32_16x16x32_bf16 v[8:11], a[0:3], a[136:139], v[8:11]
ds_read_b128 a[144:147], v197 offset:0
v_mfma_f32_16x16x32_bf16 v[12:15], a[0:3], a[140:143], v[12:15]
ds_read_b128 a[148:151], v197 offset:2048
v_mfma_f32_16x16x32_bf16 v[16:19], a[4:7], a[128:131], v[16:19]
buffer_load_dwordx4 v213, s[20:23], s32 offen offset:3072 sc1 lds
v_mfma_f32_16x16x32_bf16 v[20:23], a[4:7], a[132:135], v[20:23]
ds_read_b128 a[152:155], v197 offset:4096
v_mfma_f32_16x16x32_bf16 v[24:27], a[4:7], a[136:139], v[24:27]
ds_read_b128 a[156:159], v197 offset:6144
v_mfma_f32_16x16x32_bf16 v[28:31], a[4:7], a[140:143], v[28:31]
ds_read_b64_tr_b16 a[36:37], v199 offset:8320
v_mfma_f32_16x16x32_bf16 v[32:35], a[8:11], a[128:131], v[32:35]
ds_read_b64_tr_b16 a[38:39], v199 offset:9344
s_add_u32 m0, s57, 65536
v_mfma_f32_16x16x32_bf16 v[36:39], a[8:11], a[132:135], v[36:39]
buffer_load_dwordx4 v208, s[36:39], s33 offen offset:0 lds
v_mfma_f32_16x16x32_bf16 v[40:43], a[8:11], a[136:139], v[40:43]
ds_read_b64_tr_b16 a[40:41], v198 offset:8448
v_mfma_f32_16x16x32_bf16 v[44:47], a[8:11], a[140:143], v[44:47]
ds_read_b64_tr_b16 a[42:43], v198 offset:9472
v_mfma_f32_16x16x32_bf16 v[48:51], a[12:15], a[128:131], v[48:51]
ds_read_b64_tr_b16 a[44:45], v199 offset:8576
v_mfma_f32_16x16x32_bf16 v[52:55], a[12:15], a[132:135], v[52:55]
ds_read_b64_tr_b16 a[46:47], v199 offset:9600
v_mfma_f32_16x16x32_bf16 v[56:59], a[12:15], a[136:139], v[56:59]
buffer_load_dwordx4 v209, s[36:39], s33 offen offset:1024 lds
v_mfma_f32_16x16x32_bf16 v[60:63], a[12:15], a[140:143], v[60:63]
ds_read_b64_tr_b16 a[48:49], v198 offset:8704
v_mfma_f32_16x16x32_bf16 v[64:67], a[16:19], a[128:131], v[64:67]
ds_read_b64_tr_b16 a[50:51], v198 offset:9728
v_mfma_f32_16x16x32_bf16 v[68:71], a[16:19], a[132:135], v[68:71]
ds_read_b64_tr_b16 a[52:53], v199 offset:8832
v_mfma_f32_16x16x32_bf16 v[72:75], a[16:19], a[136:139], v[72:75]
ds_read_b64_tr_b16 a[54:55], v199 offset:9856
v_mfma_f32_16x16x32_bf16 v[76:79], a[16:19], a[140:143], v[76:79]
buffer_load_dwordx4 v210, s[36:39], s33 offen offset:2048 lds
v_mfma_f32_16x16x32_bf16 v[80:83], a[20:23], a[128:131], v[80:83]
ds_read_b64_tr_b16 a[56:57], v198 offset:8960
v_mfma_f32_16x16x32_bf16 v[84:87], a[20:23], a[132:135], v[84:87]
ds_read_b64_tr_b16 a[58:59], v198 offset:9984
v_mfma_f32_16x16x32_bf16 v[88:91], a[20:23], a[136:139], v[88:91]
ds_read_b64_tr_b16 a[60:61], v199 offset:9088
v_mfma_f32_16x16x32_bf16 v[92:95], a[20:23], a[140:143], v[92:95]
ds_read_b64_tr_b16 a[62:63], v199 offset:10112
v_mfma_f32_16x16x32_bf16 v[96:99], a[24:27], a[128:131], v[96:99]
buffer_load_dwordx4 v211, s[36:39], s33 offen offset:3072 lds
v_mfma_f32_16x16x32_bf16 v[100:103], a[24:27], a[132:135], v[100:103]
s_add_u32 s24, s24, 1
s_and_b32 s24, s24, 7
v_mfma_f32_16x16x32_bf16 v[104:107], a[24:27], a[136:139], v[104:107]
s_lshl_b32 s32, s24, 7
s_add_u32 s31, s32, s60
v_mfma_f32_16x16x32_bf16 v[108:111], a[24:27], a[140:143], v[108:111]
s_add_u32 s32, s32, s96
s_lshl_b32 s33, s24, 19
v_mfma_f32_16x16x32_bf16 v[112:115], a[28:31], a[128:131], v[112:115]
s_add_u32 s33, s33, s97
s_add_u32 s61, s63, 6144
buffer_store_dwordx4 a[188:191], v252, s[8:11], s61 offen sc1
v_mfma_f32_16x16x32_bf16 v[116:119], a[28:31], a[132:135], v[116:119]
s_add_u32 s61, s63, 7168
buffer_store_dwordx4 a[192:195], v252, s[8:11], s61 offen sc1
v_mfma_f32_16x16x32_bf16 v[120:123], a[28:31], a[136:139], v[120:123]
v_mfma_f32_16x16x32_bf16 v[124:127], a[28:31], a[140:143], v[124:127]
s_waitcnt vmcnt(16) lgkmcnt(0)
s_barrier
v_add_u32 v196, 0xfffe8000, v196
v_add_u32 v197, 0xfffe8000, v197
v_add_u32 v198, 0xfffe8000, v198
v_add_u32 v199, 0xfffe8000, v199
s_nop 0
v_mfma_f32_16x16x32_bf16 v[0:3], a[32:35], a[144:147], v[0:3]
ds_read_b64_tr_b16 a[0:1], v198 offset:0
s_add_u32 m0, s56, 114688
v_mfma_f32_16x16x32_bf16 v[4:7], a[32:35], a[148:151], v[4:7]
buffer_load_dwordx4 v200, s[20:23], s31 offen offset:0 sc1 lds
v_mfma_f32_16x16x32_bf16 v[8:11], a[32:35], a[152:155], v[8:11]
ds_read_b64_tr_b16 a[2:3], v198 offset:1024
v_mfma_f32_16x16x32_bf16 v[12:15], a[32:35], a[156:159], v[12:15]
ds_read_b128 a[128:131], v196 offset:0
v_mfma_f32_16x16x32_bf16 v[16:19], a[36:39], a[144:147], v[16:19]
ds_read_b128 a[132:135], v196 offset:2048
v_mfma_f32_16x16x32_bf16 v[20:23], a[36:39], a[148:151], v[20:23]
ds_read_b128 a[136:139], v196 offset:4096
v_mfma_f32_16x16x32_bf16 v[24:27], a[36:39], a[152:155], v[24:27]
buffer_load_dwordx4 v201, s[20:23], s31 offen offset:1024 sc1 lds
v_mfma_f32_16x16x32_bf16 v[28:31], a[36:39], a[156:159], v[28:31]
ds_read_b128 a[140:143], v196 offset:6144
v_mfma_f32_16x16x32_bf16 v[32:35], a[40:43], a[144:147], v[32:35]
ds_read_b64_tr_b16 a[4:5], v199 offset:128
v_mfma_f32_16x16x32_bf16 v[36:39], a[40:43], a[148:151], v[36:39]
ds_read_b64_tr_b16 a[6:7], v199 offset:1152
v_mfma_f32_16x16x32_bf16 v[40:43], a[40:43], a[152:155], v[40:43]
ds_read_b64_tr_b16 a[8:9], v198 offset:256
v_mfma_f32_16x16x32_bf16 v[44:47], a[40:43], a[156:159], v[44:47]
buffer_load_dwordx4 v212, s[20:23], s31 offen offset:2048 sc1 lds
v_mfma_f32_16x16x32_bf16 v[48:51], a[44:47], a[144:147], v[48:51]
ds_read_b64_tr_b16 a[10:11], v198 offset:1280
v_mfma_f32_16x16x32_bf16 v[52:55], a[44:47], a[148:151], v[52:55]
ds_read_b64_tr_b16 a[12:13], v199 offset:384
v_mfma_f32_16x16x32_bf16 v[56:59], a[44:47], a[152:155], v[56:59]
ds_read_b64_tr_b16 a[14:15], v199 offset:1408
v_mfma_f32_16x16x32_bf16 v[60:63], a[44:47], a[156:159], v[60:63]
ds_read_b64_tr_b16 a[16:17], v198 offset:512
v_mfma_f32_16x16x32_bf16 v[64:67], a[48:51], a[144:147], v[64:67]
buffer_load_dwordx4 v213, s[20:23], s31 offen offset:3072 sc1 lds
v_mfma_f32_16x16x32_bf16 v[68:71], a[48:51], a[148:151], v[68:71]
ds_read_b64_tr_b16 a[18:19], v198 offset:1536
v_mfma_f32_16x16x32_bf16 v[72:75], a[48:51], a[152:155], v[72:75]
)ASM"
R"ASM(ds_read_b64_tr_b16 a[20:21], v199 offset:640
v_mfma_f32_16x16x32_bf16 v[76:79], a[48:51], a[156:159], v[76:79]
ds_read_b64_tr_b16 a[22:23], v199 offset:1664
v_mfma_f32_16x16x32_bf16 v[80:83], a[52:55], a[144:147], v[80:83]
ds_read_b64_tr_b16 a[24:25], v198 offset:768
s_add_u32 m0, s56, 118784
v_mfma_f32_16x16x32_bf16 v[84:87], a[52:55], a[148:151], v[84:87]
buffer_load_dwordx4 v200, s[20:23], s32 offen offset:0 sc1 lds
v_mfma_f32_16x16x32_bf16 v[88:91], a[52:55], a[152:155], v[88:91]
ds_read_b64_tr_b16 a[26:27], v198 offset:1792
v_mfma_f32_16x16x32_bf16 v[92:95], a[52:55], a[156:159], v[92:95]
ds_read_b64_tr_b16 a[28:29], v199 offset:896
v_mfma_f32_16x16x32_bf16 v[96:99], a[56:59], a[144:147], v[96:99]
ds_read_b64_tr_b16 a[30:31], v199 offset:1920
v_mfma_f32_16x16x32_bf16 v[100:103], a[56:59], a[148:151], v[100:103]
v_mfma_f32_16x16x32_bf16 v[104:107], a[56:59], a[152:155], v[104:107]
buffer_load_dwordx4 v201, s[20:23], s32 offen offset:1024 sc1 lds
v_mfma_f32_16x16x32_bf16 v[108:111], a[56:59], a[156:159], v[108:111]
v_mfma_f32_16x16x32_bf16 v[112:115], a[60:63], a[144:147], v[112:115]
v_mfma_f32_16x16x32_bf16 v[116:119], a[60:63], a[148:151], v[116:119]
v_mfma_f32_16x16x32_bf16 v[120:123], a[60:63], a[152:155], v[120:123]
v_mfma_f32_16x16x32_bf16 v[124:127], a[60:63], a[156:159], v[124:127]
buffer_load_dwordx4 v212, s[20:23], s32 offen offset:2048 sc1 lds
s_waitcnt lgkmcnt(0)
v_mfma_f32_16x16x32_bf16 v[0:3], a[0:3], a[128:131], v[0:3]
ds_read_b64_tr_b16 a[32:33], v198 offset:8192
v_mfma_f32_16x16x32_bf16 v[4:7], a[0:3], a[132:135], v[4:7]
ds_read_b64_tr_b16 a[34:35], v198 offset:9216
v_mfma_f32_16x16x32_bf16 v[8:11], a[0:3], a[136:139], v[8:11]
ds_read_b128 a[144:147], v197 offset:0
v_mfma_f32_16x16x32_bf16 v[12:15], a[0:3], a[140:143], v[12:15]
ds_read_b128 a[148:151], v197 offset:2048
v_mfma_f32_16x16x32_bf16 v[16:19], a[4:7], a[128:131], v[16:19]
buffer_load_dwordx4 v213, s[20:23], s32 offen offset:3072 sc1 lds
v_mfma_f32_16x16x32_bf16 v[20:23], a[4:7], a[132:135], v[20:23]
ds_read_b128 a[152:155], v197 offset:4096
v_mfma_f32_16x16x32_bf16 v[24:27], a[4:7], a[136:139], v[24:27]
ds_read_b128 a[156:159], v197 offset:6144
v_mfma_f32_16x16x32_bf16 v[28:31], a[4:7], a[140:143], v[28:31]
ds_read_b64_tr_b16 a[36:37], v199 offset:8320
v_mfma_f32_16x16x32_bf16 v[32:35], a[8:11], a[128:131], v[32:35]
ds_read_b64_tr_b16 a[38:39], v199 offset:9344
s_add_u32 m0, s57, 114688
v_mfma_f32_16x16x32_bf16 v[36:39], a[8:11], a[132:135], v[36:39]
buffer_load_dwordx4 v208, s[36:39], s33 offen offset:0 lds
v_mfma_f32_16x16x32_bf16 v[40:43], a[8:11], a[136:139], v[40:43]
ds_read_b64_tr_b16 a[40:41], v198 offset:8448
v_mfma_f32_16x16x32_bf16 v[44:47], a[8:11], a[140:143], v[44:47]
ds_read_b64_tr_b16 a[42:43], v198 offset:9472
v_mfma_f32_16x16x32_bf16 v[48:51], a[12:15], a[128:131], v[48:51]
ds_read_b64_tr_b16 a[44:45], v199 offset:8576
v_mfma_f32_16x16x32_bf16 v[52:55], a[12:15], a[132:135], v[52:55]
ds_read_b64_tr_b16 a[46:47], v199 offset:9600
v_mfma_f32_16x16x32_bf16 v[56:59], a[12:15], a[136:139], v[56:59]
buffer_load_dwordx4 v209, s[36:39], s33 offen offset:1024 lds
v_mfma_f32_16x16x32_bf16 v[60:63], a[12:15], a[140:143], v[60:63]
ds_read_b64_tr_b16 a[48:49], v198 offset:8704
v_mfma_f32_16x16x32_bf16 v[64:67], a[16:19], a[128:131], v[64:67]
ds_read_b64_tr_b16 a[50:51], v198 offset:9728
v_mfma_f32_16x16x32_bf16 v[68:71], a[16:19], a[132:135], v[68:71]
ds_read_b64_tr_b16 a[52:53], v199 offset:8832
v_mfma_f32_16x16x32_bf16 v[72:75], a[16:19], a[136:139], v[72:75]
ds_read_b64_tr_b16 a[54:55], v199 offset:9856
v_mfma_f32_16x16x32_bf16 v[76:79], a[16:19], a[140:143], v[76:79]
buffer_load_dwordx4 v210, s[36:39], s33 offen offset:2048 lds
v_mfma_f32_16x16x32_bf16 v[80:83], a[20:23], a[128:131], v[80:83]
ds_read_b64_tr_b16 a[56:57], v198 offset:8960
v_mfma_f32_16x16x32_bf16 v[84:87], a[20:23], a[132:135], v[84:87]
ds_read_b64_tr_b16 a[58:59], v198 offset:9984
v_mfma_f32_16x16x32_bf16 v[88:91], a[20:23], a[136:139], v[88:91]
ds_read_b64_tr_b16 a[60:61], v199 offset:9088
v_mfma_f32_16x16x32_bf16 v[92:95], a[20:23], a[140:143], v[92:95]
ds_read_b64_tr_b16 a[62:63], v199 offset:10112
v_mfma_f32_16x16x32_bf16 v[96:99], a[24:27], a[128:131], v[96:99]
buffer_load_dwordx4 v211, s[36:39], s33 offen offset:3072 lds
v_mfma_f32_16x16x32_bf16 v[100:103], a[24:27], a[132:135], v[100:103]
s_add_u32 s24, s24, 1
s_and_b32 s24, s24, 7
v_mfma_f32_16x16x32_bf16 v[104:107], a[24:27], a[136:139], v[104:107]
s_add_u32 s61, s63, 8192
buffer_store_dwordx4 a[196:199], v252, s[8:11], s61 offen sc1
s_add_u32 s61, s63, 9216
buffer_store_dwordx4 a[200:203], v252, s[8:11], s61 offen sc1
v_mfma_f32_16x16x32_bf16 v[108:111], a[24:27], a[140:143], v[108:111]
v_mfma_f32_16x16x32_bf16 v[112:115], a[28:31], a[128:131], v[112:115]
v_mfma_f32_16x16x32_bf16 v[116:119], a[28:31], a[132:135], v[116:119]
v_mfma_f32_16x16x32_bf16 v[120:123], a[28:31], a[136:139], v[120:123]
v_mfma_f32_16x16x32_bf16 v[124:127], a[28:31], a[140:143], v[124:127]
s_waitcnt vmcnt(16) lgkmcnt(0)
s_barrier
v_min_u32 v226, 7, v253
v_lshlrev_b32 v226, 3, v226
s_lshl_b32 s25, s26, 6
s_add_u32 s25, s25, 8192
buffer_load_dwordx2 v[228:229], v226, s[88:91], s25 offen sc1
v_add_u32 v196, 0xc000, v196
v_add_u32 v197, 0xc000, v197
v_add_u32 v198, 0xc000, v198
v_add_u32 v199, 0xc000, v199
s_nop 0
v_mfma_f32_16x16x32_bf16 v[0:3], a[32:35], a[144:147], v[0:3]
ds_read_b64_tr_b16 a[0:1], v198 offset:0
v_mfma_f32_16x16x32_bf16 v[4:7], a[32:35], a[148:151], v[4:7]
ds_read_b64_tr_b16 a[2:3], v198 offset:1024
v_mfma_f32_16x16x32_bf16 v[8:11], a[32:35], a[152:155], v[8:11]
ds_read_b128 a[128:131], v196 offset:0
s_add_u32 s61, s63, 10240
buffer_store_dwordx4 a[204:207], v252, s[8:11], s61 offen sc1
v_mfma_f32_16x16x32_bf16 v[12:15], a[32:35], a[156:159], v[12:15]
ds_read_b128 a[132:135], v196 offset:2048
v_mfma_f32_16x16x32_bf16 v[16:19], a[36:39], a[144:147], v[16:19]
ds_read_b128 a[136:139], v196 offset:4096
v_mfma_f32_16x16x32_bf16 v[20:23], a[36:39], a[148:151], v[20:23]
ds_read_b128 a[140:143], v196 offset:6144
v_mfma_f32_16x16x32_bf16 v[24:27], a[36:39], a[152:155], v[24:27]
ds_read_b64_tr_b16 a[4:5], v199 offset:128
s_add_u32 s61, s63, 11264
buffer_store_dwordx4 a[208:211], v252, s[8:11], s61 offen sc1
v_mfma_f32_16x16x32_bf16 v[28:31], a[36:39], a[156:159], v[28:31]
ds_read_b64_tr_b16 a[6:7], v199 offset:1152
v_mfma_f32_16x16x32_bf16 v[32:35], a[40:43], a[144:147], v[32:35]
ds_read_b64_tr_b16 a[8:9], v198 offset:256
v_mfma_f32_16x16x32_bf16 v[36:39], a[40:43], a[148:151], v[36:39]
ds_read_b64_tr_b16 a[10:11], v198 offset:1280
v_mfma_f32_16x16x32_bf16 v[40:43], a[40:43], a[152:155], v[40:43]
ds_read_b64_tr_b16 a[12:13], v199 offset:384
v_mfma_f32_16x16x32_bf16 v[44:47], a[40:43], a[156:159], v[44:47]
ds_read_b64_tr_b16 a[14:15], v199 offset:1408
v_mfma_f32_16x16x32_bf16 v[48:51], a[44:47], a[144:147], v[48:51]
ds_read_b64_tr_b16 a[16:17], v198 offset:512
v_mfma_f32_16x16x32_bf16 v[52:55], a[44:47], a[148:151], v[52:55]
ds_read_b64_tr_b16 a[18:19], v198 offset:1536
v_mfma_f32_16x16x32_bf16 v[56:59], a[44:47], a[152:155], v[56:59]
ds_read_b64_tr_b16 a[20:21], v199 offset:640
v_mfma_f32_16x16x32_bf16 v[60:63], a[44:47], a[156:159], v[60:63]
ds_read_b64_tr_b16 a[22:23], v199 offset:1664
v_mfma_f32_16x16x32_bf16 v[64:67], a[48:51], a[144:147], v[64:67]
ds_read_b64_tr_b16 a[24:25], v198 offset:768
v_mfma_f32_16x16x32_bf16 v[68:71], a[48:51], a[148:151], v[68:71]
ds_read_b64_tr_b16 a[26:27], v198 offset:1792
v_mfma_f32_16x16x32_bf16 v[72:75], a[48:51], a[152:155], v[72:75]
ds_read_b64_tr_b16 a[28:29], v199 offset:896
v_mfma_f32_16x16x32_bf16 v[76:79], a[48:51], a[156:159], v[76:79]
ds_read_b64_tr_b16 a[30:31], v199 offset:1920
v_mfma_f32_16x16x32_bf16 v[80:83], a[52:55], a[144:147], v[80:83]
v_mfma_f32_16x16x32_bf16 v[84:87], a[52:55], a[148:151], v[84:87]
v_mfma_f32_16x16x32_bf16 v[88:91], a[52:55], a[152:155], v[88:91]
v_mfma_f32_16x16x32_bf16 v[92:95], a[52:55], a[156:159], v[92:95]
v_mfma_f32_16x16x32_bf16 v[96:99], a[56:59], a[144:147], v[96:99]
v_mfma_f32_16x16x32_bf16 v[100:103], a[56:59], a[148:151], v[100:103]
v_mfma_f32_16x16x32_bf16 v[104:107], a[56:59], a[152:155], v[104:107]
v_mfma_f32_16x16x32_bf16 v[108:111], a[56:59], a[156:159], v[108:111]
v_mfma_f32_16x16x32_bf16 v[112:115], a[60:63], a[144:147], v[112:115]
v_mfma_f32_16x16x32_bf16 v[116:119], a[60:63], a[148:151], v[116:119]
v_mfma_f32_16x16x32_bf16 v[120:123], a[60:63], a[152:155], v[120:123]
v_mfma_f32_16x16x32_bf16 v[124:127], a[60:63], a[156:159], v[124:127]
s_waitcnt lgkmcnt(0)
v_mfma_f32_16x16x32_bf16 v[0:3], a[0:3], a[128:131], v[0:3]
ds_read_b64_tr_b16 a[32:33], v198 offset:8192
v_mfma_f32_16x16x32_bf16 v[4:7], a[0:3], a[132:135], v[4:7]
ds_read_b64_tr_b16 a[34:35], v198 offset:9216
v_mfma_f32_16x16x32_bf16 v[8:11], a[0:3], a[136:139], v[8:11]
ds_read_b128 a[144:147], v197 offset:0
v_mfma_f32_16x16x32_bf16 v[12:15], a[0:3], a[140:143], v[12:15]
ds_read_b128 a[148:151], v197 offset:2048
v_mfma_f32_16x16x32_bf16 v[16:19], a[4:7], a[128:131], v[16:19]
ds_read_b128 a[152:155], v197 offset:4096
v_mfma_f32_16x16x32_bf16 v[20:23], a[4:7], a[132:135], v[20:23]
ds_read_b128 a[156:159], v197 offset:6144
v_mfma_f32_16x16x32_bf16 v[24:27], a[4:7], a[136:139], v[24:27]
ds_read_b64_tr_b16 a[36:37], v199 offset:8320
v_mfma_f32_16x16x32_bf16 v[28:31], a[4:7], a[140:143], v[28:31]
ds_read_b64_tr_b16 a[38:39], v199 offset:9344
v_mfma_f32_16x16x32_bf16 v[32:35], a[8:11], a[128:131], v[32:35]
ds_read_b64_tr_b16 a[40:41], v198 offset:8448
v_mfma_f32_16x16x32_bf16 v[36:39], a[8:11], a[132:135], v[36:39]
ds_read_b64_tr_b16 a[42:43], v198 offset:9472
v_mfma_f32_16x16x32_bf16 v[40:43], a[8:11], a[136:139], v[40:43]
ds_read_b64_tr_b16 a[44:45], v199 offset:8576
v_mfma_f32_16x16x32_bf16 v[44:47], a[8:11], a[140:143], v[44:47]
ds_read_b64_tr_b16 a[46:47], v199 offset:9600
v_mfma_f32_16x16x32_bf16 v[48:51], a[12:15], a[128:131], v[48:51]
ds_read_b64_tr_b16 a[48:49], v198 offset:8704
v_mfma_f32_16x16x32_bf16 v[52:55], a[12:15], a[132:135], v[52:55]
ds_read_b64_tr_b16 a[50:51], v198 offset:9728
v_mfma_f32_16x16x32_bf16 v[56:59], a[12:15], a[136:139], v[56:59]
ds_read_b64_tr_b16 a[52:53], v199 offset:8832
v_mfma_f32_16x16x32_bf16 v[60:63], a[12:15], a[140:143], v[60:63]
ds_read_b64_tr_b16 a[54:55], v199 offset:9856
v_mfma_f32_16x16x32_bf16 v[64:67], a[16:19], a[128:131], v[64:67]
ds_read_b64_tr_b16 a[56:57], v198 offset:8960
v_mfma_f32_16x16x32_bf16 v[68:71], a[16:19], a[132:135], v[68:71]
ds_read_b64_tr_b16 a[58:59], v198 offset:9984
v_mfma_f32_16x16x32_bf16 v[72:75], a[16:19], a[136:139], v[72:75]
ds_read_b64_tr_b16 a[60:61], v199 offset:9088
v_mfma_f32_16x16x32_bf16 v[76:79], a[16:19], a[140:143], v[76:79]
ds_read_b64_tr_b16 a[62:63], v199 offset:10112
v_mfma_f32_16x16x32_bf16 v[80:83], a[20:23], a[128:131], v[80:83]
v_mfma_f32_16x16x32_bf16 v[84:87], a[20:23], a[132:135], v[84:87]
v_mfma_f32_16x16x32_bf16 v[88:91], a[20:23], a[136:139], v[88:91]
v_mfma_f32_16x16x32_bf16 v[92:95], a[20:23], a[140:143], v[92:95]
v_mfma_f32_16x16x32_bf16 v[96:99], a[24:27], a[128:131], v[96:99]
v_mfma_f32_16x16x32_bf16 v[100:103], a[24:27], a[132:135], v[100:103]
v_mfma_f32_16x16x32_bf16 v[104:107], a[24:27], a[136:139], v[104:107]
v_mfma_f32_16x16x32_bf16 v[108:111], a[24:27], a[140:143], v[108:111]
v_mfma_f32_16x16x32_bf16 v[112:115], a[28:31], a[128:131], v[112:115]
v_mfma_f32_16x16x32_bf16 v[116:119], a[28:31], a[132:135], v[116:119]
v_mfma_f32_16x16x32_bf16 v[120:123], a[28:31], a[136:139], v[120:123]
v_mfma_f32_16x16x32_bf16 v[124:127], a[28:31], a[140:143], v[124:127]
s_waitcnt vmcnt(5) lgkmcnt(0)
s_barrier
s_waitcnt vmcnt(2)
s_mov_b32 s86, 0
Lpoll_8:
v_cmp_eq_u64 vcc, s[84:85], v[228:229]
s_nop 1
s_cmp_eq_u64 vcc, exec
s_cbranch_scc1 Lpdone_9
s_sleep 1
buffer_load_dwordx2 v[228:229], v226, s[88:91], s25 offen sc1
s_waitcnt vmcnt(0)
s_add_u32 s86, s86, 1
s_cmp_lt_u32 s86, 0x40000
s_cbranch_scc1 Lpoll_8
Lpdone_9:
s_add_u32 s20, s82, 0x2800000
s_addc_u32 s21, s83, 0
s_and_b32 s21, s21, 0xffff
s_mov_b32 s22, 0x1000000
s_mov_b32 s23, 0x20000
s_add_u32 s16, s82, 0x400000
s_addc_u32 s17, s83, 0
s_and_b32 s17, s17, 0xffff
s_mov_b32 s18, 0x2000000
s_mov_b32 s19, 0x20000
s_and_b32 s28, s7, 1
s_lshr_b32 s29, s7, 1
s_sub_u32 s49, 16, s27
s_add_u32 s50, s27, 1
s_lshl_b32 s52, s26, 5
s_add_u32 s52, s52, s29
s_lshl_b32 s52, s52, 15
s_lshl_b32 s36, s28, 13
s_add_u32 s52, s52, s36
s_add_u32 s56, s52, 16384
s_mov_b32 s40, s52
s_mov_b32 s41, s56
s_lshl_b32 s37, s49, 16
s_add_u32 s45, s52, s37
s_add_u32 s47, s56, s37
s_lshl_b32 s37, s50, 16
s_add_u32 s57, s52, s37
s_add_u32 s58, s56, s37
s_lshl_b32 s42, s7, 13
s_add_u32 s43, s42, 65536
v_add_u32 v244, 4096, v252
v_mov_b32 v220, v252
v_add_u32 v221, 98304, v252
v_add_u32 v196, 0xc000, v196
v_add_u32 v197, 0xc000, v197
v_add_u32 v198, 0xc000, v198
v_add_u32 v199, 0xc000, v199
s_nop 0
v_mfma_f32_16x16x32_bf16 v[0:3], a[32:35], a[144:147], v[0:3]
ds_read_b64_tr_b16 a[0:1], v198 offset:0
s_mov_b32 m0, s42
v_mfma_f32_16x16x32_bf16 v[4:7], a[32:35], a[148:151], v[4:7]
buffer_load_dwordx4 v252, s[16:19], s40 offen offset:0 sc1 lds
v_mfma_f32_16x16x32_bf16 v[8:11], a[32:35], a[152:155], v[8:11]
ds_read_b64_tr_b16 a[2:3], v198 offset:1024
v_mfma_f32_16x16x32_bf16 v[12:15], a[32:35], a[156:159], v[12:15]
ds_read_b128 a[128:131], v196 offset:0
v_mfma_f32_16x16x32_bf16 v[16:19], a[36:39], a[144:147], v[16:19]
ds_read_b128 a[132:135], v196 offset:2048
v_mfma_f32_16x16x32_bf16 v[20:23], a[36:39], a[148:151], v[20:23]
buffer_load_dwordx4 v252, s[16:19], s40 offen offset:1024 sc1 lds
v_mfma_f32_16x16x32_bf16 v[24:27], a[36:39], a[152:155], v[24:27]
ds_read_b128 a[136:139], v196 offset:4096
v_mfma_f32_16x16x32_bf16 v[28:31], a[36:39], a[156:159], v[28:31]
ds_read_b128 a[140:143], v196 offset:6144
v_mfma_f32_16x16x32_bf16 v[32:35], a[40:43], a[144:147], v[32:35]
ds_read_b64_tr_b16 a[4:5], v199 offset:128
v_mfma_f32_16x16x32_bf16 v[36:39], a[40:43], a[148:151], v[36:39]
buffer_load_dwordx4 v252, s[16:19], s40 offen offset:2048 sc1 lds
v_mfma_f32_16x16x32_bf16 v[40:43], a[40:43], a[152:155], v[40:43]
ds_read_b64_tr_b16 a[6:7], v199 offset:1152
v_mfma_f32_16x16x32_bf16 v[44:47], a[40:43], a[156:159], v[44:47]
ds_read_b64_tr_b16 a[8:9], v198 offset:256
v_mfma_f32_16x16x32_bf16 v[48:51], a[44:47], a[144:147], v[48:51]
ds_read_b64_tr_b16 a[10:11], v198 offset:1280
v_mfma_f32_16x16x32_bf16 v[52:55], a[44:47], a[148:151], v[52:55]
buffer_load_dwordx4 v252, s[16:19], s40 offen offset:3072 sc1 lds
v_mfma_f32_16x16x32_bf16 v[56:59], a[44:47], a[152:155], v[56:59]
ds_read_b64_tr_b16 a[12:13], v199 offset:384
v_mfma_f32_16x16x32_bf16 v[60:63], a[44:47], a[156:159], v[60:63]
ds_read_b64_tr_b16 a[14:15], v199 offset:1408
v_mfma_f32_16x16x32_bf16 v[64:67], a[48:51], a[144:147], v[64:67]
ds_read_b64_tr_b16 a[16:17], v198 offset:512
s_add_u32 m0, s42, 4096
v_mfma_f32_16x16x32_bf16 v[68:71], a[48:51], a[148:151], v[68:71]
buffer_load_dwordx4 v244, s[16:19], s40 offen offset:0 sc1 lds
v_mfma_f32_16x16x32_bf16 v[72:75], a[48:51], a[152:155], v[72:75]
ds_read_b64_tr_b16 a[18:19], v198 offset:1536
v_mfma_f32_16x16x32_bf16 v[76:79], a[48:51], a[156:159], v[76:79]
ds_read_b64_tr_b16 a[20:21], v199 offset:640
v_mfma_f32_16x16x32_bf16 v[80:83], a[52:55], a[144:147], v[80:83]
ds_read_b64_tr_b16 a[22:23], v199 offset:1664
v_mfma_f32_16x16x32_bf16 v[84:87], a[52:55], a[148:151], v[84:87]
buffer_load_dwordx4 v244, s[16:19], s40 offen offset:1024 sc1 lds
v_mfma_f32_16x16x32_bf16 v[88:91], a[52:55], a[152:155], v[88:91]
ds_read_b64_tr_b16 a[24:25], v198 offset:768
v_mfma_f32_16x16x32_bf16 v[92:95], a[52:55], a[156:159], v[92:95]
ds_read_b64_tr_b16 a[26:27], v198 offset:1792
v_mfma_f32_16x16x32_bf16 v[96:99], a[56:59], a[144:147], v[96:99]
ds_read_b64_tr_b16 a[28:29], v199 offset:896
v_mfma_f32_16x16x32_bf16 v[100:103], a[56:59], a[148:151], v[100:103]
buffer_load_dwordx4 v244, s[16:19], s40 offen offset:2048 sc1 lds
v_mfma_f32_16x16x32_bf16 v[104:107], a[56:59], a[152:155], v[104:107]
ds_read_b64_tr_b16 a[30:31], v199 offset:1920
v_mfma_f32_16x16x32_bf16 v[108:111], a[56:59], a[156:159], v[108:111]
v_mfma_f32_16x16x32_bf16 v[112:115], a[60:63], a[144:147], v[112:115]
v_mfma_f32_16x16x32_bf16 v[116:119], a[60:63], a[148:151], v[116:119]
buffer_load_dwordx4 v244, s[16:19], s40 offen offset:3072 sc1 lds
v_mfma_f32_16x16x32_bf16 v[120:123], a[60:63], a[152:155], v[120:123]
s_add_u32 s40, s40, 0x10000
v_mfma_f32_16x16x32_bf16 v[124:127], a[60:63], a[156:159], v[124:127]
s_cmp_eq_u32 s40, s45
s_cselect_b32 s40, s52, s40
s_cselect_b32 s45, s57, s45
s_waitcnt lgkmcnt(0)
v_mfma_f32_16x16x32_bf16 v[0:3], a[0:3], a[128:131], v[0:3]
ds_read_b64_tr_b16 a[32:33], v198 offset:8192
s_xor_b32 s42, s42, 0x8000
v_mfma_f32_16x16x32_bf16 v[4:7], a[0:3], a[132:135], v[4:7]
ds_read_b64_tr_b16 a[34:35], v198 offset:9216
s_add_u32 s61, s63, 12288
buffer_store_dwordx4 a[212:215], v252, s[8:11], s61 offen sc1
v_mfma_f32_16x16x32_bf16 v[8:11], a[0:3], a[136:139], v[8:11]
ds_read_b128 a[144:147], v197 offset:0
v_mfma_f32_16x16x32_bf16 v[12:15], a[0:3], a[140:143], v[12:15]
ds_read_b128 a[148:151], v197 offset:2048
v_mfma_f32_16x16x32_bf16 v[16:19], a[4:7], a[128:131], v[16:19]
ds_read_b128 a[152:155], v197 offset:4096
v_mfma_f32_16x16x32_bf16 v[20:23], a[4:7], a[132:135], v[20:23]
ds_read_b128 a[156:159], v197 offset:6144
v_mfma_f32_16x16x32_bf16 v[24:27], a[4:7], a[136:139], v[24:27]
ds_read_b64_tr_b16 a[36:37], v199 offset:8320
v_mfma_f32_16x16x32_bf16 v[28:31], a[4:7], a[140:143], v[28:31]
ds_read_b64_tr_b16 a[38:39], v199 offset:9344
v_mfma_f32_16x16x32_bf16 v[32:35], a[8:11], a[128:131], v[32:35]
ds_read_b64_tr_b16 a[40:41], v198 offset:8448
v_mfma_f32_16x16x32_bf16 v[36:39], a[8:11], a[132:135], v[36:39]
ds_read_b64_tr_b16 a[42:43], v198 offset:9472
s_add_u32 s61, s63, 13312
buffer_store_dwordx4 a[216:219], v252, s[8:11], s61 offen sc1
v_mfma_f32_16x16x32_bf16 v[40:43], a[8:11], a[136:139], v[40:43]
ds_read_b64_tr_b16 a[44:45], v199 offset:8576
v_mfma_f32_16x16x32_bf16 v[44:47], a[8:11], a[140:143], v[44:47]
ds_read_b64_tr_b16 a[46:47], v199 offset:9600
v_mfma_f32_16x16x32_bf16 v[48:51], a[12:15], a[128:131], v[48:51]
ds_read_b64_tr_b16 a[48:49], v198 offset:8704
v_mfma_f32_16x16x32_bf16 v[52:55], a[12:15], a[132:135], v[52:55]
ds_read_b64_tr_b16 a[50:51], v198 offset:9728
v_mfma_f32_16x16x32_bf16 v[56:59], a[12:15], a[136:139], v[56:59]
ds_read_b64_tr_b16 a[52:53], v199 offset:8832
v_mfma_f32_16x16x32_bf16 v[60:63], a[12:15], a[140:143], v[60:63]
ds_read_b64_tr_b16 a[54:55], v199 offset:9856
v_mfma_f32_16x16x32_bf16 v[64:67], a[16:19], a[128:131], v[64:67]
ds_read_b64_tr_b16 a[56:57], v198 offset:8960
v_mfma_f32_16x16x32_bf16 v[68:71], a[16:19], a[132:135], v[68:71]
ds_read_b64_tr_b16 a[58:59], v198 offset:9984
v_mfma_f32_16x16x32_bf16 v[72:75], a[16:19], a[136:139], v[72:75]
ds_read_b64_tr_b16 a[60:61], v199 offset:9088
v_mfma_f32_16x16x32_bf16 v[76:79], a[16:19], a[140:143], v[76:79]
ds_read_b64_tr_b16 a[62:63], v199 offset:10112
v_mfma_f32_16x16x32_bf16 v[80:83], a[20:23], a[128:131], v[80:83]
v_mfma_f32_16x16x32_bf16 v[84:87], a[20:23], a[132:135], v[84:87]
v_mfma_f32_16x16x32_bf16 v[88:91], a[20:23], a[136:139], v[88:91]
v_mfma_f32_16x16x32_bf16 v[92:95], a[20:23], a[140:143], v[92:95]
v_mfma_f32_16x16x32_bf16 v[96:99], a[24:27], a[128:131], v[96:99]
v_mfma_f32_16x16x32_bf16 v[100:103], a[24:27], a[132:135], v[100:103]
v_mfma_f32_16x16x32_bf16 v[104:107], a[24:27], a[136:139], v[104:107]
v_mfma_f32_16x16x32_bf16 v[108:111], a[24:27], a[140:143], v[108:111]
v_mfma_f32_16x16x32_bf16 v[112:115], a[28:31], a[128:131], v[112:115]
v_mfma_f32_16x16x32_bf16 v[116:119], a[28:31], a[132:135], v[116:119]
v_mfma_f32_16x16x32_bf16 v[120:123], a[28:31], a[136:139], v[120:123]
v_mfma_f32_16x16x32_bf16 v[124:127], a[28:31], a[140:143], v[124:127]
s_waitcnt lgkmcnt(0)
s_barrier
v_mfma_f32_16x16x32_bf16 v[0:3], a[32:35], a[144:147], v[0:3]
s_mov_b32 m0, s43
v_mfma_f32_16x16x32_bf16 v[4:7], a[32:35], a[148:151], v[4:7]
buffer_load_dwordx4 v252, s[16:19], s41 offen offset:0 sc1 lds
v_mfma_f32_16x16x32_bf16 v[8:11], a[32:35], a[152:155], v[8:11]
v_mfma_f32_16x16x32_bf16 v[12:15], a[32:35], a[156:159], v[12:15]
v_mfma_f32_16x16x32_bf16 v[16:19], a[36:39], a[144:147], v[16:19]
buffer_load_dwordx4 v252, s[16:19], s41 offen offset:1024 sc1 lds
v_mfma_f32_16x16x32_bf16 v[20:23], a[36:39], a[148:151], v[20:23]
v_mfma_f32_16x16x32_bf16 v[24:27], a[36:39], a[152:155], v[24:27]
v_mfma_f32_16x16x32_bf16 v[28:31], a[36:39], a[156:159], v[28:31]
buffer_load_dwordx4 v252, s[16:19], s41 offen offset:2048 sc1 lds
v_mfma_f32_16x16x32_bf16 v[32:35], a[40:43], a[144:147], v[32:35]
v_mfma_f32_16x16x32_bf16 v[36:39], a[40:43], a[148:151], v[36:39]
v_mfma_f32_16x16x32_bf16 v[40:43], a[40:43], a[152:155], v[40:43]
buffer_load_dwordx4 v252, s[16:19], s41 offen offset:3072 sc1 lds
v_mfma_f32_16x16x32_bf16 v[44:47], a[40:43], a[156:159], v[44:47]
v_mfma_f32_16x16x32_bf16 v[48:51], a[44:47], a[144:147], v[48:51]
s_add_u32 m0, s43, 4096
v_mfma_f32_16x16x32_bf16 v[52:55], a[44:47], a[148:151], v[52:55]
buffer_load_dwordx4 v244, s[16:19], s41 offen offset:0 sc1 lds
v_mfma_f32_16x16x32_bf16 v[56:59], a[44:47], a[152:155], v[56:59]
v_mfma_f32_16x16x32_bf16 v[60:63], a[44:47], a[156:159], v[60:63]
v_mfma_f32_16x16x32_bf16 v[64:67], a[48:51], a[144:147], v[64:67]
buffer_load_dwordx4 v244, s[16:19], s41 offen offset:1024 sc1 lds
v_mfma_f32_16x16x32_bf16 v[68:71], a[48:51], a[148:151], v[68:71]
v_mfma_f32_16x16x32_bf16 v[72:75], a[48:51], a[152:155], v[72:75]
v_mfma_f32_16x16x32_bf16 v[76:79], a[48:51], a[156:159], v[76:79]
buffer_load_dwordx4 v244, s[16:19], s41 offen offset:2048 sc1 lds
v_mfma_f32_16x16x32_bf16 v[80:83], a[52:55], a[144:147], v[80:83]
v_mfma_f32_16x16x32_bf16 v[84:87], a[52:55], a[148:151], v[84:87]
v_mfma_f32_16x16x32_bf16 v[88:91], a[52:55], a[152:155], v[88:91]
buffer_load_dwordx4 v244, s[16:19], s41 offen offset:3072 sc1 lds
v_mfma_f32_16x16x32_bf16 v[92:95], a[52:55], a[156:159], v[92:95]
s_add_u32 s41, s41, 0x10000
v_mfma_f32_16x16x32_bf16 v[96:99], a[56:59], a[144:147], v[96:99]
s_cmp_eq_u32 s41, s47
s_cselect_b32 s41, s56, s41
s_cselect_b32 s47, s58, s47
s_add_u32 s61, s63, 14336
buffer_store_dwordx4 a[220:223], v252, s[8:11], s61 offen sc1
v_mfma_f32_16x16x32_bf16 v[100:103], a[56:59], a[148:151], v[100:103]
s_xor_b32 s43, s43, 0x8000
v_mfma_f32_16x16x32_bf16 v[104:107], a[56:59], a[152:155], v[104:107]
v_mfma_f32_16x16x32_bf16 v[108:111], a[56:59], a[156:159], v[108:111]
s_add_u32 s61, s63, 15360
buffer_store_dwordx4 a[224:227], v252, s[8:11], s61 offen sc1
v_mfma_f32_16x16x32_bf16 v[112:115], a[60:63], a[144:147], v[112:115]
v_mfma_f32_16x16x32_bf16 v[116:119], a[60:63], a[148:151], v[116:119]
v_mfma_f32_16x16x32_bf16 v[120:123], a[60:63], a[152:155], v[120:123]
v_mfma_f32_16x16x32_bf16 v[124:127], a[60:63], a[156:159], v[124:127]
s_mov_b32 s60, 0x3e0293ee
s_mov_b32 s61, 0x3e0293ee
v_mov_b32 v226, 0x3f803f80
s_nop 2
v_accvgpr_write_b32 a160, v226
v_accvgpr_write_b32 a161, v226
v_accvgpr_write_b32 a162, v226
v_accvgpr_write_b32 a163, v226
v_pk_mul_f32 v[8:9], v[8:9], s[60:61]
v_pk_mul_f32 v[10:11], v[10:11], s[60:61]
v_pk_mul_f32 v[24:25], v[24:25], s[60:61]
v_pk_mul_f32 v[26:27], v[26:27], s[60:61]
v_cvt_pk_bf16_f32 v8, v8, v9
v_cvt_pk_bf16_f32 v9, v10, v11
v_cvt_pk_bf16_f32 v10, v24, v25
v_cvt_pk_bf16_f32 v11, v26, v27
v_accvgpr_write_b32 a128, v8
v_accvgpr_write_b32 a129, v9
v_accvgpr_write_b32 a130, v10
v_accvgpr_write_b32 a131, v11
v_pk_mul_f32 v[40:41], v[40:41], s[60:61]
v_pk_mul_f32 v[42:43], v[42:43], s[60:61]
v_pk_mul_f32 v[56:57], v[56:57], s[60:61]
v_pk_mul_f32 v[58:59], v[58:59], s[60:61]
v_cvt_pk_bf16_f32 v40, v40, v41
v_cvt_pk_bf16_f32 v41, v42, v43
v_cvt_pk_bf16_f32 v42, v56, v57
v_cvt_pk_bf16_f32 v43, v58, v59
v_accvgpr_write_b32 a132, v40
v_accvgpr_write_b32 a133, v41
v_accvgpr_write_b32 a134, v42
v_accvgpr_write_b32 a135, v43
v_pk_mul_f32 v[72:73], v[72:73], s[60:61]
v_pk_mul_f32 v[74:75], v[74:75], s[60:61]
v_pk_mul_f32 v[88:89], v[88:89], s[60:61]
v_pk_mul_f32 v[90:91], v[90:91], s[60:61]
v_cvt_pk_bf16_f32 v72, v72, v73
v_cvt_pk_bf16_f32 v73, v74, v75
v_cvt_pk_bf16_f32 v74, v88, v89
v_cvt_pk_bf16_f32 v75, v90, v91
v_accvgpr_write_b32 a136, v72
v_accvgpr_write_b32 a137, v73
v_accvgpr_write_b32 a138, v74
v_accvgpr_write_b32 a139, v75
v_pk_mul_f32 v[104:105], v[104:105], s[60:61]
v_pk_mul_f32 v[106:107], v[106:107], s[60:61]
v_pk_mul_f32 v[120:121], v[120:121], s[60:61]
v_pk_mul_f32 v[122:123], v[122:123], s[60:61]
v_cvt_pk_bf16_f32 v104, v104, v105
v_cvt_pk_bf16_f32 v105, v106, v107
v_cvt_pk_bf16_f32 v106, v120, v121
v_cvt_pk_bf16_f32 v107, v122, v123
v_accvgpr_write_b32 a140, v104
v_accvgpr_write_b32 a141, v105
v_accvgpr_write_b32 a142, v106
v_accvgpr_write_b32 a143, v107
v_pk_mul_f32 v[12:13], v[12:13], s[60:61]
v_pk_mul_f32 v[14:15], v[14:15], s[60:61]
v_pk_mul_f32 v[28:29], v[28:29], s[60:61]
v_pk_mul_f32 v[30:31], v[30:31], s[60:61]
v_cvt_pk_bf16_f32 v12, v12, v13
v_cvt_pk_bf16_f32 v13, v14, v15
v_cvt_pk_bf16_f32 v14, v28, v29
v_cvt_pk_bf16_f32 v15, v30, v31
v_accvgpr_write_b32 a144, v12
v_accvgpr_write_b32 a145, v13
v_accvgpr_write_b32 a146, v14
v_accvgpr_write_b32 a147, v15
v_pk_mul_f32 v[44:45], v[44:45], s[60:61]
v_pk_mul_f32 v[46:47], v[46:47], s[60:61]
v_pk_mul_f32 v[60:61], v[60:61], s[60:61]
v_pk_mul_f32 v[62:63], v[62:63], s[60:61]
v_cvt_pk_bf16_f32 v44, v44, v45
v_cvt_pk_bf16_f32 v45, v46, v47
v_cvt_pk_bf16_f32 v46, v60, v61
v_cvt_pk_bf16_f32 v47, v62, v63
v_accvgpr_write_b32 a148, v44
v_accvgpr_write_b32 a149, v45
v_accvgpr_write_b32 a150, v46
v_accvgpr_write_b32 a151, v47
v_pk_mul_f32 v[76:77], v[76:77], s[60:61]
v_pk_mul_f32 v[78:79], v[78:79], s[60:61]
v_pk_mul_f32 v[92:93], v[92:93], s[60:61]
v_pk_mul_f32 v[94:95], v[94:95], s[60:61]
v_cvt_pk_bf16_f32 v76, v76, v77
v_cvt_pk_bf16_f32 v77, v78, v79
v_cvt_pk_bf16_f32 v78, v92, v93
v_cvt_pk_bf16_f32 v79, v94, v95
v_accvgpr_write_b32 a152, v76
v_accvgpr_write_b32 a153, v77
v_accvgpr_write_b32 a154, v78
v_accvgpr_write_b32 a155, v79
v_pk_mul_f32 v[108:109], v[108:109], s[60:61]
v_pk_mul_f32 v[110:111], v[110:111], s[60:61]
v_pk_mul_f32 v[124:125], v[124:125], s[60:61]
v_pk_mul_f32 v[126:127], v[126:127], s[60:61]
v_cvt_pk_bf16_f32 v108, v108, v109
v_cvt_pk_bf16_f32 v109, v110, v111
v_cvt_pk_bf16_f32 v110, v124, v125
v_cvt_pk_bf16_f32 v111, v126, v127
v_accvgpr_write_b32 a156, v108
v_accvgpr_write_b32 a157, v109
v_accvgpr_write_b32 a158, v110
v_accvgpr_write_b32 a159, v111
v_pk_mul_f32 v[0:1], v[0:1], s[60:61]
v_pk_mul_f32 v[2:3], v[2:3], s[60:61]
v_pk_mul_f32 v[16:17], v[16:17], s[60:61]
v_pk_mul_f32 v[18:19], v[18:19], s[60:61]
v_cvt_pk_bf16_f32 v160, v0, v1
v_cvt_pk_bf16_f32 v161, v2, v3
v_cvt_pk_bf16_f32 v162, v16, v17
v_cvt_pk_bf16_f32 v163, v18, v19
v_pk_mul_f32 v[32:33], v[32:33], s[60:61]
v_pk_mul_f32 v[34:35], v[34:35], s[60:61]
v_pk_mul_f32 v[48:49], v[48:49], s[60:61]
v_pk_mul_f32 v[50:51], v[50:51], s[60:61]
v_cvt_pk_bf16_f32 v164, v32, v33
v_cvt_pk_bf16_f32 v165, v34, v35
v_cvt_pk_bf16_f32 v166, v48, v49
v_cvt_pk_bf16_f32 v167, v50, v51
v_pk_mul_f32 v[64:65], v[64:65], s[60:61]
v_pk_mul_f32 v[66:67], v[66:67], s[60:61]
v_pk_mul_f32 v[80:81], v[80:81], s[60:61]
v_pk_mul_f32 v[82:83], v[82:83], s[60:61]
v_cvt_pk_bf16_f32 v168, v64, v65
v_cvt_pk_bf16_f32 v169, v66, v67
v_cvt_pk_bf16_f32 v170, v80, v81
v_cvt_pk_bf16_f32 v171, v82, v83
v_pk_mul_f32 v[96:97], v[96:97], s[60:61]
v_pk_mul_f32 v[98:99], v[98:99], s[60:61]
v_pk_mul_f32 v[112:113], v[112:113], s[60:61]
v_pk_mul_f32 v[114:115], v[114:115], s[60:61]
v_cvt_pk_bf16_f32 v172, v96, v97
v_cvt_pk_bf16_f32 v173, v98, v99
v_cvt_pk_bf16_f32 v174, v112, v113
v_cvt_pk_bf16_f32 v175, v114, v115
v_pk_mul_f32 v[4:5], v[4:5], s[60:61]
v_pk_mul_f32 v[6:7], v[6:7], s[60:61]
v_pk_mul_f32 v[20:21], v[20:21], s[60:61]
v_pk_mul_f32 v[22:23], v[22:23], s[60:61]
v_cvt_pk_bf16_f32 v176, v4, v5
v_cvt_pk_bf16_f32 v177, v6, v7
v_cvt_pk_bf16_f32 v178, v20, v21
v_cvt_pk_bf16_f32 v179, v22, v23
v_pk_mul_f32 v[36:37], v[36:37], s[60:61]
v_pk_mul_f32 v[38:39], v[38:39], s[60:61]
v_pk_mul_f32 v[52:53], v[52:53], s[60:61]
v_pk_mul_f32 v[54:55], v[54:55], s[60:61]
v_cvt_pk_bf16_f32 v180, v36, v37
v_cvt_pk_bf16_f32 v181, v38, v39
v_cvt_pk_bf16_f32 v182, v52, v53
v_cvt_pk_bf16_f32 v183, v54, v55
v_pk_mul_f32 v[68:69], v[68:69], s[60:61]
v_pk_mul_f32 v[70:71], v[70:71], s[60:61]
v_pk_mul_f32 v[84:85], v[84:85], s[60:61]
v_pk_mul_f32 v[86:87], v[86:87], s[60:61]
v_cvt_pk_bf16_f32 v184, v68, v69
v_cvt_pk_bf16_f32 v185, v70, v71
v_cvt_pk_bf16_f32 v186, v84, v85
v_cvt_pk_bf16_f32 v187, v86, v87
v_pk_mul_f32 v[100:101], v[100:101], s[60:61]
v_pk_mul_f32 v[102:103], v[102:103], s[60:61]
v_pk_mul_f32 v[116:117], v[116:117], s[60:61]
v_pk_mul_f32 v[118:119], v[118:119], s[60:61]
v_cvt_pk_bf16_f32 v188, v100, v101
v_cvt_pk_bf16_f32 v189, v102, v103
v_cvt_pk_bf16_f32 v190, v116, v117
v_cvt_pk_bf16_f32 v191, v118, v119
s_waitcnt vmcnt(12)
s_barrier
s_mov_b32 s35, 0
s_mov_b32 s51, 0
s_sub_u32 s55, 15, s27
K2_BLOCK:
s_mov_b32 s48, s49
ds_read_b128 a[0:3], v220 offset:0
ds_read_b128 a[4:7], v220 offset:1024
ds_read_b128 a[8:11], v220 offset:2048
ds_read_b128 a[12:15], v220 offset:3072
ds_read_b128 a[16:19], v220 offset:4096
ds_read_b128 a[20:23], v220 offset:5120
ds_read_b128 a[24:27], v220 offset:6144
ds_read_b128 a[28:31], v220 offset:7168
s_cmp_eq_u32 s48, 1
s_cbranch_scc1 K2_F1M
s_waitcnt lgkmcnt(4)
v_mfma_f32_16x16x32_bf16 v[0:3], a[0:3], v[160:163], 0
v_mfma_f32_16x16x32_bf16 v[4:7], a[0:3], v[176:179], 0
ds_read_b128 a[32:35], v220 offset:8192
v_mfma_f32_16x16x32_bf16 v[0:3], a[4:7], v[164:167], v[0:3]
s_mov_b32 m0, s42
v_mfma_f32_16x16x32_bf16 v[4:7], a[4:7], v[180:183], v[4:7]
ds_read_b128 a[36:39], v220 offset:9216
buffer_load_dwordx4 v252, s[16:19], s40 offen offset:0 sc1 lds
v_mfma_f32_16x16x32_bf16 v[0:3], a[8:11], v[168:171], v[0:3]
v_mfma_f32_16x16x32_bf16 v[4:7], a[8:11], v[184:187], v[4:7]
ds_read_b128 a[40:43], v220 offset:10240
v_mfma_f32_16x16x32_bf16 v[0:3], a[12:15], v[172:175], v[0:3]
v_mfma_f32_16x16x32_bf16 v[4:7], a[12:15], v[188:191], v[4:7]
ds_read_b128 a[44:47], v220 offset:11264
buffer_load_dwordx4 v252, s[16:19], s40 offen offset:1024 sc1 lds
s_waitcnt lgkmcnt(4)
v_mfma_f32_16x16x32_bf16 v[8:11], a[16:19], v[160:163], 0
v_mfma_f32_16x16x32_bf16 v[12:15], a[16:19], v[176:179], 0
ds_read_b128 a[48:51], v220 offset:12288
v_mfma_f32_16x16x32_bf16 v[8:11], a[20:23], v[164:167], v[8:11]
v_mfma_f32_16x16x32_bf16 v[12:15], a[20:23], v[180:183], v[12:15]
ds_read_b128 a[52:55], v220 offset:13312
buffer_load_dwordx4 v252, s[16:19], s40 offen offset:2048 sc1 lds
v_mfma_f32_16x16x32_bf16 v[8:11], a[24:27], v[168:171], v[8:11]
v_max3_f32 v216, v0, v1, v2
v_mfma_f32_16x16x32_bf16 v[12:15], a[24:27], v[184:187], v[12:15]
ds_read_b128 a[56:59], v220 offset:14336
v_max_f32 v216, v216, v3
v_mfma_f32_16x16x32_bf16 v[8:11], a[28:31], v[172:175], v[8:11]
v_max3_f32 v218, v4, v5, v6
v_mfma_f32_16x16x32_bf16 v[12:15], a[28:31], v[188:191], v[12:15]
ds_read_b128 a[60:63], v220 offset:15360
buffer_load_dwordx4 v252, s[16:19], s40 offen offset:3072 sc1 lds
v_max_f32 v218, v218, v7
s_add_u32 m0, s42, 4096
s_waitcnt lgkmcnt(4)
v_mfma_f32_16x16x32_bf16 v[16:19], a[32:35], v[160:163], 0
v_mfma_f32_16x16x32_bf16 v[20:23], a[32:35], v[176:179], 0
ds_read_b128 a[0:3], v220 offset:16384
v_mfma_f32_16x16x32_bf16 v[16:19], a[36:39], v[164:167], v[16:19]
v_mfma_f32_16x16x32_bf16 v[20:23], a[36:39], v[180:183], v[20:23]
ds_read_b128 a[4:7], v220 offset:17408
buffer_load_dwordx4 v244, s[16:19], s40 offen offset:0 sc1 lds
v_mfma_f32_16x16x32_bf16 v[16:19], a[40:43], v[168:171], v[16:19]
v_max3_f32 v216, v216, v8, v9
v_mfma_f32_16x16x32_bf16 v[20:23], a[40:43], v[184:187], v[20:23]
ds_read_b128 a[8:11], v220 offset:18432
v_max3_f32 v216, v216, v10, v11
v_mfma_f32_16x16x32_bf16 v[16:19], a[44:47], v[172:175], v[16:19]
v_max3_f32 v218, v218, v12, v13
v_mfma_f32_16x16x32_bf16 v[20:23], a[44:47], v[188:191], v[20:23]
ds_read_b128 a[12:15], v220 offset:19456
buffer_load_dwordx4 v244, s[16:19], s40 offen offset:1024 sc1 lds
v_max3_f32 v218, v218, v14, v15
s_waitcnt lgkmcnt(4)
v_mfma_f32_16x16x32_bf16 v[24:27], a[48:51], v[160:163], 0
v_mfma_f32_16x16x32_bf16 v[28:31], a[48:51], v[176:179], 0
ds_read_b128 a[16:19], v220 offset:20480
v_mfma_f32_16x16x32_bf16 v[24:27], a[52:55], v[164:167], v[24:27]
v_mfma_f32_16x16x32_bf16 v[28:31], a[52:55], v[180:183], v[28:31]
ds_read_b128 a[20:23], v220 offset:21504
buffer_load_dwordx4 v244, s[16:19], s40 offen offset:2048 sc1 lds
v_mfma_f32_16x16x32_bf16 v[24:27], a[56:59], v[168:171], v[24:27]
v_max3_f32 v216, v216, v16, v17
v_mfma_f32_16x16x32_bf16 v[28:31], a[56:59], v[184:187], v[28:31]
ds_read_b128 a[24:27], v220 offset:22528
v_max3_f32 v216, v216, v18, v19
v_mfma_f32_16x16x32_bf16 v[24:27], a[60:63], v[172:175], v[24:27]
v_max3_f32 v218, v218, v20, v21
v_mfma_f32_16x16x32_bf16 v[28:31], a[60:63], v[188:191], v[28:31]
ds_read_b128 a[28:31], v220 offset:23552
buffer_load_dwordx4 v244, s[16:19], s40 offen offset:3072 sc1 lds
v_max3_f32 v218, v218, v22, v23
s_waitcnt lgkmcnt(4)
v_mfma_f32_16x16x32_bf16 v[32:35], a[0:3], v[160:163], 0
v_mfma_f32_16x16x32_bf16 v[36:39], a[0:3], v[176:179], 0
ds_read_b128 a[32:35], v220 offset:24576
s_add_u32 s40, s40, 0x10000
v_mfma_f32_16x16x32_bf16 v[32:35], a[4:7], v[164:167], v[32:35]
v_mfma_f32_16x16x32_bf16 v[36:39], a[4:7], v[180:183], v[36:39]
ds_read_b128 a[36:39], v220 offset:25600
s_cmp_eq_u32 s40, s45
s_cselect_b32 s40, s52, s40
s_cselect_b32 s45, s57, s45
v_mfma_f32_16x16x32_bf16 v[32:35], a[8:11], v[168:171], v[32:35]
v_max3_f32 v216, v216, v24, v25
v_mfma_f32_16x16x32_bf16 v[36:39], a[8:11], v[184:187], v[36:39]
ds_read_b128 a[40:43], v220 offset:26624
v_max3_f32 v216, v216, v26, v27
s_xor_b32 s42, s42, 0x8000
v_mfma_f32_16x16x32_bf16 v[32:35], a[12:15], v[172:175], v[32:35]
v_max3_f32 v218, v218, v28, v29
v_mfma_f32_16x16x32_bf16 v[36:39], a[12:15], v[188:191], v[36:39]
ds_read_b128 a[44:47], v220 offset:27648
v_max3_f32 v218, v218, v30, v31
s_waitcnt lgkmcnt(4)
v_mfma_f32_16x16x32_bf16 v[40:43], a[16:19], v[160:163], 0
v_mfma_f32_16x16x32_bf16 v[44:47], a[16:19], v[176:179], 0
ds_read_b128 a[48:51], v220 offset:28672
v_mfma_f32_16x16x32_bf16 v[40:43], a[20:23], v[164:167], v[40:43]
v_mfma_f32_16x16x32_bf16 v[44:47], a[20:23], v[180:183], v[44:47]
ds_read_b128 a[52:55], v220 offset:29696
v_mfma_f32_16x16x32_bf16 v[40:43], a[24:27], v[168:171], v[40:43]
v_max3_f32 v216, v216, v32, v33
v_mfma_f32_16x16x32_bf16 v[44:47], a[24:27], v[184:187], v[44:47]
ds_read_b128 a[56:59], v220 offset:30720
v_max3_f32 v216, v216, v34, v35
v_mfma_f32_16x16x32_bf16 v[40:43], a[28:31], v[172:175], v[40:43]
v_max3_f32 v218, v218, v36, v37
v_mfma_f32_16x16x32_bf16 v[44:47], a[28:31], v[188:191], v[44:47]
ds_read_b128 a[60:63], v220 offset:31744
v_max3_f32 v218, v218, v38, v39
s_waitcnt lgkmcnt(4)
v_mfma_f32_16x16x32_bf16 v[48:51], a[32:35], v[160:163], 0
v_mfma_f32_16x16x32_bf16 v[52:55], a[32:35], v[176:179], 0
v_xor_b32 v220, 0x8000, v220
v_mfma_f32_16x16x32_bf16 v[48:51], a[36:39], v[164:167], v[48:51]
v_mfma_f32_16x16x32_bf16 v[52:55], a[36:39], v[180:183], v[52:55]
v_mfma_f32_16x16x32_bf16 v[48:51], a[40:43], v[168:171], v[48:51]
v_max3_f32 v216, v216, v40, v41
v_mfma_f32_16x16x32_bf16 v[52:55], a[40:43], v[184:187], v[52:55]
v_max3_f32 v216, v216, v42, v43
v_mfma_f32_16x16x32_bf16 v[48:51], a[44:47], v[172:175], v[48:51]
v_max3_f32 v218, v218, v44, v45
v_mfma_f32_16x16x32_bf16 v[52:55], a[44:47], v[188:191], v[52:55]
v_max3_f32 v218, v218, v46, v47
s_waitcnt lgkmcnt(0)
v_mfma_f32_16x16x32_bf16 v[56:59], a[48:51], v[160:163], 0
v_mfma_f32_16x16x32_bf16 v[60:63], a[48:51], v[176:179], 0
v_mfma_f32_16x16x32_bf16 v[56:59], a[52:55], v[164:167], v[56:59]
v_mfma_f32_16x16x32_bf16 v[60:63], a[52:55], v[180:183], v[60:63]
v_mfma_f32_16x16x32_bf16 v[56:59], a[56:59], v[168:171], v[56:59]
v_mfma_f32_16x16x32_bf16 v[60:63], a[56:59], v[184:187], v[60:63]
v_mfma_f32_16x16x32_bf16 v[56:59], a[60:63], v[172:175], v[56:59]
v_mfma_f32_16x16x32_bf16 v[60:63], a[60:63], v[188:191], v[60:63]
s_nop 4
v_max3_f32 v216, v216, v48, v49
v_max3_f32 v216, v216, v50, v51
v_max3_f32 v218, v218, v52, v53
v_max3_f32 v218, v218, v54, v55
v_max3_f32 v216, v216, v56, v57
v_max3_f32 v216, v216, v58, v59
v_max3_f32 v218, v218, v60, v61
v_max3_f32 v218, v218, v62, v63
v_mov_b32 v226, v216
v_mov_b32 v227, v218
s_nop 0
v_permlane16_swap_b32 v226, v216
v_permlane16_swap_b32 v227, v218
v_max_f32 v216, v216, v226
v_max_f32 v218, v218, v227
v_mov_b32 v226, v216
v_mov_b32 v227, v218
s_nop 0
v_permlane32_swap_b32 v226, v216
v_permlane32_swap_b32 v227, v218
v_max_f32 v216, v216, v226
v_max_f32 v218, v218, v227
v_max_f32 v216, 0xf149f2ca, v216
v_max_f32 v218, 0xf149f2ca, v218
v_pk_add_f32 v[0:1], v[0:1], v[216:217] op_sel_hi:[1,0] neg_lo:[0,1] neg_hi:[0,1]
v_pk_add_f32 v[2:3], v[2:3], v[216:217] op_sel_hi:[1,0] neg_lo:[0,1] neg_hi:[0,1]
v_pk_add_f32 v[4:5], v[4:5], v[218:219] op_sel_hi:[1,0] neg_lo:[0,1] neg_hi:[0,1]
v_pk_add_f32 v[6:7], v[6:7], v[218:219] op_sel_hi:[1,0] neg_lo:[0,1] neg_hi:[0,1]
v_pk_add_f32 v[8:9], v[8:9], v[216:217] op_sel_hi:[1,0] neg_lo:[0,1] neg_hi:[0,1]
v_pk_add_f32 v[10:11], v[10:11], v[216:217] op_sel_hi:[1,0] neg_lo:[0,1] neg_hi:[0,1]
v_pk_add_f32 v[12:13], v[12:13], v[218:219] op_sel_hi:[1,0] neg_lo:[0,1] neg_hi:[0,1]
v_pk_add_f32 v[14:15], v[14:15], v[218:219] op_sel_hi:[1,0] neg_lo:[0,1] neg_hi:[0,1]
v_pk_add_f32 v[16:17], v[16:17], v[216:217] op_sel_hi:[1,0] neg_lo:[0,1] neg_hi:[0,1]
v_pk_add_f32 v[18:19], v[18:19], v[216:217] op_sel_hi:[1,0] neg_lo:[0,1] neg_hi:[0,1]
v_pk_add_f32 v[20:21], v[20:21], v[218:219] op_sel_hi:[1,0] neg_lo:[0,1] neg_hi:[0,1]
v_pk_add_f32 v[22:23], v[22:23], v[218:219] op_sel_hi:[1,0] neg_lo:[0,1] neg_hi:[0,1]
v_pk_add_f32 v[24:25], v[24:25], v[216:217] op_sel_hi:[1,0] neg_lo:[0,1] neg_hi:[0,1]
v_pk_add_f32 v[26:27], v[26:27], v[216:217] op_sel_hi:[1,0] neg_lo:[0,1] neg_hi:[0,1]
v_pk_add_f32 v[28:29], v[28:29], v[218:219] op_sel_hi:[1,0] neg_lo:[0,1] neg_hi:[0,1]
v_pk_add_f32 v[30:31], v[30:31], v[218:219] op_sel_hi:[1,0] neg_lo:[0,1] neg_hi:[0,1]
v_pk_add_f32 v[32:33], v[32:33], v[216:217] op_sel_hi:[1,0] neg_lo:[0,1] neg_hi:[0,1]
v_pk_add_f32 v[34:35], v[34:35], v[216:217] op_sel_hi:[1,0] neg_lo:[0,1] neg_hi:[0,1]
v_pk_add_f32 v[36:37], v[36:37], v[218:219] op_sel_hi:[1,0] neg_lo:[0,1] neg_hi:[0,1]
v_pk_add_f32 v[38:39], v[38:39], v[218:219] op_sel_hi:[1,0] neg_lo:[0,1] neg_hi:[0,1]
v_pk_add_f32 v[40:41], v[40:41], v[216:217] op_sel_hi:[1,0] neg_lo:[0,1] neg_hi:[0,1]
v_pk_add_f32 v[42:43], v[42:43], v[216:217] op_sel_hi:[1,0] neg_lo:[0,1] neg_hi:[0,1]
v_pk_add_f32 v[44:45], v[44:45], v[218:219] op_sel_hi:[1,0] neg_lo:[0,1] neg_hi:[0,1]
v_pk_add_f32 v[46:47], v[46:47], v[218:219] op_sel_hi:[1,0] neg_lo:[0,1] neg_hi:[0,1]
v_pk_add_f32 v[48:49], v[48:49], v[216:217] op_sel_hi:[1,0] neg_lo:[0,1] neg_hi:[0,1]
v_pk_add_f32 v[50:51], v[50:51], v[216:217] op_sel_hi:[1,0] neg_lo:[0,1] neg_hi:[0,1]
v_pk_add_f32 v[52:53], v[52:53], v[218:219] op_sel_hi:[1,0] neg_lo:[0,1] neg_hi:[0,1]
v_pk_add_f32 v[54:55], v[54:55], v[218:219] op_sel_hi:[1,0] neg_lo:[0,1] neg_hi:[0,1]
v_pk_add_f32 v[56:57], v[56:57], v[216:217] op_sel_hi:[1,0] neg_lo:[0,1] neg_hi:[0,1]
v_pk_add_f32 v[58:59], v[58:59], v[216:217] op_sel_hi:[1,0] neg_lo:[0,1] neg_hi:[0,1]
v_pk_add_f32 v[60:61], v[60:61], v[218:219] op_sel_hi:[1,0] neg_lo:[0,1] neg_hi:[0,1]
v_pk_add_f32 v[62:63], v[62:63], v[218:219] op_sel_hi:[1,0] neg_lo:[0,1] neg_hi:[0,1]
v_pk_mul_f32 v[192:193], v[216:217], -1.0 op_sel_hi:[0,0]
v_pk_mul_f32 v[194:195], v[216:217], -1.0 op_sel_hi:[0,0]
v_pk_mul_f32 v[196:197], v[218:219], -1.0 op_sel_hi:[0,0]
v_pk_mul_f32 v[198:199], v[218:219], -1.0 op_sel_hi:[0,0]
s_branch K2_F1E
K2_F1M:
v_mov_b32 v192, 0
v_mov_b32 v193, 0
v_mov_b32 v194, 0
v_mov_b32 v195, 0
v_mov_b32 v196, 0
v_mov_b32 v197, 0
v_mov_b32 v198, 0
v_mov_b32 v199, 0
v_mov_b32 v226, 0xff800000
v_lshrrev_b32 v227, 4, v253
v_lshlrev_b32 v227, 2, v227
v_and_b32 v228, 15, v253
v_sub_u32 v228, v228, v227
v_cmp_gt_i32 vcc, 0, v228
s_nop 1
v_cndmask_b32 v200, v192, v226, vcc
v_cndmask_b32 v204, v196, v226, vcc
v_cmp_gt_i32 vcc, 1, v228
s_nop 1
v_cndmask_b32 v201, v193, v226, vcc
v_cndmask_b32 v205, v197, v226, vcc
v_cmp_gt_i32 vcc, 2, v228
s_nop 1
v_cndmask_b32 v202, v194, v226, vcc
v_cndmask_b32 v206, v198, v226, vcc
v_cmp_gt_i32 vcc, 3, v228
s_nop 1
v_cndmask_b32 v203, v195, v226, vcc
v_cndmask_b32 v207, v199, v226, vcc
s_nop 1
s_cmp_lg_u32 s7, 0
s_cbranch_scc1 K2_F1M1
s_waitcnt lgkmcnt(4)
v_mfma_f32_16x16x32_bf16 v[0:3], a[0:3], v[160:163], v[200:203]
v_mfma_f32_16x16x32_bf16 v[4:7], a[0:3], v[176:179], 0
ds_read_b128 a[32:35], v220 offset:8192
v_mfma_f32_16x16x32_bf16 v[0:3], a[4:7], v[164:167], v[0:3]
v_mov_b32 v8, v226
s_mov_b32 m0, s42
v_mfma_f32_16x16x32_bf16 v[4:7], a[4:7], v[180:183], v[4:7]
ds_read_b128 a[36:39], v220 offset:9216
buffer_load_dwordx4 v252, s[16:19], s40 offen offset:0 sc1 lds
v_mov_b32 v9, v226
v_mfma_f32_16x16x32_bf16 v[0:3], a[8:11], v[168:171], v[0:3]
v_mov_b32 v10, v226
v_mfma_f32_16x16x32_bf16 v[4:7], a[8:11], v[184:187], v[4:7]
ds_read_b128 a[40:43], v220 offset:10240
v_mov_b32 v11, v226
v_mfma_f32_16x16x32_bf16 v[0:3], a[12:15], v[172:175], v[0:3]
v_mov_b32 v16, v226
v_mfma_f32_16x16x32_bf16 v[4:7], a[12:15], v[188:191], v[4:7]
ds_read_b128 a[44:47], v220 offset:11264
buffer_load_dwordx4 v252, s[16:19], s40 offen offset:1024 sc1 lds
v_mov_b32 v17, v226
v_mov_b32 v18, v226
s_waitcnt lgkmcnt(7)
v_mfma_f32_16x16x32_bf16 v[12:15], a[16:19], v[176:179], 0
ds_read_b128 a[48:51], v220 offset:12288
v_mov_b32 v19, v226
v_mov_b32 v24, v226
s_waitcnt lgkmcnt(7)
v_mfma_f32_16x16x32_bf16 v[12:15], a[20:23], v[180:183], v[12:15]
ds_read_b128 a[52:55], v220 offset:13312
buffer_load_dwordx4 v252, s[16:19], s40 offen offset:2048 sc1 lds
v_mov_b32 v25, v226
v_mov_b32 v26, v226
s_waitcnt lgkmcnt(7)
v_mfma_f32_16x16x32_bf16 v[12:15], a[24:27], v[184:187], v[12:15]
ds_read_b128 a[56:59], v220 offset:14336
v_mov_b32 v27, v226
v_mov_b32 v32, v226
s_waitcnt lgkmcnt(7)
v_mfma_f32_16x16x32_bf16 v[12:15], a[28:31], v[188:191], v[12:15]
ds_read_b128 a[60:63], v220 offset:15360
buffer_load_dwordx4 v252, s[16:19], s40 offen offset:3072 sc1 lds
v_mov_b32 v33, v226
s_add_u32 m0, s42, 4096
v_mov_b32 v34, v226
s_waitcnt lgkmcnt(7)
v_mfma_f32_16x16x32_bf16 v[20:23], a[32:35], v[176:179], 0
ds_read_b128 a[0:3], v220 offset:16384
v_mov_b32 v35, v226
v_mov_b32 v40, v226
s_waitcnt lgkmcnt(7)
v_mfma_f32_16x16x32_bf16 v[20:23], a[36:39], v[180:183], v[20:23]
ds_read_b128 a[4:7], v220 offset:17408
buffer_load_dwordx4 v244, s[16:19], s40 offen offset:0 sc1 lds
v_mov_b32 v41, v226
v_mov_b32 v42, v226
s_waitcnt lgkmcnt(7)
v_mfma_f32_16x16x32_bf16 v[20:23], a[40:43], v[184:187], v[20:23]
ds_read_b128 a[8:11], v220 offset:18432
v_mov_b32 v43, v226
v_mov_b32 v48, v226
s_waitcnt lgkmcnt(7)
v_mfma_f32_16x16x32_bf16 v[20:23], a[44:47], v[188:191], v[20:23]
ds_read_b128 a[12:15], v220 offset:19456
buffer_load_dwordx4 v244, s[16:19], s40 offen offset:1024 sc1 lds
v_mov_b32 v49, v226
v_mov_b32 v50, v226
s_waitcnt lgkmcnt(7)
v_mfma_f32_16x16x32_bf16 v[28:31], a[48:51], v[176:179], 0
ds_read_b128 a[16:19], v220 offset:20480
v_mov_b32 v51, v226
v_mov_b32 v56, v226
s_waitcnt lgkmcnt(7)
v_mfma_f32_16x16x32_bf16 v[28:31], a[52:55], v[180:183], v[28:31]
ds_read_b128 a[20:23], v220 offset:21504
buffer_load_dwordx4 v244, s[16:19], s40 offen offset:2048 sc1 lds
v_mov_b32 v57, v226
v_mov_b32 v58, v226
s_waitcnt lgkmcnt(7)
v_mfma_f32_16x16x32_bf16 v[28:31], a[56:59], v[184:187], v[28:31]
ds_read_b128 a[24:27], v220 offset:22528
v_mov_b32 v59, v226
s_waitcnt lgkmcnt(7)
v_mfma_f32_16x16x32_bf16 v[28:31], a[60:63], v[188:191], v[28:31]
ds_read_b128 a[28:31], v220 offset:23552
buffer_load_dwordx4 v244, s[16:19], s40 offen offset:3072 sc1 lds
s_waitcnt lgkmcnt(7)
v_mfma_f32_16x16x32_bf16 v[36:39], a[0:3], v[176:179], 0
ds_read_b128 a[32:35], v220 offset:24576
s_add_u32 s40, s40, 0x10000
s_waitcnt lgkmcnt(7)
v_mfma_f32_16x16x32_bf16 v[36:39], a[4:7], v[180:183], v[36:39]
ds_read_b128 a[36:39], v220 offset:25600
s_cmp_eq_u32 s40, s45
s_cselect_b32 s40, s52, s40
s_cselect_b32 s45, s57, s45
s_waitcnt lgkmcnt(7)
v_mfma_f32_16x16x32_bf16 v[36:39], a[8:11], v[184:187], v[36:39]
ds_read_b128 a[40:43], v220 offset:26624
s_xor_b32 s42, s42, 0x8000
s_waitcnt lgkmcnt(7)
v_mfma_f32_16x16x32_bf16 v[36:39], a[12:15], v[188:191], v[36:39]
ds_read_b128 a[44:47], v220 offset:27648
s_waitcnt lgkmcnt(7)
v_mfma_f32_16x16x32_bf16 v[44:47], a[16:19], v[176:179], 0
ds_read_b128 a[48:51], v220 offset:28672
s_waitcnt lgkmcnt(7)
v_mfma_f32_16x16x32_bf16 v[44:47], a[20:23], v[180:183], v[44:47]
ds_read_b128 a[52:55], v220 offset:29696
s_waitcnt lgkmcnt(7)
v_mfma_f32_16x16x32_bf16 v[44:47], a[24:27], v[184:187], v[44:47]
ds_read_b128 a[56:59], v220 offset:30720
s_waitcnt lgkmcnt(7)
v_mfma_f32_16x16x32_bf16 v[44:47], a[28:31], v[188:191], v[44:47]
ds_read_b128 a[60:63], v220 offset:31744
s_waitcnt lgkmcnt(7)
v_mfma_f32_16x16x32_bf16 v[52:55], a[32:35], v[176:179], 0
v_xor_b32 v220, 0x8000, v220
s_waitcnt lgkmcnt(6)
v_mfma_f32_16x16x32_bf16 v[52:55], a[36:39], v[180:183], v[52:55]
s_waitcnt lgkmcnt(5)
v_mfma_f32_16x16x32_bf16 v[52:55], a[40:43], v[184:187], v[52:55]
s_waitcnt lgkmcnt(4)
v_mfma_f32_16x16x32_bf16 v[52:55], a[44:47], v[188:191], v[52:55]
s_waitcnt lgkmcnt(3)
v_mfma_f32_16x16x32_bf16 v[60:63], a[48:51], v[176:179], v[204:207]
s_waitcnt lgkmcnt(2)
v_mfma_f32_16x16x32_bf16 v[60:63], a[52:55], v[180:183], v[60:63]
s_waitcnt lgkmcnt(1)
v_mfma_f32_16x16x32_bf16 v[60:63], a[56:59], v[184:187], v[60:63]
s_waitcnt lgkmcnt(0)
v_mfma_f32_16x16x32_bf16 v[60:63], a[60:63], v[188:191], v[60:63]
s_branch K2_F1MX
K2_F1M1:
s_cmp_lg_u32 s7, 1
s_cbranch_scc1 K2_F1M2
s_waitcnt lgkmcnt(4)
v_mfma_f32_16x16x32_bf16 v[0:3], a[0:3], v[160:163], 0
v_mfma_f32_16x16x32_bf16 v[4:7], a[0:3], v[176:179], 0
ds_read_b128 a[32:35], v220 offset:8192
v_mfma_f32_16x16x32_bf16 v[0:3], a[4:7], v[164:167], v[0:3]
v_mov_b32 v16, v226
s_mov_b32 m0, s42
v_mfma_f32_16x16x32_bf16 v[4:7], a[4:7], v[180:183], v[4:7]
ds_read_b128 a[36:39], v220 offset:9216
buffer_load_dwordx4 v252, s[16:19], s40 offen offset:0 sc1 lds
v_mov_b32 v17, v226
v_mfma_f32_16x16x32_bf16 v[0:3], a[8:11], v[168:171], v[0:3]
v_mov_b32 v18, v226
v_mfma_f32_16x16x32_bf16 v[4:7], a[8:11], v[184:187], v[4:7]
ds_read_b128 a[40:43], v220 offset:10240
v_mov_b32 v19, v226
v_mfma_f32_16x16x32_bf16 v[0:3], a[12:15], v[172:175], v[0:3]
v_mov_b32 v24, v226
v_mfma_f32_16x16x32_bf16 v[4:7], a[12:15], v[188:191], v[4:7]
ds_read_b128 a[44:47], v220 offset:11264
buffer_load_dwordx4 v252, s[16:19], s40 offen offset:1024 sc1 lds
v_mov_b32 v25, v226
s_waitcnt lgkmcnt(4)
v_mfma_f32_16x16x32_bf16 v[8:11], a[16:19], v[160:163], v[200:203]
v_mov_b32 v26, v226
v_mfma_f32_16x16x32_bf16 v[12:15], a[16:19], v[176:179], 0
ds_read_b128 a[48:51], v220 offset:12288
v_mov_b32 v27, v226
v_mfma_f32_16x16x32_bf16 v[8:11], a[20:23], v[164:167], v[8:11]
v_mov_b32 v32, v226
v_mfma_f32_16x16x32_bf16 v[12:15], a[20:23], v[180:183], v[12:15]
ds_read_b128 a[52:55], v220 offset:13312
buffer_load_dwordx4 v252, s[16:19], s40 offen offset:2048 sc1 lds
v_mov_b32 v33, v226
v_mfma_f32_16x16x32_bf16 v[8:11], a[24:27], v[168:171], v[8:11]
v_mov_b32 v34, v226
v_mfma_f32_16x16x32_bf16 v[12:15], a[24:27], v[184:187], v[12:15]
ds_read_b128 a[56:59], v220 offset:14336
v_mov_b32 v35, v226
v_mfma_f32_16x16x32_bf16 v[8:11], a[28:31], v[172:175], v[8:11]
v_mov_b32 v40, v226
v_mfma_f32_16x16x32_bf16 v[12:15], a[28:31], v[188:191], v[12:15]
ds_read_b128 a[60:63], v220 offset:15360
buffer_load_dwordx4 v252, s[16:19], s40 offen offset:3072 sc1 lds
v_mov_b32 v41, v226
s_add_u32 m0, s42, 4096
v_mov_b32 v42, v226
s_waitcnt lgkmcnt(7)
v_mfma_f32_16x16x32_bf16 v[20:23], a[32:35], v[176:179], 0
ds_read_b128 a[0:3], v220 offset:16384
v_mov_b32 v43, v226
v_mov_b32 v48, v226
s_waitcnt lgkmcnt(7)
v_mfma_f32_16x16x32_bf16 v[20:23], a[36:39], v[180:183], v[20:23]
ds_read_b128 a[4:7], v220 offset:17408
buffer_load_dwordx4 v244, s[16:19], s40 offen offset:0 sc1 lds
v_mov_b32 v49, v226
v_mov_b32 v50, v226
s_waitcnt lgkmcnt(7)
v_mfma_f32_16x16x32_bf16 v[20:23], a[40:43], v[184:187], v[20:23]
ds_read_b128 a[8:11], v220 offset:18432
v_mov_b32 v51, v226
v_mov_b32 v56, v226
s_waitcnt lgkmcnt(7)
v_mfma_f32_16x16x32_bf16 v[20:23], a[44:47], v[188:191], v[20:23]
ds_read_b128 a[12:15], v220 offset:19456
buffer_load_dwordx4 v244, s[16:19], s40 offen offset:1024 sc1 lds
v_mov_b32 v57, v226
v_mov_b32 v58, v226
s_waitcnt lgkmcnt(7)
v_mfma_f32_16x16x32_bf16 v[28:31], a[48:51], v[176:179], 0
ds_read_b128 a[16:19], v220 offset:20480
v_mov_b32 v59, v226
v_mov_b32 v60, v226
s_waitcnt lgkmcnt(7)
v_mfma_f32_16x16x32_bf16 v[28:31], a[52:55], v[180:183], v[28:31]
ds_read_b128 a[20:23], v220 offset:21504
buffer_load_dwordx4 v244, s[16:19], s40 offen offset:2048 sc1 lds
v_mov_b32 v61, v226
v_mov_b32 v62, v226
s_waitcnt lgkmcnt(7)
v_mfma_f32_16x16x32_bf16 v[28:31], a[56:59], v[184:187], v[28:31]
ds_read_b128 a[24:27], v220 offset:22528
v_mov_b32 v63, v226
s_waitcnt lgkmcnt(7)
v_mfma_f32_16x16x32_bf16 v[28:31], a[60:63], v[188:191], v[28:31]
ds_read_b128 a[28:31], v220 offset:23552
buffer_load_dwordx4 v244, s[16:19], s40 offen offset:3072 sc1 lds
s_waitcnt lgkmcnt(7)
v_mfma_f32_16x16x32_bf16 v[36:39], a[0:3], v[176:179], 0
ds_read_b128 a[32:35], v220 offset:24576
s_add_u32 s40, s40, 0x10000
s_waitcnt lgkmcnt(7)
v_mfma_f32_16x16x32_bf16 v[36:39], a[4:7], v[180:183], v[36:39]
ds_read_b128 a[36:39], v220 offset:25600
s_cmp_eq_u32 s40, s45
s_cselect_b32 s40, s52, s40
s_cselect_b32 s45, s57, s45
s_waitcnt lgkmcnt(7)
v_mfma_f32_16x16x32_bf16 v[36:39], a[8:11], v[184:187], v[36:39]
ds_read_b128 a[40:43], v220 offset:26624
s_xor_b32 s42, s42, 0x8000
s_waitcnt lgkmcnt(7)
v_mfma_f32_16x16x32_bf16 v[36:39], a[12:15], v[188:191], v[36:39]
ds_read_b128 a[44:47], v220 offset:27648
s_waitcnt lgkmcnt(7)
v_mfma_f32_16x16x32_bf16 v[44:47], a[16:19], v[176:179], 0
s_waitcnt lgkmcnt(6)
v_mfma_f32_16x16x32_bf16 v[44:47], a[20:23], v[180:183], v[44:47]
s_waitcnt lgkmcnt(5)
v_mfma_f32_16x16x32_bf16 v[44:47], a[24:27], v[184:187], v[44:47]
s_waitcnt lgkmcnt(4)
v_mfma_f32_16x16x32_bf16 v[44:47], a[28:31], v[188:191], v[44:47]
s_waitcnt lgkmcnt(3)
)ASM"
R"ASM(v_mfma_f32_16x16x32_bf16 v[52:55], a[32:35], v[176:179], v[204:207]
v_xor_b32 v220, 0x8000, v220
s_waitcnt lgkmcnt(2)
v_mfma_f32_16x16x32_bf16 v[52:55], a[36:39], v[180:183], v[52:55]
s_waitcnt lgkmcnt(1)
v_mfma_f32_16x16x32_bf16 v[52:55], a[40:43], v[184:187], v[52:55]
s_waitcnt lgkmcnt(0)
v_mfma_f32_16x16x32_bf16 v[52:55], a[44:47], v[188:191], v[52:55]
s_branch K2_F1MX
K2_F1M2:
s_cmp_lg_u32 s7, 2
s_cbranch_scc1 K2_F1M3
s_waitcnt lgkmcnt(4)
v_mfma_f32_16x16x32_bf16 v[0:3], a[0:3], v[160:163], 0
v_mfma_f32_16x16x32_bf16 v[4:7], a[0:3], v[176:179], 0
ds_read_b128 a[32:35], v220 offset:8192
v_mfma_f32_16x16x32_bf16 v[0:3], a[4:7], v[164:167], v[0:3]
v_mov_b32 v24, v226
s_mov_b32 m0, s42
v_mfma_f32_16x16x32_bf16 v[4:7], a[4:7], v[180:183], v[4:7]
ds_read_b128 a[36:39], v220 offset:9216
buffer_load_dwordx4 v252, s[16:19], s40 offen offset:0 sc1 lds
v_mov_b32 v25, v226
v_mfma_f32_16x16x32_bf16 v[0:3], a[8:11], v[168:171], v[0:3]
v_mov_b32 v26, v226
v_mfma_f32_16x16x32_bf16 v[4:7], a[8:11], v[184:187], v[4:7]
ds_read_b128 a[40:43], v220 offset:10240
v_mov_b32 v27, v226
v_mfma_f32_16x16x32_bf16 v[0:3], a[12:15], v[172:175], v[0:3]
v_mov_b32 v32, v226
v_mfma_f32_16x16x32_bf16 v[4:7], a[12:15], v[188:191], v[4:7]
ds_read_b128 a[44:47], v220 offset:11264
buffer_load_dwordx4 v252, s[16:19], s40 offen offset:1024 sc1 lds
v_mov_b32 v33, v226
s_waitcnt lgkmcnt(4)
v_mfma_f32_16x16x32_bf16 v[8:11], a[16:19], v[160:163], 0
v_mov_b32 v34, v226
v_mfma_f32_16x16x32_bf16 v[12:15], a[16:19], v[176:179], 0
ds_read_b128 a[48:51], v220 offset:12288
v_mov_b32 v35, v226
v_mfma_f32_16x16x32_bf16 v[8:11], a[20:23], v[164:167], v[8:11]
v_mov_b32 v40, v226
v_mfma_f32_16x16x32_bf16 v[12:15], a[20:23], v[180:183], v[12:15]
ds_read_b128 a[52:55], v220 offset:13312
buffer_load_dwordx4 v252, s[16:19], s40 offen offset:2048 sc1 lds
v_mov_b32 v41, v226
v_mfma_f32_16x16x32_bf16 v[8:11], a[24:27], v[168:171], v[8:11]
v_mov_b32 v42, v226
v_mfma_f32_16x16x32_bf16 v[12:15], a[24:27], v[184:187], v[12:15]
ds_read_b128 a[56:59], v220 offset:14336
v_mov_b32 v43, v226
v_mfma_f32_16x16x32_bf16 v[8:11], a[28:31], v[172:175], v[8:11]
v_mov_b32 v48, v226
v_mfma_f32_16x16x32_bf16 v[12:15], a[28:31], v[188:191], v[12:15]
ds_read_b128 a[60:63], v220 offset:15360
buffer_load_dwordx4 v252, s[16:19], s40 offen offset:3072 sc1 lds
v_mov_b32 v49, v226
s_add_u32 m0, s42, 4096
s_waitcnt lgkmcnt(4)
v_mfma_f32_16x16x32_bf16 v[16:19], a[32:35], v[160:163], v[200:203]
v_mov_b32 v50, v226
v_mfma_f32_16x16x32_bf16 v[20:23], a[32:35], v[176:179], 0
ds_read_b128 a[0:3], v220 offset:16384
v_mov_b32 v51, v226
v_mfma_f32_16x16x32_bf16 v[16:19], a[36:39], v[164:167], v[16:19]
v_mov_b32 v52, v226
v_mfma_f32_16x16x32_bf16 v[20:23], a[36:39], v[180:183], v[20:23]
ds_read_b128 a[4:7], v220 offset:17408
buffer_load_dwordx4 v244, s[16:19], s40 offen offset:0 sc1 lds
v_mov_b32 v53, v226
v_mfma_f32_16x16x32_bf16 v[16:19], a[40:43], v[168:171], v[16:19]
v_mov_b32 v54, v226
v_mfma_f32_16x16x32_bf16 v[20:23], a[40:43], v[184:187], v[20:23]
ds_read_b128 a[8:11], v220 offset:18432
v_mov_b32 v55, v226
v_mfma_f32_16x16x32_bf16 v[16:19], a[44:47], v[172:175], v[16:19]
v_mov_b32 v56, v226
v_mfma_f32_16x16x32_bf16 v[20:23], a[44:47], v[188:191], v[20:23]
ds_read_b128 a[12:15], v220 offset:19456
buffer_load_dwordx4 v244, s[16:19], s40 offen offset:1024 sc1 lds
v_mov_b32 v57, v226
v_mov_b32 v58, v226
s_waitcnt lgkmcnt(7)
v_mfma_f32_16x16x32_bf16 v[28:31], a[48:51], v[176:179], 0
ds_read_b128 a[16:19], v220 offset:20480
v_mov_b32 v59, v226
v_mov_b32 v60, v226
s_waitcnt lgkmcnt(7)
v_mfma_f32_16x16x32_bf16 v[28:31], a[52:55], v[180:183], v[28:31]
ds_read_b128 a[20:23], v220 offset:21504
buffer_load_dwordx4 v244, s[16:19], s40 offen offset:2048 sc1 lds
v_mov_b32 v61, v226
v_mov_b32 v62, v226
s_waitcnt lgkmcnt(7)
v_mfma_f32_16x16x32_bf16 v[28:31], a[56:59], v[184:187], v[28:31]
ds_read_b128 a[24:27], v220 offset:22528
v_mov_b32 v63, v226
s_waitcnt lgkmcnt(7)
v_mfma_f32_16x16x32_bf16 v[28:31], a[60:63], v[188:191], v[28:31]
ds_read_b128 a[28:31], v220 offset:23552
buffer_load_dwordx4 v244, s[16:19], s40 offen offset:3072 sc1 lds
s_waitcnt lgkmcnt(7)
v_mfma_f32_16x16x32_bf16 v[36:39], a[0:3], v[176:179], 0
s_add_u32 s40, s40, 0x10000
s_waitcnt lgkmcnt(6)
v_mfma_f32_16x16x32_bf16 v[36:39], a[4:7], v[180:183], v[36:39]
s_cmp_eq_u32 s40, s45
s_cselect_b32 s40, s52, s40
s_cselect_b32 s45, s57, s45
s_waitcnt lgkmcnt(5)
v_mfma_f32_16x16x32_bf16 v[36:39], a[8:11], v[184:187], v[36:39]
s_xor_b32 s42, s42, 0x8000
s_waitcnt lgkmcnt(4)
v_mfma_f32_16x16x32_bf16 v[36:39], a[12:15], v[188:191], v[36:39]
s_waitcnt lgkmcnt(3)
v_mfma_f32_16x16x32_bf16 v[44:47], a[16:19], v[176:179], v[204:207]
s_waitcnt lgkmcnt(2)
v_mfma_f32_16x16x32_bf16 v[44:47], a[20:23], v[180:183], v[44:47]
s_waitcnt lgkmcnt(1)
v_mfma_f32_16x16x32_bf16 v[44:47], a[24:27], v[184:187], v[44:47]
s_waitcnt lgkmcnt(0)
v_mfma_f32_16x16x32_bf16 v[44:47], a[28:31], v[188:191], v[44:47]
v_xor_b32 v220, 0x8000, v220
s_branch K2_F1MX
K2_F1M3:
s_waitcnt lgkmcnt(4)
v_mfma_f32_16x16x32_bf16 v[0:3], a[0:3], v[160:163], 0
v_mfma_f32_16x16x32_bf16 v[4:7], a[0:3], v[176:179], 0
ds_read_b128 a[32:35], v220 offset:8192
v_mfma_f32_16x16x32_bf16 v[0:3], a[4:7], v[164:167], v[0:3]
v_mov_b32 v32, v226
s_mov_b32 m0, s42
v_mfma_f32_16x16x32_bf16 v[4:7], a[4:7], v[180:183], v[4:7]
ds_read_b128 a[36:39], v220 offset:9216
buffer_load_dwordx4 v252, s[16:19], s40 offen offset:0 sc1 lds
v_mov_b32 v33, v226
v_mfma_f32_16x16x32_bf16 v[0:3], a[8:11], v[168:171], v[0:3]
v_mov_b32 v34, v226
v_mfma_f32_16x16x32_bf16 v[4:7], a[8:11], v[184:187], v[4:7]
ds_read_b128 a[40:43], v220 offset:10240
v_mov_b32 v35, v226
v_mfma_f32_16x16x32_bf16 v[0:3], a[12:15], v[172:175], v[0:3]
v_mov_b32 v40, v226
v_mfma_f32_16x16x32_bf16 v[4:7], a[12:15], v[188:191], v[4:7]
ds_read_b128 a[44:47], v220 offset:11264
buffer_load_dwordx4 v252, s[16:19], s40 offen offset:1024 sc1 lds
v_mov_b32 v41, v226
s_waitcnt lgkmcnt(4)
v_mfma_f32_16x16x32_bf16 v[8:11], a[16:19], v[160:163], 0
v_mov_b32 v42, v226
v_mfma_f32_16x16x32_bf16 v[12:15], a[16:19], v[176:179], 0
ds_read_b128 a[48:51], v220 offset:12288
v_mov_b32 v43, v226
v_mfma_f32_16x16x32_bf16 v[8:11], a[20:23], v[164:167], v[8:11]
v_mov_b32 v44, v226
v_mfma_f32_16x16x32_bf16 v[12:15], a[20:23], v[180:183], v[12:15]
ds_read_b128 a[52:55], v220 offset:13312
buffer_load_dwordx4 v252, s[16:19], s40 offen offset:2048 sc1 lds
v_mov_b32 v45, v226
v_mfma_f32_16x16x32_bf16 v[8:11], a[24:27], v[168:171], v[8:11]
v_mov_b32 v46, v226
v_mfma_f32_16x16x32_bf16 v[12:15], a[24:27], v[184:187], v[12:15]
ds_read_b128 a[56:59], v220 offset:14336
v_mov_b32 v47, v226
v_mfma_f32_16x16x32_bf16 v[8:11], a[28:31], v[172:175], v[8:11]
v_mov_b32 v48, v226
v_mfma_f32_16x16x32_bf16 v[12:15], a[28:31], v[188:191], v[12:15]
ds_read_b128 a[60:63], v220 offset:15360
buffer_load_dwordx4 v252, s[16:19], s40 offen offset:3072 sc1 lds
v_mov_b32 v49, v226
s_add_u32 m0, s42, 4096
s_waitcnt lgkmcnt(4)
v_mfma_f32_16x16x32_bf16 v[16:19], a[32:35], v[160:163], 0
v_mov_b32 v50, v226
v_mfma_f32_16x16x32_bf16 v[20:23], a[32:35], v[176:179], 0
ds_read_b128 a[0:3], v220 offset:16384
v_mov_b32 v51, v226
v_mfma_f32_16x16x32_bf16 v[16:19], a[36:39], v[164:167], v[16:19]
v_mov_b32 v52, v226
v_mfma_f32_16x16x32_bf16 v[20:23], a[36:39], v[180:183], v[20:23]
ds_read_b128 a[4:7], v220 offset:17408
buffer_load_dwordx4 v244, s[16:19], s40 offen offset:0 sc1 lds
v_mov_b32 v53, v226
v_mfma_f32_16x16x32_bf16 v[16:19], a[40:43], v[168:171], v[16:19]
v_mov_b32 v54, v226
v_mfma_f32_16x16x32_bf16 v[20:23], a[40:43], v[184:187], v[20:23]
ds_read_b128 a[8:11], v220 offset:18432
v_mov_b32 v55, v226
v_mfma_f32_16x16x32_bf16 v[16:19], a[44:47], v[172:175], v[16:19]
v_mov_b32 v56, v226
v_mfma_f32_16x16x32_bf16 v[20:23], a[44:47], v[188:191], v[20:23]
ds_read_b128 a[12:15], v220 offset:19456
buffer_load_dwordx4 v244, s[16:19], s40 offen offset:1024 sc1 lds
v_mov_b32 v57, v226
s_waitcnt lgkmcnt(4)
v_mfma_f32_16x16x32_bf16 v[24:27], a[48:51], v[160:163], v[200:203]
v_mov_b32 v58, v226
v_mfma_f32_16x16x32_bf16 v[28:31], a[48:51], v[176:179], 0
v_mov_b32 v59, v226
v_mfma_f32_16x16x32_bf16 v[24:27], a[52:55], v[164:167], v[24:27]
v_mov_b32 v60, v226
v_mfma_f32_16x16x32_bf16 v[28:31], a[52:55], v[180:183], v[28:31]
buffer_load_dwordx4 v244, s[16:19], s40 offen offset:2048 sc1 lds
v_mov_b32 v61, v226
v_mfma_f32_16x16x32_bf16 v[24:27], a[56:59], v[168:171], v[24:27]
v_mov_b32 v62, v226
v_mfma_f32_16x16x32_bf16 v[28:31], a[56:59], v[184:187], v[28:31]
v_mov_b32 v63, v226
v_mfma_f32_16x16x32_bf16 v[24:27], a[60:63], v[172:175], v[24:27]
v_mfma_f32_16x16x32_bf16 v[28:31], a[60:63], v[188:191], v[28:31]
buffer_load_dwordx4 v244, s[16:19], s40 offen offset:3072 sc1 lds
s_waitcnt lgkmcnt(3)
v_mfma_f32_16x16x32_bf16 v[36:39], a[0:3], v[176:179], v[204:207]
s_add_u32 s40, s40, 0x10000
s_waitcnt lgkmcnt(2)
v_mfma_f32_16x16x32_bf16 v[36:39], a[4:7], v[180:183], v[36:39]
s_cmp_eq_u32 s40, s45
s_cselect_b32 s40, s52, s40
s_cselect_b32 s45, s57, s45
s_waitcnt lgkmcnt(1)
v_mfma_f32_16x16x32_bf16 v[36:39], a[8:11], v[184:187], v[36:39]
s_xor_b32 s42, s42, 0x8000
s_waitcnt lgkmcnt(0)
v_mfma_f32_16x16x32_bf16 v[36:39], a[12:15], v[188:191], v[36:39]
v_xor_b32 v220, 0x8000, v220
s_branch K2_F1MX
K2_F1MX:
s_nop 4
v_max3_f32 v216, v0, v1, v2
v_max_f32 v216, v216, v3
v_max3_f32 v218, v4, v5, v6
v_max_f32 v218, v218, v7
v_max3_f32 v216, v216, v8, v9
v_max3_f32 v216, v216, v10, v11
v_max3_f32 v218, v218, v12, v13
v_max3_f32 v218, v218, v14, v15
v_max3_f32 v216, v216, v16, v17
v_max3_f32 v216, v216, v18, v19
v_max3_f32 v218, v218, v20, v21
v_max3_f32 v218, v218, v22, v23
v_max3_f32 v216, v216, v24, v25
v_max3_f32 v216, v216, v26, v27
v_max3_f32 v218, v218, v28, v29
v_max3_f32 v218, v218, v30, v31
v_max3_f32 v216, v216, v32, v33
v_max3_f32 v216, v216, v34, v35
v_max3_f32 v218, v218, v36, v37
v_max3_f32 v218, v218, v38, v39
v_max3_f32 v216, v216, v40, v41
v_max3_f32 v216, v216, v42, v43
v_max3_f32 v218, v218, v44, v45
v_max3_f32 v218, v218, v46, v47
v_max3_f32 v216, v216, v48, v49
v_max3_f32 v216, v216, v50, v51
v_max3_f32 v218, v218, v52, v53
v_max3_f32 v218, v218, v54, v55
v_max3_f32 v216, v216, v56, v57
v_max3_f32 v216, v216, v58, v59
v_max3_f32 v218, v218, v60, v61
v_max3_f32 v218, v218, v62, v63
v_mov_b32 v226, v216
v_mov_b32 v227, v218
s_nop 0
v_permlane16_swap_b32 v226, v216
v_permlane16_swap_b32 v227, v218
v_max_f32 v216, v216, v226
v_max_f32 v218, v218, v227
v_mov_b32 v226, v216
v_mov_b32 v227, v218
s_nop 0
v_permlane32_swap_b32 v226, v216
v_permlane32_swap_b32 v227, v218
v_max_f32 v216, v216, v226
v_max_f32 v218, v218, v227
v_max_f32 v216, 0xf149f2ca, v216
v_max_f32 v218, 0xf149f2ca, v218
v_pk_add_f32 v[0:1], v[0:1], v[216:217] op_sel_hi:[1,0] neg_lo:[0,1] neg_hi:[0,1]
v_pk_add_f32 v[2:3], v[2:3], v[216:217] op_sel_hi:[1,0] neg_lo:[0,1] neg_hi:[0,1]
v_pk_add_f32 v[4:5], v[4:5], v[218:219] op_sel_hi:[1,0] neg_lo:[0,1] neg_hi:[0,1]
v_pk_add_f32 v[6:7], v[6:7], v[218:219] op_sel_hi:[1,0] neg_lo:[0,1] neg_hi:[0,1]
v_pk_add_f32 v[8:9], v[8:9], v[216:217] op_sel_hi:[1,0] neg_lo:[0,1] neg_hi:[0,1]
v_pk_add_f32 v[10:11], v[10:11], v[216:217] op_sel_hi:[1,0] neg_lo:[0,1] neg_hi:[0,1]
v_pk_add_f32 v[12:13], v[12:13], v[218:219] op_sel_hi:[1,0] neg_lo:[0,1] neg_hi:[0,1]
v_pk_add_f32 v[14:15], v[14:15], v[218:219] op_sel_hi:[1,0] neg_lo:[0,1] neg_hi:[0,1]
v_pk_add_f32 v[16:17], v[16:17], v[216:217] op_sel_hi:[1,0] neg_lo:[0,1] neg_hi:[0,1]
v_pk_add_f32 v[18:19], v[18:19], v[216:217] op_sel_hi:[1,0] neg_lo:[0,1] neg_hi:[0,1]
v_pk_add_f32 v[20:21], v[20:21], v[218:219] op_sel_hi:[1,0] neg_lo:[0,1] neg_hi:[0,1]
v_pk_add_f32 v[22:23], v[22:23], v[218:219] op_sel_hi:[1,0] neg_lo:[0,1] neg_hi:[0,1]
v_pk_add_f32 v[24:25], v[24:25], v[216:217] op_sel_hi:[1,0] neg_lo:[0,1] neg_hi:[0,1]
v_pk_add_f32 v[26:27], v[26:27], v[216:217] op_sel_hi:[1,0] neg_lo:[0,1] neg_hi:[0,1]
v_pk_add_f32 v[28:29], v[28:29], v[218:219] op_sel_hi:[1,0] neg_lo:[0,1] neg_hi:[0,1]
v_pk_add_f32 v[30:31], v[30:31], v[218:219] op_sel_hi:[1,0] neg_lo:[0,1] neg_hi:[0,1]
v_pk_add_f32 v[32:33], v[32:33], v[216:217] op_sel_hi:[1,0] neg_lo:[0,1] neg_hi:[0,1]
v_pk_add_f32 v[34:35], v[34:35], v[216:217] op_sel_hi:[1,0] neg_lo:[0,1] neg_hi:[0,1]
v_pk_add_f32 v[36:37], v[36:37], v[218:219] op_sel_hi:[1,0] neg_lo:[0,1] neg_hi:[0,1]
v_pk_add_f32 v[38:39], v[38:39], v[218:219] op_sel_hi:[1,0] neg_lo:[0,1] neg_hi:[0,1]
v_pk_add_f32 v[40:41], v[40:41], v[216:217] op_sel_hi:[1,0] neg_lo:[0,1] neg_hi:[0,1]
v_pk_add_f32 v[42:43], v[42:43], v[216:217] op_sel_hi:[1,0] neg_lo:[0,1] neg_hi:[0,1]
v_pk_add_f32 v[44:45], v[44:45], v[218:219] op_sel_hi:[1,0] neg_lo:[0,1] neg_hi:[0,1]
v_pk_add_f32 v[46:47], v[46:47], v[218:219] op_sel_hi:[1,0] neg_lo:[0,1] neg_hi:[0,1]
v_pk_add_f32 v[48:49], v[48:49], v[216:217] op_sel_hi:[1,0] neg_lo:[0,1] neg_hi:[0,1]
v_pk_add_f32 v[50:51], v[50:51], v[216:217] op_sel_hi:[1,0] neg_lo:[0,1] neg_hi:[0,1]
v_pk_add_f32 v[52:53], v[52:53], v[218:219] op_sel_hi:[1,0] neg_lo:[0,1] neg_hi:[0,1]
v_pk_add_f32 v[54:55], v[54:55], v[218:219] op_sel_hi:[1,0] neg_lo:[0,1] neg_hi:[0,1]
v_pk_add_f32 v[56:57], v[56:57], v[216:217] op_sel_hi:[1,0] neg_lo:[0,1] neg_hi:[0,1]
v_pk_add_f32 v[58:59], v[58:59], v[216:217] op_sel_hi:[1,0] neg_lo:[0,1] neg_hi:[0,1]
v_pk_add_f32 v[60:61], v[60:61], v[218:219] op_sel_hi:[1,0] neg_lo:[0,1] neg_hi:[0,1]
v_pk_add_f32 v[62:63], v[62:63], v[218:219] op_sel_hi:[1,0] neg_lo:[0,1] neg_hi:[0,1]
v_pk_mul_f32 v[192:193], v[216:217], -1.0 op_sel_hi:[0,0]
v_pk_mul_f32 v[194:195], v[216:217], -1.0 op_sel_hi:[0,0]
v_pk_mul_f32 v[196:197], v[218:219], -1.0 op_sel_hi:[0,0]
v_pk_mul_f32 v[198:199], v[218:219], -1.0 op_sel_hi:[0,0]
K2_F1E:
v_exp_f32 v0, v0
v_exp_f32 v1, v1
v_exp_f32 v2, v2
v_cvt_pk_bf16_f32 v64, v0, v1
v_exp_f32 v3, v3
v_exp_f32 v4, v4
v_cvt_pk_bf16_f32 v65, v2, v3
v_exp_f32 v5, v5
v_exp_f32 v6, v6
v_cvt_pk_bf16_f32 v68, v4, v5
v_exp_f32 v7, v7
v_exp_f32 v8, v8
v_cvt_pk_bf16_f32 v69, v6, v7
v_exp_f32 v9, v9
v_exp_f32 v10, v10
v_cvt_pk_bf16_f32 v66, v8, v9
v_exp_f32 v11, v11
v_exp_f32 v12, v12
v_cvt_pk_bf16_f32 v67, v10, v11
v_exp_f32 v13, v13
v_exp_f32 v14, v14
v_cvt_pk_bf16_f32 v70, v12, v13
v_exp_f32 v15, v15
v_exp_f32 v16, v16
v_cvt_pk_bf16_f32 v71, v14, v15
v_exp_f32 v17, v17
v_exp_f32 v18, v18
v_cvt_pk_bf16_f32 v72, v16, v17
v_exp_f32 v19, v19
v_exp_f32 v20, v20
v_cvt_pk_bf16_f32 v73, v18, v19
v_exp_f32 v21, v21
v_exp_f32 v22, v22
v_cvt_pk_bf16_f32 v76, v20, v21
v_exp_f32 v23, v23
s_nop 0
v_cvt_pk_bf16_f32 v77, v22, v23
v_exp_f32 v24, v24
v_exp_f32 v25, v25
v_exp_f32 v26, v26
v_cvt_pk_bf16_f32 v74, v24, v25
v_exp_f32 v27, v27
v_exp_f32 v28, v28
v_cvt_pk_bf16_f32 v75, v26, v27
v_exp_f32 v29, v29
v_exp_f32 v30, v30
v_cvt_pk_bf16_f32 v78, v28, v29
v_exp_f32 v31, v31
v_exp_f32 v32, v32
v_cvt_pk_bf16_f32 v79, v30, v31
v_exp_f32 v33, v33
v_exp_f32 v34, v34
v_xor_b32 v221, 0x8000, v221
v_cvt_pk_bf16_f32 v80, v32, v33
s_waitcnt vmcnt(0) lgkmcnt(0)
s_barrier
s_cmp_eq_u32 s51, 1
s_cbranch_scc0 LpubA_10
s_sub_u32 s46, 15, s27
s_lshl_b32 s46, s46, 8
s_lshl_b32 s59, s26, 3
s_add_u32 s46, s46, s59
s_add_u32 s46, s46, 12288
s_cmp_eq_u32 s7, 0
s_cbranch_scc0 Lpub_11
v_mov_b32 v226, s84
v_mov_b32 v227, s85
v_mov_b32 v228, 0
s_mov_b64 exec, 1
buffer_store_dwordx2 v[226:227], v228, s[88:91], s46 offen sc1
s_nop 0
s_mov_b64 exec, -1
Lpub_11:
LpubA_10:
s_cmp_eq_u32 s51, 0
s_cbranch_scc0 Ltt_12
s_lshl_b32 s46, s26, 6
s_lshl_b32 s59, s27, 3
s_add_u32 s46, s46, s59
s_add_u32 s46, s46, 10240
s_cmp_eq_u32 s7, 0
s_cbranch_scc0 Lpkv_13
v_mov_b32 v254, s84
v_accvgpr_write_b32 a250, v254
v_mov_b32 v254, s85
v_accvgpr_write_b32 a251, v254
v_mov_b32 v254, 0
s_mov_b64 exec, 1
s_nop 1
buffer_store_dwordx2 a[250:251], v254, s[88:91], s46 offen sc1
s_nop 0
s_mov_b64 exec, -1
Lpkv_13:
v_lshlrev_b32 v226, 7, v253
s_cmp_lt_u32 s7, 2
s_cbranch_scc0 Ltt_14
s_mov_b32 s60, s74
s_and_b32 s61, s75, 0xffff
s_mov_b32 s62, 0x400000
s_mov_b32 s63, 0x20000
s_lshl_b32 s59, s6, 1
s_add_u32 s59, s59, s7
s_lshl_b32 s59, s59, 13
s_branch Ltt_16
Ltt_14:
s_cmp_eq_u32 s7, 2
s_cbranch_scc0 Ltt_15
s_mov_b32 s60, s76
s_and_b32 s61, s77, 0xffff
s_mov_b32 s62, 0x1c0000
s_mov_b32 s63, 0x20000
s_branch Ltt_17
Ltt_15:
s_mov_b32 s60, s78
s_and_b32 s61, s79, 0xffff
s_mov_b32 s62, 0x1c0000
s_mov_b32 s63, 0x20000
Ltt_17:
s_mul_i32 s59, s6, 7168
Ltt_16:
buffer_load_dword a252, v226, s[60:63], s59 offen
Ltt_12:
s_cmp_eq_u32 s51, 1
s_cbranch_scc0 Ltt_18
s_getpc_b64 s[60:61]
Lpc_19:
s_and_b32 s61, s61, 0xffff
s_mov_b32 s62, KEND-Lpc_19
s_mov_b32 s63, 0x20000
v_lshlrev_b32 v226, 7, v253
s_lshl_b32 s59, s7, 13
s_add_u32 s59, s59, K2_END-Lpc_19
buffer_load_dword a252, v226, s[60:63], s59 offen
Ltt_18:
v_exp_f32 v35, v35
ds_read_b128 a[64:67], v221 offset:0
ds_read_b128 a[68:71], v221 offset:1024
v_exp_f32 v36, v36
ds_read_b128 a[72:75], v221 offset:2048
v_cvt_pk_bf16_f32 v81, v34, v35
ds_read_b128 a[76:79], v221 offset:3072
v_exp_f32 v37, v37
s_mov_b32 m0, s43
ds_read_b128 a[80:83], v221 offset:4096
buffer_load_dwordx4 v252, s[16:19], s41 offen offset:0 sc1 lds
v_exp_f32 v38, v38
ds_read_b128 a[84:87], v221 offset:5120
v_cvt_pk_bf16_f32 v84, v36, v37
v_exp_f32 v39, v39
ds_read_b128 a[88:91], v221 offset:6144
buffer_load_dwordx4 v252, s[16:19], s41 offen offset:1024 sc1 lds
s_waitcnt lgkmcnt(3)
v_mfma_f32_16x16x32_bf16 v[96:99], a[64:67], v[64:67], 0
v_exp_f32 v40, v40
v_mfma_f32_16x16x32_bf16 v[100:103], a[64:67], v[68:71], 0
ds_read_b128 a[92:95], v221 offset:7168
v_cvt_pk_bf16_f32 v85, v38, v39
v_mfma_f32_16x16x32_bf16 v[104:107], a[68:71], v[64:67], 0
v_exp_f32 v41, v41
v_mfma_f32_16x16x32_bf16 v[108:111], a[68:71], v[68:71], 0
ds_read_b128 a[96:99], v221 offset:8192
buffer_load_dwordx4 v252, s[16:19], s41 offen offset:2048 sc1 lds
v_mfma_f32_16x16x32_bf16 v[112:115], a[72:75], v[64:67], 0
v_exp_f32 v42, v42
v_mfma_f32_16x16x32_bf16 v[116:119], a[72:75], v[68:71], 0
ds_read_b128 a[100:103], v221 offset:9216
v_cvt_pk_bf16_f32 v82, v40, v41
v_mfma_f32_16x16x32_bf16 v[120:123], a[76:79], v[64:67], 0
v_exp_f32 v43, v43
v_mfma_f32_16x16x32_bf16 v[124:127], a[76:79], v[68:71], 0
ds_read_b128 a[104:107], v221 offset:10240
buffer_load_dwordx4 v252, s[16:19], s41 offen offset:3072 sc1 lds
s_add_u32 m0, s43, 4096
v_mfma_f32_16x16x32_bf16 v[208:211], a[160:163], v[64:67], 0
v_mfma_f32_16x16x32_bf16 v[212:215], a[160:163], v[68:71], 0
s_waitcnt lgkmcnt(3)
v_mfma_f32_16x16x32_bf16 v[128:131], a[80:83], v[64:67], 0
v_exp_f32 v44, v44
v_mfma_f32_16x16x32_bf16 v[132:135], a[80:83], v[68:71], 0
ds_read_b128 a[108:111], v221 offset:11264
v_cvt_pk_bf16_f32 v83, v42, v43
v_mfma_f32_16x16x32_bf16 v[136:139], a[84:87], v[64:67], 0
v_exp_f32 v45, v45
v_mfma_f32_16x16x32_bf16 v[140:143], a[84:87], v[68:71], 0
ds_read_b128 a[112:115], v221 offset:12288
buffer_load_dwordx4 v244, s[16:19], s41 offen offset:0 sc1 lds
v_mfma_f32_16x16x32_bf16 v[144:147], a[88:91], v[64:67], 0
v_exp_f32 v46, v46
v_mfma_f32_16x16x32_bf16 v[148:151], a[88:91], v[68:71], 0
ds_read_b128 a[116:119], v221 offset:13312
v_cvt_pk_bf16_f32 v86, v44, v45
v_mfma_f32_16x16x32_bf16 v[152:155], a[92:95], v[64:67], 0
v_exp_f32 v47, v47
v_mfma_f32_16x16x32_bf16 v[156:159], a[92:95], v[68:71], 0
ds_read_b128 a[120:123], v221 offset:14336
buffer_load_dwordx4 v244, s[16:19], s41 offen offset:1024 sc1 lds
s_waitcnt lgkmcnt(3)
v_mfma_f32_16x16x32_bf16 v[96:99], a[96:99], v[72:75], v[96:99]
v_exp_f32 v48, v48
v_mfma_f32_16x16x32_bf16 v[100:103], a[96:99], v[76:79], v[100:103]
ds_read_b128 a[0:3], v220 offset:0
ds_read_b128 a[124:127], v221 offset:15360
v_cvt_pk_bf16_f32 v87, v46, v47
v_mfma_f32_16x16x32_bf16 v[104:107], a[100:103], v[72:75], v[104:107]
v_exp_f32 v49, v49
v_mfma_f32_16x16x32_bf16 v[108:111], a[100:103], v[76:79], v[108:111]
ds_read_b128 a[4:7], v220 offset:1024
buffer_load_dwordx4 v244, s[16:19], s41 offen offset:2048 sc1 lds
v_mfma_f32_16x16x32_bf16 v[112:115], a[104:107], v[72:75], v[112:115]
v_exp_f32 v50, v50
v_mfma_f32_16x16x32_bf16 v[116:119], a[104:107], v[76:79], v[116:119]
ds_read_b128 a[8:11], v220 offset:2048
v_cvt_pk_bf16_f32 v88, v48, v49
v_mfma_f32_16x16x32_bf16 v[120:123], a[108:111], v[72:75], v[120:123]
v_exp_f32 v51, v51
v_mfma_f32_16x16x32_bf16 v[124:127], a[108:111], v[76:79], v[124:127]
ds_read_b128 a[12:15], v220 offset:3072
buffer_load_dwordx4 v244, s[16:19], s41 offen offset:3072 sc1 lds
v_mfma_f32_16x16x32_bf16 v[208:211], a[160:163], v[72:75], v[208:211]
s_add_u32 s41, s41, 0x10000
s_xor_b32 s43, s43, 0x8000
v_mfma_f32_16x16x32_bf16 v[212:215], a[160:163], v[76:79], v[212:215]
s_cmp_eq_u32 s41, s47
s_cselect_b32 s41, s56, s41
s_cselect_b32 s47, s58, s47
s_waitcnt lgkmcnt(3)
v_mfma_f32_16x16x32_bf16 v[128:131], a[112:115], v[72:75], v[128:131]
v_exp_f32 v52, v52
v_mfma_f32_16x16x32_bf16 v[132:135], a[112:115], v[76:79], v[132:135]
ds_read_b128 a[16:19], v220 offset:4096
v_cvt_pk_bf16_f32 v89, v50, v51
v_mfma_f32_16x16x32_bf16 v[136:139], a[116:119], v[72:75], v[136:139]
v_exp_f32 v53, v53
v_mfma_f32_16x16x32_bf16 v[140:143], a[116:119], v[76:79], v[140:143]
ds_read_b128 a[20:23], v220 offset:5120
v_mfma_f32_16x16x32_bf16 v[144:147], a[120:123], v[72:75], v[144:147]
v_exp_f32 v54, v54
v_mfma_f32_16x16x32_bf16 v[148:151], a[120:123], v[76:79], v[148:151]
ds_read_b128 a[24:27], v220 offset:6144
v_cvt_pk_bf16_f32 v92, v52, v53
v_mfma_f32_16x16x32_bf16 v[152:155], a[124:127], v[72:75], v[152:155]
v_exp_f32 v55, v55
v_mfma_f32_16x16x32_bf16 v[156:159], a[124:127], v[76:79], v[156:159]
ds_read_b128 a[28:31], v220 offset:7168
s_sub_u32 s48, s48, 1
s_cmp_eq_u32 s48, 0
s_cbranch_scc1 K2_DRAING
s_cmp_eq_u32 s48, 1
s_cbranch_scc1 K2_LAST
K2_LOOP:
s_cmp_eq_u32 s51, 0
s_cbranch_scc0 Lkvall_20
s_sub_u32 s59, s49, s48
s_cmp_eq_u32 s59, 7
s_cbranch_scc0 Lkvall_20
v_min_u32 v226, 7, v253
v_lshlrev_b32 v226, 3, v226
s_lshl_b32 s46, s26, 6
s_add_u32 s46, s46, 10240
s_mov_b32 s86, 0
Lpoll_21:
buffer_load_dwordx2 v[228:229], v226, s[88:91], s46 offen sc1
s_waitcnt vmcnt(0)
v_cmp_eq_u64 vcc, s[84:85], v[228:229]
s_nop 1
s_cmp_eq_u64 vcc, exec
s_cbranch_scc1 Lpdone_22
s_sleep 1
s_add_u32 s86, s86, 1
s_cmp_lt_u32 s86, 0x40000
s_cbranch_scc1 Lpoll_21
Lpdone_22:
Lkvall_20:
s_waitcnt lgkmcnt(4)
v_mfma_f32_16x16x32_bf16 v[0:3], a[0:3], v[160:163], v[192:195]
v_exp_f32 v56, v56
v_mfma_f32_16x16x32_bf16 v[4:7], a[0:3], v[176:179], v[196:199]
ds_read_b128 a[32:35], v220 offset:8192
v_cvt_pk_bf16_f32 v93, v54, v55
v_mfma_f32_16x16x32_bf16 v[0:3], a[4:7], v[164:167], v[0:3]
v_exp_f32 v57, v57
s_mov_b32 m0, s42
v_mfma_f32_16x16x32_bf16 v[4:7], a[4:7], v[180:183], v[4:7]
ds_read_b128 a[36:39], v220 offset:9216
buffer_load_dwordx4 v252, s[16:19], s40 offen offset:0 sc1 lds
v_mfma_f32_16x16x32_bf16 v[0:3], a[8:11], v[168:171], v[0:3]
v_exp_f32 v58, v58
v_mfma_f32_16x16x32_bf16 v[4:7], a[8:11], v[184:187], v[4:7]
ds_read_b128 a[40:43], v220 offset:10240
v_cvt_pk_bf16_f32 v90, v56, v57
v_mfma_f32_16x16x32_bf16 v[0:3], a[12:15], v[172:175], v[0:3]
v_exp_f32 v59, v59
v_mfma_f32_16x16x32_bf16 v[4:7], a[12:15], v[188:191], v[4:7]
ds_read_b128 a[44:47], v220 offset:11264
buffer_load_dwordx4 v252, s[16:19], s40 offen offset:1024 sc1 lds
s_waitcnt lgkmcnt(4)
v_mfma_f32_16x16x32_bf16 v[8:11], a[16:19], v[160:163], v[192:195]
v_exp_f32 v60, v60
v_mfma_f32_16x16x32_bf16 v[12:15], a[16:19], v[176:179], v[196:199]
ds_read_b128 a[48:51], v220 offset:12288
v_cvt_pk_bf16_f32 v91, v58, v59
v_mfma_f32_16x16x32_bf16 v[8:11], a[20:23], v[164:167], v[8:11]
v_exp_f32 v61, v61
v_mfma_f32_16x16x32_bf16 v[12:15], a[20:23], v[180:183], v[12:15]
ds_read_b128 a[52:55], v220 offset:13312
buffer_load_dwordx4 v252, s[16:19], s40 offen offset:2048 sc1 lds
v_mfma_f32_16x16x32_bf16 v[8:11], a[24:27], v[168:171], v[8:11]
v_exp_f32 v62, v62
v_mfma_f32_16x16x32_bf16 v[12:15], a[24:27], v[184:187], v[12:15]
ds_read_b128 a[56:59], v220 offset:14336
v_cvt_pk_bf16_f32 v94, v60, v61
v_mfma_f32_16x16x32_bf16 v[8:11], a[28:31], v[172:175], v[8:11]
v_exp_f32 v63, v63
v_mfma_f32_16x16x32_bf16 v[12:15], a[28:31], v[188:191], v[12:15]
ds_read_b128 a[60:63], v220 offset:15360
buffer_load_dwordx4 v252, s[16:19], s40 offen offset:3072 sc1 lds
s_add_u32 m0, s42, 4096
s_waitcnt lgkmcnt(4)
v_mfma_f32_16x16x32_bf16 v[16:19], a[32:35], v[160:163], v[192:195]
v_exp_f32 v0, v0
v_mfma_f32_16x16x32_bf16 v[20:23], a[32:35], v[176:179], v[196:199]
ds_read_b128 a[0:3], v220 offset:16384
v_cvt_pk_bf16_f32 v95, v62, v63
v_mfma_f32_16x16x32_bf16 v[16:19], a[36:39], v[164:167], v[16:19]
v_exp_f32 v1, v1
v_mfma_f32_16x16x32_bf16 v[20:23], a[36:39], v[180:183], v[20:23]
ds_read_b128 a[4:7], v220 offset:17408
buffer_load_dwordx4 v244, s[16:19], s40 offen offset:0 sc1 lds
v_mfma_f32_16x16x32_bf16 v[16:19], a[40:43], v[168:171], v[16:19]
v_exp_f32 v2, v2
v_mfma_f32_16x16x32_bf16 v[20:23], a[40:43], v[184:187], v[20:23]
ds_read_b128 a[8:11], v220 offset:18432
v_cvt_pk_bf16_f32 v64, v0, v1
v_mfma_f32_16x16x32_bf16 v[16:19], a[44:47], v[172:175], v[16:19]
v_exp_f32 v3, v3
v_mfma_f32_16x16x32_bf16 v[20:23], a[44:47], v[188:191], v[20:23]
ds_read_b128 a[12:15], v220 offset:19456
buffer_load_dwordx4 v244, s[16:19], s40 offen offset:1024 sc1 lds
s_waitcnt lgkmcnt(4)
v_mfma_f32_16x16x32_bf16 v[24:27], a[48:51], v[160:163], v[192:195]
v_exp_f32 v4, v4
v_mfma_f32_16x16x32_bf16 v[28:31], a[48:51], v[176:179], v[196:199]
ds_read_b128 a[16:19], v220 offset:20480
v_cvt_pk_bf16_f32 v65, v2, v3
v_mfma_f32_16x16x32_bf16 v[24:27], a[52:55], v[164:167], v[24:27]
v_exp_f32 v5, v5
v_mfma_f32_16x16x32_bf16 v[28:31], a[52:55], v[180:183], v[28:31]
ds_read_b128 a[20:23], v220 offset:21504
buffer_load_dwordx4 v244, s[16:19], s40 offen offset:2048 sc1 lds
v_mfma_f32_16x16x32_bf16 v[24:27], a[56:59], v[168:171], v[24:27]
v_exp_f32 v6, v6
v_mfma_f32_16x16x32_bf16 v[28:31], a[56:59], v[184:187], v[28:31]
ds_read_b128 a[24:27], v220 offset:22528
v_cvt_pk_bf16_f32 v68, v4, v5
v_mfma_f32_16x16x32_bf16 v[24:27], a[60:63], v[172:175], v[24:27]
v_exp_f32 v7, v7
v_mfma_f32_16x16x32_bf16 v[28:31], a[60:63], v[188:191], v[28:31]
ds_read_b128 a[28:31], v220 offset:23552
buffer_load_dwordx4 v244, s[16:19], s40 offen offset:3072 sc1 lds
s_waitcnt lgkmcnt(4)
v_mfma_f32_16x16x32_bf16 v[32:35], a[0:3], v[160:163], v[192:195]
v_exp_f32 v8, v8
v_mfma_f32_16x16x32_bf16 v[36:39], a[0:3], v[176:179], v[196:199]
ds_read_b128 a[32:35], v220 offset:24576
v_cvt_pk_bf16_f32 v69, v6, v7
v_mfma_f32_16x16x32_bf16 v[32:35], a[4:7], v[164:167], v[32:35]
v_exp_f32 v9, v9
v_mfma_f32_16x16x32_bf16 v[36:39], a[4:7], v[180:183], v[36:39]
ds_read_b128 a[36:39], v220 offset:25600
v_mfma_f32_16x16x32_bf16 v[32:35], a[8:11], v[168:171], v[32:35]
v_exp_f32 v10, v10
v_mfma_f32_16x16x32_bf16 v[36:39], a[8:11], v[184:187], v[36:39]
ds_read_b128 a[40:43], v220 offset:26624
v_cvt_pk_bf16_f32 v66, v8, v9
v_mfma_f32_16x16x32_bf16 v[32:35], a[12:15], v[172:175], v[32:35]
v_exp_f32 v11, v11
v_mfma_f32_16x16x32_bf16 v[36:39], a[12:15], v[188:191], v[36:39]
ds_read_b128 a[44:47], v220 offset:27648
s_waitcnt lgkmcnt(4)
v_mfma_f32_16x16x32_bf16 v[40:43], a[16:19], v[160:163], v[192:195]
v_exp_f32 v12, v12
v_mfma_f32_16x16x32_bf16 v[44:47], a[16:19], v[176:179], v[196:199]
ds_read_b128 a[48:51], v220 offset:28672
v_cvt_pk_bf16_f32 v67, v10, v11
v_mfma_f32_16x16x32_bf16 v[40:43], a[20:23], v[164:167], v[40:43]
v_exp_f32 v13, v13
v_mfma_f32_16x16x32_bf16 v[44:47], a[20:23], v[180:183], v[44:47]
ds_read_b128 a[52:55], v220 offset:29696
v_mfma_f32_16x16x32_bf16 v[40:43], a[24:27], v[168:171], v[40:43]
v_exp_f32 v14, v14
v_mfma_f32_16x16x32_bf16 v[44:47], a[24:27], v[184:187], v[44:47]
ds_read_b128 a[56:59], v220 offset:30720
v_cvt_pk_bf16_f32 v70, v12, v13
v_mfma_f32_16x16x32_bf16 v[40:43], a[28:31], v[172:175], v[40:43]
v_exp_f32 v15, v15
v_mfma_f32_16x16x32_bf16 v[44:47], a[28:31], v[188:191], v[44:47]
ds_read_b128 a[60:63], v220 offset:31744
s_waitcnt lgkmcnt(4)
v_mfma_f32_16x16x32_bf16 v[48:51], a[32:35], v[160:163], v[192:195]
v_exp_f32 v16, v16
v_mfma_f32_16x16x32_bf16 v[52:55], a[32:35], v[176:179], v[196:199]
ds_read_b128 a[64:67], v221 offset:16384
v_cvt_pk_bf16_f32 v71, v14, v15
v_xor_b32 v220, 0x8000, v220
v_mfma_f32_16x16x32_bf16 v[48:51], a[36:39], v[164:167], v[48:51]
v_exp_f32 v17, v17
v_mfma_f32_16x16x32_bf16 v[52:55], a[36:39], v[180:183], v[52:55]
ds_read_b128 a[68:71], v221 offset:17408
v_mfma_f32_16x16x32_bf16 v[48:51], a[40:43], v[168:171], v[48:51]
v_exp_f32 v18, v18
v_mfma_f32_16x16x32_bf16 v[52:55], a[40:43], v[184:187], v[52:55]
ds_read_b128 a[72:75], v221 offset:18432
v_cvt_pk_bf16_f32 v72, v16, v17
v_mfma_f32_16x16x32_bf16 v[48:51], a[44:47], v[172:175], v[48:51]
v_exp_f32 v19, v19
v_mfma_f32_16x16x32_bf16 v[52:55], a[44:47], v[188:191], v[52:55]
ds_read_b128 a[76:79], v221 offset:19456
s_waitcnt lgkmcnt(4)
v_mfma_f32_16x16x32_bf16 v[56:59], a[48:51], v[160:163], v[192:195]
v_exp_f32 v20, v20
v_mfma_f32_16x16x32_bf16 v[60:63], a[48:51], v[176:179], v[196:199]
ds_read_b128 a[80:83], v221 offset:20480
v_cvt_pk_bf16_f32 v73, v18, v19
v_mfma_f32_16x16x32_bf16 v[56:59], a[52:55], v[164:167], v[56:59]
v_exp_f32 v21, v21
v_mfma_f32_16x16x32_bf16 v[60:63], a[52:55], v[180:183], v[60:63]
ds_read_b128 a[84:87], v221 offset:21504
v_mfma_f32_16x16x32_bf16 v[56:59], a[56:59], v[168:171], v[56:59]
v_exp_f32 v22, v22
v_mfma_f32_16x16x32_bf16 v[60:63], a[56:59], v[184:187], v[60:63]
ds_read_b128 a[88:91], v221 offset:22528
v_cvt_pk_bf16_f32 v76, v20, v21
v_mfma_f32_16x16x32_bf16 v[56:59], a[60:63], v[172:175], v[56:59]
v_exp_f32 v23, v23
v_mfma_f32_16x16x32_bf16 v[60:63], a[60:63], v[188:191], v[60:63]
ds_read_b128 a[92:95], v221 offset:23552
s_waitcnt lgkmcnt(4)
v_mfma_f32_16x16x32_bf16 v[96:99], a[64:67], v[80:83], v[96:99]
v_exp_f32 v24, v24
v_mfma_f32_16x16x32_bf16 v[100:103], a[64:67], v[84:87], v[100:103]
ds_read_b128 a[96:99], v221 offset:24576
v_cvt_pk_bf16_f32 v77, v22, v23
v_mfma_f32_16x16x32_bf16 v[104:107], a[68:71], v[80:83], v[104:107]
v_exp_f32 v25, v25
v_mfma_f32_16x16x32_bf16 v[108:111], a[68:71], v[84:87], v[108:111]
ds_read_b128 a[100:103], v221 offset:25600
v_mfma_f32_16x16x32_bf16 v[112:115], a[72:75], v[80:83], v[112:115]
v_exp_f32 v26, v26
v_mfma_f32_16x16x32_bf16 v[116:119], a[72:75], v[84:87], v[116:119]
ds_read_b128 a[104:107], v221 offset:26624
v_cvt_pk_bf16_f32 v74, v24, v25
v_mfma_f32_16x16x32_bf16 v[120:123], a[76:79], v[80:83], v[120:123]
v_exp_f32 v27, v27
v_mfma_f32_16x16x32_bf16 v[124:127], a[76:79], v[84:87], v[124:127]
ds_read_b128 a[108:111], v221 offset:27648
v_mfma_f32_16x16x32_bf16 v[208:211], a[160:163], v[80:83], v[208:211]
s_add_u32 s40, s40, 0x10000
s_xor_b32 s42, s42, 0x8000
v_mfma_f32_16x16x32_bf16 v[212:215], a[160:163], v[84:87], v[212:215]
s_cmp_eq_u32 s40, s45
s_cselect_b32 s40, s52, s40
s_cselect_b32 s45, s57, s45
s_waitcnt lgkmcnt(4)
v_mfma_f32_16x16x32_bf16 v[128:131], a[80:83], v[80:83], v[128:131]
v_exp_f32 v28, v28
v_mfma_f32_16x16x32_bf16 v[132:135], a[80:83], v[84:87], v[132:135]
ds_read_b128 a[112:115], v221 offset:28672
v_cvt_pk_bf16_f32 v75, v26, v27
v_mfma_f32_16x16x32_bf16 v[136:139], a[84:87], v[80:83], v[136:139]
v_exp_f32 v29, v29
v_mfma_f32_16x16x32_bf16 v[140:143], a[84:87], v[84:87], v[140:143]
ds_read_b128 a[116:119], v221 offset:29696
v_mfma_f32_16x16x32_bf16 v[144:147], a[88:91], v[80:83], v[144:147]
v_exp_f32 v30, v30
v_mfma_f32_16x16x32_bf16 v[148:151], a[88:91], v[84:87], v[148:151]
ds_read_b128 a[120:123], v221 offset:30720
v_cvt_pk_bf16_f32 v78, v28, v29
v_mfma_f32_16x16x32_bf16 v[152:155], a[92:95], v[80:83], v[152:155]
v_exp_f32 v31, v31
v_mfma_f32_16x16x32_bf16 v[156:159], a[92:95], v[84:87], v[156:159]
ds_read_b128 a[124:127], v221 offset:31744
s_waitcnt lgkmcnt(4)
v_mfma_f32_16x16x32_bf16 v[96:99], a[96:99], v[88:91], v[96:99]
v_exp_f32 v32, v32
v_mfma_f32_16x16x32_bf16 v[100:103], a[96:99], v[92:95], v[100:103]
v_cvt_pk_bf16_f32 v79, v30, v31
v_mfma_f32_16x16x32_bf16 v[104:107], a[100:103], v[88:91], v[104:107]
v_exp_f32 v33, v33
v_mfma_f32_16x16x32_bf16 v[108:111], a[100:103], v[92:95], v[108:111]
v_mfma_f32_16x16x32_bf16 v[112:115], a[104:107], v[88:91], v[112:115]
v_exp_f32 v34, v34
v_xor_b32 v221, 0x8000, v221
v_mfma_f32_16x16x32_bf16 v[116:119], a[104:107], v[92:95], v[116:119]
v_cvt_pk_bf16_f32 v80, v32, v33
s_waitcnt vmcnt(0) lgkmcnt(0)
s_barrier
v_mfma_f32_16x16x32_bf16 v[120:123], a[108:111], v[88:91], v[120:123]
v_exp_f32 v35, v35
v_mfma_f32_16x16x32_bf16 v[124:127], a[108:111], v[92:95], v[124:127]
ds_read_b128 a[64:67], v221 offset:0
v_mfma_f32_16x16x32_bf16 v[208:211], a[160:163], v[88:91], v[208:211]
v_mfma_f32_16x16x32_bf16 v[212:215], a[160:163], v[92:95], v[212:215]
v_mfma_f32_16x16x32_bf16 v[128:131], a[112:115], v[88:91], v[128:131]
ds_read_b128 a[68:71], v221 offset:1024
v_exp_f32 v36, v36
v_mfma_f32_16x16x32_bf16 v[132:135], a[112:115], v[92:95], v[132:135]
ds_read_b128 a[72:75], v221 offset:2048
v_cvt_pk_bf16_f32 v81, v34, v35
v_mfma_f32_16x16x32_bf16 v[136:139], a[116:119], v[88:91], v[136:139]
ds_read_b128 a[76:79], v221 offset:3072
v_exp_f32 v37, v37
s_mov_b32 m0, s43
v_mfma_f32_16x16x32_bf16 v[140:143], a[116:119], v[92:95], v[140:143]
ds_read_b128 a[80:83], v221 offset:4096
buffer_load_dwordx4 v252, s[16:19], s41 offen offset:0 sc1 lds
v_mfma_f32_16x16x32_bf16 v[144:147], a[120:123], v[88:91], v[144:147]
v_exp_f32 v38, v38
v_mfma_f32_16x16x32_bf16 v[148:151], a[120:123], v[92:95], v[148:151]
ds_read_b128 a[84:87], v221 offset:5120
v_cvt_pk_bf16_f32 v84, v36, v37
v_mfma_f32_16x16x32_bf16 v[152:155], a[124:127], v[88:91], v[152:155]
v_exp_f32 v39, v39
v_mfma_f32_16x16x32_bf16 v[156:159], a[124:127], v[92:95], v[156:159]
ds_read_b128 a[88:91], v221 offset:6144
buffer_load_dwordx4 v252, s[16:19], s41 offen offset:1024 sc1 lds
s_waitcnt lgkmcnt(3)
v_mfma_f32_16x16x32_bf16 v[96:99], a[64:67], v[64:67], v[96:99]
v_exp_f32 v40, v40
v_mfma_f32_16x16x32_bf16 v[100:103], a[64:67], v[68:71], v[100:103]
ds_read_b128 a[92:95], v221 offset:7168
v_cvt_pk_bf16_f32 v85, v38, v39
v_mfma_f32_16x16x32_bf16 v[104:107], a[68:71], v[64:67], v[104:107]
v_exp_f32 v41, v41
v_mfma_f32_16x16x32_bf16 v[108:111], a[68:71], v[68:71], v[108:111]
ds_read_b128 a[96:99], v221 offset:8192
buffer_load_dwordx4 v252, s[16:19], s41 offen offset:2048 sc1 lds
v_mfma_f32_16x16x32_bf16 v[112:115], a[72:75], v[64:67], v[112:115]
v_exp_f32 v42, v42
v_mfma_f32_16x16x32_bf16 v[116:119], a[72:75], v[68:71], v[116:119]
ds_read_b128 a[100:103], v221 offset:9216
v_cvt_pk_bf16_f32 v82, v40, v41
v_mfma_f32_16x16x32_bf16 v[120:123], a[76:79], v[64:67], v[120:123]
v_exp_f32 v43, v43
v_mfma_f32_16x16x32_bf16 v[124:127], a[76:79], v[68:71], v[124:127]
ds_read_b128 a[104:107], v221 offset:10240
buffer_load_dwordx4 v252, s[16:19], s41 offen offset:3072 sc1 lds
s_add_u32 m0, s43, 4096
v_mfma_f32_16x16x32_bf16 v[208:211], a[160:163], v[64:67], v[208:211]
v_mfma_f32_16x16x32_bf16 v[212:215], a[160:163], v[68:71], v[212:215]
s_waitcnt lgkmcnt(3)
v_mfma_f32_16x16x32_bf16 v[128:131], a[80:83], v[64:67], v[128:131]
v_exp_f32 v44, v44
v_mfma_f32_16x16x32_bf16 v[132:135], a[80:83], v[68:71], v[132:135]
ds_read_b128 a[108:111], v221 offset:11264
v_cvt_pk_bf16_f32 v83, v42, v43
v_mfma_f32_16x16x32_bf16 v[136:139], a[84:87], v[64:67], v[136:139]
v_exp_f32 v45, v45
v_mfma_f32_16x16x32_bf16 v[140:143], a[84:87], v[68:71], v[140:143]
ds_read_b128 a[112:115], v221 offset:12288
buffer_load_dwordx4 v244, s[16:19], s41 offen offset:0 sc1 lds
v_mfma_f32_16x16x32_bf16 v[144:147], a[88:91], v[64:67], v[144:147]
v_exp_f32 v46, v46
v_mfma_f32_16x16x32_bf16 v[148:151], a[88:91], v[68:71], v[148:151]
ds_read_b128 a[116:119], v221 offset:13312
v_cvt_pk_bf16_f32 v86, v44, v45
v_mfma_f32_16x16x32_bf16 v[152:155], a[92:95], v[64:67], v[152:155]
v_exp_f32 v47, v47
v_mfma_f32_16x16x32_bf16 v[156:159], a[92:95], v[68:71], v[156:159]
ds_read_b128 a[120:123], v221 offset:14336
buffer_load_dwordx4 v244, s[16:19], s41 offen offset:1024 sc1 lds
s_waitcnt lgkmcnt(3)
v_mfma_f32_16x16x32_bf16 v[96:99], a[96:99], v[72:75], v[96:99]
v_exp_f32 v48, v48
v_mfma_f32_16x16x32_bf16 v[100:103], a[96:99], v[76:79], v[100:103]
ds_read_b128 a[0:3], v220 offset:0
ds_read_b128 a[124:127], v221 offset:15360
v_cvt_pk_bf16_f32 v87, v46, v47
v_mfma_f32_16x16x32_bf16 v[104:107], a[100:103], v[72:75], v[104:107]
v_exp_f32 v49, v49
v_mfma_f32_16x16x32_bf16 v[108:111], a[100:103], v[76:79], v[108:111]
ds_read_b128 a[4:7], v220 offset:1024
buffer_load_dwordx4 v244, s[16:19], s41 offen offset:2048 sc1 lds
v_mfma_f32_16x16x32_bf16 v[112:115], a[104:107], v[72:75], v[112:115]
v_exp_f32 v50, v50
v_mfma_f32_16x16x32_bf16 v[116:119], a[104:107], v[76:79], v[116:119]
ds_read_b128 a[8:11], v220 offset:2048
v_cvt_pk_bf16_f32 v88, v48, v49
v_mfma_f32_16x16x32_bf16 v[120:123], a[108:111], v[72:75], v[120:123]
v_exp_f32 v51, v51
v_mfma_f32_16x16x32_bf16 v[124:127], a[108:111], v[76:79], v[124:127]
ds_read_b128 a[12:15], v220 offset:3072
buffer_load_dwordx4 v244, s[16:19], s41 offen offset:3072 sc1 lds
v_mfma_f32_16x16x32_bf16 v[208:211], a[160:163], v[72:75], v[208:211]
s_add_u32 s41, s41, 0x10000
s_xor_b32 s43, s43, 0x8000
v_mfma_f32_16x16x32_bf16 v[212:215], a[160:163], v[76:79], v[212:215]
s_cmp_eq_u32 s41, s47
s_cselect_b32 s41, s56, s41
s_cselect_b32 s47, s58, s47
s_waitcnt lgkmcnt(3)
v_mfma_f32_16x16x32_bf16 v[128:131], a[112:115], v[72:75], v[128:131]
v_exp_f32 v52, v52
v_mfma_f32_16x16x32_bf16 v[132:135], a[112:115], v[76:79], v[132:135]
ds_read_b128 a[16:19], v220 offset:4096
v_cvt_pk_bf16_f32 v89, v50, v51
v_mfma_f32_16x16x32_bf16 v[136:139], a[116:119], v[72:75], v[136:139]
v_exp_f32 v53, v53
v_mfma_f32_16x16x32_bf16 v[140:143], a[116:119], v[76:79], v[140:143]
ds_read_b128 a[20:23], v220 offset:5120
v_mfma_f32_16x16x32_bf16 v[144:147], a[120:123], v[72:75], v[144:147]
v_exp_f32 v54, v54
v_mfma_f32_16x16x32_bf16 v[148:151], a[120:123], v[76:79], v[148:151]
ds_read_b128 a[24:27], v220 offset:6144
v_cvt_pk_bf16_f32 v92, v52, v53
v_mfma_f32_16x16x32_bf16 v[152:155], a[124:127], v[72:75], v[152:155]
v_exp_f32 v55, v55
v_mfma_f32_16x16x32_bf16 v[156:159], a[124:127], v[76:79], v[156:159]
ds_read_b128 a[28:31], v220 offset:7168
s_sub_u32 s48, s48, 1
s_cmp_eq_u32 s48, 1
s_cbranch_scc0 K2_LOOP
K2_LAST:
v_mov_b32 v226, 0xff800000
v_lshrrev_b32 v227, 4, v253
v_lshlrev_b32 v227, 2, v227
v_and_b32 v228, 15, v253
v_sub_u32 v228, v228, v227
v_cmp_gt_i32 vcc, 0, v228
s_nop 1
v_cndmask_b32 v200, v192, v226, vcc
v_cndmask_b32 v204, v196, v226, vcc
v_cmp_gt_i32 vcc, 1, v228
s_nop 1
v_cndmask_b32 v201, v193, v226, vcc
v_cndmask_b32 v205, v197, v226, vcc
v_cmp_gt_i32 vcc, 2, v228
s_nop 1
v_cndmask_b32 v202, v194, v226, vcc
v_cndmask_b32 v206, v198, v226, vcc
v_cmp_gt_i32 vcc, 3, v228
s_nop 1
v_cndmask_b32 v203, v195, v226, vcc
v_cndmask_b32 v207, v199, v226, vcc
s_nop 1
s_cmp_lg_u32 s7, 0
s_cbranch_scc1 K2_L1
s_waitcnt lgkmcnt(4)
v_mfma_f32_16x16x32_bf16 v[0:3], a[0:3], v[160:163], v[200:203]
v_exp_f32 v56, v56
v_mfma_f32_16x16x32_bf16 v[4:7], a[0:3], v[176:179], v[196:199]
ds_read_b128 a[32:35], v220 offset:8192
v_cvt_pk_bf16_f32 v93, v54, v55
v_mfma_f32_16x16x32_bf16 v[0:3], a[4:7], v[164:167], v[0:3]
v_exp_f32 v57, v57
s_mov_b32 m0, s42
v_mfma_f32_16x16x32_bf16 v[4:7], a[4:7], v[180:183], v[4:7]
ds_read_b128 a[36:39], v220 offset:9216
buffer_load_dwordx4 v252, s[16:19], s40 offen offset:0 sc1 lds
v_mfma_f32_16x16x32_bf16 v[0:3], a[8:11], v[168:171], v[0:3]
v_exp_f32 v58, v58
v_mfma_f32_16x16x32_bf16 v[4:7], a[8:11], v[184:187], v[4:7]
ds_read_b128 a[40:43], v220 offset:10240
v_cvt_pk_bf16_f32 v90, v56, v57
v_mfma_f32_16x16x32_bf16 v[0:3], a[12:15], v[172:175], v[0:3]
v_exp_f32 v59, v59
v_mfma_f32_16x16x32_bf16 v[4:7], a[12:15], v[188:191], v[4:7]
ds_read_b128 a[44:47], v220 offset:11264
buffer_load_dwordx4 v252, s[16:19], s40 offen offset:1024 sc1 lds
v_exp_f32 v60, v60
s_waitcnt lgkmcnt(7)
v_mfma_f32_16x16x32_bf16 v[12:15], a[16:19], v[176:179], v[196:199]
ds_read_b128 a[48:51], v220 offset:12288
v_cvt_pk_bf16_f32 v91, v58, v59
v_exp_f32 v61, v61
s_waitcnt lgkmcnt(7)
v_mfma_f32_16x16x32_bf16 v[12:15], a[20:23], v[180:183], v[12:15]
ds_read_b128 a[52:55], v220 offset:13312
buffer_load_dwordx4 v252, s[16:19], s40 offen offset:2048 sc1 lds
v_exp_f32 v62, v62
s_waitcnt lgkmcnt(7)
v_mfma_f32_16x16x32_bf16 v[12:15], a[24:27], v[184:187], v[12:15]
ds_read_b128 a[56:59], v220 offset:14336
v_cvt_pk_bf16_f32 v94, v60, v61
v_exp_f32 v63, v63
s_waitcnt lgkmcnt(7)
v_mfma_f32_16x16x32_bf16 v[12:15], a[28:31], v[188:191], v[12:15]
ds_read_b128 a[60:63], v220 offset:15360
buffer_load_dwordx4 v252, s[16:19], s40 offen offset:3072 sc1 lds
s_add_u32 m0, s42, 4096
v_exp_f32 v0, v0
s_waitcnt lgkmcnt(7)
v_mfma_f32_16x16x32_bf16 v[20:23], a[32:35], v[176:179], v[196:199]
ds_read_b128 a[0:3], v220 offset:16384
v_cvt_pk_bf16_f32 v95, v62, v63
v_exp_f32 v1, v1
s_waitcnt lgkmcnt(7)
v_mfma_f32_16x16x32_bf16 v[20:23], a[36:39], v[180:183], v[20:23]
ds_read_b128 a[4:7], v220 offset:17408
buffer_load_dwordx4 v244, s[16:19], s40 offen offset:0 sc1 lds
v_exp_f32 v2, v2
s_waitcnt lgkmcnt(7)
v_mfma_f32_16x16x32_bf16 v[20:23], a[40:43], v[184:187], v[20:23]
ds_read_b128 a[8:11], v220 offset:18432
v_cvt_pk_bf16_f32 v64, v0, v1
v_exp_f32 v3, v3
s_waitcnt lgkmcnt(7)
v_mfma_f32_16x16x32_bf16 v[20:23], a[44:47], v[188:191], v[20:23]
ds_read_b128 a[12:15], v220 offset:19456
buffer_load_dwordx4 v244, s[16:19], s40 offen offset:1024 sc1 lds
v_exp_f32 v4, v4
s_waitcnt lgkmcnt(7)
v_mfma_f32_16x16x32_bf16 v[28:31], a[48:51], v[176:179], v[196:199]
ds_read_b128 a[16:19], v220 offset:20480
v_cvt_pk_bf16_f32 v65, v2, v3
v_exp_f32 v5, v5
s_waitcnt lgkmcnt(7)
v_mfma_f32_16x16x32_bf16 v[28:31], a[52:55], v[180:183], v[28:31]
ds_read_b128 a[20:23], v220 offset:21504
buffer_load_dwordx4 v244, s[16:19], s40 offen offset:2048 sc1 lds
v_exp_f32 v6, v6
s_waitcnt lgkmcnt(7)
v_mfma_f32_16x16x32_bf16 v[28:31], a[56:59], v[184:187], v[28:31]
ds_read_b128 a[24:27], v220 offset:22528
v_cvt_pk_bf16_f32 v68, v4, v5
v_exp_f32 v7, v7
s_waitcnt lgkmcnt(7)
v_mfma_f32_16x16x32_bf16 v[28:31], a[60:63], v[188:191], v[28:31]
ds_read_b128 a[28:31], v220 offset:23552
buffer_load_dwordx4 v244, s[16:19], s40 offen offset:3072 sc1 lds
s_waitcnt lgkmcnt(7)
v_mfma_f32_16x16x32_bf16 v[36:39], a[0:3], v[176:179], v[196:199]
ds_read_b128 a[32:35], v220 offset:24576
v_cvt_pk_bf16_f32 v69, v6, v7
s_waitcnt lgkmcnt(7)
v_mfma_f32_16x16x32_bf16 v[36:39], a[4:7], v[180:183], v[36:39]
ds_read_b128 a[36:39], v220 offset:25600
s_waitcnt lgkmcnt(7)
v_mfma_f32_16x16x32_bf16 v[36:39], a[8:11], v[184:187], v[36:39]
ds_read_b128 a[40:43], v220 offset:26624
v_mov_b32 v66, 0
s_waitcnt lgkmcnt(7)
v_mfma_f32_16x16x32_bf16 v[36:39], a[12:15], v[188:191], v[36:39]
ds_read_b128 a[44:47], v220 offset:27648
v_exp_f32 v12, v12
s_waitcnt lgkmcnt(7)
v_mfma_f32_16x16x32_bf16 v[44:47], a[16:19], v[176:179], v[196:199]
ds_read_b128 a[48:51], v220 offset:28672
v_mov_b32 v67, 0
v_exp_f32 v13, v13
s_waitcnt lgkmcnt(7)
v_mfma_f32_16x16x32_bf16 v[44:47], a[20:23], v[180:183], v[44:47]
ds_read_b128 a[52:55], v220 offset:29696
v_exp_f32 v14, v14
s_waitcnt lgkmcnt(7)
v_mfma_f32_16x16x32_bf16 v[44:47], a[24:27], v[184:187], v[44:47]
ds_read_b128 a[56:59], v220 offset:30720
v_cvt_pk_bf16_f32 v70, v12, v13
v_exp_f32 v15, v15
s_waitcnt lgkmcnt(7)
v_mfma_f32_16x16x32_bf16 v[44:47], a[28:31], v[188:191], v[44:47]
ds_read_b128 a[60:63], v220 offset:31744
s_waitcnt lgkmcnt(7)
v_mfma_f32_16x16x32_bf16 v[52:55], a[32:35], v[176:179], v[196:199]
ds_read_b128 a[64:67], v221 offset:16384
v_cvt_pk_bf16_f32 v71, v14, v15
v_xor_b32 v220, 0x8000, v220
s_waitcnt lgkmcnt(7)
v_mfma_f32_16x16x32_bf16 v[52:55], a[36:39], v[180:183], v[52:55]
ds_read_b128 a[68:71], v221 offset:17408
s_waitcnt lgkmcnt(7)
v_mfma_f32_16x16x32_bf16 v[52:55], a[40:43], v[184:187], v[52:55]
ds_read_b128 a[72:75], v221 offset:18432
s_waitcnt lgkmcnt(7)
v_mfma_f32_16x16x32_bf16 v[52:55], a[44:47], v[188:191], v[52:55]
ds_read_b128 a[76:79], v221 offset:19456
v_exp_f32 v20, v20
s_waitcnt lgkmcnt(7)
v_mfma_f32_16x16x32_bf16 v[60:63], a[48:51], v[176:179], v[204:207]
ds_read_b128 a[80:83], v221 offset:20480
v_exp_f32 v21, v21
s_waitcnt lgkmcnt(7)
v_mfma_f32_16x16x32_bf16 v[60:63], a[52:55], v[180:183], v[60:63]
ds_read_b128 a[84:87], v221 offset:21504
v_exp_f32 v22, v22
s_waitcnt lgkmcnt(7)
v_mfma_f32_16x16x32_bf16 v[60:63], a[56:59], v[184:187], v[60:63]
ds_read_b128 a[88:91], v221 offset:22528
v_cvt_pk_bf16_f32 v76, v20, v21
v_exp_f32 v23, v23
s_waitcnt lgkmcnt(7)
v_mfma_f32_16x16x32_bf16 v[60:63], a[60:63], v[188:191], v[60:63]
ds_read_b128 a[92:95], v221 offset:23552
s_waitcnt lgkmcnt(4)
v_mfma_f32_16x16x32_bf16 v[96:99], a[64:67], v[80:83], v[96:99]
v_mfma_f32_16x16x32_bf16 v[100:103], a[64:67], v[84:87], v[100:103]
ds_read_b128 a[96:99], v221 offset:24576
v_cvt_pk_bf16_f32 v77, v22, v23
v_mfma_f32_16x16x32_bf16 v[104:107], a[68:71], v[80:83], v[104:107]
v_mfma_f32_16x16x32_bf16 v[108:111], a[68:71], v[84:87], v[108:111]
ds_read_b128 a[100:103], v221 offset:25600
v_mfma_f32_16x16x32_bf16 v[112:115], a[72:75], v[80:83], v[112:115]
v_mfma_f32_16x16x32_bf16 v[116:119], a[72:75], v[84:87], v[116:119]
ds_read_b128 a[104:107], v221 offset:26624
v_mfma_f32_16x16x32_bf16 v[120:123], a[76:79], v[80:83], v[120:123]
v_mfma_f32_16x16x32_bf16 v[124:127], a[76:79], v[84:87], v[124:127]
ds_read_b128 a[108:111], v221 offset:27648
v_mfma_f32_16x16x32_bf16 v[208:211], a[160:163], v[80:83], v[208:211]
s_add_u32 s40, s40, 0x10000
s_xor_b32 s42, s42, 0x8000
v_mfma_f32_16x16x32_bf16 v[212:215], a[160:163], v[84:87], v[212:215]
s_cmp_eq_u32 s40, s45
s_cselect_b32 s40, s52, s40
s_cselect_b32 s45, s57, s45
s_waitcnt lgkmcnt(4)
v_mfma_f32_16x16x32_bf16 v[128:131], a[80:83], v[80:83], v[128:131]
v_exp_f32 v28, v28
v_mfma_f32_16x16x32_bf16 v[132:135], a[80:83], v[84:87], v[132:135]
ds_read_b128 a[112:115], v221 offset:28672
v_mfma_f32_16x16x32_bf16 v[136:139], a[84:87], v[80:83], v[136:139]
v_exp_f32 v29, v29
v_mfma_f32_16x16x32_bf16 v[140:143], a[84:87], v[84:87], v[140:143]
ds_read_b128 a[116:119], v221 offset:29696
v_mfma_f32_16x16x32_bf16 v[144:147], a[88:91], v[80:83], v[144:147]
v_exp_f32 v30, v30
v_mfma_f32_16x16x32_bf16 v[148:151], a[88:91], v[84:87], v[148:151]
ds_read_b128 a[120:123], v221 offset:30720
v_cvt_pk_bf16_f32 v78, v28, v29
v_mfma_f32_16x16x32_bf16 v[152:155], a[92:95], v[80:83], v[152:155]
v_exp_f32 v31, v31
v_mfma_f32_16x16x32_bf16 v[156:159], a[92:95], v[84:87], v[156:159]
ds_read_b128 a[124:127], v221 offset:31744
s_waitcnt lgkmcnt(4)
v_mfma_f32_16x16x32_bf16 v[96:99], a[96:99], v[88:91], v[96:99]
v_mfma_f32_16x16x32_bf16 v[100:103], a[96:99], v[92:95], v[100:103]
v_cvt_pk_bf16_f32 v79, v30, v31
v_mfma_f32_16x16x32_bf16 v[104:107], a[100:103], v[88:91], v[104:107]
v_mfma_f32_16x16x32_bf16 v[108:111], a[100:103], v[92:95], v[108:111]
v_mfma_f32_16x16x32_bf16 v[112:115], a[104:107], v[88:91], v[112:115]
v_xor_b32 v221, 0x8000, v221
v_mfma_f32_16x16x32_bf16 v[116:119], a[104:107], v[92:95], v[116:119]
s_waitcnt vmcnt(0) lgkmcnt(0)
s_barrier
v_mfma_f32_16x16x32_bf16 v[120:123], a[108:111], v[88:91], v[120:123]
v_mfma_f32_16x16x32_bf16 v[124:127], a[108:111], v[92:95], v[124:127]
ds_read_b128 a[64:67], v221 offset:0
v_mfma_f32_16x16x32_bf16 v[208:211], a[160:163], v[88:91], v[208:211]
v_mfma_f32_16x16x32_bf16 v[212:215], a[160:163], v[92:95], v[212:215]
v_mfma_f32_16x16x32_bf16 v[128:131], a[112:115], v[88:91], v[128:131]
ds_read_b128 a[68:71], v221 offset:1024
v_exp_f32 v36, v36
v_mfma_f32_16x16x32_bf16 v[132:135], a[112:115], v[92:95], v[132:135]
ds_read_b128 a[72:75], v221 offset:2048
v_mfma_f32_16x16x32_bf16 v[136:139], a[116:119], v[88:91], v[136:139]
ds_read_b128 a[76:79], v221 offset:3072
v_exp_f32 v37, v37
s_mov_b32 m0, s43
v_mfma_f32_16x16x32_bf16 v[140:143], a[116:119], v[92:95], v[140:143]
ds_read_b128 a[80:83], v221 offset:4096
buffer_load_dwordx4 v252, s[16:19], s41 offen offset:0 sc1 lds
v_mfma_f32_16x16x32_bf16 v[144:147], a[120:123], v[88:91], v[144:147]
v_exp_f32 v38, v38
v_mfma_f32_16x16x32_bf16 v[148:151], a[120:123], v[92:95], v[148:151]
ds_read_b128 a[84:87], v221 offset:5120
v_cvt_pk_bf16_f32 v84, v36, v37
v_mfma_f32_16x16x32_bf16 v[152:155], a[124:127], v[88:91], v[152:155]
v_exp_f32 v39, v39
v_mfma_f32_16x16x32_bf16 v[156:159], a[124:127], v[92:95], v[156:159]
ds_read_b128 a[88:91], v221 offset:6144
buffer_load_dwordx4 v252, s[16:19], s41 offen offset:1024 sc1 lds
s_waitcnt lgkmcnt(3)
v_mfma_f32_16x16x32_bf16 v[96:99], a[64:67], v[64:67], v[96:99]
v_mfma_f32_16x16x32_bf16 v[100:103], a[64:67], v[68:71], v[100:103]
ds_read_b128 a[92:95], v221 offset:7168
v_cvt_pk_bf16_f32 v85, v38, v39
v_mfma_f32_16x16x32_bf16 v[104:107], a[68:71], v[64:67], v[104:107]
v_mfma_f32_16x16x32_bf16 v[108:111], a[68:71], v[68:71], v[108:111]
ds_read_b128 a[96:99], v221 offset:8192
buffer_load_dwordx4 v252, s[16:19], s41 offen offset:2048 sc1 lds
v_mfma_f32_16x16x32_bf16 v[112:115], a[72:75], v[64:67], v[112:115]
v_mfma_f32_16x16x32_bf16 v[116:119], a[72:75], v[68:71], v[116:119]
ds_read_b128 a[100:103], v221 offset:9216
v_mfma_f32_16x16x32_bf16 v[120:123], a[76:79], v[64:67], v[120:123]
v_mfma_f32_16x16x32_bf16 v[124:127], a[76:79], v[68:71], v[124:127]
ds_read_b128 a[104:107], v221 offset:10240
buffer_load_dwordx4 v252, s[16:19], s41 offen offset:3072 sc1 lds
s_add_u32 m0, s43, 4096
v_mfma_f32_16x16x32_bf16 v[208:211], a[160:163], v[64:67], v[208:211]
v_mfma_f32_16x16x32_bf16 v[212:215], a[160:163], v[68:71], v[212:215]
s_waitcnt lgkmcnt(3)
v_mfma_f32_16x16x32_bf16 v[128:131], a[80:83], v[64:67], v[128:131]
v_exp_f32 v44, v44
v_mfma_f32_16x16x32_bf16 v[132:135], a[80:83], v[68:71], v[132:135]
ds_read_b128 a[108:111], v221 offset:11264
v_mfma_f32_16x16x32_bf16 v[136:139], a[84:87], v[64:67], v[136:139]
v_exp_f32 v45, v45
v_mfma_f32_16x16x32_bf16 v[140:143], a[84:87], v[68:71], v[140:143]
ds_read_b128 a[112:115], v221 offset:12288
buffer_load_dwordx4 v244, s[16:19], s41 offen offset:0 sc1 lds
v_mfma_f32_16x16x32_bf16 v[144:147], a[88:91], v[64:67], v[144:147]
v_exp_f32 v46, v46
v_mfma_f32_16x16x32_bf16 v[148:151], a[88:91], v[68:71], v[148:151]
ds_read_b128 a[116:119], v221 offset:13312
v_cvt_pk_bf16_f32 v86, v44, v45
v_mfma_f32_16x16x32_bf16 v[152:155], a[92:95], v[64:67], v[152:155]
v_exp_f32 v47, v47
v_mfma_f32_16x16x32_bf16 v[156:159], a[92:95], v[68:71], v[156:159]
ds_read_b128 a[120:123], v221 offset:14336
buffer_load_dwordx4 v244, s[16:19], s41 offen offset:1024 sc1 lds
s_waitcnt lgkmcnt(6)
)ASM"
R"ASM(v_mfma_f32_16x16x32_bf16 v[100:103], a[96:99], v[76:79], v[100:103]
ds_read_b128 a[124:127], v221 offset:15360
v_cvt_pk_bf16_f32 v87, v46, v47
s_waitcnt lgkmcnt(6)
v_mfma_f32_16x16x32_bf16 v[108:111], a[100:103], v[76:79], v[108:111]
buffer_load_dwordx4 v244, s[16:19], s41 offen offset:2048 sc1 lds
s_waitcnt lgkmcnt(5)
v_mfma_f32_16x16x32_bf16 v[116:119], a[104:107], v[76:79], v[116:119]
s_waitcnt lgkmcnt(4)
v_mfma_f32_16x16x32_bf16 v[124:127], a[108:111], v[76:79], v[124:127]
buffer_load_dwordx4 v244, s[16:19], s41 offen offset:3072 sc1 lds
v_mfma_f32_16x16x32_bf16 v[212:215], a[160:163], v[76:79], v[212:215]
v_exp_f32 v52, v52
s_waitcnt lgkmcnt(3)
v_mfma_f32_16x16x32_bf16 v[132:135], a[112:115], v[76:79], v[132:135]
s_add_u32 s41, s41, 0x10000
v_exp_f32 v53, v53
s_waitcnt lgkmcnt(2)
v_mfma_f32_16x16x32_bf16 v[140:143], a[116:119], v[76:79], v[140:143]
s_cmp_eq_u32 s41, s47
s_cselect_b32 s41, s56, s41
s_cselect_b32 s47, s58, s47
v_exp_f32 v54, v54
s_waitcnt lgkmcnt(1)
v_mfma_f32_16x16x32_bf16 v[148:151], a[120:123], v[76:79], v[148:151]
v_cvt_pk_bf16_f32 v92, v52, v53
s_xor_b32 s43, s43, 0x8000
v_exp_f32 v55, v55
s_waitcnt lgkmcnt(0)
v_mfma_f32_16x16x32_bf16 v[156:159], a[124:127], v[76:79], v[156:159]
v_cvt_pk_bf16_f32 v93, v54, v55
v_exp_f32 v60, v60
v_exp_f32 v61, v61
v_exp_f32 v62, v62
v_cvt_pk_bf16_f32 v94, v60, v61
v_exp_f32 v63, v63
s_nop 0
v_cvt_pk_bf16_f32 v95, v62, v63
ds_read_b128 a[64:67], v221 offset:16384
ds_read_b128 a[68:71], v221 offset:17408
ds_read_b128 a[72:75], v221 offset:24576
ds_read_b128 a[76:79], v221 offset:25600
ds_read_b128 a[80:83], v221 offset:18432
ds_read_b128 a[84:87], v221 offset:19456
ds_read_b128 a[88:91], v221 offset:26624
ds_read_b128 a[92:95], v221 offset:27648
v_mfma_f32_16x16x32_bf16 v[212:215], a[160:163], v[84:87], v[212:215]
v_mfma_f32_16x16x32_bf16 v[212:215], a[160:163], v[92:95], v[212:215]
s_waitcnt lgkmcnt(7)
v_mfma_f32_16x16x32_bf16 v[100:103], a[64:67], v[84:87], v[100:103]
ds_read_b128 a[96:99], v221 offset:20480
v_cmp_ngt_f32 vcc, 0x5d800000, v208
s_nop 1
s_mov_b64 s[92:93], vcc
s_nop 0
v_cmp_ngt_f32 vcc, 0x5d800000, v212
s_nop 1
s_or_b64 vcc, vcc, s[92:93]
s_cmp_lg_u64 vcc, 0
s_cbranch_scc1 K2_SAFE
v_rcp_f32 v226, v208
v_rcp_f32 v228, v212
s_lshl_b32 s36, s55, 7
s_lshl_b32 s37, s26, 2
s_add_u32 s36, s36, s37
s_lshl_b32 s36, s36, 13
s_sub_u32 s35, 7, s7
s_lshl_b32 s35, s35, 10
s_add_u32 s35, s35, s36
s_lshl_b32 s37, s7, 10
s_add_u32 s36, s36, s37
s_waitcnt lgkmcnt(7)
v_mfma_f32_16x16x32_bf16 v[108:111], a[68:71], v[84:87], v[108:111]
ds_read_b128 a[100:103], v221 offset:21504
s_waitcnt lgkmcnt(7)
v_mfma_f32_16x16x32_bf16 v[100:103], a[72:75], v[92:95], v[100:103]
ds_read_b128 a[104:107], v221 offset:28672
s_waitcnt lgkmcnt(7)
v_mfma_f32_16x16x32_bf16 v[108:111], a[76:79], v[92:95], v[108:111]
ds_read_b128 a[108:111], v221 offset:29696
s_waitcnt lgkmcnt(7)
v_mfma_f32_16x16x32_bf16 v[116:119], a[80:83], v[84:87], v[116:119]
ds_read_b128 a[112:115], v221 offset:22528
v_pk_mul_f32 v[96:97], v[96:97], v[226:227] op_sel_hi:[1,0]
v_pk_mul_f32 v[98:99], v[98:99], v[226:227] op_sel_hi:[1,0]
v_pk_mul_f32 v[104:105], v[104:105], v[226:227] op_sel_hi:[1,0]
v_pk_mul_f32 v[106:107], v[106:107], v[226:227] op_sel_hi:[1,0]
s_waitcnt lgkmcnt(7)
v_mfma_f32_16x16x32_bf16 v[124:127], a[84:87], v[84:87], v[124:127]
ds_read_b128 a[116:119], v221 offset:23552
v_cvt_pk_bf16_f32 v0, v96, v97
v_cvt_pk_bf16_f32 v1, v98, v99
v_cvt_pk_bf16_f32 v2, v104, v105
v_cvt_pk_bf16_f32 v3, v106, v107
s_add_u32 s37, s36, 0
buffer_store_dwordx4 v[0:3], v252, s[20:23], s37 offen sc1
v_pk_mul_f32 v[100:101], v[100:101], v[228:229] op_sel_hi:[1,0]
v_pk_mul_f32 v[102:103], v[102:103], v[228:229] op_sel_hi:[1,0]
s_waitcnt lgkmcnt(7)
v_mfma_f32_16x16x32_bf16 v[116:119], a[88:91], v[92:95], v[116:119]
ds_read_b128 a[120:123], v221 offset:30720
v_pk_mul_f32 v[108:109], v[108:109], v[228:229] op_sel_hi:[1,0]
v_pk_mul_f32 v[110:111], v[110:111], v[228:229] op_sel_hi:[1,0]
v_cvt_pk_bf16_f32 v4, v100, v101
v_cvt_pk_bf16_f32 v5, v102, v103
v_cvt_pk_bf16_f32 v6, v108, v109
v_cvt_pk_bf16_f32 v7, v110, v111
s_add_u32 s37, s35, 0
buffer_store_dwordx4 v[4:7], v252, s[20:23], s37 offen sc1
s_waitcnt lgkmcnt(7)
v_mfma_f32_16x16x32_bf16 v[124:127], a[92:95], v[92:95], v[124:127]
ds_read_b128 a[124:127], v221 offset:31744
s_waitcnt lgkmcnt(7)
v_mfma_f32_16x16x32_bf16 v[132:135], a[96:99], v[84:87], v[132:135]
v_pk_mul_f32 v[112:113], v[112:113], v[226:227] op_sel_hi:[1,0]
v_pk_mul_f32 v[114:115], v[114:115], v[226:227] op_sel_hi:[1,0]
v_pk_mul_f32 v[120:121], v[120:121], v[226:227] op_sel_hi:[1,0]
v_pk_mul_f32 v[122:123], v[122:123], v[226:227] op_sel_hi:[1,0]
s_waitcnt lgkmcnt(6)
v_mfma_f32_16x16x32_bf16 v[140:143], a[100:103], v[84:87], v[140:143]
v_cvt_pk_bf16_f32 v8, v112, v113
v_cvt_pk_bf16_f32 v9, v114, v115
v_cvt_pk_bf16_f32 v10, v120, v121
v_cvt_pk_bf16_f32 v11, v122, v123
s_add_u32 s37, s36, 8192
buffer_store_dwordx4 v[8:11], v252, s[20:23], s37 offen sc1
v_pk_mul_f32 v[116:117], v[116:117], v[228:229] op_sel_hi:[1,0]
v_pk_mul_f32 v[118:119], v[118:119], v[228:229] op_sel_hi:[1,0]
s_waitcnt lgkmcnt(5)
v_mfma_f32_16x16x32_bf16 v[132:135], a[104:107], v[92:95], v[132:135]
v_pk_mul_f32 v[124:125], v[124:125], v[228:229] op_sel_hi:[1,0]
v_pk_mul_f32 v[126:127], v[126:127], v[228:229] op_sel_hi:[1,0]
v_cvt_pk_bf16_f32 v12, v116, v117
v_cvt_pk_bf16_f32 v13, v118, v119
v_cvt_pk_bf16_f32 v14, v124, v125
v_cvt_pk_bf16_f32 v15, v126, v127
s_add_u32 s37, s35, 8192
buffer_store_dwordx4 v[12:15], v252, s[20:23], s37 offen sc1
s_waitcnt lgkmcnt(4)
v_mfma_f32_16x16x32_bf16 v[140:143], a[108:111], v[92:95], v[140:143]
s_waitcnt lgkmcnt(3)
v_mfma_f32_16x16x32_bf16 v[148:151], a[112:115], v[84:87], v[148:151]
v_pk_mul_f32 v[128:129], v[128:129], v[226:227] op_sel_hi:[1,0]
v_pk_mul_f32 v[130:131], v[130:131], v[226:227] op_sel_hi:[1,0]
v_pk_mul_f32 v[136:137], v[136:137], v[226:227] op_sel_hi:[1,0]
v_pk_mul_f32 v[138:139], v[138:139], v[226:227] op_sel_hi:[1,0]
s_waitcnt lgkmcnt(2)
v_mfma_f32_16x16x32_bf16 v[156:159], a[116:119], v[84:87], v[156:159]
v_cvt_pk_bf16_f32 v0, v128, v129
v_cvt_pk_bf16_f32 v1, v130, v131
v_cvt_pk_bf16_f32 v2, v136, v137
v_cvt_pk_bf16_f32 v3, v138, v139
s_add_u32 s37, s36, 16384
buffer_store_dwordx4 v[0:3], v252, s[20:23], s37 offen sc1
v_pk_mul_f32 v[132:133], v[132:133], v[228:229] op_sel_hi:[1,0]
v_pk_mul_f32 v[134:135], v[134:135], v[228:229] op_sel_hi:[1,0]
s_waitcnt lgkmcnt(1)
v_mfma_f32_16x16x32_bf16 v[148:151], a[120:123], v[92:95], v[148:151]
v_pk_mul_f32 v[140:141], v[140:141], v[228:229] op_sel_hi:[1,0]
v_pk_mul_f32 v[142:143], v[142:143], v[228:229] op_sel_hi:[1,0]
v_cvt_pk_bf16_f32 v4, v132, v133
v_cvt_pk_bf16_f32 v5, v134, v135
v_cvt_pk_bf16_f32 v6, v140, v141
v_cvt_pk_bf16_f32 v7, v142, v143
s_add_u32 s37, s35, 16384
buffer_store_dwordx4 v[4:7], v252, s[20:23], s37 offen sc1
s_waitcnt lgkmcnt(0)
v_mfma_f32_16x16x32_bf16 v[156:159], a[124:127], v[92:95], v[156:159]
s_nop 7
v_pk_mul_f32 v[144:145], v[144:145], v[226:227] op_sel_hi:[1,0]
v_pk_mul_f32 v[146:147], v[146:147], v[226:227] op_sel_hi:[1,0]
v_pk_mul_f32 v[152:153], v[152:153], v[226:227] op_sel_hi:[1,0]
v_pk_mul_f32 v[154:155], v[154:155], v[226:227] op_sel_hi:[1,0]
v_cvt_pk_bf16_f32 v8, v144, v145
v_cvt_pk_bf16_f32 v9, v146, v147
v_cvt_pk_bf16_f32 v10, v152, v153
v_cvt_pk_bf16_f32 v11, v154, v155
s_add_u32 s37, s36, 24576
buffer_store_dwordx4 v[8:11], v252, s[20:23], s37 offen sc1
v_pk_mul_f32 v[148:149], v[148:149], v[228:229] op_sel_hi:[1,0]
v_pk_mul_f32 v[150:151], v[150:151], v[228:229] op_sel_hi:[1,0]
v_pk_mul_f32 v[156:157], v[156:157], v[228:229] op_sel_hi:[1,0]
v_pk_mul_f32 v[158:159], v[158:159], v[228:229] op_sel_hi:[1,0]
v_cvt_pk_bf16_f32 v12, v148, v149
v_cvt_pk_bf16_f32 v13, v150, v151
v_cvt_pk_bf16_f32 v14, v156, v157
v_cvt_pk_bf16_f32 v15, v158, v159
s_add_u32 s37, s35, 24576
buffer_store_dwordx4 v[12:15], v252, s[20:23], s37 offen sc1
s_branch K2_EPI
K2_L1:
s_cmp_lg_u32 s7, 1
s_cbranch_scc1 K2_L2
s_waitcnt lgkmcnt(4)
v_mfma_f32_16x16x32_bf16 v[0:3], a[0:3], v[160:163], v[192:195]
v_exp_f32 v56, v56
v_mfma_f32_16x16x32_bf16 v[4:7], a[0:3], v[176:179], v[196:199]
ds_read_b128 a[32:35], v220 offset:8192
v_cvt_pk_bf16_f32 v93, v54, v55
v_mfma_f32_16x16x32_bf16 v[0:3], a[4:7], v[164:167], v[0:3]
v_exp_f32 v57, v57
s_mov_b32 m0, s42
v_mfma_f32_16x16x32_bf16 v[4:7], a[4:7], v[180:183], v[4:7]
ds_read_b128 a[36:39], v220 offset:9216
buffer_load_dwordx4 v252, s[16:19], s40 offen offset:0 sc1 lds
v_mfma_f32_16x16x32_bf16 v[0:3], a[8:11], v[168:171], v[0:3]
v_exp_f32 v58, v58
v_mfma_f32_16x16x32_bf16 v[4:7], a[8:11], v[184:187], v[4:7]
ds_read_b128 a[40:43], v220 offset:10240
v_cvt_pk_bf16_f32 v90, v56, v57
v_mfma_f32_16x16x32_bf16 v[0:3], a[12:15], v[172:175], v[0:3]
v_exp_f32 v59, v59
v_mfma_f32_16x16x32_bf16 v[4:7], a[12:15], v[188:191], v[4:7]
ds_read_b128 a[44:47], v220 offset:11264
buffer_load_dwordx4 v252, s[16:19], s40 offen offset:1024 sc1 lds
s_waitcnt lgkmcnt(4)
v_mfma_f32_16x16x32_bf16 v[8:11], a[16:19], v[160:163], v[200:203]
v_exp_f32 v60, v60
v_mfma_f32_16x16x32_bf16 v[12:15], a[16:19], v[176:179], v[196:199]
ds_read_b128 a[48:51], v220 offset:12288
v_cvt_pk_bf16_f32 v91, v58, v59
v_mfma_f32_16x16x32_bf16 v[8:11], a[20:23], v[164:167], v[8:11]
v_exp_f32 v61, v61
v_mfma_f32_16x16x32_bf16 v[12:15], a[20:23], v[180:183], v[12:15]
ds_read_b128 a[52:55], v220 offset:13312
buffer_load_dwordx4 v252, s[16:19], s40 offen offset:2048 sc1 lds
v_mfma_f32_16x16x32_bf16 v[8:11], a[24:27], v[168:171], v[8:11]
v_exp_f32 v62, v62
v_mfma_f32_16x16x32_bf16 v[12:15], a[24:27], v[184:187], v[12:15]
ds_read_b128 a[56:59], v220 offset:14336
v_cvt_pk_bf16_f32 v94, v60, v61
v_mfma_f32_16x16x32_bf16 v[8:11], a[28:31], v[172:175], v[8:11]
v_exp_f32 v63, v63
v_mfma_f32_16x16x32_bf16 v[12:15], a[28:31], v[188:191], v[12:15]
ds_read_b128 a[60:63], v220 offset:15360
buffer_load_dwordx4 v252, s[16:19], s40 offen offset:3072 sc1 lds
s_add_u32 m0, s42, 4096
v_exp_f32 v0, v0
s_waitcnt lgkmcnt(7)
v_mfma_f32_16x16x32_bf16 v[20:23], a[32:35], v[176:179], v[196:199]
ds_read_b128 a[0:3], v220 offset:16384
v_cvt_pk_bf16_f32 v95, v62, v63
v_exp_f32 v1, v1
s_waitcnt lgkmcnt(7)
v_mfma_f32_16x16x32_bf16 v[20:23], a[36:39], v[180:183], v[20:23]
ds_read_b128 a[4:7], v220 offset:17408
buffer_load_dwordx4 v244, s[16:19], s40 offen offset:0 sc1 lds
v_exp_f32 v2, v2
s_waitcnt lgkmcnt(7)
v_mfma_f32_16x16x32_bf16 v[20:23], a[40:43], v[184:187], v[20:23]
ds_read_b128 a[8:11], v220 offset:18432
v_cvt_pk_bf16_f32 v64, v0, v1
v_exp_f32 v3, v3
s_waitcnt lgkmcnt(7)
v_mfma_f32_16x16x32_bf16 v[20:23], a[44:47], v[188:191], v[20:23]
ds_read_b128 a[12:15], v220 offset:19456
buffer_load_dwordx4 v244, s[16:19], s40 offen offset:1024 sc1 lds
v_exp_f32 v4, v4
s_waitcnt lgkmcnt(7)
v_mfma_f32_16x16x32_bf16 v[28:31], a[48:51], v[176:179], v[196:199]
ds_read_b128 a[16:19], v220 offset:20480
v_cvt_pk_bf16_f32 v65, v2, v3
v_exp_f32 v5, v5
s_waitcnt lgkmcnt(7)
v_mfma_f32_16x16x32_bf16 v[28:31], a[52:55], v[180:183], v[28:31]
ds_read_b128 a[20:23], v220 offset:21504
buffer_load_dwordx4 v244, s[16:19], s40 offen offset:2048 sc1 lds
v_exp_f32 v6, v6
s_waitcnt lgkmcnt(7)
v_mfma_f32_16x16x32_bf16 v[28:31], a[56:59], v[184:187], v[28:31]
ds_read_b128 a[24:27], v220 offset:22528
v_cvt_pk_bf16_f32 v68, v4, v5
v_exp_f32 v7, v7
s_waitcnt lgkmcnt(7)
v_mfma_f32_16x16x32_bf16 v[28:31], a[60:63], v[188:191], v[28:31]
ds_read_b128 a[28:31], v220 offset:23552
buffer_load_dwordx4 v244, s[16:19], s40 offen offset:3072 sc1 lds
v_exp_f32 v8, v8
s_waitcnt lgkmcnt(7)
v_mfma_f32_16x16x32_bf16 v[36:39], a[0:3], v[176:179], v[196:199]
ds_read_b128 a[32:35], v220 offset:24576
v_cvt_pk_bf16_f32 v69, v6, v7
v_exp_f32 v9, v9
s_waitcnt lgkmcnt(7)
v_mfma_f32_16x16x32_bf16 v[36:39], a[4:7], v[180:183], v[36:39]
ds_read_b128 a[36:39], v220 offset:25600
v_exp_f32 v10, v10
s_waitcnt lgkmcnt(7)
v_mfma_f32_16x16x32_bf16 v[36:39], a[8:11], v[184:187], v[36:39]
ds_read_b128 a[40:43], v220 offset:26624
v_cvt_pk_bf16_f32 v66, v8, v9
v_exp_f32 v11, v11
s_waitcnt lgkmcnt(7)
v_mfma_f32_16x16x32_bf16 v[36:39], a[12:15], v[188:191], v[36:39]
ds_read_b128 a[44:47], v220 offset:27648
v_exp_f32 v12, v12
s_waitcnt lgkmcnt(7)
v_mfma_f32_16x16x32_bf16 v[44:47], a[16:19], v[176:179], v[196:199]
v_cvt_pk_bf16_f32 v67, v10, v11
v_exp_f32 v13, v13
s_waitcnt lgkmcnt(6)
v_mfma_f32_16x16x32_bf16 v[44:47], a[20:23], v[180:183], v[44:47]
v_exp_f32 v14, v14
s_waitcnt lgkmcnt(5)
v_mfma_f32_16x16x32_bf16 v[44:47], a[24:27], v[184:187], v[44:47]
v_cvt_pk_bf16_f32 v70, v12, v13
v_exp_f32 v15, v15
s_waitcnt lgkmcnt(4)
v_mfma_f32_16x16x32_bf16 v[44:47], a[28:31], v[188:191], v[44:47]
s_waitcnt lgkmcnt(3)
v_mfma_f32_16x16x32_bf16 v[52:55], a[32:35], v[176:179], v[204:207]
ds_read_b128 a[64:67], v221 offset:16384
v_cvt_pk_bf16_f32 v71, v14, v15
v_xor_b32 v220, 0x8000, v220
s_waitcnt lgkmcnt(3)
v_mfma_f32_16x16x32_bf16 v[52:55], a[36:39], v[180:183], v[52:55]
ds_read_b128 a[68:71], v221 offset:17408
s_waitcnt lgkmcnt(3)
v_mfma_f32_16x16x32_bf16 v[52:55], a[40:43], v[184:187], v[52:55]
ds_read_b128 a[72:75], v221 offset:18432
s_waitcnt lgkmcnt(3)
v_mfma_f32_16x16x32_bf16 v[52:55], a[44:47], v[188:191], v[52:55]
ds_read_b128 a[76:79], v221 offset:19456
v_exp_f32 v20, v20
ds_read_b128 a[80:83], v221 offset:20480
v_exp_f32 v21, v21
ds_read_b128 a[84:87], v221 offset:21504
v_exp_f32 v22, v22
ds_read_b128 a[88:91], v221 offset:22528
v_cvt_pk_bf16_f32 v76, v20, v21
v_exp_f32 v23, v23
ds_read_b128 a[92:95], v221 offset:23552
s_waitcnt lgkmcnt(4)
v_mfma_f32_16x16x32_bf16 v[96:99], a[64:67], v[80:83], v[96:99]
v_mfma_f32_16x16x32_bf16 v[100:103], a[64:67], v[84:87], v[100:103]
ds_read_b128 a[96:99], v221 offset:24576
v_cvt_pk_bf16_f32 v77, v22, v23
v_mfma_f32_16x16x32_bf16 v[104:107], a[68:71], v[80:83], v[104:107]
v_mfma_f32_16x16x32_bf16 v[108:111], a[68:71], v[84:87], v[108:111]
ds_read_b128 a[100:103], v221 offset:25600
v_mfma_f32_16x16x32_bf16 v[112:115], a[72:75], v[80:83], v[112:115]
v_mfma_f32_16x16x32_bf16 v[116:119], a[72:75], v[84:87], v[116:119]
ds_read_b128 a[104:107], v221 offset:26624
v_mfma_f32_16x16x32_bf16 v[120:123], a[76:79], v[80:83], v[120:123]
v_mfma_f32_16x16x32_bf16 v[124:127], a[76:79], v[84:87], v[124:127]
ds_read_b128 a[108:111], v221 offset:27648
v_mfma_f32_16x16x32_bf16 v[208:211], a[160:163], v[80:83], v[208:211]
s_add_u32 s40, s40, 0x10000
s_xor_b32 s42, s42, 0x8000
v_mfma_f32_16x16x32_bf16 v[212:215], a[160:163], v[84:87], v[212:215]
s_cmp_eq_u32 s40, s45
s_cselect_b32 s40, s52, s40
s_cselect_b32 s45, s57, s45
s_waitcnt lgkmcnt(4)
v_mfma_f32_16x16x32_bf16 v[128:131], a[80:83], v[80:83], v[128:131]
v_exp_f32 v28, v28
v_mfma_f32_16x16x32_bf16 v[132:135], a[80:83], v[84:87], v[132:135]
ds_read_b128 a[112:115], v221 offset:28672
v_mfma_f32_16x16x32_bf16 v[136:139], a[84:87], v[80:83], v[136:139]
v_exp_f32 v29, v29
v_mfma_f32_16x16x32_bf16 v[140:143], a[84:87], v[84:87], v[140:143]
ds_read_b128 a[116:119], v221 offset:29696
v_mfma_f32_16x16x32_bf16 v[144:147], a[88:91], v[80:83], v[144:147]
v_exp_f32 v30, v30
v_mfma_f32_16x16x32_bf16 v[148:151], a[88:91], v[84:87], v[148:151]
ds_read_b128 a[120:123], v221 offset:30720
v_cvt_pk_bf16_f32 v78, v28, v29
v_mfma_f32_16x16x32_bf16 v[152:155], a[92:95], v[80:83], v[152:155]
v_exp_f32 v31, v31
v_mfma_f32_16x16x32_bf16 v[156:159], a[92:95], v[84:87], v[156:159]
ds_read_b128 a[124:127], v221 offset:31744
s_waitcnt lgkmcnt(4)
v_mfma_f32_16x16x32_bf16 v[96:99], a[96:99], v[88:91], v[96:99]
v_mfma_f32_16x16x32_bf16 v[100:103], a[96:99], v[92:95], v[100:103]
v_cvt_pk_bf16_f32 v79, v30, v31
v_mfma_f32_16x16x32_bf16 v[104:107], a[100:103], v[88:91], v[104:107]
v_mfma_f32_16x16x32_bf16 v[108:111], a[100:103], v[92:95], v[108:111]
v_mfma_f32_16x16x32_bf16 v[112:115], a[104:107], v[88:91], v[112:115]
v_xor_b32 v221, 0x8000, v221
v_mfma_f32_16x16x32_bf16 v[116:119], a[104:107], v[92:95], v[116:119]
s_waitcnt vmcnt(0) lgkmcnt(0)
s_barrier
v_mfma_f32_16x16x32_bf16 v[120:123], a[108:111], v[88:91], v[120:123]
v_mfma_f32_16x16x32_bf16 v[124:127], a[108:111], v[92:95], v[124:127]
ds_read_b128 a[64:67], v221 offset:0
v_mfma_f32_16x16x32_bf16 v[208:211], a[160:163], v[88:91], v[208:211]
v_mfma_f32_16x16x32_bf16 v[212:215], a[160:163], v[92:95], v[212:215]
v_mfma_f32_16x16x32_bf16 v[128:131], a[112:115], v[88:91], v[128:131]
ds_read_b128 a[68:71], v221 offset:1024
v_exp_f32 v36, v36
v_mfma_f32_16x16x32_bf16 v[132:135], a[112:115], v[92:95], v[132:135]
ds_read_b128 a[72:75], v221 offset:2048
v_mfma_f32_16x16x32_bf16 v[136:139], a[116:119], v[88:91], v[136:139]
ds_read_b128 a[76:79], v221 offset:3072
v_exp_f32 v37, v37
s_mov_b32 m0, s43
v_mfma_f32_16x16x32_bf16 v[140:143], a[116:119], v[92:95], v[140:143]
ds_read_b128 a[80:83], v221 offset:4096
buffer_load_dwordx4 v252, s[16:19], s41 offen offset:0 sc1 lds
v_mfma_f32_16x16x32_bf16 v[144:147], a[120:123], v[88:91], v[144:147]
v_exp_f32 v38, v38
v_mfma_f32_16x16x32_bf16 v[148:151], a[120:123], v[92:95], v[148:151]
ds_read_b128 a[84:87], v221 offset:5120
v_cvt_pk_bf16_f32 v84, v36, v37
v_mfma_f32_16x16x32_bf16 v[152:155], a[124:127], v[88:91], v[152:155]
v_exp_f32 v39, v39
v_mfma_f32_16x16x32_bf16 v[156:159], a[124:127], v[92:95], v[156:159]
ds_read_b128 a[88:91], v221 offset:6144
buffer_load_dwordx4 v252, s[16:19], s41 offen offset:1024 sc1 lds
s_waitcnt lgkmcnt(3)
v_mfma_f32_16x16x32_bf16 v[96:99], a[64:67], v[64:67], v[96:99]
v_mfma_f32_16x16x32_bf16 v[100:103], a[64:67], v[68:71], v[100:103]
ds_read_b128 a[92:95], v221 offset:7168
v_cvt_pk_bf16_f32 v85, v38, v39
v_mfma_f32_16x16x32_bf16 v[104:107], a[68:71], v[64:67], v[104:107]
v_mfma_f32_16x16x32_bf16 v[108:111], a[68:71], v[68:71], v[108:111]
ds_read_b128 a[96:99], v221 offset:8192
buffer_load_dwordx4 v252, s[16:19], s41 offen offset:2048 sc1 lds
v_mfma_f32_16x16x32_bf16 v[112:115], a[72:75], v[64:67], v[112:115]
v_mfma_f32_16x16x32_bf16 v[116:119], a[72:75], v[68:71], v[116:119]
ds_read_b128 a[100:103], v221 offset:9216
v_mfma_f32_16x16x32_bf16 v[120:123], a[76:79], v[64:67], v[120:123]
v_mfma_f32_16x16x32_bf16 v[124:127], a[76:79], v[68:71], v[124:127]
ds_read_b128 a[104:107], v221 offset:10240
buffer_load_dwordx4 v252, s[16:19], s41 offen offset:3072 sc1 lds
s_add_u32 m0, s43, 4096
v_mfma_f32_16x16x32_bf16 v[208:211], a[160:163], v[64:67], v[208:211]
v_mfma_f32_16x16x32_bf16 v[212:215], a[160:163], v[68:71], v[212:215]
s_waitcnt lgkmcnt(3)
v_mfma_f32_16x16x32_bf16 v[128:131], a[80:83], v[64:67], v[128:131]
v_exp_f32 v44, v44
v_mfma_f32_16x16x32_bf16 v[132:135], a[80:83], v[68:71], v[132:135]
ds_read_b128 a[108:111], v221 offset:11264
v_mfma_f32_16x16x32_bf16 v[136:139], a[84:87], v[64:67], v[136:139]
v_exp_f32 v45, v45
v_mfma_f32_16x16x32_bf16 v[140:143], a[84:87], v[68:71], v[140:143]
ds_read_b128 a[112:115], v221 offset:12288
buffer_load_dwordx4 v244, s[16:19], s41 offen offset:0 sc1 lds
v_mfma_f32_16x16x32_bf16 v[144:147], a[88:91], v[64:67], v[144:147]
v_exp_f32 v46, v46
v_mfma_f32_16x16x32_bf16 v[148:151], a[88:91], v[68:71], v[148:151]
ds_read_b128 a[116:119], v221 offset:13312
v_cvt_pk_bf16_f32 v86, v44, v45
v_mfma_f32_16x16x32_bf16 v[152:155], a[92:95], v[64:67], v[152:155]
v_exp_f32 v47, v47
v_mfma_f32_16x16x32_bf16 v[156:159], a[92:95], v[68:71], v[156:159]
ds_read_b128 a[120:123], v221 offset:14336
buffer_load_dwordx4 v244, s[16:19], s41 offen offset:1024 sc1 lds
s_waitcnt lgkmcnt(6)
v_mfma_f32_16x16x32_bf16 v[100:103], a[96:99], v[76:79], v[100:103]
ds_read_b128 a[124:127], v221 offset:15360
v_cvt_pk_bf16_f32 v87, v46, v47
s_waitcnt lgkmcnt(6)
v_mfma_f32_16x16x32_bf16 v[108:111], a[100:103], v[76:79], v[108:111]
buffer_load_dwordx4 v244, s[16:19], s41 offen offset:2048 sc1 lds
s_waitcnt lgkmcnt(5)
v_mfma_f32_16x16x32_bf16 v[116:119], a[104:107], v[76:79], v[116:119]
s_waitcnt lgkmcnt(4)
v_mfma_f32_16x16x32_bf16 v[124:127], a[108:111], v[76:79], v[124:127]
buffer_load_dwordx4 v244, s[16:19], s41 offen offset:3072 sc1 lds
v_mfma_f32_16x16x32_bf16 v[212:215], a[160:163], v[76:79], v[212:215]
v_exp_f32 v52, v52
s_waitcnt lgkmcnt(3)
v_mfma_f32_16x16x32_bf16 v[132:135], a[112:115], v[76:79], v[132:135]
s_add_u32 s41, s41, 0x10000
v_exp_f32 v53, v53
s_waitcnt lgkmcnt(2)
v_mfma_f32_16x16x32_bf16 v[140:143], a[116:119], v[76:79], v[140:143]
s_cmp_eq_u32 s41, s47
s_cselect_b32 s41, s56, s41
s_cselect_b32 s47, s58, s47
v_exp_f32 v54, v54
s_waitcnt lgkmcnt(1)
v_mfma_f32_16x16x32_bf16 v[148:151], a[120:123], v[76:79], v[148:151]
v_cvt_pk_bf16_f32 v92, v52, v53
s_xor_b32 s43, s43, 0x8000
v_exp_f32 v55, v55
s_waitcnt lgkmcnt(0)
v_mfma_f32_16x16x32_bf16 v[156:159], a[124:127], v[76:79], v[156:159]
v_cvt_pk_bf16_f32 v93, v54, v55
v_mov_b32 v94, 0
v_mov_b32 v95, 0
ds_read_b128 a[64:67], v221 offset:16384
ds_read_b128 a[68:71], v221 offset:17408
ds_read_b128 a[72:75], v221 offset:24576
ds_read_b128 a[76:79], v221 offset:25600
ds_read_b128 a[80:83], v221 offset:18432
ds_read_b128 a[84:87], v221 offset:19456
ds_read_b128 a[88:91], v221 offset:26624
ds_read_b128 a[92:95], v221 offset:27648
v_mfma_f32_16x16x32_bf16 v[212:215], a[160:163], v[84:87], v[212:215]
v_mfma_f32_16x16x32_bf16 v[212:215], a[160:163], v[92:95], v[212:215]
s_waitcnt lgkmcnt(7)
v_mfma_f32_16x16x32_bf16 v[100:103], a[64:67], v[84:87], v[100:103]
ds_read_b128 a[96:99], v221 offset:20480
v_cmp_ngt_f32 vcc, 0x5d800000, v208
s_nop 1
s_mov_b64 s[92:93], vcc
s_nop 0
v_cmp_ngt_f32 vcc, 0x5d800000, v212
s_nop 1
s_or_b64 vcc, vcc, s[92:93]
s_cmp_lg_u64 vcc, 0
s_cbranch_scc1 K2_SAFE
v_rcp_f32 v226, v208
v_rcp_f32 v228, v212
s_lshl_b32 s36, s55, 7
s_lshl_b32 s37, s26, 2
s_add_u32 s36, s36, s37
s_lshl_b32 s36, s36, 13
s_sub_u32 s35, 7, s7
s_lshl_b32 s35, s35, 10
s_add_u32 s35, s35, s36
s_lshl_b32 s37, s7, 10
s_add_u32 s36, s36, s37
s_waitcnt lgkmcnt(7)
v_mfma_f32_16x16x32_bf16 v[108:111], a[68:71], v[84:87], v[108:111]
ds_read_b128 a[100:103], v221 offset:21504
s_waitcnt lgkmcnt(7)
v_mfma_f32_16x16x32_bf16 v[100:103], a[72:75], v[92:95], v[100:103]
ds_read_b128 a[104:107], v221 offset:28672
s_waitcnt lgkmcnt(7)
v_mfma_f32_16x16x32_bf16 v[108:111], a[76:79], v[92:95], v[108:111]
ds_read_b128 a[108:111], v221 offset:29696
s_waitcnt lgkmcnt(7)
v_mfma_f32_16x16x32_bf16 v[116:119], a[80:83], v[84:87], v[116:119]
ds_read_b128 a[112:115], v221 offset:22528
v_pk_mul_f32 v[96:97], v[96:97], v[226:227] op_sel_hi:[1,0]
v_pk_mul_f32 v[98:99], v[98:99], v[226:227] op_sel_hi:[1,0]
v_pk_mul_f32 v[104:105], v[104:105], v[226:227] op_sel_hi:[1,0]
v_pk_mul_f32 v[106:107], v[106:107], v[226:227] op_sel_hi:[1,0]
s_waitcnt lgkmcnt(7)
v_mfma_f32_16x16x32_bf16 v[124:127], a[84:87], v[84:87], v[124:127]
ds_read_b128 a[116:119], v221 offset:23552
v_cvt_pk_bf16_f32 v0, v96, v97
v_cvt_pk_bf16_f32 v1, v98, v99
v_cvt_pk_bf16_f32 v2, v104, v105
v_cvt_pk_bf16_f32 v3, v106, v107
s_add_u32 s37, s36, 0
buffer_store_dwordx4 v[0:3], v252, s[20:23], s37 offen sc1
v_pk_mul_f32 v[100:101], v[100:101], v[228:229] op_sel_hi:[1,0]
v_pk_mul_f32 v[102:103], v[102:103], v[228:229] op_sel_hi:[1,0]
s_waitcnt lgkmcnt(7)
v_mfma_f32_16x16x32_bf16 v[116:119], a[88:91], v[92:95], v[116:119]
ds_read_b128 a[120:123], v221 offset:30720
v_pk_mul_f32 v[108:109], v[108:109], v[228:229] op_sel_hi:[1,0]
v_pk_mul_f32 v[110:111], v[110:111], v[228:229] op_sel_hi:[1,0]
v_cvt_pk_bf16_f32 v4, v100, v101
v_cvt_pk_bf16_f32 v5, v102, v103
v_cvt_pk_bf16_f32 v6, v108, v109
v_cvt_pk_bf16_f32 v7, v110, v111
s_add_u32 s37, s35, 0
buffer_store_dwordx4 v[4:7], v252, s[20:23], s37 offen sc1
s_waitcnt lgkmcnt(7)
v_mfma_f32_16x16x32_bf16 v[124:127], a[92:95], v[92:95], v[124:127]
ds_read_b128 a[124:127], v221 offset:31744
s_waitcnt lgkmcnt(7)
v_mfma_f32_16x16x32_bf16 v[132:135], a[96:99], v[84:87], v[132:135]
v_pk_mul_f32 v[112:113], v[112:113], v[226:227] op_sel_hi:[1,0]
v_pk_mul_f32 v[114:115], v[114:115], v[226:227] op_sel_hi:[1,0]
v_pk_mul_f32 v[120:121], v[120:121], v[226:227] op_sel_hi:[1,0]
v_pk_mul_f32 v[122:123], v[122:123], v[226:227] op_sel_hi:[1,0]
s_waitcnt lgkmcnt(6)
v_mfma_f32_16x16x32_bf16 v[140:143], a[100:103], v[84:87], v[140:143]
v_cvt_pk_bf16_f32 v8, v112, v113
v_cvt_pk_bf16_f32 v9, v114, v115
v_cvt_pk_bf16_f32 v10, v120, v121
v_cvt_pk_bf16_f32 v11, v122, v123
s_add_u32 s37, s36, 8192
buffer_store_dwordx4 v[8:11], v252, s[20:23], s37 offen sc1
v_pk_mul_f32 v[116:117], v[116:117], v[228:229] op_sel_hi:[1,0]
v_pk_mul_f32 v[118:119], v[118:119], v[228:229] op_sel_hi:[1,0]
s_waitcnt lgkmcnt(5)
v_mfma_f32_16x16x32_bf16 v[132:135], a[104:107], v[92:95], v[132:135]
v_pk_mul_f32 v[124:125], v[124:125], v[228:229] op_sel_hi:[1,0]
v_pk_mul_f32 v[126:127], v[126:127], v[228:229] op_sel_hi:[1,0]
v_cvt_pk_bf16_f32 v12, v116, v117
v_cvt_pk_bf16_f32 v13, v118, v119
v_cvt_pk_bf16_f32 v14, v124, v125
v_cvt_pk_bf16_f32 v15, v126, v127
s_add_u32 s37, s35, 8192
buffer_store_dwordx4 v[12:15], v252, s[20:23], s37 offen sc1
s_waitcnt lgkmcnt(4)
v_mfma_f32_16x16x32_bf16 v[140:143], a[108:111], v[92:95], v[140:143]
s_waitcnt lgkmcnt(3)
v_mfma_f32_16x16x32_bf16 v[148:151], a[112:115], v[84:87], v[148:151]
v_pk_mul_f32 v[128:129], v[128:129], v[226:227] op_sel_hi:[1,0]
v_pk_mul_f32 v[130:131], v[130:131], v[226:227] op_sel_hi:[1,0]
v_pk_mul_f32 v[136:137], v[136:137], v[226:227] op_sel_hi:[1,0]
v_pk_mul_f32 v[138:139], v[138:139], v[226:227] op_sel_hi:[1,0]
s_waitcnt lgkmcnt(2)
v_mfma_f32_16x16x32_bf16 v[156:159], a[116:119], v[84:87], v[156:159]
v_cvt_pk_bf16_f32 v0, v128, v129
v_cvt_pk_bf16_f32 v1, v130, v131
v_cvt_pk_bf16_f32 v2, v136, v137
v_cvt_pk_bf16_f32 v3, v138, v139
s_add_u32 s37, s36, 16384
buffer_store_dwordx4 v[0:3], v252, s[20:23], s37 offen sc1
v_pk_mul_f32 v[132:133], v[132:133], v[228:229] op_sel_hi:[1,0]
v_pk_mul_f32 v[134:135], v[134:135], v[228:229] op_sel_hi:[1,0]
s_waitcnt lgkmcnt(1)
v_mfma_f32_16x16x32_bf16 v[148:151], a[120:123], v[92:95], v[148:151]
v_pk_mul_f32 v[140:141], v[140:141], v[228:229] op_sel_hi:[1,0]
v_pk_mul_f32 v[142:143], v[142:143], v[228:229] op_sel_hi:[1,0]
v_cvt_pk_bf16_f32 v4, v132, v133
v_cvt_pk_bf16_f32 v5, v134, v135
v_cvt_pk_bf16_f32 v6, v140, v141
v_cvt_pk_bf16_f32 v7, v142, v143
s_add_u32 s37, s35, 16384
buffer_store_dwordx4 v[4:7], v252, s[20:23], s37 offen sc1
s_waitcnt lgkmcnt(0)
v_mfma_f32_16x16x32_bf16 v[156:159], a[124:127], v[92:95], v[156:159]
s_nop 7
v_pk_mul_f32 v[144:145], v[144:145], v[226:227] op_sel_hi:[1,0]
v_pk_mul_f32 v[146:147], v[146:147], v[226:227] op_sel_hi:[1,0]
v_pk_mul_f32 v[152:153], v[152:153], v[226:227] op_sel_hi:[1,0]
v_pk_mul_f32 v[154:155], v[154:155], v[226:227] op_sel_hi:[1,0]
v_cvt_pk_bf16_f32 v8, v144, v145
v_cvt_pk_bf16_f32 v9, v146, v147
v_cvt_pk_bf16_f32 v10, v152, v153
v_cvt_pk_bf16_f32 v11, v154, v155
s_add_u32 s37, s36, 24576
buffer_store_dwordx4 v[8:11], v252, s[20:23], s37 offen sc1
v_pk_mul_f32 v[148:149], v[148:149], v[228:229] op_sel_hi:[1,0]
v_pk_mul_f32 v[150:151], v[150:151], v[228:229] op_sel_hi:[1,0]
v_pk_mul_f32 v[156:157], v[156:157], v[228:229] op_sel_hi:[1,0]
v_pk_mul_f32 v[158:159], v[158:159], v[228:229] op_sel_hi:[1,0]
v_cvt_pk_bf16_f32 v12, v148, v149
v_cvt_pk_bf16_f32 v13, v150, v151
v_cvt_pk_bf16_f32 v14, v156, v157
v_cvt_pk_bf16_f32 v15, v158, v159
s_add_u32 s37, s35, 24576
buffer_store_dwordx4 v[12:15], v252, s[20:23], s37 offen sc1
s_branch K2_EPI
K2_L2:
s_cmp_lg_u32 s7, 2
s_cbranch_scc1 K2_L3
s_waitcnt lgkmcnt(4)
v_mfma_f32_16x16x32_bf16 v[0:3], a[0:3], v[160:163], v[192:195]
v_exp_f32 v56, v56
v_mfma_f32_16x16x32_bf16 v[4:7], a[0:3], v[176:179], v[196:199]
ds_read_b128 a[32:35], v220 offset:8192
v_cvt_pk_bf16_f32 v93, v54, v55
v_mfma_f32_16x16x32_bf16 v[0:3], a[4:7], v[164:167], v[0:3]
v_exp_f32 v57, v57
s_mov_b32 m0, s42
v_mfma_f32_16x16x32_bf16 v[4:7], a[4:7], v[180:183], v[4:7]
ds_read_b128 a[36:39], v220 offset:9216
buffer_load_dwordx4 v252, s[16:19], s40 offen offset:0 sc1 lds
v_mfma_f32_16x16x32_bf16 v[0:3], a[8:11], v[168:171], v[0:3]
v_exp_f32 v58, v58
v_mfma_f32_16x16x32_bf16 v[4:7], a[8:11], v[184:187], v[4:7]
ds_read_b128 a[40:43], v220 offset:10240
v_cvt_pk_bf16_f32 v90, v56, v57
v_mfma_f32_16x16x32_bf16 v[0:3], a[12:15], v[172:175], v[0:3]
v_exp_f32 v59, v59
v_mfma_f32_16x16x32_bf16 v[4:7], a[12:15], v[188:191], v[4:7]
ds_read_b128 a[44:47], v220 offset:11264
buffer_load_dwordx4 v252, s[16:19], s40 offen offset:1024 sc1 lds
s_waitcnt lgkmcnt(4)
v_mfma_f32_16x16x32_bf16 v[8:11], a[16:19], v[160:163], v[192:195]
v_exp_f32 v60, v60
v_mfma_f32_16x16x32_bf16 v[12:15], a[16:19], v[176:179], v[196:199]
ds_read_b128 a[48:51], v220 offset:12288
v_cvt_pk_bf16_f32 v91, v58, v59
v_mfma_f32_16x16x32_bf16 v[8:11], a[20:23], v[164:167], v[8:11]
v_exp_f32 v61, v61
v_mfma_f32_16x16x32_bf16 v[12:15], a[20:23], v[180:183], v[12:15]
ds_read_b128 a[52:55], v220 offset:13312
buffer_load_dwordx4 v252, s[16:19], s40 offen offset:2048 sc1 lds
v_mfma_f32_16x16x32_bf16 v[8:11], a[24:27], v[168:171], v[8:11]
v_exp_f32 v62, v62
v_mfma_f32_16x16x32_bf16 v[12:15], a[24:27], v[184:187], v[12:15]
ds_read_b128 a[56:59], v220 offset:14336
v_cvt_pk_bf16_f32 v94, v60, v61
v_mfma_f32_16x16x32_bf16 v[8:11], a[28:31], v[172:175], v[8:11]
v_exp_f32 v63, v63
v_mfma_f32_16x16x32_bf16 v[12:15], a[28:31], v[188:191], v[12:15]
ds_read_b128 a[60:63], v220 offset:15360
buffer_load_dwordx4 v252, s[16:19], s40 offen offset:3072 sc1 lds
s_add_u32 m0, s42, 4096
s_waitcnt lgkmcnt(4)
v_mfma_f32_16x16x32_bf16 v[16:19], a[32:35], v[160:163], v[200:203]
v_exp_f32 v0, v0
v_mfma_f32_16x16x32_bf16 v[20:23], a[32:35], v[176:179], v[196:199]
ds_read_b128 a[0:3], v220 offset:16384
v_cvt_pk_bf16_f32 v95, v62, v63
v_mfma_f32_16x16x32_bf16 v[16:19], a[36:39], v[164:167], v[16:19]
v_exp_f32 v1, v1
v_mfma_f32_16x16x32_bf16 v[20:23], a[36:39], v[180:183], v[20:23]
ds_read_b128 a[4:7], v220 offset:17408
buffer_load_dwordx4 v244, s[16:19], s40 offen offset:0 sc1 lds
v_mfma_f32_16x16x32_bf16 v[16:19], a[40:43], v[168:171], v[16:19]
v_exp_f32 v2, v2
v_mfma_f32_16x16x32_bf16 v[20:23], a[40:43], v[184:187], v[20:23]
ds_read_b128 a[8:11], v220 offset:18432
v_cvt_pk_bf16_f32 v64, v0, v1
v_mfma_f32_16x16x32_bf16 v[16:19], a[44:47], v[172:175], v[16:19]
v_exp_f32 v3, v3
v_mfma_f32_16x16x32_bf16 v[20:23], a[44:47], v[188:191], v[20:23]
ds_read_b128 a[12:15], v220 offset:19456
buffer_load_dwordx4 v244, s[16:19], s40 offen offset:1024 sc1 lds
v_exp_f32 v4, v4
s_waitcnt lgkmcnt(7)
v_mfma_f32_16x16x32_bf16 v[28:31], a[48:51], v[176:179], v[196:199]
ds_read_b128 a[16:19], v220 offset:20480
v_cvt_pk_bf16_f32 v65, v2, v3
v_exp_f32 v5, v5
s_waitcnt lgkmcnt(7)
v_mfma_f32_16x16x32_bf16 v[28:31], a[52:55], v[180:183], v[28:31]
ds_read_b128 a[20:23], v220 offset:21504
buffer_load_dwordx4 v244, s[16:19], s40 offen offset:2048 sc1 lds
v_exp_f32 v6, v6
s_waitcnt lgkmcnt(7)
v_mfma_f32_16x16x32_bf16 v[28:31], a[56:59], v[184:187], v[28:31]
ds_read_b128 a[24:27], v220 offset:22528
v_cvt_pk_bf16_f32 v68, v4, v5
v_exp_f32 v7, v7
s_waitcnt lgkmcnt(7)
v_mfma_f32_16x16x32_bf16 v[28:31], a[60:63], v[188:191], v[28:31]
ds_read_b128 a[28:31], v220 offset:23552
buffer_load_dwordx4 v244, s[16:19], s40 offen offset:3072 sc1 lds
v_exp_f32 v8, v8
s_waitcnt lgkmcnt(7)
v_mfma_f32_16x16x32_bf16 v[36:39], a[0:3], v[176:179], v[196:199]
v_cvt_pk_bf16_f32 v69, v6, v7
v_exp_f32 v9, v9
s_waitcnt lgkmcnt(6)
v_mfma_f32_16x16x32_bf16 v[36:39], a[4:7], v[180:183], v[36:39]
v_exp_f32 v10, v10
s_waitcnt lgkmcnt(5)
v_mfma_f32_16x16x32_bf16 v[36:39], a[8:11], v[184:187], v[36:39]
v_cvt_pk_bf16_f32 v66, v8, v9
v_exp_f32 v11, v11
s_waitcnt lgkmcnt(4)
v_mfma_f32_16x16x32_bf16 v[36:39], a[12:15], v[188:191], v[36:39]
v_exp_f32 v12, v12
s_waitcnt lgkmcnt(3)
v_mfma_f32_16x16x32_bf16 v[44:47], a[16:19], v[176:179], v[204:207]
v_cvt_pk_bf16_f32 v67, v10, v11
v_exp_f32 v13, v13
s_waitcnt lgkmcnt(2)
v_mfma_f32_16x16x32_bf16 v[44:47], a[20:23], v[180:183], v[44:47]
v_exp_f32 v14, v14
s_waitcnt lgkmcnt(1)
v_mfma_f32_16x16x32_bf16 v[44:47], a[24:27], v[184:187], v[44:47]
v_cvt_pk_bf16_f32 v70, v12, v13
v_exp_f32 v15, v15
s_waitcnt lgkmcnt(0)
v_mfma_f32_16x16x32_bf16 v[44:47], a[28:31], v[188:191], v[44:47]
v_exp_f32 v16, v16
ds_read_b128 a[64:67], v221 offset:16384
v_cvt_pk_bf16_f32 v71, v14, v15
v_xor_b32 v220, 0x8000, v220
v_exp_f32 v17, v17
ds_read_b128 a[68:71], v221 offset:17408
v_exp_f32 v18, v18
ds_read_b128 a[72:75], v221 offset:18432
v_cvt_pk_bf16_f32 v72, v16, v17
v_exp_f32 v19, v19
ds_read_b128 a[76:79], v221 offset:19456
v_exp_f32 v20, v20
ds_read_b128 a[80:83], v221 offset:20480
v_cvt_pk_bf16_f32 v73, v18, v19
v_exp_f32 v21, v21
ds_read_b128 a[84:87], v221 offset:21504
v_exp_f32 v22, v22
ds_read_b128 a[88:91], v221 offset:22528
v_cvt_pk_bf16_f32 v76, v20, v21
v_exp_f32 v23, v23
ds_read_b128 a[92:95], v221 offset:23552
s_waitcnt lgkmcnt(4)
v_mfma_f32_16x16x32_bf16 v[96:99], a[64:67], v[80:83], v[96:99]
v_mfma_f32_16x16x32_bf16 v[100:103], a[64:67], v[84:87], v[100:103]
ds_read_b128 a[96:99], v221 offset:24576
v_cvt_pk_bf16_f32 v77, v22, v23
v_mfma_f32_16x16x32_bf16 v[104:107], a[68:71], v[80:83], v[104:107]
v_mfma_f32_16x16x32_bf16 v[108:111], a[68:71], v[84:87], v[108:111]
ds_read_b128 a[100:103], v221 offset:25600
v_mfma_f32_16x16x32_bf16 v[112:115], a[72:75], v[80:83], v[112:115]
v_mfma_f32_16x16x32_bf16 v[116:119], a[72:75], v[84:87], v[116:119]
ds_read_b128 a[104:107], v221 offset:26624
v_mov_b32 v74, 0
v_mfma_f32_16x16x32_bf16 v[120:123], a[76:79], v[80:83], v[120:123]
v_mfma_f32_16x16x32_bf16 v[124:127], a[76:79], v[84:87], v[124:127]
ds_read_b128 a[108:111], v221 offset:27648
v_mfma_f32_16x16x32_bf16 v[208:211], a[160:163], v[80:83], v[208:211]
s_add_u32 s40, s40, 0x10000
s_xor_b32 s42, s42, 0x8000
v_mfma_f32_16x16x32_bf16 v[212:215], a[160:163], v[84:87], v[212:215]
s_cmp_eq_u32 s40, s45
s_cselect_b32 s40, s52, s40
s_cselect_b32 s45, s57, s45
s_waitcnt lgkmcnt(4)
v_mfma_f32_16x16x32_bf16 v[128:131], a[80:83], v[80:83], v[128:131]
v_exp_f32 v28, v28
v_mfma_f32_16x16x32_bf16 v[132:135], a[80:83], v[84:87], v[132:135]
ds_read_b128 a[112:115], v221 offset:28672
v_mov_b32 v75, 0
v_mfma_f32_16x16x32_bf16 v[136:139], a[84:87], v[80:83], v[136:139]
v_exp_f32 v29, v29
v_mfma_f32_16x16x32_bf16 v[140:143], a[84:87], v[84:87], v[140:143]
ds_read_b128 a[116:119], v221 offset:29696
v_mfma_f32_16x16x32_bf16 v[144:147], a[88:91], v[80:83], v[144:147]
v_exp_f32 v30, v30
v_mfma_f32_16x16x32_bf16 v[148:151], a[88:91], v[84:87], v[148:151]
ds_read_b128 a[120:123], v221 offset:30720
v_cvt_pk_bf16_f32 v78, v28, v29
v_mfma_f32_16x16x32_bf16 v[152:155], a[92:95], v[80:83], v[152:155]
v_exp_f32 v31, v31
v_mfma_f32_16x16x32_bf16 v[156:159], a[92:95], v[84:87], v[156:159]
ds_read_b128 a[124:127], v221 offset:31744
s_waitcnt lgkmcnt(4)
v_mfma_f32_16x16x32_bf16 v[96:99], a[96:99], v[88:91], v[96:99]
v_mfma_f32_16x16x32_bf16 v[100:103], a[96:99], v[92:95], v[100:103]
v_cvt_pk_bf16_f32 v79, v30, v31
v_mfma_f32_16x16x32_bf16 v[104:107], a[100:103], v[88:91], v[104:107]
v_mfma_f32_16x16x32_bf16 v[108:111], a[100:103], v[92:95], v[108:111]
v_mfma_f32_16x16x32_bf16 v[112:115], a[104:107], v[88:91], v[112:115]
v_xor_b32 v221, 0x8000, v221
v_mfma_f32_16x16x32_bf16 v[116:119], a[104:107], v[92:95], v[116:119]
s_waitcnt vmcnt(0) lgkmcnt(0)
s_barrier
v_mfma_f32_16x16x32_bf16 v[120:123], a[108:111], v[88:91], v[120:123]
v_mfma_f32_16x16x32_bf16 v[124:127], a[108:111], v[92:95], v[124:127]
ds_read_b128 a[64:67], v221 offset:0
v_mfma_f32_16x16x32_bf16 v[208:211], a[160:163], v[88:91], v[208:211]
v_mfma_f32_16x16x32_bf16 v[212:215], a[160:163], v[92:95], v[212:215]
v_mfma_f32_16x16x32_bf16 v[128:131], a[112:115], v[88:91], v[128:131]
ds_read_b128 a[68:71], v221 offset:1024
v_exp_f32 v36, v36
v_mfma_f32_16x16x32_bf16 v[132:135], a[112:115], v[92:95], v[132:135]
ds_read_b128 a[72:75], v221 offset:2048
v_mfma_f32_16x16x32_bf16 v[136:139], a[116:119], v[88:91], v[136:139]
ds_read_b128 a[76:79], v221 offset:3072
v_exp_f32 v37, v37
s_mov_b32 m0, s43
v_mfma_f32_16x16x32_bf16 v[140:143], a[116:119], v[92:95], v[140:143]
ds_read_b128 a[80:83], v221 offset:4096
buffer_load_dwordx4 v252, s[16:19], s41 offen offset:0 sc1 lds
v_mfma_f32_16x16x32_bf16 v[144:147], a[120:123], v[88:91], v[144:147]
v_exp_f32 v38, v38
v_mfma_f32_16x16x32_bf16 v[148:151], a[120:123], v[92:95], v[148:151]
ds_read_b128 a[84:87], v221 offset:5120
v_cvt_pk_bf16_f32 v84, v36, v37
v_mfma_f32_16x16x32_bf16 v[152:155], a[124:127], v[88:91], v[152:155]
v_exp_f32 v39, v39
v_mfma_f32_16x16x32_bf16 v[156:159], a[124:127], v[92:95], v[156:159]
ds_read_b128 a[88:91], v221 offset:6144
buffer_load_dwordx4 v252, s[16:19], s41 offen offset:1024 sc1 lds
s_waitcnt lgkmcnt(3)
v_mfma_f32_16x16x32_bf16 v[96:99], a[64:67], v[64:67], v[96:99]
v_mfma_f32_16x16x32_bf16 v[100:103], a[64:67], v[68:71], v[100:103]
ds_read_b128 a[92:95], v221 offset:7168
v_cvt_pk_bf16_f32 v85, v38, v39
v_mfma_f32_16x16x32_bf16 v[104:107], a[68:71], v[64:67], v[104:107]
v_mfma_f32_16x16x32_bf16 v[108:111], a[68:71], v[68:71], v[108:111]
ds_read_b128 a[96:99], v221 offset:8192
buffer_load_dwordx4 v252, s[16:19], s41 offen offset:2048 sc1 lds
v_mfma_f32_16x16x32_bf16 v[112:115], a[72:75], v[64:67], v[112:115]
v_mfma_f32_16x16x32_bf16 v[116:119], a[72:75], v[68:71], v[116:119]
ds_read_b128 a[100:103], v221 offset:9216
v_mfma_f32_16x16x32_bf16 v[120:123], a[76:79], v[64:67], v[120:123]
v_mfma_f32_16x16x32_bf16 v[124:127], a[76:79], v[68:71], v[124:127]
ds_read_b128 a[104:107], v221 offset:10240
buffer_load_dwordx4 v252, s[16:19], s41 offen offset:3072 sc1 lds
s_add_u32 m0, s43, 4096
v_mfma_f32_16x16x32_bf16 v[208:211], a[160:163], v[64:67], v[208:211]
v_mfma_f32_16x16x32_bf16 v[212:215], a[160:163], v[68:71], v[212:215]
s_waitcnt lgkmcnt(3)
v_mfma_f32_16x16x32_bf16 v[128:131], a[80:83], v[64:67], v[128:131]
v_exp_f32 v44, v44
v_mfma_f32_16x16x32_bf16 v[132:135], a[80:83], v[68:71], v[132:135]
ds_read_b128 a[108:111], v221 offset:11264
v_mfma_f32_16x16x32_bf16 v[136:139], a[84:87], v[64:67], v[136:139]
v_exp_f32 v45, v45
v_mfma_f32_16x16x32_bf16 v[140:143], a[84:87], v[68:71], v[140:143]
ds_read_b128 a[112:115], v221 offset:12288
buffer_load_dwordx4 v244, s[16:19], s41 offen offset:0 sc1 lds
v_mfma_f32_16x16x32_bf16 v[144:147], a[88:91], v[64:67], v[144:147]
v_exp_f32 v46, v46
v_mfma_f32_16x16x32_bf16 v[148:151], a[88:91], v[68:71], v[148:151]
ds_read_b128 a[116:119], v221 offset:13312
v_cvt_pk_bf16_f32 v86, v44, v45
v_mfma_f32_16x16x32_bf16 v[152:155], a[92:95], v[64:67], v[152:155]
v_exp_f32 v47, v47
v_mfma_f32_16x16x32_bf16 v[156:159], a[92:95], v[68:71], v[156:159]
ds_read_b128 a[120:123], v221 offset:14336
buffer_load_dwordx4 v244, s[16:19], s41 offen offset:1024 sc1 lds
s_waitcnt lgkmcnt(3)
v_mfma_f32_16x16x32_bf16 v[96:99], a[96:99], v[72:75], v[96:99]
v_mfma_f32_16x16x32_bf16 v[100:103], a[96:99], v[76:79], v[100:103]
ds_read_b128 a[124:127], v221 offset:15360
v_cvt_pk_bf16_f32 v87, v46, v47
v_mfma_f32_16x16x32_bf16 v[104:107], a[100:103], v[72:75], v[104:107]
v_mfma_f32_16x16x32_bf16 v[108:111], a[100:103], v[76:79], v[108:111]
buffer_load_dwordx4 v244, s[16:19], s41 offen offset:2048 sc1 lds
v_mfma_f32_16x16x32_bf16 v[112:115], a[104:107], v[72:75], v[112:115]
v_mfma_f32_16x16x32_bf16 v[116:119], a[104:107], v[76:79], v[116:119]
v_mfma_f32_16x16x32_bf16 v[120:123], a[108:111], v[72:75], v[120:123]
v_mfma_f32_16x16x32_bf16 v[124:127], a[108:111], v[76:79], v[124:127]
buffer_load_dwordx4 v244, s[16:19], s41 offen offset:3072 sc1 lds
v_mfma_f32_16x16x32_bf16 v[208:211], a[160:163], v[72:75], v[208:211]
s_add_u32 s41, s41, 0x10000
s_xor_b32 s43, s43, 0x8000
v_mfma_f32_16x16x32_bf16 v[212:215], a[160:163], v[76:79], v[212:215]
s_cmp_eq_u32 s41, s47
s_cselect_b32 s41, s56, s41
s_cselect_b32 s47, s58, s47
s_waitcnt lgkmcnt(0)
v_mfma_f32_16x16x32_bf16 v[128:131], a[112:115], v[72:75], v[128:131]
v_mfma_f32_16x16x32_bf16 v[132:135], a[112:115], v[76:79], v[132:135]
v_mfma_f32_16x16x32_bf16 v[136:139], a[116:119], v[72:75], v[136:139]
v_mfma_f32_16x16x32_bf16 v[140:143], a[116:119], v[76:79], v[140:143]
v_mfma_f32_16x16x32_bf16 v[144:147], a[120:123], v[72:75], v[144:147]
v_mfma_f32_16x16x32_bf16 v[148:151], a[120:123], v[76:79], v[148:151]
v_mfma_f32_16x16x32_bf16 v[152:155], a[124:127], v[72:75], v[152:155]
v_mfma_f32_16x16x32_bf16 v[156:159], a[124:127], v[76:79], v[156:159]
ds_read_b128 a[64:67], v221 offset:16384
ds_read_b128 a[68:71], v221 offset:17408
ds_read_b128 a[80:83], v221 offset:18432
ds_read_b128 a[84:87], v221 offset:19456
v_mfma_f32_16x16x32_bf16 v[212:215], a[160:163], v[84:87], v[212:215]
s_waitcnt lgkmcnt(3)
v_mfma_f32_16x16x32_bf16 v[100:103], a[64:67], v[84:87], v[100:103]
ds_read_b128 a[96:99], v221 offset:20480
v_cmp_ngt_f32 vcc, 0x5d800000, v208
s_nop 1
s_mov_b64 s[92:93], vcc
s_nop 0
v_cmp_ngt_f32 vcc, 0x5d800000, v212
s_nop 1
s_or_b64 vcc, vcc, s[92:93]
s_cmp_lg_u64 vcc, 0
s_cbranch_scc1 K2_SAFE
v_rcp_f32 v226, v208
v_rcp_f32 v228, v212
s_lshl_b32 s36, s55, 7
s_lshl_b32 s37, s26, 2
s_add_u32 s36, s36, s37
s_lshl_b32 s36, s36, 13
s_sub_u32 s35, 7, s7
s_lshl_b32 s35, s35, 10
s_add_u32 s35, s35, s36
s_lshl_b32 s37, s7, 10
s_add_u32 s36, s36, s37
s_waitcnt lgkmcnt(3)
v_mfma_f32_16x16x32_bf16 v[108:111], a[68:71], v[84:87], v[108:111]
ds_read_b128 a[100:103], v221 offset:21504
s_waitcnt lgkmcnt(3)
v_mfma_f32_16x16x32_bf16 v[116:119], a[80:83], v[84:87], v[116:119]
ds_read_b128 a[112:115], v221 offset:22528
v_pk_mul_f32 v[96:97], v[96:97], v[226:227] op_sel_hi:[1,0]
v_pk_mul_f32 v[98:99], v[98:99], v[226:227] op_sel_hi:[1,0]
v_pk_mul_f32 v[104:105], v[104:105], v[226:227] op_sel_hi:[1,0]
v_pk_mul_f32 v[106:107], v[106:107], v[226:227] op_sel_hi:[1,0]
s_waitcnt lgkmcnt(3)
v_mfma_f32_16x16x32_bf16 v[124:127], a[84:87], v[84:87], v[124:127]
ds_read_b128 a[116:119], v221 offset:23552
v_cvt_pk_bf16_f32 v0, v96, v97
v_cvt_pk_bf16_f32 v1, v98, v99
v_cvt_pk_bf16_f32 v2, v104, v105
v_cvt_pk_bf16_f32 v3, v106, v107
s_add_u32 s37, s36, 0
buffer_store_dwordx4 v[0:3], v252, s[20:23], s37 offen sc1
v_pk_mul_f32 v[100:101], v[100:101], v[228:229] op_sel_hi:[1,0]
v_pk_mul_f32 v[102:103], v[102:103], v[228:229] op_sel_hi:[1,0]
v_pk_mul_f32 v[108:109], v[108:109], v[228:229] op_sel_hi:[1,0]
v_pk_mul_f32 v[110:111], v[110:111], v[228:229] op_sel_hi:[1,0]
v_cvt_pk_bf16_f32 v4, v100, v101
v_cvt_pk_bf16_f32 v5, v102, v103
v_cvt_pk_bf16_f32 v6, v108, v109
v_cvt_pk_bf16_f32 v7, v110, v111
s_add_u32 s37, s35, 0
buffer_store_dwordx4 v[4:7], v252, s[20:23], s37 offen sc1
s_waitcnt lgkmcnt(3)
v_mfma_f32_16x16x32_bf16 v[132:135], a[96:99], v[84:87], v[132:135]
v_pk_mul_f32 v[112:113], v[112:113], v[226:227] op_sel_hi:[1,0]
v_pk_mul_f32 v[114:115], v[114:115], v[226:227] op_sel_hi:[1,0]
v_pk_mul_f32 v[120:121], v[120:121], v[226:227] op_sel_hi:[1,0]
v_pk_mul_f32 v[122:123], v[122:123], v[226:227] op_sel_hi:[1,0]
s_waitcnt lgkmcnt(2)
v_mfma_f32_16x16x32_bf16 v[140:143], a[100:103], v[84:87], v[140:143]
v_cvt_pk_bf16_f32 v8, v112, v113
v_cvt_pk_bf16_f32 v9, v114, v115
v_cvt_pk_bf16_f32 v10, v120, v121
v_cvt_pk_bf16_f32 v11, v122, v123
s_add_u32 s37, s36, 8192
buffer_store_dwordx4 v[8:11], v252, s[20:23], s37 offen sc1
v_pk_mul_f32 v[116:117], v[116:117], v[228:229] op_sel_hi:[1,0]
v_pk_mul_f32 v[118:119], v[118:119], v[228:229] op_sel_hi:[1,0]
v_pk_mul_f32 v[124:125], v[124:125], v[228:229] op_sel_hi:[1,0]
v_pk_mul_f32 v[126:127], v[126:127], v[228:229] op_sel_hi:[1,0]
v_cvt_pk_bf16_f32 v12, v116, v117
v_cvt_pk_bf16_f32 v13, v118, v119
v_cvt_pk_bf16_f32 v14, v124, v125
v_cvt_pk_bf16_f32 v15, v126, v127
s_add_u32 s37, s35, 8192
buffer_store_dwordx4 v[12:15], v252, s[20:23], s37 offen sc1
s_waitcnt lgkmcnt(1)
v_mfma_f32_16x16x32_bf16 v[148:151], a[112:115], v[84:87], v[148:151]
v_pk_mul_f32 v[128:129], v[128:129], v[226:227] op_sel_hi:[1,0]
v_pk_mul_f32 v[130:131], v[130:131], v[226:227] op_sel_hi:[1,0]
v_pk_mul_f32 v[136:137], v[136:137], v[226:227] op_sel_hi:[1,0]
v_pk_mul_f32 v[138:139], v[138:139], v[226:227] op_sel_hi:[1,0]
s_waitcnt lgkmcnt(0)
v_mfma_f32_16x16x32_bf16 v[156:159], a[116:119], v[84:87], v[156:159]
v_cvt_pk_bf16_f32 v0, v128, v129
v_cvt_pk_bf16_f32 v1, v130, v131
v_cvt_pk_bf16_f32 v2, v136, v137
v_cvt_pk_bf16_f32 v3, v138, v139
s_add_u32 s37, s36, 16384
buffer_store_dwordx4 v[0:3], v252, s[20:23], s37 offen sc1
v_pk_mul_f32 v[132:133], v[132:133], v[228:229] op_sel_hi:[1,0]
v_pk_mul_f32 v[134:135], v[134:135], v[228:229] op_sel_hi:[1,0]
v_pk_mul_f32 v[140:141], v[140:141], v[228:229] op_sel_hi:[1,0]
v_pk_mul_f32 v[142:143], v[142:143], v[228:229] op_sel_hi:[1,0]
v_cvt_pk_bf16_f32 v4, v132, v133
v_cvt_pk_bf16_f32 v5, v134, v135
v_cvt_pk_bf16_f32 v6, v140, v141
v_cvt_pk_bf16_f32 v7, v142, v143
s_add_u32 s37, s35, 16384
buffer_store_dwordx4 v[4:7], v252, s[20:23], s37 offen sc1
s_nop 7
v_pk_mul_f32 v[144:145], v[144:145], v[226:227] op_sel_hi:[1,0]
v_pk_mul_f32 v[146:147], v[146:147], v[226:227] op_sel_hi:[1,0]
v_pk_mul_f32 v[152:153], v[152:153], v[226:227] op_sel_hi:[1,0]
v_pk_mul_f32 v[154:155], v[154:155], v[226:227] op_sel_hi:[1,0]
v_cvt_pk_bf16_f32 v8, v144, v145
v_cvt_pk_bf16_f32 v9, v146, v147
v_cvt_pk_bf16_f32 v10, v152, v153
v_cvt_pk_bf16_f32 v11, v154, v155
s_add_u32 s37, s36, 24576
buffer_store_dwordx4 v[8:11], v252, s[20:23], s37 offen sc1
v_pk_mul_f32 v[148:149], v[148:149], v[228:229] op_sel_hi:[1,0]
v_pk_mul_f32 v[150:151], v[150:151], v[228:229] op_sel_hi:[1,0]
v_pk_mul_f32 v[156:157], v[156:157], v[228:229] op_sel_hi:[1,0]
v_pk_mul_f32 v[158:159], v[158:159], v[228:229] op_sel_hi:[1,0]
v_cvt_pk_bf16_f32 v12, v148, v149
v_cvt_pk_bf16_f32 v13, v150, v151
v_cvt_pk_bf16_f32 v14, v156, v157
v_cvt_pk_bf16_f32 v15, v158, v159
s_add_u32 s37, s35, 24576
buffer_store_dwordx4 v[12:15], v252, s[20:23], s37 offen sc1
s_branch K2_EPI
K2_L3:
s_waitcnt lgkmcnt(4)
v_mfma_f32_16x16x32_bf16 v[0:3], a[0:3], v[160:163], v[192:195]
v_exp_f32 v56, v56
v_mfma_f32_16x16x32_bf16 v[4:7], a[0:3], v[176:179], v[196:199]
ds_read_b128 a[32:35], v220 offset:8192
v_cvt_pk_bf16_f32 v93, v54, v55
v_mfma_f32_16x16x32_bf16 v[0:3], a[4:7], v[164:167], v[0:3]
v_exp_f32 v57, v57
s_mov_b32 m0, s42
v_mfma_f32_16x16x32_bf16 v[4:7], a[4:7], v[180:183], v[4:7]
ds_read_b128 a[36:39], v220 offset:9216
buffer_load_dwordx4 v252, s[16:19], s40 offen offset:0 sc1 lds
v_mfma_f32_16x16x32_bf16 v[0:3], a[8:11], v[168:171], v[0:3]
v_exp_f32 v58, v58
v_mfma_f32_16x16x32_bf16 v[4:7], a[8:11], v[184:187], v[4:7]
ds_read_b128 a[40:43], v220 offset:10240
v_cvt_pk_bf16_f32 v90, v56, v57
v_mfma_f32_16x16x32_bf16 v[0:3], a[12:15], v[172:175], v[0:3]
v_exp_f32 v59, v59
v_mfma_f32_16x16x32_bf16 v[4:7], a[12:15], v[188:191], v[4:7]
ds_read_b128 a[44:47], v220 offset:11264
buffer_load_dwordx4 v252, s[16:19], s40 offen offset:1024 sc1 lds
s_waitcnt lgkmcnt(4)
v_mfma_f32_16x16x32_bf16 v[8:11], a[16:19], v[160:163], v[192:195]
v_exp_f32 v60, v60
v_mfma_f32_16x16x32_bf16 v[12:15], a[16:19], v[176:179], v[196:199]
ds_read_b128 a[48:51], v220 offset:12288
v_cvt_pk_bf16_f32 v91, v58, v59
v_mfma_f32_16x16x32_bf16 v[8:11], a[20:23], v[164:167], v[8:11]
v_exp_f32 v61, v61
v_mfma_f32_16x16x32_bf16 v[12:15], a[20:23], v[180:183], v[12:15]
ds_read_b128 a[52:55], v220 offset:13312
buffer_load_dwordx4 v252, s[16:19], s40 offen offset:2048 sc1 lds
v_mfma_f32_16x16x32_bf16 v[8:11], a[24:27], v[168:171], v[8:11]
v_exp_f32 v62, v62
v_mfma_f32_16x16x32_bf16 v[12:15], a[24:27], v[184:187], v[12:15]
ds_read_b128 a[56:59], v220 offset:14336
v_cvt_pk_bf16_f32 v94, v60, v61
v_mfma_f32_16x16x32_bf16 v[8:11], a[28:31], v[172:175], v[8:11]
v_exp_f32 v63, v63
v_mfma_f32_16x16x32_bf16 v[12:15], a[28:31], v[188:191], v[12:15]
ds_read_b128 a[60:63], v220 offset:15360
buffer_load_dwordx4 v252, s[16:19], s40 offen offset:3072 sc1 lds
s_add_u32 m0, s42, 4096
s_waitcnt lgkmcnt(4)
v_mfma_f32_16x16x32_bf16 v[16:19], a[32:35], v[160:163], v[192:195]
v_exp_f32 v0, v0
v_mfma_f32_16x16x32_bf16 v[20:23], a[32:35], v[176:179], v[196:199]
ds_read_b128 a[0:3], v220 offset:16384
v_cvt_pk_bf16_f32 v95, v62, v63
v_mfma_f32_16x16x32_bf16 v[16:19], a[36:39], v[164:167], v[16:19]
v_exp_f32 v1, v1
v_mfma_f32_16x16x32_bf16 v[20:23], a[36:39], v[180:183], v[20:23]
ds_read_b128 a[4:7], v220 offset:17408
buffer_load_dwordx4 v244, s[16:19], s40 offen offset:0 sc1 lds
v_mfma_f32_16x16x32_bf16 v[16:19], a[40:43], v[168:171], v[16:19]
v_exp_f32 v2, v2
v_mfma_f32_16x16x32_bf16 v[20:23], a[40:43], v[184:187], v[20:23]
ds_read_b128 a[8:11], v220 offset:18432
v_cvt_pk_bf16_f32 v64, v0, v1
v_mfma_f32_16x16x32_bf16 v[16:19], a[44:47], v[172:175], v[16:19]
v_exp_f32 v3, v3
v_mfma_f32_16x16x32_bf16 v[20:23], a[44:47], v[188:191], v[20:23]
ds_read_b128 a[12:15], v220 offset:19456
buffer_load_dwordx4 v244, s[16:19], s40 offen offset:1024 sc1 lds
s_waitcnt lgkmcnt(4)
v_mfma_f32_16x16x32_bf16 v[24:27], a[48:51], v[160:163], v[200:203]
v_exp_f32 v4, v4
v_mfma_f32_16x16x32_bf16 v[28:31], a[48:51], v[176:179], v[196:199]
v_cvt_pk_bf16_f32 v65, v2, v3
v_mfma_f32_16x16x32_bf16 v[24:27], a[52:55], v[164:167], v[24:27]
v_exp_f32 v5, v5
v_mfma_f32_16x16x32_bf16 v[28:31], a[52:55], v[180:183], v[28:31]
buffer_load_dwordx4 v244, s[16:19], s40 offen offset:2048 sc1 lds
v_mfma_f32_16x16x32_bf16 v[24:27], a[56:59], v[168:171], v[24:27]
v_exp_f32 v6, v6
v_mfma_f32_16x16x32_bf16 v[28:31], a[56:59], v[184:187], v[28:31]
v_cvt_pk_bf16_f32 v68, v4, v5
v_mfma_f32_16x16x32_bf16 v[24:27], a[60:63], v[172:175], v[24:27]
v_exp_f32 v7, v7
v_mfma_f32_16x16x32_bf16 v[28:31], a[60:63], v[188:191], v[28:31]
buffer_load_dwordx4 v244, s[16:19], s40 offen offset:3072 sc1 lds
v_exp_f32 v8, v8
s_waitcnt lgkmcnt(3)
v_mfma_f32_16x16x32_bf16 v[36:39], a[0:3], v[176:179], v[204:207]
v_cvt_pk_bf16_f32 v69, v6, v7
v_exp_f32 v9, v9
s_waitcnt lgkmcnt(2)
v_mfma_f32_16x16x32_bf16 v[36:39], a[4:7], v[180:183], v[36:39]
v_exp_f32 v10, v10
s_waitcnt lgkmcnt(1)
v_mfma_f32_16x16x32_bf16 v[36:39], a[8:11], v[184:187], v[36:39]
v_cvt_pk_bf16_f32 v66, v8, v9
v_exp_f32 v11, v11
s_waitcnt lgkmcnt(0)
v_mfma_f32_16x16x32_bf16 v[36:39], a[12:15], v[188:191], v[36:39]
v_exp_f32 v12, v12
v_cvt_pk_bf16_f32 v67, v10, v11
v_exp_f32 v13, v13
v_exp_f32 v14, v14
v_cvt_pk_bf16_f32 v70, v12, v13
v_exp_f32 v15, v15
v_exp_f32 v16, v16
ds_read_b128 a[64:67], v221 offset:16384
v_cvt_pk_bf16_f32 v71, v14, v15
v_xor_b32 v220, 0x8000, v220
v_exp_f32 v17, v17
ds_read_b128 a[68:71], v221 offset:17408
v_exp_f32 v18, v18
ds_read_b128 a[72:75], v221 offset:18432
v_cvt_pk_bf16_f32 v72, v16, v17
v_exp_f32 v19, v19
ds_read_b128 a[76:79], v221 offset:19456
v_exp_f32 v20, v20
)ASM"
R"ASM(ds_read_b128 a[80:83], v221 offset:20480
v_cvt_pk_bf16_f32 v73, v18, v19
v_exp_f32 v21, v21
ds_read_b128 a[84:87], v221 offset:21504
v_exp_f32 v22, v22
ds_read_b128 a[88:91], v221 offset:22528
v_cvt_pk_bf16_f32 v76, v20, v21
v_exp_f32 v23, v23
ds_read_b128 a[92:95], v221 offset:23552
s_waitcnt lgkmcnt(4)
v_mfma_f32_16x16x32_bf16 v[96:99], a[64:67], v[80:83], v[96:99]
v_exp_f32 v24, v24
v_mfma_f32_16x16x32_bf16 v[100:103], a[64:67], v[84:87], v[100:103]
ds_read_b128 a[96:99], v221 offset:24576
v_cvt_pk_bf16_f32 v77, v22, v23
v_mfma_f32_16x16x32_bf16 v[104:107], a[68:71], v[80:83], v[104:107]
v_exp_f32 v25, v25
v_mfma_f32_16x16x32_bf16 v[108:111], a[68:71], v[84:87], v[108:111]
ds_read_b128 a[100:103], v221 offset:25600
v_mfma_f32_16x16x32_bf16 v[112:115], a[72:75], v[80:83], v[112:115]
v_exp_f32 v26, v26
v_mfma_f32_16x16x32_bf16 v[116:119], a[72:75], v[84:87], v[116:119]
ds_read_b128 a[104:107], v221 offset:26624
v_cvt_pk_bf16_f32 v74, v24, v25
v_mfma_f32_16x16x32_bf16 v[120:123], a[76:79], v[80:83], v[120:123]
v_exp_f32 v27, v27
v_mfma_f32_16x16x32_bf16 v[124:127], a[76:79], v[84:87], v[124:127]
ds_read_b128 a[108:111], v221 offset:27648
v_mfma_f32_16x16x32_bf16 v[208:211], a[160:163], v[80:83], v[208:211]
s_add_u32 s40, s40, 0x10000
s_xor_b32 s42, s42, 0x8000
v_mfma_f32_16x16x32_bf16 v[212:215], a[160:163], v[84:87], v[212:215]
s_cmp_eq_u32 s40, s45
s_cselect_b32 s40, s52, s40
s_cselect_b32 s45, s57, s45
s_waitcnt lgkmcnt(4)
v_mfma_f32_16x16x32_bf16 v[128:131], a[80:83], v[80:83], v[128:131]
v_exp_f32 v28, v28
v_mfma_f32_16x16x32_bf16 v[132:135], a[80:83], v[84:87], v[132:135]
ds_read_b128 a[112:115], v221 offset:28672
v_cvt_pk_bf16_f32 v75, v26, v27
v_mfma_f32_16x16x32_bf16 v[136:139], a[84:87], v[80:83], v[136:139]
v_exp_f32 v29, v29
v_mfma_f32_16x16x32_bf16 v[140:143], a[84:87], v[84:87], v[140:143]
ds_read_b128 a[116:119], v221 offset:29696
v_mfma_f32_16x16x32_bf16 v[144:147], a[88:91], v[80:83], v[144:147]
v_exp_f32 v30, v30
v_mfma_f32_16x16x32_bf16 v[148:151], a[88:91], v[84:87], v[148:151]
ds_read_b128 a[120:123], v221 offset:30720
v_cvt_pk_bf16_f32 v78, v28, v29
v_mfma_f32_16x16x32_bf16 v[152:155], a[92:95], v[80:83], v[152:155]
v_exp_f32 v31, v31
v_mfma_f32_16x16x32_bf16 v[156:159], a[92:95], v[84:87], v[156:159]
ds_read_b128 a[124:127], v221 offset:31744
s_waitcnt lgkmcnt(4)
v_mfma_f32_16x16x32_bf16 v[96:99], a[96:99], v[88:91], v[96:99]
v_mfma_f32_16x16x32_bf16 v[100:103], a[96:99], v[92:95], v[100:103]
v_cvt_pk_bf16_f32 v79, v30, v31
v_mfma_f32_16x16x32_bf16 v[104:107], a[100:103], v[88:91], v[104:107]
v_mfma_f32_16x16x32_bf16 v[108:111], a[100:103], v[92:95], v[108:111]
v_mfma_f32_16x16x32_bf16 v[112:115], a[104:107], v[88:91], v[112:115]
v_xor_b32 v221, 0x8000, v221
v_mfma_f32_16x16x32_bf16 v[116:119], a[104:107], v[92:95], v[116:119]
s_waitcnt vmcnt(0) lgkmcnt(0)
s_barrier
v_mfma_f32_16x16x32_bf16 v[120:123], a[108:111], v[88:91], v[120:123]
v_mfma_f32_16x16x32_bf16 v[124:127], a[108:111], v[92:95], v[124:127]
ds_read_b128 a[64:67], v221 offset:0
v_mfma_f32_16x16x32_bf16 v[208:211], a[160:163], v[88:91], v[208:211]
v_mfma_f32_16x16x32_bf16 v[212:215], a[160:163], v[92:95], v[212:215]
v_mfma_f32_16x16x32_bf16 v[128:131], a[112:115], v[88:91], v[128:131]
ds_read_b128 a[68:71], v221 offset:1024
v_exp_f32 v36, v36
v_mfma_f32_16x16x32_bf16 v[132:135], a[112:115], v[92:95], v[132:135]
ds_read_b128 a[72:75], v221 offset:2048
v_mfma_f32_16x16x32_bf16 v[136:139], a[116:119], v[88:91], v[136:139]
ds_read_b128 a[76:79], v221 offset:3072
v_exp_f32 v37, v37
s_mov_b32 m0, s43
v_mfma_f32_16x16x32_bf16 v[140:143], a[116:119], v[92:95], v[140:143]
ds_read_b128 a[80:83], v221 offset:4096
buffer_load_dwordx4 v252, s[16:19], s41 offen offset:0 sc1 lds
v_mfma_f32_16x16x32_bf16 v[144:147], a[120:123], v[88:91], v[144:147]
v_exp_f32 v38, v38
v_mfma_f32_16x16x32_bf16 v[148:151], a[120:123], v[92:95], v[148:151]
ds_read_b128 a[84:87], v221 offset:5120
v_cvt_pk_bf16_f32 v84, v36, v37
v_mfma_f32_16x16x32_bf16 v[152:155], a[124:127], v[88:91], v[152:155]
v_exp_f32 v39, v39
v_mfma_f32_16x16x32_bf16 v[156:159], a[124:127], v[92:95], v[156:159]
ds_read_b128 a[88:91], v221 offset:6144
buffer_load_dwordx4 v252, s[16:19], s41 offen offset:1024 sc1 lds
s_waitcnt lgkmcnt(3)
v_mfma_f32_16x16x32_bf16 v[96:99], a[64:67], v[64:67], v[96:99]
v_mfma_f32_16x16x32_bf16 v[100:103], a[64:67], v[68:71], v[100:103]
ds_read_b128 a[92:95], v221 offset:7168
v_cvt_pk_bf16_f32 v85, v38, v39
v_mfma_f32_16x16x32_bf16 v[104:107], a[68:71], v[64:67], v[104:107]
v_mfma_f32_16x16x32_bf16 v[108:111], a[68:71], v[68:71], v[108:111]
ds_read_b128 a[96:99], v221 offset:8192
buffer_load_dwordx4 v252, s[16:19], s41 offen offset:2048 sc1 lds
v_mfma_f32_16x16x32_bf16 v[112:115], a[72:75], v[64:67], v[112:115]
v_mfma_f32_16x16x32_bf16 v[116:119], a[72:75], v[68:71], v[116:119]
ds_read_b128 a[100:103], v221 offset:9216
v_mfma_f32_16x16x32_bf16 v[120:123], a[76:79], v[64:67], v[120:123]
v_mfma_f32_16x16x32_bf16 v[124:127], a[76:79], v[68:71], v[124:127]
ds_read_b128 a[104:107], v221 offset:10240
buffer_load_dwordx4 v252, s[16:19], s41 offen offset:3072 sc1 lds
s_add_u32 m0, s43, 4096
v_mfma_f32_16x16x32_bf16 v[208:211], a[160:163], v[64:67], v[208:211]
v_mfma_f32_16x16x32_bf16 v[212:215], a[160:163], v[68:71], v[212:215]
s_waitcnt lgkmcnt(3)
v_mfma_f32_16x16x32_bf16 v[128:131], a[80:83], v[64:67], v[128:131]
v_mfma_f32_16x16x32_bf16 v[132:135], a[80:83], v[68:71], v[132:135]
ds_read_b128 a[108:111], v221 offset:11264
v_mfma_f32_16x16x32_bf16 v[136:139], a[84:87], v[64:67], v[136:139]
v_mfma_f32_16x16x32_bf16 v[140:143], a[84:87], v[68:71], v[140:143]
ds_read_b128 a[112:115], v221 offset:12288
buffer_load_dwordx4 v244, s[16:19], s41 offen offset:0 sc1 lds
v_mfma_f32_16x16x32_bf16 v[144:147], a[88:91], v[64:67], v[144:147]
v_mfma_f32_16x16x32_bf16 v[148:151], a[88:91], v[68:71], v[148:151]
ds_read_b128 a[116:119], v221 offset:13312
v_mov_b32 v86, 0
v_mfma_f32_16x16x32_bf16 v[152:155], a[92:95], v[64:67], v[152:155]
v_mfma_f32_16x16x32_bf16 v[156:159], a[92:95], v[68:71], v[156:159]
ds_read_b128 a[120:123], v221 offset:14336
buffer_load_dwordx4 v244, s[16:19], s41 offen offset:1024 sc1 lds
s_waitcnt lgkmcnt(3)
v_mfma_f32_16x16x32_bf16 v[96:99], a[96:99], v[72:75], v[96:99]
v_mfma_f32_16x16x32_bf16 v[100:103], a[96:99], v[76:79], v[100:103]
ds_read_b128 a[124:127], v221 offset:15360
v_mov_b32 v87, 0
v_mfma_f32_16x16x32_bf16 v[104:107], a[100:103], v[72:75], v[104:107]
v_mfma_f32_16x16x32_bf16 v[108:111], a[100:103], v[76:79], v[108:111]
buffer_load_dwordx4 v244, s[16:19], s41 offen offset:2048 sc1 lds
v_mfma_f32_16x16x32_bf16 v[112:115], a[104:107], v[72:75], v[112:115]
v_mfma_f32_16x16x32_bf16 v[116:119], a[104:107], v[76:79], v[116:119]
v_mfma_f32_16x16x32_bf16 v[120:123], a[108:111], v[72:75], v[120:123]
v_mfma_f32_16x16x32_bf16 v[124:127], a[108:111], v[76:79], v[124:127]
buffer_load_dwordx4 v244, s[16:19], s41 offen offset:3072 sc1 lds
v_mfma_f32_16x16x32_bf16 v[208:211], a[160:163], v[72:75], v[208:211]
s_add_u32 s41, s41, 0x10000
s_xor_b32 s43, s43, 0x8000
v_mfma_f32_16x16x32_bf16 v[212:215], a[160:163], v[76:79], v[212:215]
s_cmp_eq_u32 s41, s47
s_cselect_b32 s41, s56, s41
s_cselect_b32 s47, s58, s47
s_waitcnt lgkmcnt(0)
v_mfma_f32_16x16x32_bf16 v[128:131], a[112:115], v[72:75], v[128:131]
v_mfma_f32_16x16x32_bf16 v[132:135], a[112:115], v[76:79], v[132:135]
v_mfma_f32_16x16x32_bf16 v[136:139], a[116:119], v[72:75], v[136:139]
v_mfma_f32_16x16x32_bf16 v[140:143], a[116:119], v[76:79], v[140:143]
v_mfma_f32_16x16x32_bf16 v[144:147], a[120:123], v[72:75], v[144:147]
v_mfma_f32_16x16x32_bf16 v[148:151], a[120:123], v[76:79], v[148:151]
v_mfma_f32_16x16x32_bf16 v[152:155], a[124:127], v[72:75], v[152:155]
v_mfma_f32_16x16x32_bf16 v[156:159], a[124:127], v[76:79], v[156:159]
ds_read_b128 a[64:67], v221 offset:16384
ds_read_b128 a[68:71], v221 offset:17408
ds_read_b128 a[80:83], v221 offset:18432
ds_read_b128 a[84:87], v221 offset:19456
v_mfma_f32_16x16x32_bf16 v[212:215], a[160:163], v[84:87], v[212:215]
s_waitcnt lgkmcnt(3)
v_mfma_f32_16x16x32_bf16 v[100:103], a[64:67], v[84:87], v[100:103]
ds_read_b128 a[96:99], v221 offset:20480
v_cmp_ngt_f32 vcc, 0x5d800000, v208
s_nop 1
s_mov_b64 s[92:93], vcc
s_nop 0
v_cmp_ngt_f32 vcc, 0x5d800000, v212
s_nop 1
s_or_b64 vcc, vcc, s[92:93]
s_cmp_lg_u64 vcc, 0
s_cbranch_scc1 K2_SAFE
v_rcp_f32 v226, v208
v_rcp_f32 v228, v212
s_lshl_b32 s36, s55, 7
s_lshl_b32 s37, s26, 2
s_add_u32 s36, s36, s37
s_lshl_b32 s36, s36, 13
s_sub_u32 s35, 7, s7
s_lshl_b32 s35, s35, 10
s_add_u32 s35, s35, s36
s_lshl_b32 s37, s7, 10
s_add_u32 s36, s36, s37
s_waitcnt lgkmcnt(3)
v_mfma_f32_16x16x32_bf16 v[108:111], a[68:71], v[84:87], v[108:111]
ds_read_b128 a[100:103], v221 offset:21504
s_waitcnt lgkmcnt(3)
v_mfma_f32_16x16x32_bf16 v[116:119], a[80:83], v[84:87], v[116:119]
ds_read_b128 a[112:115], v221 offset:22528
v_pk_mul_f32 v[96:97], v[96:97], v[226:227] op_sel_hi:[1,0]
v_pk_mul_f32 v[98:99], v[98:99], v[226:227] op_sel_hi:[1,0]
v_pk_mul_f32 v[104:105], v[104:105], v[226:227] op_sel_hi:[1,0]
v_pk_mul_f32 v[106:107], v[106:107], v[226:227] op_sel_hi:[1,0]
s_waitcnt lgkmcnt(3)
v_mfma_f32_16x16x32_bf16 v[124:127], a[84:87], v[84:87], v[124:127]
ds_read_b128 a[116:119], v221 offset:23552
v_cvt_pk_bf16_f32 v0, v96, v97
v_cvt_pk_bf16_f32 v1, v98, v99
v_cvt_pk_bf16_f32 v2, v104, v105
v_cvt_pk_bf16_f32 v3, v106, v107
s_add_u32 s37, s36, 0
buffer_store_dwordx4 v[0:3], v252, s[20:23], s37 offen sc1
v_pk_mul_f32 v[100:101], v[100:101], v[228:229] op_sel_hi:[1,0]
v_pk_mul_f32 v[102:103], v[102:103], v[228:229] op_sel_hi:[1,0]
v_pk_mul_f32 v[108:109], v[108:109], v[228:229] op_sel_hi:[1,0]
v_pk_mul_f32 v[110:111], v[110:111], v[228:229] op_sel_hi:[1,0]
v_cvt_pk_bf16_f32 v4, v100, v101
v_cvt_pk_bf16_f32 v5, v102, v103
v_cvt_pk_bf16_f32 v6, v108, v109
v_cvt_pk_bf16_f32 v7, v110, v111
s_add_u32 s37, s35, 0
buffer_store_dwordx4 v[4:7], v252, s[20:23], s37 offen sc1
s_waitcnt lgkmcnt(3)
v_mfma_f32_16x16x32_bf16 v[132:135], a[96:99], v[84:87], v[132:135]
v_pk_mul_f32 v[112:113], v[112:113], v[226:227] op_sel_hi:[1,0]
v_pk_mul_f32 v[114:115], v[114:115], v[226:227] op_sel_hi:[1,0]
v_pk_mul_f32 v[120:121], v[120:121], v[226:227] op_sel_hi:[1,0]
v_pk_mul_f32 v[122:123], v[122:123], v[226:227] op_sel_hi:[1,0]
s_waitcnt lgkmcnt(2)
v_mfma_f32_16x16x32_bf16 v[140:143], a[100:103], v[84:87], v[140:143]
v_cvt_pk_bf16_f32 v8, v112, v113
v_cvt_pk_bf16_f32 v9, v114, v115
v_cvt_pk_bf16_f32 v10, v120, v121
v_cvt_pk_bf16_f32 v11, v122, v123
s_add_u32 s37, s36, 8192
buffer_store_dwordx4 v[8:11], v252, s[20:23], s37 offen sc1
v_pk_mul_f32 v[116:117], v[116:117], v[228:229] op_sel_hi:[1,0]
v_pk_mul_f32 v[118:119], v[118:119], v[228:229] op_sel_hi:[1,0]
v_pk_mul_f32 v[124:125], v[124:125], v[228:229] op_sel_hi:[1,0]
v_pk_mul_f32 v[126:127], v[126:127], v[228:229] op_sel_hi:[1,0]
v_cvt_pk_bf16_f32 v12, v116, v117
v_cvt_pk_bf16_f32 v13, v118, v119
v_cvt_pk_bf16_f32 v14, v124, v125
v_cvt_pk_bf16_f32 v15, v126, v127
s_add_u32 s37, s35, 8192
buffer_store_dwordx4 v[12:15], v252, s[20:23], s37 offen sc1
s_waitcnt lgkmcnt(1)
v_mfma_f32_16x16x32_bf16 v[148:151], a[112:115], v[84:87], v[148:151]
v_pk_mul_f32 v[128:129], v[128:129], v[226:227] op_sel_hi:[1,0]
v_pk_mul_f32 v[130:131], v[130:131], v[226:227] op_sel_hi:[1,0]
v_pk_mul_f32 v[136:137], v[136:137], v[226:227] op_sel_hi:[1,0]
v_pk_mul_f32 v[138:139], v[138:139], v[226:227] op_sel_hi:[1,0]
s_waitcnt lgkmcnt(0)
v_mfma_f32_16x16x32_bf16 v[156:159], a[116:119], v[84:87], v[156:159]
v_cvt_pk_bf16_f32 v0, v128, v129
v_cvt_pk_bf16_f32 v1, v130, v131
v_cvt_pk_bf16_f32 v2, v136, v137
v_cvt_pk_bf16_f32 v3, v138, v139
s_add_u32 s37, s36, 16384
buffer_store_dwordx4 v[0:3], v252, s[20:23], s37 offen sc1
v_pk_mul_f32 v[132:133], v[132:133], v[228:229] op_sel_hi:[1,0]
v_pk_mul_f32 v[134:135], v[134:135], v[228:229] op_sel_hi:[1,0]
v_pk_mul_f32 v[140:141], v[140:141], v[228:229] op_sel_hi:[1,0]
v_pk_mul_f32 v[142:143], v[142:143], v[228:229] op_sel_hi:[1,0]
v_cvt_pk_bf16_f32 v4, v132, v133
v_cvt_pk_bf16_f32 v5, v134, v135
v_cvt_pk_bf16_f32 v6, v140, v141
v_cvt_pk_bf16_f32 v7, v142, v143
s_add_u32 s37, s35, 16384
buffer_store_dwordx4 v[4:7], v252, s[20:23], s37 offen sc1
s_nop 7
v_pk_mul_f32 v[144:145], v[144:145], v[226:227] op_sel_hi:[1,0]
v_pk_mul_f32 v[146:147], v[146:147], v[226:227] op_sel_hi:[1,0]
v_pk_mul_f32 v[152:153], v[152:153], v[226:227] op_sel_hi:[1,0]
v_pk_mul_f32 v[154:155], v[154:155], v[226:227] op_sel_hi:[1,0]
v_cvt_pk_bf16_f32 v8, v144, v145
v_cvt_pk_bf16_f32 v9, v146, v147
v_cvt_pk_bf16_f32 v10, v152, v153
v_cvt_pk_bf16_f32 v11, v154, v155
s_add_u32 s37, s36, 24576
buffer_store_dwordx4 v[8:11], v252, s[20:23], s37 offen sc1
v_pk_mul_f32 v[148:149], v[148:149], v[228:229] op_sel_hi:[1,0]
v_pk_mul_f32 v[150:151], v[150:151], v[228:229] op_sel_hi:[1,0]
v_pk_mul_f32 v[156:157], v[156:157], v[228:229] op_sel_hi:[1,0]
v_pk_mul_f32 v[158:159], v[158:159], v[228:229] op_sel_hi:[1,0]
v_cvt_pk_bf16_f32 v12, v148, v149
v_cvt_pk_bf16_f32 v13, v150, v151
v_cvt_pk_bf16_f32 v14, v156, v157
v_cvt_pk_bf16_f32 v15, v158, v159
s_add_u32 s37, s35, 24576
buffer_store_dwordx4 v[12:15], v252, s[20:23], s37 offen sc1
s_branch K2_EPI
K2_DRAING:
v_exp_f32 v56, v56
v_cvt_pk_bf16_f32 v93, v54, v55
v_exp_f32 v57, v57
v_exp_f32 v58, v58
v_cvt_pk_bf16_f32 v90, v56, v57
v_exp_f32 v59, v59
v_exp_f32 v60, v60
v_cvt_pk_bf16_f32 v91, v58, v59
v_exp_f32 v61, v61
v_exp_f32 v62, v62
v_cvt_pk_bf16_f32 v94, v60, v61
v_exp_f32 v63, v63
s_nop 0
v_cvt_pk_bf16_f32 v95, v62, v63
ds_read_b128 a[64:67], v221 offset:16384
ds_read_b128 a[68:71], v221 offset:17408
ds_read_b128 a[72:75], v221 offset:24576
ds_read_b128 a[76:79], v221 offset:25600
ds_read_b128 a[80:83], v221 offset:18432
ds_read_b128 a[84:87], v221 offset:19456
ds_read_b128 a[88:91], v221 offset:26624
ds_read_b128 a[92:95], v221 offset:27648
v_mfma_f32_16x16x32_bf16 v[208:211], a[160:163], v[80:83], v[208:211]
v_mfma_f32_16x16x32_bf16 v[212:215], a[160:163], v[84:87], v[212:215]
v_mfma_f32_16x16x32_bf16 v[208:211], a[160:163], v[88:91], v[208:211]
v_mfma_f32_16x16x32_bf16 v[212:215], a[160:163], v[92:95], v[212:215]
s_waitcnt lgkmcnt(4)
v_mfma_f32_16x16x32_bf16 v[96:99], a[64:67], v[80:83], v[96:99]
v_mfma_f32_16x16x32_bf16 v[100:103], a[64:67], v[84:87], v[100:103]
ds_read_b128 a[96:99], v221 offset:20480
v_mfma_f32_16x16x32_bf16 v[104:107], a[68:71], v[80:83], v[104:107]
s_nop 1
v_cmp_ngt_f32 vcc, 0x5d800000, v208
s_nop 1
s_mov_b64 s[92:93], vcc
v_cmp_ngt_f32 vcc, 0x5d800000, v212
s_nop 1
s_or_b64 vcc, vcc, s[92:93]
s_cmp_lg_u64 vcc, 0
s_cbranch_scc1 K2_SAFE
v_rcp_f32 v226, v208
v_rcp_f32 v228, v212
s_lshl_b32 s36, s55, 7
s_lshl_b32 s37, s26, 2
s_add_u32 s36, s36, s37
s_lshl_b32 s36, s36, 13
s_sub_u32 s35, 7, s7
s_lshl_b32 s35, s35, 10
s_add_u32 s35, s35, s36
s_lshl_b32 s37, s7, 10
s_add_u32 s36, s36, s37
v_mfma_f32_16x16x32_bf16 v[108:111], a[68:71], v[84:87], v[108:111]
ds_read_b128 a[100:103], v221 offset:21504
v_mfma_f32_16x16x32_bf16 v[96:99], a[72:75], v[88:91], v[96:99]
v_mfma_f32_16x16x32_bf16 v[100:103], a[72:75], v[92:95], v[100:103]
ds_read_b128 a[104:107], v221 offset:28672
v_mfma_f32_16x16x32_bf16 v[104:107], a[76:79], v[88:91], v[104:107]
v_mfma_f32_16x16x32_bf16 v[108:111], a[76:79], v[92:95], v[108:111]
ds_read_b128 a[108:111], v221 offset:29696
s_waitcnt lgkmcnt(4)
v_mfma_f32_16x16x32_bf16 v[112:115], a[80:83], v[80:83], v[112:115]
v_mfma_f32_16x16x32_bf16 v[116:119], a[80:83], v[84:87], v[116:119]
ds_read_b128 a[112:115], v221 offset:22528
v_mfma_f32_16x16x32_bf16 v[120:123], a[84:87], v[80:83], v[120:123]
v_pk_mul_f32 v[96:97], v[96:97], v[226:227] op_sel_hi:[1,0]
v_pk_mul_f32 v[98:99], v[98:99], v[226:227] op_sel_hi:[1,0]
v_pk_mul_f32 v[104:105], v[104:105], v[226:227] op_sel_hi:[1,0]
v_pk_mul_f32 v[106:107], v[106:107], v[226:227] op_sel_hi:[1,0]
v_mfma_f32_16x16x32_bf16 v[124:127], a[84:87], v[84:87], v[124:127]
ds_read_b128 a[116:119], v221 offset:23552
v_cvt_pk_bf16_f32 v0, v96, v97
v_cvt_pk_bf16_f32 v1, v98, v99
v_cvt_pk_bf16_f32 v2, v104, v105
v_cvt_pk_bf16_f32 v3, v106, v107
v_mfma_f32_16x16x32_bf16 v[112:115], a[88:91], v[88:91], v[112:115]
s_add_u32 s37, s36, 0
buffer_store_dwordx4 v[0:3], v252, s[20:23], s37 offen sc1
v_pk_mul_f32 v[100:101], v[100:101], v[228:229] op_sel_hi:[1,0]
v_pk_mul_f32 v[102:103], v[102:103], v[228:229] op_sel_hi:[1,0]
v_mfma_f32_16x16x32_bf16 v[116:119], a[88:91], v[92:95], v[116:119]
ds_read_b128 a[120:123], v221 offset:30720
v_pk_mul_f32 v[108:109], v[108:109], v[228:229] op_sel_hi:[1,0]
v_pk_mul_f32 v[110:111], v[110:111], v[228:229] op_sel_hi:[1,0]
v_cvt_pk_bf16_f32 v4, v100, v101
v_cvt_pk_bf16_f32 v5, v102, v103
v_mfma_f32_16x16x32_bf16 v[120:123], a[92:95], v[88:91], v[120:123]
v_cvt_pk_bf16_f32 v6, v108, v109
v_cvt_pk_bf16_f32 v7, v110, v111
s_add_u32 s37, s35, 0
buffer_store_dwordx4 v[4:7], v252, s[20:23], s37 offen sc1
v_mfma_f32_16x16x32_bf16 v[124:127], a[92:95], v[92:95], v[124:127]
ds_read_b128 a[124:127], v221 offset:31744
s_waitcnt lgkmcnt(4)
v_mfma_f32_16x16x32_bf16 v[128:131], a[96:99], v[80:83], v[128:131]
v_mfma_f32_16x16x32_bf16 v[132:135], a[96:99], v[84:87], v[132:135]
v_mfma_f32_16x16x32_bf16 v[136:139], a[100:103], v[80:83], v[136:139]
v_pk_mul_f32 v[112:113], v[112:113], v[226:227] op_sel_hi:[1,0]
v_pk_mul_f32 v[114:115], v[114:115], v[226:227] op_sel_hi:[1,0]
v_pk_mul_f32 v[120:121], v[120:121], v[226:227] op_sel_hi:[1,0]
v_pk_mul_f32 v[122:123], v[122:123], v[226:227] op_sel_hi:[1,0]
v_mfma_f32_16x16x32_bf16 v[140:143], a[100:103], v[84:87], v[140:143]
v_cvt_pk_bf16_f32 v8, v112, v113
v_cvt_pk_bf16_f32 v9, v114, v115
v_cvt_pk_bf16_f32 v10, v120, v121
v_cvt_pk_bf16_f32 v11, v122, v123
v_mfma_f32_16x16x32_bf16 v[128:131], a[104:107], v[88:91], v[128:131]
s_add_u32 s37, s36, 8192
buffer_store_dwordx4 v[8:11], v252, s[20:23], s37 offen sc1
v_pk_mul_f32 v[116:117], v[116:117], v[228:229] op_sel_hi:[1,0]
v_pk_mul_f32 v[118:119], v[118:119], v[228:229] op_sel_hi:[1,0]
v_mfma_f32_16x16x32_bf16 v[132:135], a[104:107], v[92:95], v[132:135]
v_pk_mul_f32 v[124:125], v[124:125], v[228:229] op_sel_hi:[1,0]
v_pk_mul_f32 v[126:127], v[126:127], v[228:229] op_sel_hi:[1,0]
v_cvt_pk_bf16_f32 v12, v116, v117
v_cvt_pk_bf16_f32 v13, v118, v119
v_mfma_f32_16x16x32_bf16 v[136:139], a[108:111], v[88:91], v[136:139]
v_cvt_pk_bf16_f32 v14, v124, v125
v_cvt_pk_bf16_f32 v15, v126, v127
s_add_u32 s37, s35, 8192
buffer_store_dwordx4 v[12:15], v252, s[20:23], s37 offen sc1
v_mfma_f32_16x16x32_bf16 v[140:143], a[108:111], v[92:95], v[140:143]
s_waitcnt lgkmcnt(0)
v_mfma_f32_16x16x32_bf16 v[144:147], a[112:115], v[80:83], v[144:147]
v_mfma_f32_16x16x32_bf16 v[148:151], a[112:115], v[84:87], v[148:151]
v_mfma_f32_16x16x32_bf16 v[152:155], a[116:119], v[80:83], v[152:155]
v_pk_mul_f32 v[128:129], v[128:129], v[226:227] op_sel_hi:[1,0]
v_pk_mul_f32 v[130:131], v[130:131], v[226:227] op_sel_hi:[1,0]
v_pk_mul_f32 v[136:137], v[136:137], v[226:227] op_sel_hi:[1,0]
v_pk_mul_f32 v[138:139], v[138:139], v[226:227] op_sel_hi:[1,0]
v_mfma_f32_16x16x32_bf16 v[156:159], a[116:119], v[84:87], v[156:159]
v_cvt_pk_bf16_f32 v0, v128, v129
v_cvt_pk_bf16_f32 v1, v130, v131
v_cvt_pk_bf16_f32 v2, v136, v137
v_cvt_pk_bf16_f32 v3, v138, v139
v_mfma_f32_16x16x32_bf16 v[144:147], a[120:123], v[88:91], v[144:147]
s_add_u32 s37, s36, 16384
buffer_store_dwordx4 v[0:3], v252, s[20:23], s37 offen sc1
v_pk_mul_f32 v[132:133], v[132:133], v[228:229] op_sel_hi:[1,0]
v_pk_mul_f32 v[134:135], v[134:135], v[228:229] op_sel_hi:[1,0]
v_mfma_f32_16x16x32_bf16 v[148:151], a[120:123], v[92:95], v[148:151]
v_pk_mul_f32 v[140:141], v[140:141], v[228:229] op_sel_hi:[1,0]
v_pk_mul_f32 v[142:143], v[142:143], v[228:229] op_sel_hi:[1,0]
v_cvt_pk_bf16_f32 v4, v132, v133
v_cvt_pk_bf16_f32 v5, v134, v135
v_mfma_f32_16x16x32_bf16 v[152:155], a[124:127], v[88:91], v[152:155]
v_cvt_pk_bf16_f32 v6, v140, v141
v_cvt_pk_bf16_f32 v7, v142, v143
s_add_u32 s37, s35, 16384
buffer_store_dwordx4 v[4:7], v252, s[20:23], s37 offen sc1
v_mfma_f32_16x16x32_bf16 v[156:159], a[124:127], v[92:95], v[156:159]
s_nop 7
v_pk_mul_f32 v[144:145], v[144:145], v[226:227] op_sel_hi:[1,0]
v_pk_mul_f32 v[146:147], v[146:147], v[226:227] op_sel_hi:[1,0]
v_pk_mul_f32 v[152:153], v[152:153], v[226:227] op_sel_hi:[1,0]
v_pk_mul_f32 v[154:155], v[154:155], v[226:227] op_sel_hi:[1,0]
v_cvt_pk_bf16_f32 v8, v144, v145
v_cvt_pk_bf16_f32 v9, v146, v147
v_cvt_pk_bf16_f32 v10, v152, v153
v_cvt_pk_bf16_f32 v11, v154, v155
s_add_u32 s37, s36, 24576
buffer_store_dwordx4 v[8:11], v252, s[20:23], s37 offen sc1
v_pk_mul_f32 v[148:149], v[148:149], v[228:229] op_sel_hi:[1,0]
v_pk_mul_f32 v[150:151], v[150:151], v[228:229] op_sel_hi:[1,0]
v_pk_mul_f32 v[156:157], v[156:157], v[228:229] op_sel_hi:[1,0]
v_pk_mul_f32 v[158:159], v[158:159], v[228:229] op_sel_hi:[1,0]
v_cvt_pk_bf16_f32 v12, v148, v149
v_cvt_pk_bf16_f32 v13, v150, v151
v_cvt_pk_bf16_f32 v14, v156, v157
v_cvt_pk_bf16_f32 v15, v158, v159
s_add_u32 s37, s35, 24576
buffer_store_dwordx4 v[12:15], v252, s[20:23], s37 offen sc1
K2_EPI:
s_cmp_eq_u32 s51, 1
s_cbranch_scc1 K2_END
s_mov_b32 s51, 1
s_mov_b32 s49, s50
s_mov_b32 s55, s27
v_accvgpr_read_b32 v160, a128
v_accvgpr_read_b32 v161, a129
v_accvgpr_read_b32 v162, a130
v_accvgpr_read_b32 v163, a131
v_accvgpr_read_b32 v164, a132
v_accvgpr_read_b32 v165, a133
v_accvgpr_read_b32 v166, a134
v_accvgpr_read_b32 v167, a135
v_accvgpr_read_b32 v168, a136
v_accvgpr_read_b32 v169, a137
v_accvgpr_read_b32 v170, a138
v_accvgpr_read_b32 v171, a139
v_accvgpr_read_b32 v172, a140
v_accvgpr_read_b32 v173, a141
v_accvgpr_read_b32 v174, a142
v_accvgpr_read_b32 v175, a143
v_accvgpr_read_b32 v176, a144
v_accvgpr_read_b32 v177, a145
v_accvgpr_read_b32 v178, a146
v_accvgpr_read_b32 v179, a147
v_accvgpr_read_b32 v180, a148
v_accvgpr_read_b32 v181, a149
v_accvgpr_read_b32 v182, a150
v_accvgpr_read_b32 v183, a151
v_accvgpr_read_b32 v184, a152
v_accvgpr_read_b32 v185, a153
v_accvgpr_read_b32 v186, a154
v_accvgpr_read_b32 v187, a155
v_accvgpr_read_b32 v188, a156
v_accvgpr_read_b32 v189, a157
v_accvgpr_read_b32 v190, a158
v_accvgpr_read_b32 v191, a159
s_branch K2_BLOCK
s_branch K2_END
K2_SAFE:
s_waitcnt lgkmcnt(0)
s_nop 7
v_mov_b32 v96, 0
v_mov_b32 v97, 0
v_mov_b32 v98, 0
v_mov_b32 v99, 0
v_mov_b32 v100, 0
v_mov_b32 v101, 0
v_mov_b32 v102, 0
v_mov_b32 v103, 0
v_mov_b32 v104, 0
v_mov_b32 v105, 0
v_mov_b32 v106, 0
v_mov_b32 v107, 0
v_mov_b32 v108, 0
v_mov_b32 v109, 0
v_mov_b32 v110, 0
v_mov_b32 v111, 0
v_mov_b32 v112, 0
v_mov_b32 v113, 0
v_mov_b32 v114, 0
v_mov_b32 v115, 0
v_mov_b32 v116, 0
v_mov_b32 v117, 0
v_mov_b32 v118, 0
v_mov_b32 v119, 0
v_mov_b32 v120, 0
v_mov_b32 v121, 0
v_mov_b32 v122, 0
v_mov_b32 v123, 0
v_mov_b32 v124, 0
v_mov_b32 v125, 0
v_mov_b32 v126, 0
v_mov_b32 v127, 0
v_mov_b32 v128, 0
v_mov_b32 v129, 0
v_mov_b32 v130, 0
v_mov_b32 v131, 0
v_mov_b32 v132, 0
v_mov_b32 v133, 0
v_mov_b32 v134, 0
v_mov_b32 v135, 0
v_mov_b32 v136, 0
v_mov_b32 v137, 0
v_mov_b32 v138, 0
v_mov_b32 v139, 0
v_mov_b32 v140, 0
v_mov_b32 v141, 0
v_mov_b32 v142, 0
v_mov_b32 v143, 0
v_mov_b32 v144, 0
v_mov_b32 v145, 0
v_mov_b32 v146, 0
v_mov_b32 v147, 0
v_mov_b32 v148, 0
v_mov_b32 v149, 0
v_mov_b32 v150, 0
v_mov_b32 v151, 0
v_mov_b32 v152, 0
v_mov_b32 v153, 0
v_mov_b32 v154, 0
v_mov_b32 v155, 0
v_mov_b32 v156, 0
v_mov_b32 v157, 0
v_mov_b32 v158, 0
v_mov_b32 v159, 0
v_mov_b32 v208, 0
v_mov_b32 v209, 0
v_mov_b32 v210, 0
v_mov_b32 v211, 0
v_mov_b32 v212, 0
v_mov_b32 v213, 0
v_mov_b32 v214, 0
v_mov_b32 v215, 0
v_mov_b32 v216, 0xf149f2ca
v_mov_b32 v218, 0xf149f2ca
s_mov_b32 s30, 0
s_lshl_b32 s31, s26, 20
K2_SAFE_LOOP:
s_add_u32 s32, s31, 0
buffer_load_dwordx4 a[0:3], v252, s[16:19], s32 offen offset:0 sc1
buffer_load_dwordx4 a[4:7], v252, s[16:19], s32 offen offset:1024 sc1
buffer_load_dwordx4 a[8:11], v252, s[16:19], s32 offen offset:2048 sc1
buffer_load_dwordx4 a[12:15], v252, s[16:19], s32 offen offset:3072 sc1
s_add_u32 s32, s31, 4096
buffer_load_dwordx4 a[16:19], v252, s[16:19], s32 offen offset:0 sc1
buffer_load_dwordx4 a[20:23], v252, s[16:19], s32 offen offset:1024 sc1
buffer_load_dwordx4 a[24:27], v252, s[16:19], s32 offen offset:2048 sc1
buffer_load_dwordx4 a[28:31], v252, s[16:19], s32 offen offset:3072 sc1
s_add_u32 s32, s31, 8192
buffer_load_dwordx4 a[32:35], v252, s[16:19], s32 offen offset:0 sc1
buffer_load_dwordx4 a[36:39], v252, s[16:19], s32 offen offset:1024 sc1
buffer_load_dwordx4 a[40:43], v252, s[16:19], s32 offen offset:2048 sc1
buffer_load_dwordx4 a[44:47], v252, s[16:19], s32 offen offset:3072 sc1
s_add_u32 s32, s31, 12288
buffer_load_dwordx4 a[48:51], v252, s[16:19], s32 offen offset:0 sc1
buffer_load_dwordx4 a[52:55], v252, s[16:19], s32 offen offset:1024 sc1
buffer_load_dwordx4 a[56:59], v252, s[16:19], s32 offen offset:2048 sc1
buffer_load_dwordx4 a[60:63], v252, s[16:19], s32 offen offset:3072 sc1
s_add_u32 s32, s31, 32768
buffer_load_dwordx4 a[64:67], v252, s[16:19], s32 offen offset:0 sc1
buffer_load_dwordx4 a[68:71], v252, s[16:19], s32 offen offset:1024 sc1
buffer_load_dwordx4 a[72:75], v252, s[16:19], s32 offen offset:2048 sc1
buffer_load_dwordx4 a[76:79], v252, s[16:19], s32 offen offset:3072 sc1
s_add_u32 s32, s31, 36864
buffer_load_dwordx4 a[80:83], v252, s[16:19], s32 offen offset:0 sc1
buffer_load_dwordx4 a[84:87], v252, s[16:19], s32 offen offset:1024 sc1
buffer_load_dwordx4 a[88:91], v252, s[16:19], s32 offen offset:2048 sc1
buffer_load_dwordx4 a[92:95], v252, s[16:19], s32 offen offset:3072 sc1
s_add_u32 s32, s31, 40960
buffer_load_dwordx4 a[96:99], v252, s[16:19], s32 offen offset:0 sc1
buffer_load_dwordx4 a[100:103], v252, s[16:19], s32 offen offset:1024 sc1
buffer_load_dwordx4 a[104:107], v252, s[16:19], s32 offen offset:2048 sc1
buffer_load_dwordx4 a[108:111], v252, s[16:19], s32 offen offset:3072 sc1
s_add_u32 s32, s31, 45056
buffer_load_dwordx4 a[112:115], v252, s[16:19], s32 offen offset:0 sc1
buffer_load_dwordx4 a[116:119], v252, s[16:19], s32 offen offset:1024 sc1
buffer_load_dwordx4 a[120:123], v252, s[16:19], s32 offen offset:2048 sc1
buffer_load_dwordx4 a[124:127], v252, s[16:19], s32 offen offset:3072 sc1
s_waitcnt vmcnt(0)
v_mfma_f32_16x16x32_bf16 v[0:3], a[0:3], v[160:163], 0
v_mfma_f32_16x16x32_bf16 v[4:7], a[0:3], v[176:179], 0
v_mfma_f32_16x16x32_bf16 v[0:3], a[4:7], v[164:167], v[0:3]
v_mfma_f32_16x16x32_bf16 v[4:7], a[4:7], v[180:183], v[4:7]
v_mfma_f32_16x16x32_bf16 v[0:3], a[8:11], v[168:171], v[0:3]
v_mfma_f32_16x16x32_bf16 v[4:7], a[8:11], v[184:187], v[4:7]
v_mfma_f32_16x16x32_bf16 v[0:3], a[12:15], v[172:175], v[0:3]
v_mfma_f32_16x16x32_bf16 v[4:7], a[12:15], v[188:191], v[4:7]
v_mfma_f32_16x16x32_bf16 v[8:11], a[16:19], v[160:163], 0
v_mfma_f32_16x16x32_bf16 v[12:15], a[16:19], v[176:179], 0
v_mfma_f32_16x16x32_bf16 v[8:11], a[20:23], v[164:167], v[8:11]
v_mfma_f32_16x16x32_bf16 v[12:15], a[20:23], v[180:183], v[12:15]
v_mfma_f32_16x16x32_bf16 v[8:11], a[24:27], v[168:171], v[8:11]
v_mfma_f32_16x16x32_bf16 v[12:15], a[24:27], v[184:187], v[12:15]
v_mfma_f32_16x16x32_bf16 v[8:11], a[28:31], v[172:175], v[8:11]
v_mfma_f32_16x16x32_bf16 v[12:15], a[28:31], v[188:191], v[12:15]
v_mfma_f32_16x16x32_bf16 v[16:19], a[32:35], v[160:163], 0
v_mfma_f32_16x16x32_bf16 v[20:23], a[32:35], v[176:179], 0
v_mfma_f32_16x16x32_bf16 v[16:19], a[36:39], v[164:167], v[16:19]
v_mfma_f32_16x16x32_bf16 v[20:23], a[36:39], v[180:183], v[20:23]
v_mfma_f32_16x16x32_bf16 v[16:19], a[40:43], v[168:171], v[16:19]
v_mfma_f32_16x16x32_bf16 v[20:23], a[40:43], v[184:187], v[20:23]
v_mfma_f32_16x16x32_bf16 v[16:19], a[44:47], v[172:175], v[16:19]
v_mfma_f32_16x16x32_bf16 v[20:23], a[44:47], v[188:191], v[20:23]
v_mfma_f32_16x16x32_bf16 v[24:27], a[48:51], v[160:163], 0
v_mfma_f32_16x16x32_bf16 v[28:31], a[48:51], v[176:179], 0
v_mfma_f32_16x16x32_bf16 v[24:27], a[52:55], v[164:167], v[24:27]
v_mfma_f32_16x16x32_bf16 v[28:31], a[52:55], v[180:183], v[28:31]
v_mfma_f32_16x16x32_bf16 v[24:27], a[56:59], v[168:171], v[24:27]
v_mfma_f32_16x16x32_bf16 v[28:31], a[56:59], v[184:187], v[28:31]
v_mfma_f32_16x16x32_bf16 v[24:27], a[60:63], v[172:175], v[24:27]
v_mfma_f32_16x16x32_bf16 v[28:31], a[60:63], v[188:191], v[28:31]
v_mfma_f32_16x16x32_bf16 v[32:35], a[64:67], v[160:163], 0
v_mfma_f32_16x16x32_bf16 v[36:39], a[64:67], v[176:179], 0
v_mfma_f32_16x16x32_bf16 v[32:35], a[68:71], v[164:167], v[32:35]
v_mfma_f32_16x16x32_bf16 v[36:39], a[68:71], v[180:183], v[36:39]
v_mfma_f32_16x16x32_bf16 v[32:35], a[72:75], v[168:171], v[32:35]
v_mfma_f32_16x16x32_bf16 v[36:39], a[72:75], v[184:187], v[36:39]
v_mfma_f32_16x16x32_bf16 v[32:35], a[76:79], v[172:175], v[32:35]
v_mfma_f32_16x16x32_bf16 v[36:39], a[76:79], v[188:191], v[36:39]
v_mfma_f32_16x16x32_bf16 v[40:43], a[80:83], v[160:163], 0
v_mfma_f32_16x16x32_bf16 v[44:47], a[80:83], v[176:179], 0
v_mfma_f32_16x16x32_bf16 v[40:43], a[84:87], v[164:167], v[40:43]
v_mfma_f32_16x16x32_bf16 v[44:47], a[84:87], v[180:183], v[44:47]
v_mfma_f32_16x16x32_bf16 v[40:43], a[88:91], v[168:171], v[40:43]
v_mfma_f32_16x16x32_bf16 v[44:47], a[88:91], v[184:187], v[44:47]
v_mfma_f32_16x16x32_bf16 v[40:43], a[92:95], v[172:175], v[40:43]
v_mfma_f32_16x16x32_bf16 v[44:47], a[92:95], v[188:191], v[44:47]
v_mfma_f32_16x16x32_bf16 v[48:51], a[96:99], v[160:163], 0
v_mfma_f32_16x16x32_bf16 v[52:55], a[96:99], v[176:179], 0
v_mfma_f32_16x16x32_bf16 v[48:51], a[100:103], v[164:167], v[48:51]
v_mfma_f32_16x16x32_bf16 v[52:55], a[100:103], v[180:183], v[52:55]
v_mfma_f32_16x16x32_bf16 v[48:51], a[104:107], v[168:171], v[48:51]
v_mfma_f32_16x16x32_bf16 v[52:55], a[104:107], v[184:187], v[52:55]
v_mfma_f32_16x16x32_bf16 v[48:51], a[108:111], v[172:175], v[48:51]
v_mfma_f32_16x16x32_bf16 v[52:55], a[108:111], v[188:191], v[52:55]
v_mfma_f32_16x16x32_bf16 v[56:59], a[112:115], v[160:163], 0
v_mfma_f32_16x16x32_bf16 v[60:63], a[112:115], v[176:179], 0
v_mfma_f32_16x16x32_bf16 v[56:59], a[116:119], v[164:167], v[56:59]
v_mfma_f32_16x16x32_bf16 v[60:63], a[116:119], v[180:183], v[60:63]
v_mfma_f32_16x16x32_bf16 v[56:59], a[120:123], v[168:171], v[56:59]
v_mfma_f32_16x16x32_bf16 v[60:63], a[120:123], v[184:187], v[60:63]
v_mfma_f32_16x16x32_bf16 v[56:59], a[124:127], v[172:175], v[56:59]
v_mfma_f32_16x16x32_bf16 v[60:63], a[124:127], v[188:191], v[60:63]
s_nop 7
s_add_u32 s32, s31, 16384
buffer_load_dwordx4 a[0:3], v252, s[16:19], s32 offen offset:0 sc1
buffer_load_dwordx4 a[4:7], v252, s[16:19], s32 offen offset:1024 sc1
buffer_load_dwordx4 a[8:11], v252, s[16:19], s32 offen offset:2048 sc1
buffer_load_dwordx4 a[12:15], v252, s[16:19], s32 offen offset:3072 sc1
s_add_u32 s32, s31, 20480
buffer_load_dwordx4 a[16:19], v252, s[16:19], s32 offen offset:0 sc1
buffer_load_dwordx4 a[20:23], v252, s[16:19], s32 offen offset:1024 sc1
buffer_load_dwordx4 a[24:27], v252, s[16:19], s32 offen offset:2048 sc1
buffer_load_dwordx4 a[28:31], v252, s[16:19], s32 offen offset:3072 sc1
s_add_u32 s32, s31, 24576
buffer_load_dwordx4 a[32:35], v252, s[16:19], s32 offen offset:0 sc1
buffer_load_dwordx4 a[36:39], v252, s[16:19], s32 offen offset:1024 sc1
buffer_load_dwordx4 a[40:43], v252, s[16:19], s32 offen offset:2048 sc1
buffer_load_dwordx4 a[44:47], v252, s[16:19], s32 offen offset:3072 sc1
s_add_u32 s32, s31, 28672
buffer_load_dwordx4 a[48:51], v252, s[16:19], s32 offen offset:0 sc1
buffer_load_dwordx4 a[52:55], v252, s[16:19], s32 offen offset:1024 sc1
buffer_load_dwordx4 a[56:59], v252, s[16:19], s32 offen offset:2048 sc1
buffer_load_dwordx4 a[60:63], v252, s[16:19], s32 offen offset:3072 sc1
s_add_u32 s32, s31, 49152
buffer_load_dwordx4 a[64:67], v252, s[16:19], s32 offen offset:0 sc1
buffer_load_dwordx4 a[68:71], v252, s[16:19], s32 offen offset:1024 sc1
buffer_load_dwordx4 a[72:75], v252, s[16:19], s32 offen offset:2048 sc1
buffer_load_dwordx4 a[76:79], v252, s[16:19], s32 offen offset:3072 sc1
s_add_u32 s32, s31, 53248
buffer_load_dwordx4 a[80:83], v252, s[16:19], s32 offen offset:0 sc1
buffer_load_dwordx4 a[84:87], v252, s[16:19], s32 offen offset:1024 sc1
buffer_load_dwordx4 a[88:91], v252, s[16:19], s32 offen offset:2048 sc1
buffer_load_dwordx4 a[92:95], v252, s[16:19], s32 offen offset:3072 sc1
s_add_u32 s32, s31, 57344
buffer_load_dwordx4 a[96:99], v252, s[16:19], s32 offen offset:0 sc1
buffer_load_dwordx4 a[100:103], v252, s[16:19], s32 offen offset:1024 sc1
buffer_load_dwordx4 a[104:107], v252, s[16:19], s32 offen offset:2048 sc1
buffer_load_dwordx4 a[108:111], v252, s[16:19], s32 offen offset:3072 sc1
s_add_u32 s32, s31, 61440
buffer_load_dwordx4 a[112:115], v252, s[16:19], s32 offen offset:0 sc1
buffer_load_dwordx4 a[116:119], v252, s[16:19], s32 offen offset:1024 sc1
buffer_load_dwordx4 a[120:123], v252, s[16:19], s32 offen offset:2048 sc1
buffer_load_dwordx4 a[124:127], v252, s[16:19], s32 offen offset:3072 sc1
s_add_u32 s32, s30, 1
s_cmp_eq_u32 s32, s49
s_cbranch_scc0 K2_SAFE_NOMASK
v_mov_b32 v226, 0xff800000
v_lshrrev_b32 v227, 4, v253
v_lshlrev_b32 v227, 2, v227
v_and_b32 v228, 15, v253
v_sub_u32 v228, v228, v227
s_sub_u32 s32, 7, s7
s_lshl_b32 s32, s32, 4
v_add_u32 v229, s32, v228
s_lshl_b32 s32, s7, 4
v_add_u32 v228, s32, v228
s_movk_i32 s32, 0
v_cmp_gt_i32 s[92:93], s32, v228
s_movk_i32 s32, 1
v_cmp_gt_i32 s[94:95], s32, v228
s_movk_i32 s32, 2
v_cmp_gt_i32 s[96:97], s32, v228
s_movk_i32 s32, 3
v_cmp_gt_i32 s[98:99], s32, v228
v_cndmask_b32 v0, v0, v226, s[92:93]
v_cndmask_b32 v1, v1, v226, s[94:95]
v_cndmask_b32 v2, v2, v226, s[96:97]
v_cndmask_b32 v3, v3, v226, s[98:99]
s_movk_i32 s32, 0
v_cmp_gt_i32 s[92:93], s32, v229
s_movk_i32 s32, 1
v_cmp_gt_i32 s[94:95], s32, v229
s_movk_i32 s32, 2
v_cmp_gt_i32 s[96:97], s32, v229
s_movk_i32 s32, 3
v_cmp_gt_i32 s[98:99], s32, v229
v_cndmask_b32 v4, v4, v226, s[92:93]
v_cndmask_b32 v5, v5, v226, s[94:95]
v_cndmask_b32 v6, v6, v226, s[96:97]
v_cndmask_b32 v7, v7, v226, s[98:99]
s_movk_i32 s32, 16
v_cmp_gt_i32 s[92:93], s32, v228
s_movk_i32 s32, 17
v_cmp_gt_i32 s[94:95], s32, v228
s_movk_i32 s32, 18
v_cmp_gt_i32 s[96:97], s32, v228
s_movk_i32 s32, 19
v_cmp_gt_i32 s[98:99], s32, v228
v_cndmask_b32 v8, v8, v226, s[92:93]
v_cndmask_b32 v9, v9, v226, s[94:95]
v_cndmask_b32 v10, v10, v226, s[96:97]
v_cndmask_b32 v11, v11, v226, s[98:99]
s_movk_i32 s32, 16
v_cmp_gt_i32 s[92:93], s32, v229
s_movk_i32 s32, 17
v_cmp_gt_i32 s[94:95], s32, v229
s_movk_i32 s32, 18
v_cmp_gt_i32 s[96:97], s32, v229
s_movk_i32 s32, 19
v_cmp_gt_i32 s[98:99], s32, v229
v_cndmask_b32 v12, v12, v226, s[92:93]
v_cndmask_b32 v13, v13, v226, s[94:95]
v_cndmask_b32 v14, v14, v226, s[96:97]
v_cndmask_b32 v15, v15, v226, s[98:99]
s_movk_i32 s32, 32
v_cmp_gt_i32 s[92:93], s32, v228
s_movk_i32 s32, 33
v_cmp_gt_i32 s[94:95], s32, v228
s_movk_i32 s32, 34
v_cmp_gt_i32 s[96:97], s32, v228
s_movk_i32 s32, 35
v_cmp_gt_i32 s[98:99], s32, v228
v_cndmask_b32 v16, v16, v226, s[92:93]
v_cndmask_b32 v17, v17, v226, s[94:95]
v_cndmask_b32 v18, v18, v226, s[96:97]
v_cndmask_b32 v19, v19, v226, s[98:99]
s_movk_i32 s32, 32
v_cmp_gt_i32 s[92:93], s32, v229
s_movk_i32 s32, 33
v_cmp_gt_i32 s[94:95], s32, v229
s_movk_i32 s32, 34
v_cmp_gt_i32 s[96:97], s32, v229
s_movk_i32 s32, 35
v_cmp_gt_i32 s[98:99], s32, v229
v_cndmask_b32 v20, v20, v226, s[92:93]
v_cndmask_b32 v21, v21, v226, s[94:95]
v_cndmask_b32 v22, v22, v226, s[96:97]
v_cndmask_b32 v23, v23, v226, s[98:99]
s_movk_i32 s32, 48
v_cmp_gt_i32 s[92:93], s32, v228
s_movk_i32 s32, 49
v_cmp_gt_i32 s[94:95], s32, v228
s_movk_i32 s32, 50
v_cmp_gt_i32 s[96:97], s32, v228
s_movk_i32 s32, 51
v_cmp_gt_i32 s[98:99], s32, v228
v_cndmask_b32 v24, v24, v226, s[92:93]
v_cndmask_b32 v25, v25, v226, s[94:95]
v_cndmask_b32 v26, v26, v226, s[96:97]
v_cndmask_b32 v27, v27, v226, s[98:99]
s_movk_i32 s32, 48
v_cmp_gt_i32 s[92:93], s32, v229
s_movk_i32 s32, 49
v_cmp_gt_i32 s[94:95], s32, v229
s_movk_i32 s32, 50
v_cmp_gt_i32 s[96:97], s32, v229
s_movk_i32 s32, 51
v_cmp_gt_i32 s[98:99], s32, v229
v_cndmask_b32 v28, v28, v226, s[92:93]
v_cndmask_b32 v29, v29, v226, s[94:95]
v_cndmask_b32 v30, v30, v226, s[96:97]
v_cndmask_b32 v31, v31, v226, s[98:99]
s_movk_i32 s32, 64
v_cmp_gt_i32 s[92:93], s32, v228
s_movk_i32 s32, 65
v_cmp_gt_i32 s[94:95], s32, v228
s_movk_i32 s32, 66
v_cmp_gt_i32 s[96:97], s32, v228
s_movk_i32 s32, 67
v_cmp_gt_i32 s[98:99], s32, v228
v_cndmask_b32 v32, v32, v226, s[92:93]
v_cndmask_b32 v33, v33, v226, s[94:95]
v_cndmask_b32 v34, v34, v226, s[96:97]
v_cndmask_b32 v35, v35, v226, s[98:99]
s_movk_i32 s32, 64
v_cmp_gt_i32 s[92:93], s32, v229
s_movk_i32 s32, 65
v_cmp_gt_i32 s[94:95], s32, v229
s_movk_i32 s32, 66
v_cmp_gt_i32 s[96:97], s32, v229
s_movk_i32 s32, 67
v_cmp_gt_i32 s[98:99], s32, v229
v_cndmask_b32 v36, v36, v226, s[92:93]
v_cndmask_b32 v37, v37, v226, s[94:95]
v_cndmask_b32 v38, v38, v226, s[96:97]
v_cndmask_b32 v39, v39, v226, s[98:99]
s_movk_i32 s32, 80
v_cmp_gt_i32 s[92:93], s32, v228
s_movk_i32 s32, 81
v_cmp_gt_i32 s[94:95], s32, v228
s_movk_i32 s32, 82
v_cmp_gt_i32 s[96:97], s32, v228
s_movk_i32 s32, 83
v_cmp_gt_i32 s[98:99], s32, v228
v_cndmask_b32 v40, v40, v226, s[92:93]
v_cndmask_b32 v41, v41, v226, s[94:95]
v_cndmask_b32 v42, v42, v226, s[96:97]
v_cndmask_b32 v43, v43, v226, s[98:99]
s_movk_i32 s32, 80
v_cmp_gt_i32 s[92:93], s32, v229
s_movk_i32 s32, 81
v_cmp_gt_i32 s[94:95], s32, v229
s_movk_i32 s32, 82
v_cmp_gt_i32 s[96:97], s32, v229
s_movk_i32 s32, 83
v_cmp_gt_i32 s[98:99], s32, v229
v_cndmask_b32 v44, v44, v226, s[92:93]
v_cndmask_b32 v45, v45, v226, s[94:95]
v_cndmask_b32 v46, v46, v226, s[96:97]
v_cndmask_b32 v47, v47, v226, s[98:99]
s_movk_i32 s32, 96
v_cmp_gt_i32 s[92:93], s32, v228
s_movk_i32 s32, 97
v_cmp_gt_i32 s[94:95], s32, v228
s_movk_i32 s32, 98
v_cmp_gt_i32 s[96:97], s32, v228
s_movk_i32 s32, 99
v_cmp_gt_i32 s[98:99], s32, v228
v_cndmask_b32 v48, v48, v226, s[92:93]
v_cndmask_b32 v49, v49, v226, s[94:95]
v_cndmask_b32 v50, v50, v226, s[96:97]
v_cndmask_b32 v51, v51, v226, s[98:99]
s_movk_i32 s32, 96
v_cmp_gt_i32 s[92:93], s32, v229
s_movk_i32 s32, 97
v_cmp_gt_i32 s[94:95], s32, v229
s_movk_i32 s32, 98
v_cmp_gt_i32 s[96:97], s32, v229
s_movk_i32 s32, 99
v_cmp_gt_i32 s[98:99], s32, v229
v_cndmask_b32 v52, v52, v226, s[92:93]
v_cndmask_b32 v53, v53, v226, s[94:95]
v_cndmask_b32 v54, v54, v226, s[96:97]
v_cndmask_b32 v55, v55, v226, s[98:99]
s_movk_i32 s32, 112
v_cmp_gt_i32 s[92:93], s32, v228
s_movk_i32 s32, 113
v_cmp_gt_i32 s[94:95], s32, v228
s_movk_i32 s32, 114
v_cmp_gt_i32 s[96:97], s32, v228
s_movk_i32 s32, 115
v_cmp_gt_i32 s[98:99], s32, v228
v_cndmask_b32 v56, v56, v226, s[92:93]
v_cndmask_b32 v57, v57, v226, s[94:95]
v_cndmask_b32 v58, v58, v226, s[96:97]
v_cndmask_b32 v59, v59, v226, s[98:99]
s_movk_i32 s32, 112
v_cmp_gt_i32 s[92:93], s32, v229
s_movk_i32 s32, 113
v_cmp_gt_i32 s[94:95], s32, v229
s_movk_i32 s32, 114
v_cmp_gt_i32 s[96:97], s32, v229
s_movk_i32 s32, 115
v_cmp_gt_i32 s[98:99], s32, v229
v_cndmask_b32 v60, v60, v226, s[92:93]
v_cndmask_b32 v61, v61, v226, s[94:95]
v_cndmask_b32 v62, v62, v226, s[96:97]
v_cndmask_b32 v63, v63, v226, s[98:99]
K2_SAFE_NOMASK:
v_max3_f32 v230, v0, v1, v2
v_max_f32 v230, v230, v3
v_max3_f32 v230, v230, v8, v9
v_max3_f32 v230, v230, v10, v11
v_max3_f32 v230, v230, v16, v17
v_max3_f32 v230, v230, v18, v19
v_max3_f32 v230, v230, v24, v25
v_max3_f32 v230, v230, v26, v27
v_max3_f32 v230, v230, v32, v33
v_max3_f32 v230, v230, v34, v35
v_max3_f32 v230, v230, v40, v41
v_max3_f32 v230, v230, v42, v43
v_max3_f32 v230, v230, v48, v49
v_max3_f32 v230, v230, v50, v51
v_max3_f32 v230, v230, v56, v57
v_max3_f32 v230, v230, v58, v59
v_max3_f32 v232, v4, v5, v6
v_max_f32 v232, v232, v7
v_max3_f32 v232, v232, v12, v13
v_max3_f32 v232, v232, v14, v15
v_max3_f32 v232, v232, v20, v21
v_max3_f32 v232, v232, v22, v23
v_max3_f32 v232, v232, v28, v29
v_max3_f32 v232, v232, v30, v31
v_max3_f32 v232, v232, v36, v37
v_max3_f32 v232, v232, v38, v39
v_max3_f32 v232, v232, v44, v45
v_max3_f32 v232, v232, v46, v47
v_max3_f32 v232, v232, v52, v53
v_max3_f32 v232, v232, v54, v55
v_max3_f32 v232, v232, v60, v61
v_max3_f32 v232, v232, v62, v63
v_mov_b32 v234, v230
v_mov_b32 v235, v232
s_nop 0
v_permlane16_swap_b32 v234, v230
v_permlane16_swap_b32 v235, v232
v_max_f32 v230, v230, v234
v_max_f32 v232, v232, v235
v_mov_b32 v234, v230
v_mov_b32 v235, v232
s_nop 0
v_permlane32_swap_b32 v234, v230
v_permlane32_swap_b32 v235, v232
v_max_f32 v230, v230, v234
v_max_f32 v232, v232, v235
v_max_f32 v230, v230, v216
v_sub_f32 v234, v216, v230
v_exp_f32 v234, v234
v_mov_b32 v216, v230
v_max_f32 v232, v232, v218
v_sub_f32 v236, v218, v232
v_exp_f32 v236, v236
v_mov_b32 v218, v232
v_pk_mul_f32 v[96:97], v[96:97], v[234:235] op_sel_hi:[1,0]
v_pk_mul_f32 v[98:99], v[98:99], v[234:235] op_sel_hi:[1,0]
v_pk_mul_f32 v[104:105], v[104:105], v[234:235] op_sel_hi:[1,0]
v_pk_mul_f32 v[106:107], v[106:107], v[234:235] op_sel_hi:[1,0]
v_pk_mul_f32 v[112:113], v[112:113], v[234:235] op_sel_hi:[1,0]
v_pk_mul_f32 v[114:115], v[114:115], v[234:235] op_sel_hi:[1,0]
v_pk_mul_f32 v[120:121], v[120:121], v[234:235] op_sel_hi:[1,0]
v_pk_mul_f32 v[122:123], v[122:123], v[234:235] op_sel_hi:[1,0]
v_pk_mul_f32 v[128:129], v[128:129], v[234:235] op_sel_hi:[1,0]
v_pk_mul_f32 v[130:131], v[130:131], v[234:235] op_sel_hi:[1,0]
v_pk_mul_f32 v[136:137], v[136:137], v[234:235] op_sel_hi:[1,0]
v_pk_mul_f32 v[138:139], v[138:139], v[234:235] op_sel_hi:[1,0]
v_pk_mul_f32 v[144:145], v[144:145], v[234:235] op_sel_hi:[1,0]
v_pk_mul_f32 v[146:147], v[146:147], v[234:235] op_sel_hi:[1,0]
v_pk_mul_f32 v[152:153], v[152:153], v[234:235] op_sel_hi:[1,0]
v_pk_mul_f32 v[154:155], v[154:155], v[234:235] op_sel_hi:[1,0]
v_pk_mul_f32 v[208:209], v[208:209], v[234:235] op_sel_hi:[1,0]
v_pk_mul_f32 v[210:211], v[210:211], v[234:235] op_sel_hi:[1,0]
v_pk_mul_f32 v[100:101], v[100:101], v[236:237] op_sel_hi:[1,0]
v_pk_mul_f32 v[102:103], v[102:103], v[236:237] op_sel_hi:[1,0]
v_pk_mul_f32 v[108:109], v[108:109], v[236:237] op_sel_hi:[1,0]
v_pk_mul_f32 v[110:111], v[110:111], v[236:237] op_sel_hi:[1,0]
v_pk_mul_f32 v[116:117], v[116:117], v[236:237] op_sel_hi:[1,0]
v_pk_mul_f32 v[118:119], v[118:119], v[236:237] op_sel_hi:[1,0]
v_pk_mul_f32 v[124:125], v[124:125], v[236:237] op_sel_hi:[1,0]
v_pk_mul_f32 v[126:127], v[126:127], v[236:237] op_sel_hi:[1,0]
v_pk_mul_f32 v[132:133], v[132:133], v[236:237] op_sel_hi:[1,0]
v_pk_mul_f32 v[134:135], v[134:135], v[236:237] op_sel_hi:[1,0]
v_pk_mul_f32 v[140:141], v[140:141], v[236:237] op_sel_hi:[1,0]
v_pk_mul_f32 v[142:143], v[142:143], v[236:237] op_sel_hi:[1,0]
v_pk_mul_f32 v[148:149], v[148:149], v[236:237] op_sel_hi:[1,0]
v_pk_mul_f32 v[150:151], v[150:151], v[236:237] op_sel_hi:[1,0]
v_pk_mul_f32 v[156:157], v[156:157], v[236:237] op_sel_hi:[1,0]
v_pk_mul_f32 v[158:159], v[158:159], v[236:237] op_sel_hi:[1,0]
v_pk_mul_f32 v[212:213], v[212:213], v[236:237] op_sel_hi:[1,0]
v_pk_mul_f32 v[214:215], v[214:215], v[236:237] op_sel_hi:[1,0]
v_pk_add_f32 v[0:1], v[0:1], v[216:217] op_sel_hi:[1,0] neg_lo:[0,1] neg_hi:[0,1]
v_pk_add_f32 v[2:3], v[2:3], v[216:217] op_sel_hi:[1,0] neg_lo:[0,1] neg_hi:[0,1]
v_pk_add_f32 v[4:5], v[4:5], v[218:219] op_sel_hi:[1,0] neg_lo:[0,1] neg_hi:[0,1]
v_pk_add_f32 v[6:7], v[6:7], v[218:219] op_sel_hi:[1,0] neg_lo:[0,1] neg_hi:[0,1]
v_pk_add_f32 v[8:9], v[8:9], v[216:217] op_sel_hi:[1,0] neg_lo:[0,1] neg_hi:[0,1]
v_pk_add_f32 v[10:11], v[10:11], v[216:217] op_sel_hi:[1,0] neg_lo:[0,1] neg_hi:[0,1]
v_pk_add_f32 v[12:13], v[12:13], v[218:219] op_sel_hi:[1,0] neg_lo:[0,1] neg_hi:[0,1]
v_pk_add_f32 v[14:15], v[14:15], v[218:219] op_sel_hi:[1,0] neg_lo:[0,1] neg_hi:[0,1]
v_pk_add_f32 v[16:17], v[16:17], v[216:217] op_sel_hi:[1,0] neg_lo:[0,1] neg_hi:[0,1]
v_pk_add_f32 v[18:19], v[18:19], v[216:217] op_sel_hi:[1,0] neg_lo:[0,1] neg_hi:[0,1]
v_pk_add_f32 v[20:21], v[20:21], v[218:219] op_sel_hi:[1,0] neg_lo:[0,1] neg_hi:[0,1]
v_pk_add_f32 v[22:23], v[22:23], v[218:219] op_sel_hi:[1,0] neg_lo:[0,1] neg_hi:[0,1]
v_pk_add_f32 v[24:25], v[24:25], v[216:217] op_sel_hi:[1,0] neg_lo:[0,1] neg_hi:[0,1]
v_pk_add_f32 v[26:27], v[26:27], v[216:217] op_sel_hi:[1,0] neg_lo:[0,1] neg_hi:[0,1]
v_pk_add_f32 v[28:29], v[28:29], v[218:219] op_sel_hi:[1,0] neg_lo:[0,1] neg_hi:[0,1]
v_pk_add_f32 v[30:31], v[30:31], v[218:219] op_sel_hi:[1,0] neg_lo:[0,1] neg_hi:[0,1]
v_pk_add_f32 v[32:33], v[32:33], v[216:217] op_sel_hi:[1,0] neg_lo:[0,1] neg_hi:[0,1]
v_pk_add_f32 v[34:35], v[34:35], v[216:217] op_sel_hi:[1,0] neg_lo:[0,1] neg_hi:[0,1]
v_pk_add_f32 v[36:37], v[36:37], v[218:219] op_sel_hi:[1,0] neg_lo:[0,1] neg_hi:[0,1]
v_pk_add_f32 v[38:39], v[38:39], v[218:219] op_sel_hi:[1,0] neg_lo:[0,1] neg_hi:[0,1]
v_pk_add_f32 v[40:41], v[40:41], v[216:217] op_sel_hi:[1,0] neg_lo:[0,1] neg_hi:[0,1]
v_pk_add_f32 v[42:43], v[42:43], v[216:217] op_sel_hi:[1,0] neg_lo:[0,1] neg_hi:[0,1]
v_pk_add_f32 v[44:45], v[44:45], v[218:219] op_sel_hi:[1,0] neg_lo:[0,1] neg_hi:[0,1]
v_pk_add_f32 v[46:47], v[46:47], v[218:219] op_sel_hi:[1,0] neg_lo:[0,1] neg_hi:[0,1]
v_pk_add_f32 v[48:49], v[48:49], v[216:217] op_sel_hi:[1,0] neg_lo:[0,1] neg_hi:[0,1]
v_pk_add_f32 v[50:51], v[50:51], v[216:217] op_sel_hi:[1,0] neg_lo:[0,1] neg_hi:[0,1]
v_pk_add_f32 v[52:53], v[52:53], v[218:219] op_sel_hi:[1,0] neg_lo:[0,1] neg_hi:[0,1]
v_pk_add_f32 v[54:55], v[54:55], v[218:219] op_sel_hi:[1,0] neg_lo:[0,1] neg_hi:[0,1]
v_pk_add_f32 v[56:57], v[56:57], v[216:217] op_sel_hi:[1,0] neg_lo:[0,1] neg_hi:[0,1]
v_pk_add_f32 v[58:59], v[58:59], v[216:217] op_sel_hi:[1,0] neg_lo:[0,1] neg_hi:[0,1]
v_pk_add_f32 v[60:61], v[60:61], v[218:219] op_sel_hi:[1,0] neg_lo:[0,1] neg_hi:[0,1]
v_pk_add_f32 v[62:63], v[62:63], v[218:219] op_sel_hi:[1,0] neg_lo:[0,1] neg_hi:[0,1]
v_exp_f32 v0, v0
v_exp_f32 v1, v1
v_exp_f32 v2, v2
v_exp_f32 v3, v3
v_exp_f32 v4, v4
v_exp_f32 v5, v5
v_exp_f32 v6, v6
v_exp_f32 v7, v7
v_exp_f32 v8, v8
v_exp_f32 v9, v9
v_exp_f32 v10, v10
v_exp_f32 v11, v11
v_exp_f32 v12, v12
v_exp_f32 v13, v13
v_exp_f32 v14, v14
v_exp_f32 v15, v15
v_exp_f32 v16, v16
v_exp_f32 v17, v17
v_exp_f32 v18, v18
v_exp_f32 v19, v19
v_exp_f32 v20, v20
v_exp_f32 v21, v21
v_exp_f32 v22, v22
v_exp_f32 v23, v23
v_exp_f32 v24, v24
v_exp_f32 v25, v25
v_exp_f32 v26, v26
v_exp_f32 v27, v27
v_exp_f32 v28, v28
v_exp_f32 v29, v29
v_exp_f32 v30, v30
v_exp_f32 v31, v31
v_exp_f32 v32, v32
v_exp_f32 v33, v33
v_exp_f32 v34, v34
v_exp_f32 v35, v35
v_exp_f32 v36, v36
v_exp_f32 v37, v37
v_exp_f32 v38, v38
v_exp_f32 v39, v39
v_exp_f32 v40, v40
v_exp_f32 v41, v41
v_exp_f32 v42, v42
v_exp_f32 v43, v43
v_exp_f32 v44, v44
v_exp_f32 v45, v45
v_exp_f32 v46, v46
v_exp_f32 v47, v47
v_exp_f32 v48, v48
v_exp_f32 v49, v49
v_exp_f32 v50, v50
v_exp_f32 v51, v51
v_exp_f32 v52, v52
v_exp_f32 v53, v53
v_exp_f32 v54, v54
v_exp_f32 v55, v55
v_exp_f32 v56, v56
v_exp_f32 v57, v57
v_exp_f32 v58, v58
v_exp_f32 v59, v59
v_exp_f32 v60, v60
v_exp_f32 v61, v61
v_exp_f32 v62, v62
v_exp_f32 v63, v63
v_cvt_pk_bf16_f32 v64, v0, v1
v_cvt_pk_bf16_f32 v65, v2, v3
v_cvt_pk_bf16_f32 v68, v4, v5
v_cvt_pk_bf16_f32 v69, v6, v7
v_cvt_pk_bf16_f32 v66, v8, v9
v_cvt_pk_bf16_f32 v67, v10, v11
v_cvt_pk_bf16_f32 v70, v12, v13
v_cvt_pk_bf16_f32 v71, v14, v15
v_cvt_pk_bf16_f32 v72, v16, v17
v_cvt_pk_bf16_f32 v73, v18, v19
v_cvt_pk_bf16_f32 v76, v20, v21
v_cvt_pk_bf16_f32 v77, v22, v23
v_cvt_pk_bf16_f32 v74, v24, v25
v_cvt_pk_bf16_f32 v75, v26, v27
v_cvt_pk_bf16_f32 v78, v28, v29
v_cvt_pk_bf16_f32 v79, v30, v31
v_cvt_pk_bf16_f32 v80, v32, v33
v_cvt_pk_bf16_f32 v81, v34, v35
v_cvt_pk_bf16_f32 v84, v36, v37
v_cvt_pk_bf16_f32 v85, v38, v39
v_cvt_pk_bf16_f32 v82, v40, v41
v_cvt_pk_bf16_f32 v83, v42, v43
v_cvt_pk_bf16_f32 v86, v44, v45
v_cvt_pk_bf16_f32 v87, v46, v47
v_cvt_pk_bf16_f32 v88, v48, v49
v_cvt_pk_bf16_f32 v89, v50, v51
v_cvt_pk_bf16_f32 v92, v52, v53
v_cvt_pk_bf16_f32 v93, v54, v55
v_cvt_pk_bf16_f32 v90, v56, v57
v_cvt_pk_bf16_f32 v91, v58, v59
v_cvt_pk_bf16_f32 v94, v60, v61
v_cvt_pk_bf16_f32 v95, v62, v63
s_waitcnt vmcnt(0)
v_mfma_f32_16x16x32_bf16 v[208:211], a[160:163], v[64:67], v[208:211]
v_mfma_f32_16x16x32_bf16 v[212:215], a[160:163], v[68:71], v[212:215]
v_mfma_f32_16x16x32_bf16 v[96:99], a[0:3], v[64:67], v[96:99]
v_mfma_f32_16x16x32_bf16 v[100:103], a[0:3], v[68:71], v[100:103]
v_mfma_f32_16x16x32_bf16 v[104:107], a[4:7], v[64:67], v[104:107]
v_mfma_f32_16x16x32_bf16 v[108:111], a[4:7], v[68:71], v[108:111]
v_mfma_f32_16x16x32_bf16 v[112:115], a[8:11], v[64:67], v[112:115]
v_mfma_f32_16x16x32_bf16 v[116:119], a[8:11], v[68:71], v[116:119]
v_mfma_f32_16x16x32_bf16 v[120:123], a[12:15], v[64:67], v[120:123]
v_mfma_f32_16x16x32_bf16 v[124:127], a[12:15], v[68:71], v[124:127]
v_mfma_f32_16x16x32_bf16 v[128:131], a[16:19], v[64:67], v[128:131]
v_mfma_f32_16x16x32_bf16 v[132:135], a[16:19], v[68:71], v[132:135]
v_mfma_f32_16x16x32_bf16 v[136:139], a[20:23], v[64:67], v[136:139]
v_mfma_f32_16x16x32_bf16 v[140:143], a[20:23], v[68:71], v[140:143]
v_mfma_f32_16x16x32_bf16 v[144:147], a[24:27], v[64:67], v[144:147]
v_mfma_f32_16x16x32_bf16 v[148:151], a[24:27], v[68:71], v[148:151]
v_mfma_f32_16x16x32_bf16 v[152:155], a[28:31], v[64:67], v[152:155]
v_mfma_f32_16x16x32_bf16 v[156:159], a[28:31], v[68:71], v[156:159]
v_mfma_f32_16x16x32_bf16 v[208:211], a[160:163], v[72:75], v[208:211]
v_mfma_f32_16x16x32_bf16 v[212:215], a[160:163], v[76:79], v[212:215]
v_mfma_f32_16x16x32_bf16 v[96:99], a[32:35], v[72:75], v[96:99]
v_mfma_f32_16x16x32_bf16 v[100:103], a[32:35], v[76:79], v[100:103]
v_mfma_f32_16x16x32_bf16 v[104:107], a[36:39], v[72:75], v[104:107]
v_mfma_f32_16x16x32_bf16 v[108:111], a[36:39], v[76:79], v[108:111]
v_mfma_f32_16x16x32_bf16 v[112:115], a[40:43], v[72:75], v[112:115]
v_mfma_f32_16x16x32_bf16 v[116:119], a[40:43], v[76:79], v[116:119]
v_mfma_f32_16x16x32_bf16 v[120:123], a[44:47], v[72:75], v[120:123]
v_mfma_f32_16x16x32_bf16 v[124:127], a[44:47], v[76:79], v[124:127]
v_mfma_f32_16x16x32_bf16 v[128:131], a[48:51], v[72:75], v[128:131]
v_mfma_f32_16x16x32_bf16 v[132:135], a[48:51], v[76:79], v[132:135]
v_mfma_f32_16x16x32_bf16 v[136:139], a[52:55], v[72:75], v[136:139]
v_mfma_f32_16x16x32_bf16 v[140:143], a[52:55], v[76:79], v[140:143]
v_mfma_f32_16x16x32_bf16 v[144:147], a[56:59], v[72:75], v[144:147]
v_mfma_f32_16x16x32_bf16 v[148:151], a[56:59], v[76:79], v[148:151]
v_mfma_f32_16x16x32_bf16 v[152:155], a[60:63], v[72:75], v[152:155]
v_mfma_f32_16x16x32_bf16 v[156:159], a[60:63], v[76:79], v[156:159]
v_mfma_f32_16x16x32_bf16 v[208:211], a[160:163], v[80:83], v[208:211]
)ASM"
R"ASM(v_mfma_f32_16x16x32_bf16 v[212:215], a[160:163], v[84:87], v[212:215]
v_mfma_f32_16x16x32_bf16 v[96:99], a[64:67], v[80:83], v[96:99]
v_mfma_f32_16x16x32_bf16 v[100:103], a[64:67], v[84:87], v[100:103]
v_mfma_f32_16x16x32_bf16 v[104:107], a[68:71], v[80:83], v[104:107]
v_mfma_f32_16x16x32_bf16 v[108:111], a[68:71], v[84:87], v[108:111]
v_mfma_f32_16x16x32_bf16 v[112:115], a[72:75], v[80:83], v[112:115]
v_mfma_f32_16x16x32_bf16 v[116:119], a[72:75], v[84:87], v[116:119]
v_mfma_f32_16x16x32_bf16 v[120:123], a[76:79], v[80:83], v[120:123]
v_mfma_f32_16x16x32_bf16 v[124:127], a[76:79], v[84:87], v[124:127]
v_mfma_f32_16x16x32_bf16 v[128:131], a[80:83], v[80:83], v[128:131]
v_mfma_f32_16x16x32_bf16 v[132:135], a[80:83], v[84:87], v[132:135]
v_mfma_f32_16x16x32_bf16 v[136:139], a[84:87], v[80:83], v[136:139]
v_mfma_f32_16x16x32_bf16 v[140:143], a[84:87], v[84:87], v[140:143]
v_mfma_f32_16x16x32_bf16 v[144:147], a[88:91], v[80:83], v[144:147]
v_mfma_f32_16x16x32_bf16 v[148:151], a[88:91], v[84:87], v[148:151]
v_mfma_f32_16x16x32_bf16 v[152:155], a[92:95], v[80:83], v[152:155]
v_mfma_f32_16x16x32_bf16 v[156:159], a[92:95], v[84:87], v[156:159]
v_mfma_f32_16x16x32_bf16 v[208:211], a[160:163], v[88:91], v[208:211]
v_mfma_f32_16x16x32_bf16 v[212:215], a[160:163], v[92:95], v[212:215]
v_mfma_f32_16x16x32_bf16 v[96:99], a[96:99], v[88:91], v[96:99]
v_mfma_f32_16x16x32_bf16 v[100:103], a[96:99], v[92:95], v[100:103]
v_mfma_f32_16x16x32_bf16 v[104:107], a[100:103], v[88:91], v[104:107]
v_mfma_f32_16x16x32_bf16 v[108:111], a[100:103], v[92:95], v[108:111]
v_mfma_f32_16x16x32_bf16 v[112:115], a[104:107], v[88:91], v[112:115]
v_mfma_f32_16x16x32_bf16 v[116:119], a[104:107], v[92:95], v[116:119]
v_mfma_f32_16x16x32_bf16 v[120:123], a[108:111], v[88:91], v[120:123]
v_mfma_f32_16x16x32_bf16 v[124:127], a[108:111], v[92:95], v[124:127]
v_mfma_f32_16x16x32_bf16 v[128:131], a[112:115], v[88:91], v[128:131]
v_mfma_f32_16x16x32_bf16 v[132:135], a[112:115], v[92:95], v[132:135]
v_mfma_f32_16x16x32_bf16 v[136:139], a[116:119], v[88:91], v[136:139]
v_mfma_f32_16x16x32_bf16 v[140:143], a[116:119], v[92:95], v[140:143]
v_mfma_f32_16x16x32_bf16 v[144:147], a[120:123], v[88:91], v[144:147]
v_mfma_f32_16x16x32_bf16 v[148:151], a[120:123], v[92:95], v[148:151]
v_mfma_f32_16x16x32_bf16 v[152:155], a[124:127], v[88:91], v[152:155]
v_mfma_f32_16x16x32_bf16 v[156:159], a[124:127], v[92:95], v[156:159]
s_nop 7
s_add_u32 s31, s31, 65536
s_add_u32 s30, s30, 1
s_cmp_lt_u32 s30, s49
s_cbranch_scc1 K2_SAFE_LOOP
s_nop 7
v_rcp_f32 v226, v208
v_rcp_f32 v228, v212
s_lshl_b32 s36, s55, 7
s_lshl_b32 s37, s26, 2
s_add_u32 s36, s36, s37
s_lshl_b32 s36, s36, 13
s_sub_u32 s35, 7, s7
s_lshl_b32 s35, s35, 10
s_add_u32 s35, s35, s36
s_lshl_b32 s37, s7, 10
s_add_u32 s36, s36, s37
v_pk_mul_f32 v[96:97], v[96:97], v[226:227] op_sel_hi:[1,0]
v_pk_mul_f32 v[98:99], v[98:99], v[226:227] op_sel_hi:[1,0]
v_pk_mul_f32 v[104:105], v[104:105], v[226:227] op_sel_hi:[1,0]
v_pk_mul_f32 v[106:107], v[106:107], v[226:227] op_sel_hi:[1,0]
v_cvt_pk_bf16_f32 v0, v96, v97
v_cvt_pk_bf16_f32 v1, v98, v99
v_cvt_pk_bf16_f32 v2, v104, v105
v_cvt_pk_bf16_f32 v3, v106, v107
s_add_u32 s37, s36, 0
buffer_store_dwordx4 v[0:3], v252, s[20:23], s37 offen sc1
v_pk_mul_f32 v[100:101], v[100:101], v[228:229] op_sel_hi:[1,0]
v_pk_mul_f32 v[102:103], v[102:103], v[228:229] op_sel_hi:[1,0]
v_pk_mul_f32 v[108:109], v[108:109], v[228:229] op_sel_hi:[1,0]
v_pk_mul_f32 v[110:111], v[110:111], v[228:229] op_sel_hi:[1,0]
v_cvt_pk_bf16_f32 v4, v100, v101
v_cvt_pk_bf16_f32 v5, v102, v103
v_cvt_pk_bf16_f32 v6, v108, v109
v_cvt_pk_bf16_f32 v7, v110, v111
s_add_u32 s37, s35, 0
buffer_store_dwordx4 v[4:7], v252, s[20:23], s37 offen sc1
v_pk_mul_f32 v[112:113], v[112:113], v[226:227] op_sel_hi:[1,0]
v_pk_mul_f32 v[114:115], v[114:115], v[226:227] op_sel_hi:[1,0]
v_pk_mul_f32 v[120:121], v[120:121], v[226:227] op_sel_hi:[1,0]
v_pk_mul_f32 v[122:123], v[122:123], v[226:227] op_sel_hi:[1,0]
v_cvt_pk_bf16_f32 v8, v112, v113
v_cvt_pk_bf16_f32 v9, v114, v115
v_cvt_pk_bf16_f32 v10, v120, v121
v_cvt_pk_bf16_f32 v11, v122, v123
s_add_u32 s37, s36, 8192
buffer_store_dwordx4 v[8:11], v252, s[20:23], s37 offen sc1
v_pk_mul_f32 v[116:117], v[116:117], v[228:229] op_sel_hi:[1,0]
v_pk_mul_f32 v[118:119], v[118:119], v[228:229] op_sel_hi:[1,0]
v_pk_mul_f32 v[124:125], v[124:125], v[228:229] op_sel_hi:[1,0]
v_pk_mul_f32 v[126:127], v[126:127], v[228:229] op_sel_hi:[1,0]
v_cvt_pk_bf16_f32 v12, v116, v117
v_cvt_pk_bf16_f32 v13, v118, v119
v_cvt_pk_bf16_f32 v14, v124, v125
v_cvt_pk_bf16_f32 v15, v126, v127
s_add_u32 s37, s35, 8192
buffer_store_dwordx4 v[12:15], v252, s[20:23], s37 offen sc1
v_pk_mul_f32 v[128:129], v[128:129], v[226:227] op_sel_hi:[1,0]
v_pk_mul_f32 v[130:131], v[130:131], v[226:227] op_sel_hi:[1,0]
v_pk_mul_f32 v[136:137], v[136:137], v[226:227] op_sel_hi:[1,0]
v_pk_mul_f32 v[138:139], v[138:139], v[226:227] op_sel_hi:[1,0]
v_cvt_pk_bf16_f32 v0, v128, v129
v_cvt_pk_bf16_f32 v1, v130, v131
v_cvt_pk_bf16_f32 v2, v136, v137
v_cvt_pk_bf16_f32 v3, v138, v139
s_add_u32 s37, s36, 16384
buffer_store_dwordx4 v[0:3], v252, s[20:23], s37 offen sc1
v_pk_mul_f32 v[132:133], v[132:133], v[228:229] op_sel_hi:[1,0]
v_pk_mul_f32 v[134:135], v[134:135], v[228:229] op_sel_hi:[1,0]
v_pk_mul_f32 v[140:141], v[140:141], v[228:229] op_sel_hi:[1,0]
v_pk_mul_f32 v[142:143], v[142:143], v[228:229] op_sel_hi:[1,0]
v_cvt_pk_bf16_f32 v4, v132, v133
v_cvt_pk_bf16_f32 v5, v134, v135
v_cvt_pk_bf16_f32 v6, v140, v141
v_cvt_pk_bf16_f32 v7, v142, v143
s_add_u32 s37, s35, 16384
buffer_store_dwordx4 v[4:7], v252, s[20:23], s37 offen sc1
v_pk_mul_f32 v[144:145], v[144:145], v[226:227] op_sel_hi:[1,0]
v_pk_mul_f32 v[146:147], v[146:147], v[226:227] op_sel_hi:[1,0]
v_pk_mul_f32 v[152:153], v[152:153], v[226:227] op_sel_hi:[1,0]
v_pk_mul_f32 v[154:155], v[154:155], v[226:227] op_sel_hi:[1,0]
v_cvt_pk_bf16_f32 v8, v144, v145
v_cvt_pk_bf16_f32 v9, v146, v147
v_cvt_pk_bf16_f32 v10, v152, v153
v_cvt_pk_bf16_f32 v11, v154, v155
s_add_u32 s37, s36, 24576
buffer_store_dwordx4 v[8:11], v252, s[20:23], s37 offen sc1
v_pk_mul_f32 v[148:149], v[148:149], v[228:229] op_sel_hi:[1,0]
v_pk_mul_f32 v[150:151], v[150:151], v[228:229] op_sel_hi:[1,0]
v_pk_mul_f32 v[156:157], v[156:157], v[228:229] op_sel_hi:[1,0]
v_pk_mul_f32 v[158:159], v[158:159], v[228:229] op_sel_hi:[1,0]
v_cvt_pk_bf16_f32 v12, v148, v149
v_cvt_pk_bf16_f32 v13, v150, v151
v_cvt_pk_bf16_f32 v14, v156, v157
v_cvt_pk_bf16_f32 v15, v158, v159
s_add_u32 s37, s35, 24576
buffer_store_dwordx4 v[12:15], v252, s[20:23], s37 offen sc1
s_branch K2_EPI
K2_END:
s_waitcnt vmcnt(0)
s_barrier
s_lshl_b32 s46, s27, 8
s_lshl_b32 s59, s26, 3
s_add_u32 s46, s46, s59
s_add_u32 s46, s46, 12288
s_cmp_eq_u32 s7, 0
s_cbranch_scc0 Lpub_25
v_mov_b32 v0, s84
v_mov_b32 v1, s85
v_mov_b32 v2, 0
s_mov_b64 exec, 1
buffer_store_dwordx2 v[0:1], v2, s[88:91], s46 offen sc1
s_nop 0
s_mov_b64 exec, -1
Lpub_25:
v_mbcnt_lo_u32_b32 v250, -1, 0
v_mbcnt_hi_u32_b32 v250, -1, v250
s_mov_b32 s52, s74
s_and_b32 s53, s75, 0xffff
s_mov_b32 s54, 0x400000
s_mov_b32 s55, 0x20000
s_and_b32 s58, s6, 3
s_lshl_b32 s56, s58, 20
s_bfe_u32 s58, s6, 0x20003
s_lshl_b32 s58, s58, 8
s_add_u32 s56, s56, s58
v_lshrrev_b32 v243, 3, v250
v_lshlrev_b32 v243, 5, v243
v_and_b32 v244, 1, v250
v_lshl_add_u32 v243, v244, 4, v243
v_bfe_u32 v244, v250, 1, 2
v_mul_u32_u24 v244, 1024, v244
v_add_u32 v246, v244, v243
v_xor_b32 v245, 32, v246
v_add_u32 v247, 3072, v245
v_add_u32 v249, 9216, v245
v_add_u32 v248, 6144, v246
s_mul_i32 s58, s7, 16384
s_add_u32 s56, s56, s58
s_lshl_b32 s57, s7, 12
s_cmp_lg_u32 s7, 0
s_cbranch_scc0 Low_24
s_add_u32 m0, s57, 0
s_nop 0
buffer_load_dwordx4 v246, s[52:55], s56 offen offset:0 lds
buffer_load_dwordx4 v247, s[52:55], s56 offen offset:1024 lds
buffer_load_dwordx4 v248, s[52:55], s56 offen offset:2048 lds
buffer_load_dwordx4 v249, s[52:55], s56 offen offset:3072 lds
s_add_u32 s56, s56, 65536
s_add_u32 m0, s57, 16384
s_nop 0
buffer_load_dwordx4 v246, s[52:55], s56 offen offset:0 lds
buffer_load_dwordx4 v247, s[52:55], s56 offen offset:1024 lds
buffer_load_dwordx4 v248, s[52:55], s56 offen offset:2048 lds
buffer_load_dwordx4 v249, s[52:55], s56 offen offset:3072 lds
s_add_u32 s56, s56, 65536
s_add_u32 m0, s57, 32768
s_nop 0
buffer_load_dwordx4 v246, s[52:55], s56 offen offset:0 lds
buffer_load_dwordx4 v247, s[52:55], s56 offen offset:1024 lds
buffer_load_dwordx4 v248, s[52:55], s56 offen offset:2048 lds
buffer_load_dwordx4 v249, s[52:55], s56 offen offset:3072 lds
s_add_u32 s56, s56, 65536
s_add_u32 m0, s57, 49152
s_nop 0
buffer_load_dwordx4 v246, s[52:55], s56 offen offset:0 lds
buffer_load_dwordx4 v247, s[52:55], s56 offen offset:1024 lds
buffer_load_dwordx4 v248, s[52:55], s56 offen offset:2048 lds
buffer_load_dwordx4 v249, s[52:55], s56 offen offset:3072 lds
s_add_u32 s56, s56, 65536
s_add_u32 m0, s57, 65536
s_nop 0
buffer_load_dwordx4 v246, s[52:55], s56 offen offset:0 lds
buffer_load_dwordx4 v247, s[52:55], s56 offen offset:1024 lds
buffer_load_dwordx4 v248, s[52:55], s56 offen offset:2048 lds
buffer_load_dwordx4 v249, s[52:55], s56 offen offset:3072 lds
s_add_u32 s56, s56, 65536
s_add_u32 m0, s57, 81920
s_nop 0
buffer_load_dwordx4 v246, s[52:55], s56 offen offset:0 lds
buffer_load_dwordx4 v247, s[52:55], s56 offen offset:1024 lds
buffer_load_dwordx4 v248, s[52:55], s56 offen offset:2048 lds
buffer_load_dwordx4 v249, s[52:55], s56 offen offset:3072 lds
s_add_u32 s56, s56, 65536
s_mul_i32 s58, s7, 16384
s_sub_u32 s58, s56, s58
s_sub_u32 s58, s58, 393216
s_add_u32 s59, s7, 1
s_mul_i32 s60, s59, 65536
s_add_u32 s58, s58, s60
s_lshl_b32 s59, s59, 14
s_mov_b32 m0, s59
s_nop 0
buffer_load_dwordx4 v246, s[52:55], s58 offen offset:0 lds
buffer_load_dwordx4 v247, s[52:55], s58 offen offset:1024 lds
buffer_load_dwordx4 v248, s[52:55], s58 offen offset:2048 lds
buffer_load_dwordx4 v249, s[52:55], s58 offen offset:3072 lds
s_add_u32 s58, s58, 196608
s_add_u32 s59, s59, 49152
s_cmp_le_u32 s7, 1
s_cbranch_scc0 Low_23
s_mov_b32 m0, s59
s_nop 0
buffer_load_dwordx4 v246, s[52:55], s58 offen offset:0 lds
buffer_load_dwordx4 v247, s[52:55], s58 offen offset:1024 lds
buffer_load_dwordx4 v248, s[52:55], s58 offen offset:2048 lds
buffer_load_dwordx4 v249, s[52:55], s58 offen offset:3072 lds
Low_23:
Low_24:
s_and_b32 s40, s6, 3
s_bfe_u32 s41, s6, 0x10002
s_lshl_b32 s41, s41, 3
s_lshr_b32 s42, s6, 5
s_add_u32 s41, s41, s42
s_bfe_u32 s42, s6, 0x20003
s_lshl_b32 s48, s42, 2
s_add_u32 s48, s48, s40
v_mbcnt_lo_u32_b32 v250, -1, 0
v_mbcnt_hi_u32_b32 v250, -1, v250
s_add_u32 s8, s82, 0x2800000
s_addc_u32 s9, s83, 0
s_and_b32 s9, s9, 0xffff
s_mov_b32 s10, 0x1000000
s_mov_b32 s11, 0x20000
s_add_u32 s16, s82, 0x3c00000
s_addc_u32 s17, s83, 0
s_and_b32 s17, s17, 0xffff
s_mov_b32 s18, 0x800000
s_mov_b32 s19, 0x20000
s_lshl_b32 s28, s41, 7
s_lshl_b32 s43, s40, 5
s_add_u32 s28, s28, s43
s_lshl_b32 s28, s28, 13
v_lshlrev_b32 v251, 4, v250
v_bfe_u32 v0, v250, 4, 1
v_lshlrev_b32 v1, 10, v0
v_lshrrev_b32 v2, 5, v250
v_lshl_add_u32 v1, v2, 8, v1
v_and_b32 v245, 15, v250
v_lshl_add_u32 v245, v245, 4, v1
v_xor_b32 v0, v0, v2
v_lshlrev_b32 v0, 7, v0
v_mul_u32_u24 v1, 1024, v2
v_add_u32 v1, v1, v0
v_bfe_u32 v2, v250, 2, 2
v_lshl_add_u32 v1, v2, 5, v1
v_and_b32 v2, 3, v250
v_lshl_add_u32 v244, v2, 3, v1
s_lshl_b32 s26, s7, 11
s_add_u32 s28, s28, s26
v_min_u32 v200, 7, v250
v_lshlrev_b32 v200, 3, v200
s_lshl_b32 s46, s41, 8
s_lshl_b32 s47, s40, 6
s_add_u32 s46, s46, s47
s_add_u32 s46, s46, 12288
s_cmp_eq_u32 s6, 0
s_cbranch_scc0 Lep_26
s_cmp_eq_u32 s7, 1
s_cbranch_scc0 Lep_26
v_lshlrev_b32 v100, 3, v250
s_mov_b32 s58, 4096
s_mov_b32 s86, 0
Lpoll_27:
buffer_load_dwordx2 v[102:103], v100, s[88:91], s58 offen sc1
s_waitcnt vmcnt(0)
v_cmp_eq_u64 vcc, s[84:85], v[102:103]
s_nop 1
s_cmp_eq_u64 vcc, exec
s_cbranch_scc1 Lpdone_28
s_sleep 1
s_add_u32 s86, s86, 1
s_cmp_lt_u32 s86, 0x40000
s_cbranch_scc1 Lpoll_27
Lpdone_28:
s_mov_b32 s58, 4608
s_mov_b32 s86, 0
Lpoll_29:
buffer_load_dwordx2 v[102:103], v100, s[88:91], s58 offen sc1
s_waitcnt vmcnt(0)
v_cmp_eq_u64 vcc, s[84:85], v[102:103]
s_nop 1
s_cmp_eq_u64 vcc, exec
s_cbranch_scc1 Lpdone_30
s_sleep 1
s_add_u32 s86, s86, 1
s_cmp_lt_u32 s86, 0x40000
s_cbranch_scc1 Lpoll_29
Lpdone_30:
s_mov_b32 s58, 5120
s_mov_b32 s86, 0
Lpoll_31:
buffer_load_dwordx2 v[102:103], v100, s[88:91], s58 offen sc1
s_waitcnt vmcnt(0)
v_cmp_eq_u64 vcc, s[84:85], v[102:103]
s_nop 1
s_cmp_eq_u64 vcc, exec
s_cbranch_scc1 Lpdone_32
s_sleep 1
s_add_u32 s86, s86, 1
s_cmp_lt_u32 s86, 0x40000
s_cbranch_scc1 Lpoll_31
Lpdone_32:
s_mov_b32 s58, 5632
s_mov_b32 s86, 0
Lpoll_33:
buffer_load_dwordx2 v[102:103], v100, s[88:91], s58 offen sc1
s_waitcnt vmcnt(0)
v_cmp_eq_u64 vcc, s[84:85], v[102:103]
s_nop 1
s_cmp_eq_u64 vcc, exec
s_cbranch_scc1 Lpdone_34
s_sleep 1
s_add_u32 s86, s86, 1
s_cmp_lt_u32 s86, 0x40000
s_cbranch_scc1 Lpoll_33
Lpdone_34:
v_mov_b32 v102, s84
v_mov_b32 v103, s85
v_mov_b32 v104, 0
s_mov_b64 exec, 1
buffer_store_dwordx2 v[102:103], v104, s[88:91], 0 offen sc1
s_nop 0
s_mov_b64 exec, -1
Lep_26:
s_cmp_eq_u32 s7, 0
s_cbranch_scc0 Lp0_35
s_mov_b32 s86, 0
Lpoll_36:
buffer_load_dwordx2 v[202:203], v200, s[88:91], s46 offen sc1
s_waitcnt vmcnt(0)
v_cmp_eq_u64 vcc, s[84:85], v[202:203]
s_nop 1
s_cmp_eq_u64 vcc, exec
s_cbranch_scc1 Lpdone_37
s_sleep 1
s_add_u32 s86, s86, 1
s_cmp_lt_u32 s86, 0x40000
s_cbranch_scc1 Lpoll_36
Lpdone_37:
Lp0_35:
s_barrier
s_cmp_eq_u32 s7, 0
s_cbranch_scc1 Lw0_38
buffer_load_dwordx4 a[128:131], v245, s[8:11], s28 offen offset:0 sc1
buffer_load_dwordx4 a[132:135], v245, s[8:11], s28 offen offset:512 sc1
s_add_u32 s27, s28, 8192
s_nop 0
buffer_load_dwordx4 a[136:139], v245, s[8:11], s27 offen offset:0 sc1
s_add_u32 s27, s28, 8192
s_nop 0
buffer_load_dwordx4 a[140:143], v245, s[8:11], s27 offen offset:512 sc1
s_add_u32 s28, s28, 16384
buffer_load_dwordx4 a[144:147], v245, s[8:11], s28 offen offset:0 sc1
buffer_load_dwordx4 a[148:151], v245, s[8:11], s28 offen offset:512 sc1
s_add_u32 s27, s28, 8192
s_nop 0
buffer_load_dwordx4 a[152:155], v245, s[8:11], s27 offen offset:0 sc1
s_add_u32 s27, s28, 8192
s_nop 0
buffer_load_dwordx4 a[156:159], v245, s[8:11], s27 offen offset:512 sc1
s_add_u32 s28, s28, 16384
buffer_load_dwordx4 a[160:163], v245, s[8:11], s28 offen offset:0 sc1
buffer_load_dwordx4 a[164:167], v245, s[8:11], s28 offen offset:512 sc1
s_add_u32 s27, s28, 8192
s_nop 0
buffer_load_dwordx4 a[168:171], v245, s[8:11], s27 offen offset:0 sc1
s_add_u32 s27, s28, 8192
s_nop 0
buffer_load_dwordx4 a[172:175], v245, s[8:11], s27 offen offset:512 sc1
s_add_u32 s28, s28, 16384
buffer_load_dwordx4 a[176:179], v245, s[8:11], s28 offen offset:0 sc1
buffer_load_dwordx4 a[180:183], v245, s[8:11], s28 offen offset:512 sc1
s_add_u32 s27, s28, 8192
s_nop 0
buffer_load_dwordx4 a[184:187], v245, s[8:11], s27 offen offset:0 sc1
s_add_u32 s27, s28, 8192
s_nop 0
buffer_load_dwordx4 a[188:191], v245, s[8:11], s27 offen offset:512 sc1
s_add_u32 s28, s28, 16384
s_branch Lw0_39
Lw0_38:
s_add_u32 m0, s57, 0
s_nop 0
buffer_load_dwordx4 v246, s[52:55], s56 offen offset:0 lds
buffer_load_dwordx4 v247, s[52:55], s56 offen offset:1024 lds
buffer_load_dwordx4 v248, s[52:55], s56 offen offset:2048 lds
buffer_load_dwordx4 v249, s[52:55], s56 offen offset:3072 lds
s_add_u32 s56, s56, 65536
s_add_u32 m0, s57, 16384
s_nop 0
buffer_load_dwordx4 v246, s[52:55], s56 offen offset:0 lds
buffer_load_dwordx4 v247, s[52:55], s56 offen offset:1024 lds
buffer_load_dwordx4 v248, s[52:55], s56 offen offset:2048 lds
buffer_load_dwordx4 v249, s[52:55], s56 offen offset:3072 lds
s_add_u32 s56, s56, 65536
s_add_u32 s56, s56, 262144
buffer_load_dwordx4 a[128:131], v245, s[8:11], s28 offen offset:0 sc1
buffer_load_dwordx4 a[132:135], v245, s[8:11], s28 offen offset:512 sc1
s_add_u32 s27, s28, 8192
s_nop 0
buffer_load_dwordx4 a[136:139], v245, s[8:11], s27 offen offset:0 sc1
s_add_u32 s27, s28, 8192
s_nop 0
buffer_load_dwordx4 a[140:143], v245, s[8:11], s27 offen offset:512 sc1
s_add_u32 s28, s28, 16384
buffer_load_dwordx4 a[144:147], v245, s[8:11], s28 offen offset:0 sc1
buffer_load_dwordx4 a[148:151], v245, s[8:11], s28 offen offset:512 sc1
s_add_u32 s27, s28, 8192
s_nop 0
buffer_load_dwordx4 a[152:155], v245, s[8:11], s27 offen offset:0 sc1
s_add_u32 s27, s28, 8192
s_nop 0
buffer_load_dwordx4 a[156:159], v245, s[8:11], s27 offen offset:512 sc1
s_add_u32 s28, s28, 16384
buffer_load_dwordx4 a[160:163], v245, s[8:11], s28 offen offset:0 sc1
buffer_load_dwordx4 a[164:167], v245, s[8:11], s28 offen offset:512 sc1
s_add_u32 s27, s28, 8192
s_nop 0
buffer_load_dwordx4 a[168:171], v245, s[8:11], s27 offen offset:0 sc1
s_add_u32 s27, s28, 8192
s_nop 0
buffer_load_dwordx4 a[172:175], v245, s[8:11], s27 offen offset:512 sc1
s_add_u32 s28, s28, 16384
buffer_load_dwordx4 a[176:179], v245, s[8:11], s28 offen offset:0 sc1
buffer_load_dwordx4 a[180:183], v245, s[8:11], s28 offen offset:512 sc1
s_add_u32 s27, s28, 8192
s_nop 0
buffer_load_dwordx4 a[184:187], v245, s[8:11], s27 offen offset:0 sc1
s_add_u32 s27, s28, 8192
s_nop 0
buffer_load_dwordx4 a[188:191], v245, s[8:11], s27 offen offset:512 sc1
s_add_u32 s28, s28, 16384
Lw0_39:
s_waitcnt vmcnt(12)
s_barrier
ds_read_b64_tr_b16 a[0:1], v244 offset:0
ds_read_b64_tr_b16 a[2:3], v244 offset:4096
ds_read_b64_tr_b16 a[4:5], v244 offset:256
ds_read_b64_tr_b16 a[6:7], v244 offset:4352
ds_read_b64_tr_b16 a[8:9], v244 offset:512
ds_read_b64_tr_b16 a[10:11], v244 offset:4608
ds_read_b64_tr_b16 a[12:13], v244 offset:768
ds_read_b64_tr_b16 a[14:15], v244 offset:4864
ds_read_b64_tr_b16 a[16:17], v244 offset:2048
ds_read_b64_tr_b16 a[18:19], v244 offset:6144
ds_read_b64_tr_b16 a[20:21], v244 offset:2304
ds_read_b64_tr_b16 a[22:23], v244 offset:6400
ds_read_b64_tr_b16 a[24:25], v244 offset:2560
ds_read_b64_tr_b16 a[26:27], v244 offset:6656
ds_read_b64_tr_b16 a[28:29], v244 offset:2816
ds_read_b64_tr_b16 a[30:31], v244 offset:6912
ds_read_b64_tr_b16 a[32:33], v244 offset:8192
ds_read_b64_tr_b16 a[34:35], v244 offset:12288
ds_read_b64_tr_b16 a[36:37], v244 offset:8448
ds_read_b64_tr_b16 a[38:39], v244 offset:12544
ds_read_b64_tr_b16 a[40:41], v244 offset:8704
ds_read_b64_tr_b16 a[42:43], v244 offset:12800
ds_read_b64_tr_b16 a[44:45], v244 offset:8960
ds_read_b64_tr_b16 a[46:47], v244 offset:13056
ds_read_b64_tr_b16 a[48:49], v244 offset:10240
ds_read_b64_tr_b16 a[50:51], v244 offset:14336
ds_read_b64_tr_b16 a[52:53], v244 offset:10496
ds_read_b64_tr_b16 a[54:55], v244 offset:14592
ds_read_b64_tr_b16 a[56:57], v244 offset:10752
ds_read_b64_tr_b16 a[58:59], v244 offset:14848
ds_read_b64_tr_b16 a[60:61], v244 offset:11008
ds_read_b64_tr_b16 a[62:63], v244 offset:15104
v_add_u32 v244, 0x4000, v244
s_waitcnt vmcnt(12) lgkmcnt(0)
s_barrier
v_mfma_f32_32x32x16_bf16 v[0:15], a[0:3], a[128:131], 0
ds_read_b64_tr_b16 a[64:65], v244 offset:0
ds_read_b64_tr_b16 a[66:67], v244 offset:4096
ds_read_b64_tr_b16 a[68:69], v244 offset:256
buffer_load_dwordx4 a[192:195], v245, s[8:11], s28 offen offset:0 sc1
v_mfma_f32_32x32x16_bf16 v[16:31], a[4:7], a[128:131], 0
ds_read_b64_tr_b16 a[70:71], v244 offset:4352
ds_read_b64_tr_b16 a[72:73], v244 offset:512
ds_read_b64_tr_b16 a[74:75], v244 offset:4608
v_mfma_f32_32x32x16_bf16 v[32:47], a[8:11], a[128:131], 0
ds_read_b64_tr_b16 a[76:77], v244 offset:768
ds_read_b64_tr_b16 a[78:79], v244 offset:4864
v_mfma_f32_32x32x16_bf16 v[48:63], a[12:15], a[128:131], 0
ds_read_b64_tr_b16 a[80:81], v244 offset:2048
ds_read_b64_tr_b16 a[82:83], v244 offset:6144
ds_read_b64_tr_b16 a[84:85], v244 offset:2304
v_mfma_f32_32x32x16_bf16 v[0:15], a[16:19], a[132:135], v[0:15]
ds_read_b64_tr_b16 a[86:87], v244 offset:6400
ds_read_b64_tr_b16 a[88:89], v244 offset:2560
ds_read_b64_tr_b16 a[90:91], v244 offset:6656
buffer_load_dwordx4 a[196:199], v245, s[8:11], s28 offen offset:512 sc1
v_mfma_f32_32x32x16_bf16 v[16:31], a[20:23], a[132:135], v[16:31]
ds_read_b64_tr_b16 a[92:93], v244 offset:2816
ds_read_b64_tr_b16 a[94:95], v244 offset:6912
v_mfma_f32_32x32x16_bf16 v[32:47], a[24:27], a[132:135], v[32:47]
ds_read_b64_tr_b16 a[96:97], v244 offset:8192
ds_read_b64_tr_b16 a[98:99], v244 offset:12288
ds_read_b64_tr_b16 a[100:101], v244 offset:8448
v_mfma_f32_32x32x16_bf16 v[48:63], a[28:31], a[132:135], v[48:63]
ds_read_b64_tr_b16 a[102:103], v244 offset:12544
ds_read_b64_tr_b16 a[104:105], v244 offset:8704
ds_read_b64_tr_b16 a[106:107], v244 offset:12800
v_mfma_f32_32x32x16_bf16 v[0:15], a[32:35], a[136:139], v[0:15]
ds_read_b64_tr_b16 a[108:109], v244 offset:8960
ds_read_b64_tr_b16 a[110:111], v244 offset:13056
s_add_u32 s27, s28, 8192
buffer_load_dwordx4 a[200:203], v245, s[8:11], s27 offen offset:0 sc1
v_mfma_f32_32x32x16_bf16 v[16:31], a[36:39], a[136:139], v[16:31]
ds_read_b64_tr_b16 a[112:113], v244 offset:10240
ds_read_b64_tr_b16 a[114:115], v244 offset:14336
ds_read_b64_tr_b16 a[116:117], v244 offset:10496
v_mfma_f32_32x32x16_bf16 v[32:47], a[40:43], a[136:139], v[32:47]
ds_read_b64_tr_b16 a[118:119], v244 offset:14592
ds_read_b64_tr_b16 a[120:121], v244 offset:10752
ds_read_b64_tr_b16 a[122:123], v244 offset:14848
v_mfma_f32_32x32x16_bf16 v[48:63], a[44:47], a[136:139], v[48:63]
ds_read_b64_tr_b16 a[124:125], v244 offset:11008
ds_read_b64_tr_b16 a[126:127], v244 offset:15104
v_mfma_f32_32x32x16_bf16 v[0:15], a[48:51], a[140:143], v[0:15]
s_add_u32 s27, s28, 8192
buffer_load_dwordx4 a[204:207], v245, s[8:11], s27 offen offset:512 sc1
v_mfma_f32_32x32x16_bf16 v[16:31], a[52:55], a[140:143], v[16:31]
v_mfma_f32_32x32x16_bf16 v[32:47], a[56:59], a[140:143], v[32:47]
v_mfma_f32_32x32x16_bf16 v[48:63], a[60:63], a[140:143], v[48:63]
s_add_u32 s28, s28, 16384
v_add_u32 v244, 0x4000, v244
s_waitcnt vmcnt(12) lgkmcnt(0)
v_mfma_f32_32x32x16_bf16 v[0:15], a[64:67], a[144:147], v[0:15]
ds_read_b64_tr_b16 a[0:1], v244 offset:0
ds_read_b64_tr_b16 a[2:3], v244 offset:4096
ds_read_b64_tr_b16 a[4:5], v244 offset:256
buffer_load_dwordx4 a[128:131], v245, s[8:11], s28 offen offset:0 sc1
v_mfma_f32_32x32x16_bf16 v[16:31], a[68:71], a[144:147], v[16:31]
ds_read_b64_tr_b16 a[6:7], v244 offset:4352
ds_read_b64_tr_b16 a[8:9], v244 offset:512
ds_read_b64_tr_b16 a[10:11], v244 offset:4608
v_mfma_f32_32x32x16_bf16 v[32:47], a[72:75], a[144:147], v[32:47]
ds_read_b64_tr_b16 a[12:13], v244 offset:768
ds_read_b64_tr_b16 a[14:15], v244 offset:4864
buffer_load_dwordx4 a[132:135], v245, s[8:11], s28 offen offset:512 sc1
v_mfma_f32_32x32x16_bf16 v[48:63], a[76:79], a[144:147], v[48:63]
ds_read_b64_tr_b16 a[16:17], v244 offset:2048
ds_read_b64_tr_b16 a[18:19], v244 offset:6144
ds_read_b64_tr_b16 a[20:21], v244 offset:2304
v_mfma_f32_32x32x16_bf16 v[0:15], a[80:83], a[148:151], v[0:15]
ds_read_b64_tr_b16 a[22:23], v244 offset:6400
ds_read_b64_tr_b16 a[24:25], v244 offset:2560
ds_read_b64_tr_b16 a[26:27], v244 offset:6656
s_add_u32 s27, s28, 8192
buffer_load_dwordx4 a[136:139], v245, s[8:11], s27 offen offset:0 sc1
v_mfma_f32_32x32x16_bf16 v[16:31], a[84:87], a[148:151], v[16:31]
ds_read_b64_tr_b16 a[28:29], v244 offset:2816
ds_read_b64_tr_b16 a[30:31], v244 offset:6912
v_mfma_f32_32x32x16_bf16 v[32:47], a[88:91], a[148:151], v[32:47]
ds_read_b64_tr_b16 a[32:33], v244 offset:8192
ds_read_b64_tr_b16 a[34:35], v244 offset:12288
ds_read_b64_tr_b16 a[36:37], v244 offset:8448
s_add_u32 s27, s28, 8192
buffer_load_dwordx4 a[140:143], v245, s[8:11], s27 offen offset:512 sc1
v_mfma_f32_32x32x16_bf16 v[48:63], a[92:95], a[148:151], v[48:63]
ds_read_b64_tr_b16 a[38:39], v244 offset:12544
ds_read_b64_tr_b16 a[40:41], v244 offset:8704
ds_read_b64_tr_b16 a[42:43], v244 offset:12800
v_mfma_f32_32x32x16_bf16 v[0:15], a[96:99], a[152:155], v[0:15]
ds_read_b64_tr_b16 a[44:45], v244 offset:8960
ds_read_b64_tr_b16 a[46:47], v244 offset:13056
s_add_u32 m0, s57, 0
s_nop 0
buffer_load_dwordx4 v246, s[52:55], s56 offen offset:0 lds
v_mfma_f32_32x32x16_bf16 v[16:31], a[100:103], a[152:155], v[16:31]
ds_read_b64_tr_b16 a[48:49], v244 offset:10240
ds_read_b64_tr_b16 a[50:51], v244 offset:14336
ds_read_b64_tr_b16 a[52:53], v244 offset:10496
v_mfma_f32_32x32x16_bf16 v[32:47], a[104:107], a[152:155], v[32:47]
ds_read_b64_tr_b16 a[54:55], v244 offset:14592
ds_read_b64_tr_b16 a[56:57], v244 offset:10752
ds_read_b64_tr_b16 a[58:59], v244 offset:14848
buffer_load_dwordx4 v247, s[52:55], s56 offen offset:1024 lds
v_mfma_f32_32x32x16_bf16 v[48:63], a[108:111], a[152:155], v[48:63]
ds_read_b64_tr_b16 a[60:61], v244 offset:11008
ds_read_b64_tr_b16 a[62:63], v244 offset:15104
v_mfma_f32_32x32x16_bf16 v[0:15], a[112:115], a[156:159], v[0:15]
buffer_load_dwordx4 v248, s[52:55], s56 offen offset:2048 lds
v_mfma_f32_32x32x16_bf16 v[16:31], a[116:119], a[156:159], v[16:31]
v_mfma_f32_32x32x16_bf16 v[32:47], a[120:123], a[156:159], v[32:47]
buffer_load_dwordx4 v249, s[52:55], s56 offen offset:3072 lds
v_mfma_f32_32x32x16_bf16 v[48:63], a[124:127], a[156:159], v[48:63]
s_add_u32 s28, s28, 16384
s_add_u32 s56, s56, 65536
v_add_u32 v244, 0x4000, v244
s_waitcnt vmcnt(16) lgkmcnt(0)
s_barrier
v_mfma_f32_32x32x16_bf16 v[0:15], a[0:3], a[160:163], v[0:15]
ds_read_b64_tr_b16 a[64:65], v244 offset:0
ds_read_b64_tr_b16 a[66:67], v244 offset:4096
ds_read_b64_tr_b16 a[68:69], v244 offset:256
buffer_load_dwordx4 a[144:147], v245, s[8:11], s28 offen offset:0 sc1
v_mfma_f32_32x32x16_bf16 v[16:31], a[4:7], a[160:163], v[16:31]
ds_read_b64_tr_b16 a[70:71], v244 offset:4352
ds_read_b64_tr_b16 a[72:73], v244 offset:512
ds_read_b64_tr_b16 a[74:75], v244 offset:4608
v_mfma_f32_32x32x16_bf16 v[32:47], a[8:11], a[160:163], v[32:47]
ds_read_b64_tr_b16 a[76:77], v244 offset:768
ds_read_b64_tr_b16 a[78:79], v244 offset:4864
buffer_load_dwordx4 a[148:151], v245, s[8:11], s28 offen offset:512 sc1
v_mfma_f32_32x32x16_bf16 v[48:63], a[12:15], a[160:163], v[48:63]
ds_read_b64_tr_b16 a[80:81], v244 offset:2048
ds_read_b64_tr_b16 a[82:83], v244 offset:6144
ds_read_b64_tr_b16 a[84:85], v244 offset:2304
v_mfma_f32_32x32x16_bf16 v[0:15], a[16:19], a[164:167], v[0:15]
ds_read_b64_tr_b16 a[86:87], v244 offset:6400
ds_read_b64_tr_b16 a[88:89], v244 offset:2560
ds_read_b64_tr_b16 a[90:91], v244 offset:6656
s_add_u32 s27, s28, 8192
buffer_load_dwordx4 a[152:155], v245, s[8:11], s27 offen offset:0 sc1
v_mfma_f32_32x32x16_bf16 v[16:31], a[20:23], a[164:167], v[16:31]
ds_read_b64_tr_b16 a[92:93], v244 offset:2816
ds_read_b64_tr_b16 a[94:95], v244 offset:6912
v_mfma_f32_32x32x16_bf16 v[32:47], a[24:27], a[164:167], v[32:47]
ds_read_b64_tr_b16 a[96:97], v244 offset:8192
ds_read_b64_tr_b16 a[98:99], v244 offset:12288
ds_read_b64_tr_b16 a[100:101], v244 offset:8448
s_add_u32 s27, s28, 8192
buffer_load_dwordx4 a[156:159], v245, s[8:11], s27 offen offset:512 sc1
v_mfma_f32_32x32x16_bf16 v[48:63], a[28:31], a[164:167], v[48:63]
ds_read_b64_tr_b16 a[102:103], v244 offset:12544
ds_read_b64_tr_b16 a[104:105], v244 offset:8704
ds_read_b64_tr_b16 a[106:107], v244 offset:12800
v_mfma_f32_32x32x16_bf16 v[0:15], a[32:35], a[168:171], v[0:15]
ds_read_b64_tr_b16 a[108:109], v244 offset:8960
ds_read_b64_tr_b16 a[110:111], v244 offset:13056
s_add_u32 m0, s57, 16384
s_nop 0
buffer_load_dwordx4 v246, s[52:55], s56 offen offset:0 lds
v_mfma_f32_32x32x16_bf16 v[16:31], a[36:39], a[168:171], v[16:31]
ds_read_b64_tr_b16 a[112:113], v244 offset:10240
ds_read_b64_tr_b16 a[114:115], v244 offset:14336
ds_read_b64_tr_b16 a[116:117], v244 offset:10496
v_mfma_f32_32x32x16_bf16 v[32:47], a[40:43], a[168:171], v[32:47]
ds_read_b64_tr_b16 a[118:119], v244 offset:14592
ds_read_b64_tr_b16 a[120:121], v244 offset:10752
ds_read_b64_tr_b16 a[122:123], v244 offset:14848
buffer_load_dwordx4 v247, s[52:55], s56 offen offset:1024 lds
v_mfma_f32_32x32x16_bf16 v[48:63], a[44:47], a[168:171], v[48:63]
ds_read_b64_tr_b16 a[124:125], v244 offset:11008
ds_read_b64_tr_b16 a[126:127], v244 offset:15104
v_mfma_f32_32x32x16_bf16 v[0:15], a[48:51], a[172:175], v[0:15]
buffer_load_dwordx4 v248, s[52:55], s56 offen offset:2048 lds
v_mfma_f32_32x32x16_bf16 v[16:31], a[52:55], a[172:175], v[16:31]
v_mfma_f32_32x32x16_bf16 v[32:47], a[56:59], a[172:175], v[32:47]
buffer_load_dwordx4 v249, s[52:55], s56 offen offset:3072 lds
v_mfma_f32_32x32x16_bf16 v[48:63], a[60:63], a[172:175], v[48:63]
s_add_u32 s28, s28, 16384
s_add_u32 s56, s56, 65536
v_add_u32 v244, 0x4000, v244
s_waitcnt vmcnt(20) lgkmcnt(0)
v_mfma_f32_32x32x16_bf16 v[0:15], a[64:67], a[176:179], v[0:15]
ds_read_b64_tr_b16 a[0:1], v244 offset:0
ds_read_b64_tr_b16 a[2:3], v244 offset:4096
ds_read_b64_tr_b16 a[4:5], v244 offset:256
buffer_load_dwordx4 a[160:163], v245, s[8:11], s28 offen offset:0 sc1
v_mfma_f32_32x32x16_bf16 v[16:31], a[68:71], a[176:179], v[16:31]
ds_read_b64_tr_b16 a[6:7], v244 offset:4352
ds_read_b64_tr_b16 a[8:9], v244 offset:512
ds_read_b64_tr_b16 a[10:11], v244 offset:4608
v_mfma_f32_32x32x16_bf16 v[32:47], a[72:75], a[176:179], v[32:47]
ds_read_b64_tr_b16 a[12:13], v244 offset:768
ds_read_b64_tr_b16 a[14:15], v244 offset:4864
buffer_load_dwordx4 a[164:167], v245, s[8:11], s28 offen offset:512 sc1
v_mfma_f32_32x32x16_bf16 v[48:63], a[76:79], a[176:179], v[48:63]
ds_read_b64_tr_b16 a[16:17], v244 offset:2048
ds_read_b64_tr_b16 a[18:19], v244 offset:6144
ds_read_b64_tr_b16 a[20:21], v244 offset:2304
v_mfma_f32_32x32x16_bf16 v[0:15], a[80:83], a[180:183], v[0:15]
ds_read_b64_tr_b16 a[22:23], v244 offset:6400
ds_read_b64_tr_b16 a[24:25], v244 offset:2560
ds_read_b64_tr_b16 a[26:27], v244 offset:6656
s_add_u32 s27, s28, 8192
buffer_load_dwordx4 a[168:171], v245, s[8:11], s27 offen offset:0 sc1
v_mfma_f32_32x32x16_bf16 v[16:31], a[84:87], a[180:183], v[16:31]
ds_read_b64_tr_b16 a[28:29], v244 offset:2816
ds_read_b64_tr_b16 a[30:31], v244 offset:6912
v_mfma_f32_32x32x16_bf16 v[32:47], a[88:91], a[180:183], v[32:47]
ds_read_b64_tr_b16 a[32:33], v244 offset:8192
ds_read_b64_tr_b16 a[34:35], v244 offset:12288
ds_read_b64_tr_b16 a[36:37], v244 offset:8448
s_add_u32 s27, s28, 8192
buffer_load_dwordx4 a[172:175], v245, s[8:11], s27 offen offset:512 sc1
v_mfma_f32_32x32x16_bf16 v[48:63], a[92:95], a[180:183], v[48:63]
ds_read_b64_tr_b16 a[38:39], v244 offset:12544
ds_read_b64_tr_b16 a[40:41], v244 offset:8704
ds_read_b64_tr_b16 a[42:43], v244 offset:12800
v_mfma_f32_32x32x16_bf16 v[0:15], a[96:99], a[184:187], v[0:15]
ds_read_b64_tr_b16 a[44:45], v244 offset:8960
ds_read_b64_tr_b16 a[46:47], v244 offset:13056
s_add_u32 m0, s57, 32768
s_nop 0
buffer_load_dwordx4 v246, s[52:55], s56 offen offset:0 lds
v_mfma_f32_32x32x16_bf16 v[16:31], a[100:103], a[184:187], v[16:31]
ds_read_b64_tr_b16 a[48:49], v244 offset:10240
ds_read_b64_tr_b16 a[50:51], v244 offset:14336
ds_read_b64_tr_b16 a[52:53], v244 offset:10496
v_mfma_f32_32x32x16_bf16 v[32:47], a[104:107], a[184:187], v[32:47]
ds_read_b64_tr_b16 a[54:55], v244 offset:14592
ds_read_b64_tr_b16 a[56:57], v244 offset:10752
ds_read_b64_tr_b16 a[58:59], v244 offset:14848
buffer_load_dwordx4 v247, s[52:55], s56 offen offset:1024 lds
v_mfma_f32_32x32x16_bf16 v[48:63], a[108:111], a[184:187], v[48:63]
ds_read_b64_tr_b16 a[60:61], v244 offset:11008
ds_read_b64_tr_b16 a[62:63], v244 offset:15104
v_mfma_f32_32x32x16_bf16 v[0:15], a[112:115], a[188:191], v[0:15]
buffer_load_dwordx4 v248, s[52:55], s56 offen offset:2048 lds
v_mfma_f32_32x32x16_bf16 v[16:31], a[116:119], a[188:191], v[16:31]
v_mfma_f32_32x32x16_bf16 v[32:47], a[120:123], a[188:191], v[32:47]
buffer_load_dwordx4 v249, s[52:55], s56 offen offset:3072 lds
v_mfma_f32_32x32x16_bf16 v[48:63], a[124:127], a[188:191], v[48:63]
s_add_u32 s28, s28, 16384
s_add_u32 s56, s56, 65536
v_add_u32 v244, 0x4000, v244
s_waitcnt vmcnt(16) lgkmcnt(0)
s_barrier
v_mfma_f32_32x32x16_bf16 v[0:15], a[0:3], a[192:195], v[0:15]
ds_read_b64_tr_b16 a[64:65], v244 offset:0
ds_read_b64_tr_b16 a[66:67], v244 offset:4096
ds_read_b64_tr_b16 a[68:69], v244 offset:256
buffer_load_dwordx4 a[176:179], v245, s[8:11], s28 offen offset:0 sc1
v_mfma_f32_32x32x16_bf16 v[16:31], a[4:7], a[192:195], v[16:31]
ds_read_b64_tr_b16 a[70:71], v244 offset:4352
ds_read_b64_tr_b16 a[72:73], v244 offset:512
ds_read_b64_tr_b16 a[74:75], v244 offset:4608
v_mfma_f32_32x32x16_bf16 v[32:47], a[8:11], a[192:195], v[32:47]
ds_read_b64_tr_b16 a[76:77], v244 offset:768
ds_read_b64_tr_b16 a[78:79], v244 offset:4864
buffer_load_dwordx4 a[180:183], v245, s[8:11], s28 offen offset:512 sc1
v_mfma_f32_32x32x16_bf16 v[48:63], a[12:15], a[192:195], v[48:63]
ds_read_b64_tr_b16 a[80:81], v244 offset:2048
ds_read_b64_tr_b16 a[82:83], v244 offset:6144
ds_read_b64_tr_b16 a[84:85], v244 offset:2304
v_mfma_f32_32x32x16_bf16 v[0:15], a[16:19], a[196:199], v[0:15]
ds_read_b64_tr_b16 a[86:87], v244 offset:6400
ds_read_b64_tr_b16 a[88:89], v244 offset:2560
ds_read_b64_tr_b16 a[90:91], v244 offset:6656
s_add_u32 s27, s28, 8192
buffer_load_dwordx4 a[184:187], v245, s[8:11], s27 offen offset:0 sc1
v_mfma_f32_32x32x16_bf16 v[16:31], a[20:23], a[196:199], v[16:31]
ds_read_b64_tr_b16 a[92:93], v244 offset:2816
ds_read_b64_tr_b16 a[94:95], v244 offset:6912
v_mfma_f32_32x32x16_bf16 v[32:47], a[24:27], a[196:199], v[32:47]
ds_read_b64_tr_b16 a[96:97], v244 offset:8192
ds_read_b64_tr_b16 a[98:99], v244 offset:12288
ds_read_b64_tr_b16 a[100:101], v244 offset:8448
s_add_u32 s27, s28, 8192
buffer_load_dwordx4 a[188:191], v245, s[8:11], s27 offen offset:512 sc1
v_mfma_f32_32x32x16_bf16 v[48:63], a[28:31], a[196:199], v[48:63]
ds_read_b64_tr_b16 a[102:103], v244 offset:12544
ds_read_b64_tr_b16 a[104:105], v244 offset:8704
ds_read_b64_tr_b16 a[106:107], v244 offset:12800
v_mfma_f32_32x32x16_bf16 v[0:15], a[32:35], a[200:203], v[0:15]
ds_read_b64_tr_b16 a[108:109], v244 offset:8960
ds_read_b64_tr_b16 a[110:111], v244 offset:13056
s_add_u32 m0, s57, 49152
s_nop 0
buffer_load_dwordx4 v246, s[52:55], s56 offen offset:0 lds
v_mfma_f32_32x32x16_bf16 v[16:31], a[36:39], a[200:203], v[16:31]
ds_read_b64_tr_b16 a[112:113], v244 offset:10240
ds_read_b64_tr_b16 a[114:115], v244 offset:14336
ds_read_b64_tr_b16 a[116:117], v244 offset:10496
v_mfma_f32_32x32x16_bf16 v[32:47], a[40:43], a[200:203], v[32:47]
ds_read_b64_tr_b16 a[118:119], v244 offset:14592
ds_read_b64_tr_b16 a[120:121], v244 offset:10752
ds_read_b64_tr_b16 a[122:123], v244 offset:14848
buffer_load_dwordx4 v247, s[52:55], s56 offen offset:1024 lds
v_mfma_f32_32x32x16_bf16 v[48:63], a[44:47], a[200:203], v[48:63]
ds_read_b64_tr_b16 a[124:125], v244 offset:11008
ds_read_b64_tr_b16 a[126:127], v244 offset:15104
v_mfma_f32_32x32x16_bf16 v[0:15], a[48:51], a[204:207], v[0:15]
buffer_load_dwordx4 v248, s[52:55], s56 offen offset:2048 lds
v_mfma_f32_32x32x16_bf16 v[16:31], a[52:55], a[204:207], v[16:31]
v_mfma_f32_32x32x16_bf16 v[32:47], a[56:59], a[204:207], v[32:47]
buffer_load_dwordx4 v249, s[52:55], s56 offen offset:3072 lds
v_mfma_f32_32x32x16_bf16 v[48:63], a[60:63], a[204:207], v[48:63]
s_add_u32 s28, s28, 16384
s_add_u32 s56, s56, 65536
v_add_u32 v244, 0xfffec000, v244
s_waitcnt vmcnt(28) lgkmcnt(0)
v_mfma_f32_32x32x16_bf16 v[0:15], a[64:67], a[128:131], v[0:15]
ds_read_b64_tr_b16 a[0:1], v244 offset:0
ds_read_b64_tr_b16 a[2:3], v244 offset:4096
ds_read_b64_tr_b16 a[4:5], v244 offset:256
buffer_load_dwordx4 a[192:195], v245, s[8:11], s28 offen offset:0 sc1
v_mfma_f32_32x32x16_bf16 v[16:31], a[68:71], a[128:131], v[16:31]
ds_read_b64_tr_b16 a[6:7], v244 offset:4352
ds_read_b64_tr_b16 a[8:9], v244 offset:512
ds_read_b64_tr_b16 a[10:11], v244 offset:4608
v_mfma_f32_32x32x16_bf16 v[32:47], a[72:75], a[128:131], v[32:47]
ds_read_b64_tr_b16 a[12:13], v244 offset:768
ds_read_b64_tr_b16 a[14:15], v244 offset:4864
buffer_load_dwordx4 a[196:199], v245, s[8:11], s28 offen offset:512 sc1
v_mfma_f32_32x32x16_bf16 v[48:63], a[76:79], a[128:131], v[48:63]
ds_read_b64_tr_b16 a[16:17], v244 offset:2048
ds_read_b64_tr_b16 a[18:19], v244 offset:6144
ds_read_b64_tr_b16 a[20:21], v244 offset:2304
v_mfma_f32_32x32x16_bf16 v[0:15], a[80:83], a[132:135], v[0:15]
ds_read_b64_tr_b16 a[22:23], v244 offset:6400
ds_read_b64_tr_b16 a[24:25], v244 offset:2560
ds_read_b64_tr_b16 a[26:27], v244 offset:6656
s_add_u32 s27, s28, 8192
buffer_load_dwordx4 a[200:203], v245, s[8:11], s27 offen offset:0 sc1
v_mfma_f32_32x32x16_bf16 v[16:31], a[84:87], a[132:135], v[16:31]
ds_read_b64_tr_b16 a[28:29], v244 offset:2816
ds_read_b64_tr_b16 a[30:31], v244 offset:6912
v_mfma_f32_32x32x16_bf16 v[32:47], a[88:91], a[132:135], v[32:47]
ds_read_b64_tr_b16 a[32:33], v244 offset:8192
ds_read_b64_tr_b16 a[34:35], v244 offset:12288
ds_read_b64_tr_b16 a[36:37], v244 offset:8448
s_add_u32 s27, s28, 8192
buffer_load_dwordx4 a[204:207], v245, s[8:11], s27 offen offset:512 sc1
v_mfma_f32_32x32x16_bf16 v[48:63], a[92:95], a[132:135], v[48:63]
ds_read_b64_tr_b16 a[38:39], v244 offset:12544
ds_read_b64_tr_b16 a[40:41], v244 offset:8704
ds_read_b64_tr_b16 a[42:43], v244 offset:12800
v_mfma_f32_32x32x16_bf16 v[0:15], a[96:99], a[136:139], v[0:15]
ds_read_b64_tr_b16 a[44:45], v244 offset:8960
ds_read_b64_tr_b16 a[46:47], v244 offset:13056
s_add_u32 m0, s57, 65536
s_nop 0
buffer_load_dwordx4 v246, s[52:55], s56 offen offset:0 lds
v_mfma_f32_32x32x16_bf16 v[16:31], a[100:103], a[136:139], v[16:31]
ds_read_b64_tr_b16 a[48:49], v244 offset:10240
ds_read_b64_tr_b16 a[50:51], v244 offset:14336
ds_read_b64_tr_b16 a[52:53], v244 offset:10496
v_mfma_f32_32x32x16_bf16 v[32:47], a[104:107], a[136:139], v[32:47]
ds_read_b64_tr_b16 a[54:55], v244 offset:14592
ds_read_b64_tr_b16 a[56:57], v244 offset:10752
ds_read_b64_tr_b16 a[58:59], v244 offset:14848
buffer_load_dwordx4 v247, s[52:55], s56 offen offset:1024 lds
v_mfma_f32_32x32x16_bf16 v[48:63], a[108:111], a[136:139], v[48:63]
ds_read_b64_tr_b16 a[60:61], v244 offset:11008
ds_read_b64_tr_b16 a[62:63], v244 offset:15104
v_mfma_f32_32x32x16_bf16 v[0:15], a[112:115], a[140:143], v[0:15]
buffer_load_dwordx4 v248, s[52:55], s56 offen offset:2048 lds
v_mfma_f32_32x32x16_bf16 v[16:31], a[116:119], a[140:143], v[16:31]
v_mfma_f32_32x32x16_bf16 v[32:47], a[120:123], a[140:143], v[32:47]
buffer_load_dwordx4 v249, s[52:55], s56 offen offset:3072 lds
v_mfma_f32_32x32x16_bf16 v[48:63], a[124:127], a[140:143], v[48:63]
s_add_u32 s28, s28, 16384
s_add_u32 s56, s56, 65536
v_add_u32 v244, 0x4000, v244
s_waitcnt vmcnt(16) lgkmcnt(0)
s_barrier
v_mfma_f32_32x32x16_bf16 v[0:15], a[0:3], a[144:147], v[0:15]
ds_read_b64_tr_b16 a[64:65], v244 offset:0
ds_read_b64_tr_b16 a[66:67], v244 offset:4096
ds_read_b64_tr_b16 a[68:69], v244 offset:256
buffer_load_dwordx4 a[128:131], v245, s[8:11], s28 offen offset:0 sc1
v_mfma_f32_32x32x16_bf16 v[16:31], a[4:7], a[144:147], v[16:31]
ds_read_b64_tr_b16 a[70:71], v244 offset:4352
ds_read_b64_tr_b16 a[72:73], v244 offset:512
ds_read_b64_tr_b16 a[74:75], v244 offset:4608
v_mfma_f32_32x32x16_bf16 v[32:47], a[8:11], a[144:147], v[32:47]
ds_read_b64_tr_b16 a[76:77], v244 offset:768
ds_read_b64_tr_b16 a[78:79], v244 offset:4864
buffer_load_dwordx4 a[132:135], v245, s[8:11], s28 offen offset:512 sc1
v_mfma_f32_32x32x16_bf16 v[48:63], a[12:15], a[144:147], v[48:63]
ds_read_b64_tr_b16 a[80:81], v244 offset:2048
ds_read_b64_tr_b16 a[82:83], v244 offset:6144
ds_read_b64_tr_b16 a[84:85], v244 offset:2304
v_mfma_f32_32x32x16_bf16 v[0:15], a[16:19], a[148:151], v[0:15]
ds_read_b64_tr_b16 a[86:87], v244 offset:6400
ds_read_b64_tr_b16 a[88:89], v244 offset:2560
ds_read_b64_tr_b16 a[90:91], v244 offset:6656
s_add_u32 s27, s28, 8192
buffer_load_dwordx4 a[136:139], v245, s[8:11], s27 offen offset:0 sc1
v_mfma_f32_32x32x16_bf16 v[16:31], a[20:23], a[148:151], v[16:31]
ds_read_b64_tr_b16 a[92:93], v244 offset:2816
ds_read_b64_tr_b16 a[94:95], v244 offset:6912
v_mfma_f32_32x32x16_bf16 v[32:47], a[24:27], a[148:151], v[32:47]
ds_read_b64_tr_b16 a[96:97], v244 offset:8192
ds_read_b64_tr_b16 a[98:99], v244 offset:12288
ds_read_b64_tr_b16 a[100:101], v244 offset:8448
s_add_u32 s27, s28, 8192
buffer_load_dwordx4 a[140:143], v245, s[8:11], s27 offen offset:512 sc1
v_mfma_f32_32x32x16_bf16 v[48:63], a[28:31], a[148:151], v[48:63]
ds_read_b64_tr_b16 a[102:103], v244 offset:12544
ds_read_b64_tr_b16 a[104:105], v244 offset:8704
ds_read_b64_tr_b16 a[106:107], v244 offset:12800
v_mfma_f32_32x32x16_bf16 v[0:15], a[32:35], a[152:155], v[0:15]
ds_read_b64_tr_b16 a[108:109], v244 offset:8960
ds_read_b64_tr_b16 a[110:111], v244 offset:13056
s_add_u32 m0, s57, 81920
s_nop 0
buffer_load_dwordx4 v246, s[52:55], s56 offen offset:0 lds
v_mfma_f32_32x32x16_bf16 v[16:31], a[36:39], a[152:155], v[16:31]
ds_read_b64_tr_b16 a[112:113], v244 offset:10240
ds_read_b64_tr_b16 a[114:115], v244 offset:14336
ds_read_b64_tr_b16 a[116:117], v244 offset:10496
v_mfma_f32_32x32x16_bf16 v[32:47], a[40:43], a[152:155], v[32:47]
ds_read_b64_tr_b16 a[118:119], v244 offset:14592
ds_read_b64_tr_b16 a[120:121], v244 offset:10752
ds_read_b64_tr_b16 a[122:123], v244 offset:14848
buffer_load_dwordx4 v247, s[52:55], s56 offen offset:1024 lds
v_mfma_f32_32x32x16_bf16 v[48:63], a[44:47], a[152:155], v[48:63]
ds_read_b64_tr_b16 a[124:125], v244 offset:11008
ds_read_b64_tr_b16 a[126:127], v244 offset:15104
v_mfma_f32_32x32x16_bf16 v[0:15], a[48:51], a[156:159], v[0:15]
buffer_load_dwordx4 v248, s[52:55], s56 offen offset:2048 lds
v_mfma_f32_32x32x16_bf16 v[16:31], a[52:55], a[156:159], v[16:31]
v_mfma_f32_32x32x16_bf16 v[32:47], a[56:59], a[156:159], v[32:47]
buffer_load_dwordx4 v249, s[52:55], s56 offen offset:3072 lds
v_mfma_f32_32x32x16_bf16 v[48:63], a[60:63], a[156:159], v[48:63]
s_add_u32 s28, s28, 16384
s_add_u32 s56, s56, 65536
v_add_u32 v244, 0x4000, v244
s_waitcnt vmcnt(28) lgkmcnt(0)
v_mfma_f32_32x32x16_bf16 v[0:15], a[64:67], a[160:163], v[0:15]
ds_read_b64_tr_b16 a[0:1], v244 offset:0
ds_read_b64_tr_b16 a[2:3], v244 offset:4096
ds_read_b64_tr_b16 a[4:5], v244 offset:256
buffer_load_dwordx4 a[144:147], v245, s[8:11], s28 offen offset:0 sc1
v_mfma_f32_32x32x16_bf16 v[16:31], a[68:71], a[160:163], v[16:31]
ds_read_b64_tr_b16 a[6:7], v244 offset:4352
ds_read_b64_tr_b16 a[8:9], v244 offset:512
ds_read_b64_tr_b16 a[10:11], v244 offset:4608
v_mfma_f32_32x32x16_bf16 v[32:47], a[72:75], a[160:163], v[32:47]
ds_read_b64_tr_b16 a[12:13], v244 offset:768
ds_read_b64_tr_b16 a[14:15], v244 offset:4864
buffer_load_dwordx4 a[148:151], v245, s[8:11], s28 offen offset:512 sc1
v_mfma_f32_32x32x16_bf16 v[48:63], a[76:79], a[160:163], v[48:63]
ds_read_b64_tr_b16 a[16:17], v244 offset:2048
ds_read_b64_tr_b16 a[18:19], v244 offset:6144
ds_read_b64_tr_b16 a[20:21], v244 offset:2304
v_mfma_f32_32x32x16_bf16 v[0:15], a[80:83], a[164:167], v[0:15]
ds_read_b64_tr_b16 a[22:23], v244 offset:6400
ds_read_b64_tr_b16 a[24:25], v244 offset:2560
ds_read_b64_tr_b16 a[26:27], v244 offset:6656
s_add_u32 s27, s28, 8192
buffer_load_dwordx4 a[152:155], v245, s[8:11], s27 offen offset:0 sc1
v_mfma_f32_32x32x16_bf16 v[16:31], a[84:87], a[164:167], v[16:31]
ds_read_b64_tr_b16 a[28:29], v244 offset:2816
ds_read_b64_tr_b16 a[30:31], v244 offset:6912
v_mfma_f32_32x32x16_bf16 v[32:47], a[88:91], a[164:167], v[32:47]
ds_read_b64_tr_b16 a[32:33], v244 offset:8192
ds_read_b64_tr_b16 a[34:35], v244 offset:12288
ds_read_b64_tr_b16 a[36:37], v244 offset:8448
s_add_u32 s27, s28, 8192
buffer_load_dwordx4 a[156:159], v245, s[8:11], s27 offen offset:512 sc1
v_mfma_f32_32x32x16_bf16 v[48:63], a[92:95], a[164:167], v[48:63]
ds_read_b64_tr_b16 a[38:39], v244 offset:12544
ds_read_b64_tr_b16 a[40:41], v244 offset:8704
ds_read_b64_tr_b16 a[42:43], v244 offset:12800
v_mfma_f32_32x32x16_bf16 v[0:15], a[96:99], a[168:171], v[0:15]
ds_read_b64_tr_b16 a[44:45], v244 offset:8960
ds_read_b64_tr_b16 a[46:47], v244 offset:13056
s_add_u32 m0, s57, 0
s_nop 0
buffer_load_dwordx4 v246, s[52:55], s56 offen offset:0 lds
v_mfma_f32_32x32x16_bf16 v[16:31], a[100:103], a[168:171], v[16:31]
ds_read_b64_tr_b16 a[48:49], v244 offset:10240
ds_read_b64_tr_b16 a[50:51], v244 offset:14336
ds_read_b64_tr_b16 a[52:53], v244 offset:10496
v_mfma_f32_32x32x16_bf16 v[32:47], a[104:107], a[168:171], v[32:47]
ds_read_b64_tr_b16 a[54:55], v244 offset:14592
ds_read_b64_tr_b16 a[56:57], v244 offset:10752
ds_read_b64_tr_b16 a[58:59], v244 offset:14848
buffer_load_dwordx4 v247, s[52:55], s56 offen offset:1024 lds
v_mfma_f32_32x32x16_bf16 v[48:63], a[108:111], a[168:171], v[48:63]
ds_read_b64_tr_b16 a[60:61], v244 offset:11008
ds_read_b64_tr_b16 a[62:63], v244 offset:15104
v_mfma_f32_32x32x16_bf16 v[0:15], a[112:115], a[172:175], v[0:15]
buffer_load_dwordx4 v248, s[52:55], s56 offen offset:2048 lds
v_mfma_f32_32x32x16_bf16 v[16:31], a[116:119], a[172:175], v[16:31]
v_mfma_f32_32x32x16_bf16 v[32:47], a[120:123], a[172:175], v[32:47]
buffer_load_dwordx4 v249, s[52:55], s56 offen offset:3072 lds
v_mfma_f32_32x32x16_bf16 v[48:63], a[124:127], a[172:175], v[48:63]
s_add_u32 s28, s28, 16384
s_add_u32 s56, s56, 65536
v_add_u32 v244, 0x4000, v244
s_waitcnt vmcnt(16) lgkmcnt(0)
s_barrier
v_mfma_f32_32x32x16_bf16 v[0:15], a[0:3], a[176:179], v[0:15]
ds_read_b64_tr_b16 a[64:65], v244 offset:0
ds_read_b64_tr_b16 a[66:67], v244 offset:4096
ds_read_b64_tr_b16 a[68:69], v244 offset:256
buffer_load_dwordx4 a[160:163], v245, s[8:11], s28 offen offset:0 sc1
v_mfma_f32_32x32x16_bf16 v[16:31], a[4:7], a[176:179], v[16:31]
ds_read_b64_tr_b16 a[70:71], v244 offset:4352
ds_read_b64_tr_b16 a[72:73], v244 offset:512
ds_read_b64_tr_b16 a[74:75], v244 offset:4608
v_mfma_f32_32x32x16_bf16 v[32:47], a[8:11], a[176:179], v[32:47]
ds_read_b64_tr_b16 a[76:77], v244 offset:768
ds_read_b64_tr_b16 a[78:79], v244 offset:4864
buffer_load_dwordx4 a[164:167], v245, s[8:11], s28 offen offset:512 sc1
v_mfma_f32_32x32x16_bf16 v[48:63], a[12:15], a[176:179], v[48:63]
ds_read_b64_tr_b16 a[80:81], v244 offset:2048
ds_read_b64_tr_b16 a[82:83], v244 offset:6144
ds_read_b64_tr_b16 a[84:85], v244 offset:2304
v_mfma_f32_32x32x16_bf16 v[0:15], a[16:19], a[180:183], v[0:15]
ds_read_b64_tr_b16 a[86:87], v244 offset:6400
ds_read_b64_tr_b16 a[88:89], v244 offset:2560
ds_read_b64_tr_b16 a[90:91], v244 offset:6656
s_add_u32 s27, s28, 8192
buffer_load_dwordx4 a[168:171], v245, s[8:11], s27 offen offset:0 sc1
v_mfma_f32_32x32x16_bf16 v[16:31], a[20:23], a[180:183], v[16:31]
ds_read_b64_tr_b16 a[92:93], v244 offset:2816
ds_read_b64_tr_b16 a[94:95], v244 offset:6912
v_mfma_f32_32x32x16_bf16 v[32:47], a[24:27], a[180:183], v[32:47]
ds_read_b64_tr_b16 a[96:97], v244 offset:8192
ds_read_b64_tr_b16 a[98:99], v244 offset:12288
ds_read_b64_tr_b16 a[100:101], v244 offset:8448
s_add_u32 s27, s28, 8192
buffer_load_dwordx4 a[172:175], v245, s[8:11], s27 offen offset:512 sc1
v_mfma_f32_32x32x16_bf16 v[48:63], a[28:31], a[180:183], v[48:63]
ds_read_b64_tr_b16 a[102:103], v244 offset:12544
ds_read_b64_tr_b16 a[104:105], v244 offset:8704
ds_read_b64_tr_b16 a[106:107], v244 offset:12800
v_mfma_f32_32x32x16_bf16 v[0:15], a[32:35], a[184:187], v[0:15]
ds_read_b64_tr_b16 a[108:109], v244 offset:8960
ds_read_b64_tr_b16 a[110:111], v244 offset:13056
s_add_u32 m0, s57, 16384
s_nop 0
buffer_load_dwordx4 v246, s[52:55], s56 offen offset:0 lds
v_mfma_f32_32x32x16_bf16 v[16:31], a[36:39], a[184:187], v[16:31]
ds_read_b64_tr_b16 a[112:113], v244 offset:10240
ds_read_b64_tr_b16 a[114:115], v244 offset:14336
ds_read_b64_tr_b16 a[116:117], v244 offset:10496
v_mfma_f32_32x32x16_bf16 v[32:47], a[40:43], a[184:187], v[32:47]
ds_read_b64_tr_b16 a[118:119], v244 offset:14592
ds_read_b64_tr_b16 a[120:121], v244 offset:10752
ds_read_b64_tr_b16 a[122:123], v244 offset:14848
buffer_load_dwordx4 v247, s[52:55], s56 offen offset:1024 lds
v_mfma_f32_32x32x16_bf16 v[48:63], a[44:47], a[184:187], v[48:63]
ds_read_b64_tr_b16 a[124:125], v244 offset:11008
ds_read_b64_tr_b16 a[126:127], v244 offset:15104
v_mfma_f32_32x32x16_bf16 v[0:15], a[48:51], a[188:191], v[0:15]
buffer_load_dwordx4 v248, s[52:55], s56 offen offset:2048 lds
v_mfma_f32_32x32x16_bf16 v[16:31], a[52:55], a[188:191], v[16:31]
v_mfma_f32_32x32x16_bf16 v[32:47], a[56:59], a[188:191], v[32:47]
buffer_load_dwordx4 v249, s[52:55], s56 offen offset:3072 lds
v_mfma_f32_32x32x16_bf16 v[48:63], a[60:63], a[188:191], v[48:63]
s_add_u32 s28, s28, 16384
s_add_u32 s56, s56, 65536
v_add_u32 v244, 0x4000, v244
s_waitcnt vmcnt(28) lgkmcnt(0)
v_mfma_f32_32x32x16_bf16 v[0:15], a[64:67], a[192:195], v[0:15]
ds_read_b64_tr_b16 a[0:1], v244 offset:0
ds_read_b64_tr_b16 a[2:3], v244 offset:4096
ds_read_b64_tr_b16 a[4:5], v244 offset:256
buffer_load_dwordx4 a[176:179], v245, s[8:11], s28 offen offset:0 sc1
v_mfma_f32_32x32x16_bf16 v[16:31], a[68:71], a[192:195], v[16:31]
ds_read_b64_tr_b16 a[6:7], v244 offset:4352
ds_read_b64_tr_b16 a[8:9], v244 offset:512
ds_read_b64_tr_b16 a[10:11], v244 offset:4608
v_mfma_f32_32x32x16_bf16 v[32:47], a[72:75], a[192:195], v[32:47]
ds_read_b64_tr_b16 a[12:13], v244 offset:768
ds_read_b64_tr_b16 a[14:15], v244 offset:4864
buffer_load_dwordx4 a[180:183], v245, s[8:11], s28 offen offset:512 sc1
v_mfma_f32_32x32x16_bf16 v[48:63], a[76:79], a[192:195], v[48:63]
ds_read_b64_tr_b16 a[16:17], v244 offset:2048
ds_read_b64_tr_b16 a[18:19], v244 offset:6144
ds_read_b64_tr_b16 a[20:21], v244 offset:2304
v_mfma_f32_32x32x16_bf16 v[0:15], a[80:83], a[196:199], v[0:15]
ds_read_b64_tr_b16 a[22:23], v244 offset:6400
ds_read_b64_tr_b16 a[24:25], v244 offset:2560
ds_read_b64_tr_b16 a[26:27], v244 offset:6656
s_add_u32 s27, s28, 8192
buffer_load_dwordx4 a[184:187], v245, s[8:11], s27 offen offset:0 sc1
v_mfma_f32_32x32x16_bf16 v[16:31], a[84:87], a[196:199], v[16:31]
ds_read_b64_tr_b16 a[28:29], v244 offset:2816
ds_read_b64_tr_b16 a[30:31], v244 offset:6912
v_mfma_f32_32x32x16_bf16 v[32:47], a[88:91], a[196:199], v[32:47]
ds_read_b64_tr_b16 a[32:33], v244 offset:8192
ds_read_b64_tr_b16 a[34:35], v244 offset:12288
ds_read_b64_tr_b16 a[36:37], v244 offset:8448
s_add_u32 s27, s28, 8192
buffer_load_dwordx4 a[188:191], v245, s[8:11], s27 offen offset:512 sc1
v_mfma_f32_32x32x16_bf16 v[48:63], a[92:95], a[196:199], v[48:63]
ds_read_b64_tr_b16 a[38:39], v244 offset:12544
ds_read_b64_tr_b16 a[40:41], v244 offset:8704
ds_read_b64_tr_b16 a[42:43], v244 offset:12800
v_mfma_f32_32x32x16_bf16 v[0:15], a[96:99], a[200:203], v[0:15]
ds_read_b64_tr_b16 a[44:45], v244 offset:8960
ds_read_b64_tr_b16 a[46:47], v244 offset:13056
s_add_u32 m0, s57, 32768
s_nop 0
buffer_load_dwordx4 v246, s[52:55], s56 offen offset:0 lds
v_mfma_f32_32x32x16_bf16 v[16:31], a[100:103], a[200:203], v[16:31]
ds_read_b64_tr_b16 a[48:49], v244 offset:10240
ds_read_b64_tr_b16 a[50:51], v244 offset:14336
ds_read_b64_tr_b16 a[52:53], v244 offset:10496
)ASM"
R"ASM(v_mfma_f32_32x32x16_bf16 v[32:47], a[104:107], a[200:203], v[32:47]
ds_read_b64_tr_b16 a[54:55], v244 offset:14592
ds_read_b64_tr_b16 a[56:57], v244 offset:10752
ds_read_b64_tr_b16 a[58:59], v244 offset:14848
buffer_load_dwordx4 v247, s[52:55], s56 offen offset:1024 lds
v_mfma_f32_32x32x16_bf16 v[48:63], a[108:111], a[200:203], v[48:63]
ds_read_b64_tr_b16 a[60:61], v244 offset:11008
ds_read_b64_tr_b16 a[62:63], v244 offset:15104
v_mfma_f32_32x32x16_bf16 v[0:15], a[112:115], a[204:207], v[0:15]
buffer_load_dwordx4 v248, s[52:55], s56 offen offset:2048 lds
v_mfma_f32_32x32x16_bf16 v[16:31], a[116:119], a[204:207], v[16:31]
v_mfma_f32_32x32x16_bf16 v[32:47], a[120:123], a[204:207], v[32:47]
buffer_load_dwordx4 v249, s[52:55], s56 offen offset:3072 lds
v_mfma_f32_32x32x16_bf16 v[48:63], a[124:127], a[204:207], v[48:63]
s_add_u32 s28, s28, 16384
s_add_u32 s56, s56, 65536
v_add_u32 v244, 0x4000, v244
s_waitcnt vmcnt(16) lgkmcnt(0)
s_barrier
v_mfma_f32_32x32x16_bf16 v[0:15], a[0:3], a[128:131], v[0:15]
ds_read_b64_tr_b16 a[64:65], v244 offset:0
ds_read_b64_tr_b16 a[66:67], v244 offset:4096
ds_read_b64_tr_b16 a[68:69], v244 offset:256
buffer_load_dwordx4 a[192:195], v245, s[8:11], s28 offen offset:0 sc1
v_mfma_f32_32x32x16_bf16 v[16:31], a[4:7], a[128:131], v[16:31]
ds_read_b64_tr_b16 a[70:71], v244 offset:4352
ds_read_b64_tr_b16 a[72:73], v244 offset:512
ds_read_b64_tr_b16 a[74:75], v244 offset:4608
v_mfma_f32_32x32x16_bf16 v[32:47], a[8:11], a[128:131], v[32:47]
ds_read_b64_tr_b16 a[76:77], v244 offset:768
ds_read_b64_tr_b16 a[78:79], v244 offset:4864
buffer_load_dwordx4 a[196:199], v245, s[8:11], s28 offen offset:512 sc1
v_mfma_f32_32x32x16_bf16 v[48:63], a[12:15], a[128:131], v[48:63]
ds_read_b64_tr_b16 a[80:81], v244 offset:2048
ds_read_b64_tr_b16 a[82:83], v244 offset:6144
ds_read_b64_tr_b16 a[84:85], v244 offset:2304
v_mfma_f32_32x32x16_bf16 v[0:15], a[16:19], a[132:135], v[0:15]
ds_read_b64_tr_b16 a[86:87], v244 offset:6400
ds_read_b64_tr_b16 a[88:89], v244 offset:2560
ds_read_b64_tr_b16 a[90:91], v244 offset:6656
s_add_u32 s27, s28, 8192
buffer_load_dwordx4 a[200:203], v245, s[8:11], s27 offen offset:0 sc1
v_mfma_f32_32x32x16_bf16 v[16:31], a[20:23], a[132:135], v[16:31]
ds_read_b64_tr_b16 a[92:93], v244 offset:2816
ds_read_b64_tr_b16 a[94:95], v244 offset:6912
v_mfma_f32_32x32x16_bf16 v[32:47], a[24:27], a[132:135], v[32:47]
ds_read_b64_tr_b16 a[96:97], v244 offset:8192
ds_read_b64_tr_b16 a[98:99], v244 offset:12288
ds_read_b64_tr_b16 a[100:101], v244 offset:8448
s_add_u32 s27, s28, 8192
buffer_load_dwordx4 a[204:207], v245, s[8:11], s27 offen offset:512 sc1
v_mfma_f32_32x32x16_bf16 v[48:63], a[28:31], a[132:135], v[48:63]
ds_read_b64_tr_b16 a[102:103], v244 offset:12544
ds_read_b64_tr_b16 a[104:105], v244 offset:8704
ds_read_b64_tr_b16 a[106:107], v244 offset:12800
v_mfma_f32_32x32x16_bf16 v[0:15], a[32:35], a[136:139], v[0:15]
ds_read_b64_tr_b16 a[108:109], v244 offset:8960
ds_read_b64_tr_b16 a[110:111], v244 offset:13056
s_add_u32 m0, s57, 49152
s_nop 0
buffer_load_dwordx4 v246, s[52:55], s56 offen offset:0 lds
v_mfma_f32_32x32x16_bf16 v[16:31], a[36:39], a[136:139], v[16:31]
ds_read_b64_tr_b16 a[112:113], v244 offset:10240
ds_read_b64_tr_b16 a[114:115], v244 offset:14336
ds_read_b64_tr_b16 a[116:117], v244 offset:10496
v_mfma_f32_32x32x16_bf16 v[32:47], a[40:43], a[136:139], v[32:47]
ds_read_b64_tr_b16 a[118:119], v244 offset:14592
ds_read_b64_tr_b16 a[120:121], v244 offset:10752
ds_read_b64_tr_b16 a[122:123], v244 offset:14848
buffer_load_dwordx4 v247, s[52:55], s56 offen offset:1024 lds
v_mfma_f32_32x32x16_bf16 v[48:63], a[44:47], a[136:139], v[48:63]
ds_read_b64_tr_b16 a[124:125], v244 offset:11008
ds_read_b64_tr_b16 a[126:127], v244 offset:15104
v_mfma_f32_32x32x16_bf16 v[0:15], a[48:51], a[140:143], v[0:15]
buffer_load_dwordx4 v248, s[52:55], s56 offen offset:2048 lds
v_mfma_f32_32x32x16_bf16 v[16:31], a[52:55], a[140:143], v[16:31]
v_mfma_f32_32x32x16_bf16 v[32:47], a[56:59], a[140:143], v[32:47]
buffer_load_dwordx4 v249, s[52:55], s56 offen offset:3072 lds
v_mfma_f32_32x32x16_bf16 v[48:63], a[60:63], a[140:143], v[48:63]
s_add_u32 s28, s28, 16384
s_add_u32 s56, s56, 65536
v_add_u32 v244, 0xfffec000, v244
s_waitcnt vmcnt(28) lgkmcnt(0)
v_mfma_f32_32x32x16_bf16 v[0:15], a[64:67], a[144:147], v[0:15]
ds_read_b64_tr_b16 a[0:1], v244 offset:0
ds_read_b64_tr_b16 a[2:3], v244 offset:4096
ds_read_b64_tr_b16 a[4:5], v244 offset:256
buffer_load_dwordx4 a[128:131], v245, s[8:11], s28 offen offset:0 sc1
v_mfma_f32_32x32x16_bf16 v[16:31], a[68:71], a[144:147], v[16:31]
ds_read_b64_tr_b16 a[6:7], v244 offset:4352
ds_read_b64_tr_b16 a[8:9], v244 offset:512
ds_read_b64_tr_b16 a[10:11], v244 offset:4608
v_mfma_f32_32x32x16_bf16 v[32:47], a[72:75], a[144:147], v[32:47]
ds_read_b64_tr_b16 a[12:13], v244 offset:768
ds_read_b64_tr_b16 a[14:15], v244 offset:4864
v_mfma_f32_32x32x16_bf16 v[48:63], a[76:79], a[144:147], v[48:63]
ds_read_b64_tr_b16 a[16:17], v244 offset:2048
ds_read_b64_tr_b16 a[18:19], v244 offset:6144
ds_read_b64_tr_b16 a[20:21], v244 offset:2304
v_mfma_f32_32x32x16_bf16 v[0:15], a[80:83], a[148:151], v[0:15]
ds_read_b64_tr_b16 a[22:23], v244 offset:6400
ds_read_b64_tr_b16 a[24:25], v244 offset:2560
ds_read_b64_tr_b16 a[26:27], v244 offset:6656
buffer_load_dwordx4 a[132:135], v245, s[8:11], s28 offen offset:512 sc1
v_mfma_f32_32x32x16_bf16 v[16:31], a[84:87], a[148:151], v[16:31]
ds_read_b64_tr_b16 a[28:29], v244 offset:2816
ds_read_b64_tr_b16 a[30:31], v244 offset:6912
v_mfma_f32_32x32x16_bf16 v[32:47], a[88:91], a[148:151], v[32:47]
ds_read_b64_tr_b16 a[32:33], v244 offset:8192
ds_read_b64_tr_b16 a[34:35], v244 offset:12288
ds_read_b64_tr_b16 a[36:37], v244 offset:8448
v_mfma_f32_32x32x16_bf16 v[48:63], a[92:95], a[148:151], v[48:63]
ds_read_b64_tr_b16 a[38:39], v244 offset:12544
ds_read_b64_tr_b16 a[40:41], v244 offset:8704
ds_read_b64_tr_b16 a[42:43], v244 offset:12800
v_mfma_f32_32x32x16_bf16 v[0:15], a[96:99], a[152:155], v[0:15]
ds_read_b64_tr_b16 a[44:45], v244 offset:8960
ds_read_b64_tr_b16 a[46:47], v244 offset:13056
s_add_u32 s27, s28, 8192
buffer_load_dwordx4 a[136:139], v245, s[8:11], s27 offen offset:0 sc1
v_mfma_f32_32x32x16_bf16 v[16:31], a[100:103], a[152:155], v[16:31]
ds_read_b64_tr_b16 a[48:49], v244 offset:10240
ds_read_b64_tr_b16 a[50:51], v244 offset:14336
ds_read_b64_tr_b16 a[52:53], v244 offset:10496
v_mfma_f32_32x32x16_bf16 v[32:47], a[104:107], a[152:155], v[32:47]
ds_read_b64_tr_b16 a[54:55], v244 offset:14592
ds_read_b64_tr_b16 a[56:57], v244 offset:10752
ds_read_b64_tr_b16 a[58:59], v244 offset:14848
v_mfma_f32_32x32x16_bf16 v[48:63], a[108:111], a[152:155], v[48:63]
ds_read_b64_tr_b16 a[60:61], v244 offset:11008
ds_read_b64_tr_b16 a[62:63], v244 offset:15104
v_mfma_f32_32x32x16_bf16 v[0:15], a[112:115], a[156:159], v[0:15]
s_add_u32 s27, s28, 8192
buffer_load_dwordx4 a[140:143], v245, s[8:11], s27 offen offset:512 sc1
v_mfma_f32_32x32x16_bf16 v[16:31], a[116:119], a[156:159], v[16:31]
v_mfma_f32_32x32x16_bf16 v[32:47], a[120:123], a[156:159], v[32:47]
v_mfma_f32_32x32x16_bf16 v[48:63], a[124:127], a[156:159], v[48:63]
s_add_u32 s28, s28, 16384
v_add_u32 v244, 0x4000, v244
s_waitcnt vmcnt(12) lgkmcnt(0)
s_barrier
v_mfma_f32_32x32x16_bf16 v[0:15], a[0:3], a[160:163], v[0:15]
ds_read_b64_tr_b16 a[64:65], v244 offset:0
ds_read_b64_tr_b16 a[66:67], v244 offset:4096
ds_read_b64_tr_b16 a[68:69], v244 offset:256
v_mfma_f32_32x32x16_bf16 v[16:31], a[4:7], a[160:163], v[16:31]
ds_read_b64_tr_b16 a[70:71], v244 offset:4352
ds_read_b64_tr_b16 a[72:73], v244 offset:512
ds_read_b64_tr_b16 a[74:75], v244 offset:4608
v_mfma_f32_32x32x16_bf16 v[32:47], a[8:11], a[160:163], v[32:47]
ds_read_b64_tr_b16 a[76:77], v244 offset:768
ds_read_b64_tr_b16 a[78:79], v244 offset:4864
v_mfma_f32_32x32x16_bf16 v[48:63], a[12:15], a[160:163], v[48:63]
ds_read_b64_tr_b16 a[80:81], v244 offset:2048
ds_read_b64_tr_b16 a[82:83], v244 offset:6144
ds_read_b64_tr_b16 a[84:85], v244 offset:2304
v_mfma_f32_32x32x16_bf16 v[0:15], a[16:19], a[164:167], v[0:15]
ds_read_b64_tr_b16 a[86:87], v244 offset:6400
ds_read_b64_tr_b16 a[88:89], v244 offset:2560
ds_read_b64_tr_b16 a[90:91], v244 offset:6656
v_mfma_f32_32x32x16_bf16 v[16:31], a[20:23], a[164:167], v[16:31]
ds_read_b64_tr_b16 a[92:93], v244 offset:2816
ds_read_b64_tr_b16 a[94:95], v244 offset:6912
v_mfma_f32_32x32x16_bf16 v[32:47], a[24:27], a[164:167], v[32:47]
ds_read_b64_tr_b16 a[96:97], v244 offset:8192
ds_read_b64_tr_b16 a[98:99], v244 offset:12288
ds_read_b64_tr_b16 a[100:101], v244 offset:8448
v_mfma_f32_32x32x16_bf16 v[48:63], a[28:31], a[164:167], v[48:63]
ds_read_b64_tr_b16 a[102:103], v244 offset:12544
ds_read_b64_tr_b16 a[104:105], v244 offset:8704
ds_read_b64_tr_b16 a[106:107], v244 offset:12800
v_mfma_f32_32x32x16_bf16 v[0:15], a[32:35], a[168:171], v[0:15]
ds_read_b64_tr_b16 a[108:109], v244 offset:8960
ds_read_b64_tr_b16 a[110:111], v244 offset:13056
v_mfma_f32_32x32x16_bf16 v[16:31], a[36:39], a[168:171], v[16:31]
ds_read_b64_tr_b16 a[112:113], v244 offset:10240
ds_read_b64_tr_b16 a[114:115], v244 offset:14336
ds_read_b64_tr_b16 a[116:117], v244 offset:10496
v_mfma_f32_32x32x16_bf16 v[32:47], a[40:43], a[168:171], v[32:47]
ds_read_b64_tr_b16 a[118:119], v244 offset:14592
ds_read_b64_tr_b16 a[120:121], v244 offset:10752
ds_read_b64_tr_b16 a[122:123], v244 offset:14848
v_mfma_f32_32x32x16_bf16 v[48:63], a[44:47], a[168:171], v[48:63]
ds_read_b64_tr_b16 a[124:125], v244 offset:11008
ds_read_b64_tr_b16 a[126:127], v244 offset:15104
v_mfma_f32_32x32x16_bf16 v[0:15], a[48:51], a[172:175], v[0:15]
v_mfma_f32_32x32x16_bf16 v[16:31], a[52:55], a[172:175], v[16:31]
v_mfma_f32_32x32x16_bf16 v[32:47], a[56:59], a[172:175], v[32:47]
v_mfma_f32_32x32x16_bf16 v[48:63], a[60:63], a[172:175], v[48:63]
v_add_u32 v244, 0x4000, v244
s_waitcnt vmcnt(16) lgkmcnt(0)
v_mfma_f32_32x32x16_bf16 v[0:15], a[64:67], a[176:179], v[0:15]
ds_read_b64_tr_b16 a[0:1], v244 offset:0
ds_read_b64_tr_b16 a[2:3], v244 offset:4096
ds_read_b64_tr_b16 a[4:5], v244 offset:256
v_mfma_f32_32x32x16_bf16 v[16:31], a[68:71], a[176:179], v[16:31]
ds_read_b64_tr_b16 a[6:7], v244 offset:4352
ds_read_b64_tr_b16 a[8:9], v244 offset:512
ds_read_b64_tr_b16 a[10:11], v244 offset:4608
v_mfma_f32_32x32x16_bf16 v[32:47], a[72:75], a[176:179], v[32:47]
ds_read_b64_tr_b16 a[12:13], v244 offset:768
ds_read_b64_tr_b16 a[14:15], v244 offset:4864
v_mfma_f32_32x32x16_bf16 v[48:63], a[76:79], a[176:179], v[48:63]
ds_read_b64_tr_b16 a[16:17], v244 offset:2048
ds_read_b64_tr_b16 a[18:19], v244 offset:6144
ds_read_b64_tr_b16 a[20:21], v244 offset:2304
v_mfma_f32_32x32x16_bf16 v[0:15], a[80:83], a[180:183], v[0:15]
ds_read_b64_tr_b16 a[22:23], v244 offset:6400
ds_read_b64_tr_b16 a[24:25], v244 offset:2560
ds_read_b64_tr_b16 a[26:27], v244 offset:6656
v_mfma_f32_32x32x16_bf16 v[16:31], a[84:87], a[180:183], v[16:31]
ds_read_b64_tr_b16 a[28:29], v244 offset:2816
ds_read_b64_tr_b16 a[30:31], v244 offset:6912
v_mfma_f32_32x32x16_bf16 v[32:47], a[88:91], a[180:183], v[32:47]
ds_read_b64_tr_b16 a[32:33], v244 offset:8192
ds_read_b64_tr_b16 a[34:35], v244 offset:12288
ds_read_b64_tr_b16 a[36:37], v244 offset:8448
v_mfma_f32_32x32x16_bf16 v[48:63], a[92:95], a[180:183], v[48:63]
ds_read_b64_tr_b16 a[38:39], v244 offset:12544
ds_read_b64_tr_b16 a[40:41], v244 offset:8704
ds_read_b64_tr_b16 a[42:43], v244 offset:12800
v_mfma_f32_32x32x16_bf16 v[0:15], a[96:99], a[184:187], v[0:15]
ds_read_b64_tr_b16 a[44:45], v244 offset:8960
ds_read_b64_tr_b16 a[46:47], v244 offset:13056
v_mfma_f32_32x32x16_bf16 v[16:31], a[100:103], a[184:187], v[16:31]
ds_read_b64_tr_b16 a[48:49], v244 offset:10240
ds_read_b64_tr_b16 a[50:51], v244 offset:14336
ds_read_b64_tr_b16 a[52:53], v244 offset:10496
v_mfma_f32_32x32x16_bf16 v[32:47], a[104:107], a[184:187], v[32:47]
ds_read_b64_tr_b16 a[54:55], v244 offset:14592
ds_read_b64_tr_b16 a[56:57], v244 offset:10752
ds_read_b64_tr_b16 a[58:59], v244 offset:14848
v_mfma_f32_32x32x16_bf16 v[48:63], a[108:111], a[184:187], v[48:63]
ds_read_b64_tr_b16 a[60:61], v244 offset:11008
ds_read_b64_tr_b16 a[62:63], v244 offset:15104
v_mfma_f32_32x32x16_bf16 v[0:15], a[112:115], a[188:191], v[0:15]
v_mfma_f32_32x32x16_bf16 v[16:31], a[116:119], a[188:191], v[16:31]
v_mfma_f32_32x32x16_bf16 v[32:47], a[120:123], a[188:191], v[32:47]
v_mfma_f32_32x32x16_bf16 v[48:63], a[124:127], a[188:191], v[48:63]
v_add_u32 v244, 0x4000, v244
s_waitcnt vmcnt(4) lgkmcnt(0)
s_barrier
v_mfma_f32_32x32x16_bf16 v[0:15], a[0:3], a[192:195], v[0:15]
ds_read_b64_tr_b16 a[64:65], v244 offset:0
ds_read_b64_tr_b16 a[66:67], v244 offset:4096
ds_read_b64_tr_b16 a[68:69], v244 offset:256
v_mfma_f32_32x32x16_bf16 v[16:31], a[4:7], a[192:195], v[16:31]
ds_read_b64_tr_b16 a[70:71], v244 offset:4352
ds_read_b64_tr_b16 a[72:73], v244 offset:512
ds_read_b64_tr_b16 a[74:75], v244 offset:4608
v_mfma_f32_32x32x16_bf16 v[32:47], a[8:11], a[192:195], v[32:47]
ds_read_b64_tr_b16 a[76:77], v244 offset:768
ds_read_b64_tr_b16 a[78:79], v244 offset:4864
v_mfma_f32_32x32x16_bf16 v[48:63], a[12:15], a[192:195], v[48:63]
ds_read_b64_tr_b16 a[80:81], v244 offset:2048
ds_read_b64_tr_b16 a[82:83], v244 offset:6144
ds_read_b64_tr_b16 a[84:85], v244 offset:2304
v_mfma_f32_32x32x16_bf16 v[0:15], a[16:19], a[196:199], v[0:15]
ds_read_b64_tr_b16 a[86:87], v244 offset:6400
ds_read_b64_tr_b16 a[88:89], v244 offset:2560
ds_read_b64_tr_b16 a[90:91], v244 offset:6656
v_mfma_f32_32x32x16_bf16 v[16:31], a[20:23], a[196:199], v[16:31]
ds_read_b64_tr_b16 a[92:93], v244 offset:2816
ds_read_b64_tr_b16 a[94:95], v244 offset:6912
v_mfma_f32_32x32x16_bf16 v[32:47], a[24:27], a[196:199], v[32:47]
ds_read_b64_tr_b16 a[96:97], v244 offset:8192
ds_read_b64_tr_b16 a[98:99], v244 offset:12288
ds_read_b64_tr_b16 a[100:101], v244 offset:8448
v_mfma_f32_32x32x16_bf16 v[48:63], a[28:31], a[196:199], v[48:63]
ds_read_b64_tr_b16 a[102:103], v244 offset:12544
ds_read_b64_tr_b16 a[104:105], v244 offset:8704
ds_read_b64_tr_b16 a[106:107], v244 offset:12800
v_mfma_f32_32x32x16_bf16 v[0:15], a[32:35], a[200:203], v[0:15]
ds_read_b64_tr_b16 a[108:109], v244 offset:8960
ds_read_b64_tr_b16 a[110:111], v244 offset:13056
v_mfma_f32_32x32x16_bf16 v[16:31], a[36:39], a[200:203], v[16:31]
ds_read_b64_tr_b16 a[112:113], v244 offset:10240
ds_read_b64_tr_b16 a[114:115], v244 offset:14336
ds_read_b64_tr_b16 a[116:117], v244 offset:10496
v_mfma_f32_32x32x16_bf16 v[32:47], a[40:43], a[200:203], v[32:47]
ds_read_b64_tr_b16 a[118:119], v244 offset:14592
ds_read_b64_tr_b16 a[120:121], v244 offset:10752
ds_read_b64_tr_b16 a[122:123], v244 offset:14848
v_mfma_f32_32x32x16_bf16 v[48:63], a[44:47], a[200:203], v[48:63]
ds_read_b64_tr_b16 a[124:125], v244 offset:11008
ds_read_b64_tr_b16 a[126:127], v244 offset:15104
v_mfma_f32_32x32x16_bf16 v[0:15], a[48:51], a[204:207], v[0:15]
v_mfma_f32_32x32x16_bf16 v[16:31], a[52:55], a[204:207], v[16:31]
v_mfma_f32_32x32x16_bf16 v[32:47], a[56:59], a[204:207], v[32:47]
v_mfma_f32_32x32x16_bf16 v[48:63], a[60:63], a[204:207], v[48:63]
s_waitcnt vmcnt(0) lgkmcnt(0)
s_lshl_b32 s43, s40, 4
s_add_u32 s43, s43, s41
s_lshl_b32 s43, s43, 4
s_lshl_b32 s44, s42, 2
s_add_u32 s43, s43, s44
s_lshl_b32 s43, s43, 13
s_lshl_b32 s44, s7, 11
s_add_u32 s43, s43, s44
v_mfma_f32_32x32x16_bf16 v[0:15], a[64:67], a[128:131], v[0:15]
v_mfma_f32_32x32x16_bf16 v[0:15], a[80:83], a[132:135], v[0:15]
v_mfma_f32_32x32x16_bf16 v[0:15], a[96:99], a[136:139], v[0:15]
v_mfma_f32_32x32x16_bf16 v[0:15], a[112:115], a[140:143], v[0:15]
v_mfma_f32_32x32x16_bf16 v[16:31], a[68:71], a[128:131], v[16:31]
s_nop 7
v_mfma_f32_32x32x16_bf16 v[16:31], a[84:87], a[132:135], v[16:31]
s_nop 1
v_cvt_pk_bf16_f32 v64, v0, v1
v_cvt_pk_bf16_f32 v65, v2, v3
v_cvt_pk_bf16_f32 v66, v8, v9
v_cvt_pk_bf16_f32 v67, v10, v11
v_mfma_f32_32x32x16_bf16 v[16:31], a[100:103], a[136:139], v[16:31]
s_add_u32 s44, s43, 0
buffer_store_dwordx4 v[64:67], v245, s[16:19], s44 offen offset:0 sc1
v_cvt_pk_bf16_f32 v68, v4, v5
v_cvt_pk_bf16_f32 v69, v6, v7
v_mfma_f32_32x32x16_bf16 v[16:31], a[116:119], a[140:143], v[16:31]
v_cvt_pk_bf16_f32 v70, v12, v13
v_cvt_pk_bf16_f32 v71, v14, v15
buffer_store_dwordx4 v[68:71], v245, s[16:19], s44 offen offset:512 sc1
v_mfma_f32_32x32x16_bf16 v[32:47], a[72:75], a[128:131], v[32:47]
s_nop 7
v_mfma_f32_32x32x16_bf16 v[32:47], a[88:91], a[132:135], v[32:47]
v_cvt_pk_bf16_f32 v72, v16, v17
v_cvt_pk_bf16_f32 v73, v18, v19
v_cvt_pk_bf16_f32 v74, v24, v25
v_cvt_pk_bf16_f32 v75, v26, v27
v_mfma_f32_32x32x16_bf16 v[32:47], a[104:107], a[136:139], v[32:47]
s_add_u32 s44, s43, 8192
buffer_store_dwordx4 v[72:75], v245, s[16:19], s44 offen offset:0 sc1
v_cvt_pk_bf16_f32 v76, v20, v21
v_cvt_pk_bf16_f32 v77, v22, v23
v_mfma_f32_32x32x16_bf16 v[32:47], a[120:123], a[140:143], v[32:47]
v_cvt_pk_bf16_f32 v78, v28, v29
v_cvt_pk_bf16_f32 v79, v30, v31
buffer_store_dwordx4 v[76:79], v245, s[16:19], s44 offen offset:512 sc1
v_mfma_f32_32x32x16_bf16 v[48:63], a[76:79], a[128:131], v[48:63]
s_nop 7
v_mfma_f32_32x32x16_bf16 v[48:63], a[92:95], a[132:135], v[48:63]
v_cvt_pk_bf16_f32 v80, v32, v33
v_cvt_pk_bf16_f32 v81, v34, v35
v_cvt_pk_bf16_f32 v82, v40, v41
v_cvt_pk_bf16_f32 v83, v42, v43
v_mfma_f32_32x32x16_bf16 v[48:63], a[108:111], a[136:139], v[48:63]
s_add_u32 s44, s43, 16384
buffer_store_dwordx4 v[80:83], v245, s[16:19], s44 offen offset:0 sc1
v_cvt_pk_bf16_f32 v84, v36, v37
v_cvt_pk_bf16_f32 v85, v38, v39
v_mfma_f32_32x32x16_bf16 v[48:63], a[124:127], a[140:143], v[48:63]
v_cvt_pk_bf16_f32 v86, v44, v45
v_cvt_pk_bf16_f32 v87, v46, v47
buffer_store_dwordx4 v[84:87], v245, s[16:19], s44 offen offset:512 sc1
s_nop 15
v_cvt_pk_bf16_f32 v88, v48, v49
v_cvt_pk_bf16_f32 v89, v50, v51
v_cvt_pk_bf16_f32 v90, v56, v57
v_cvt_pk_bf16_f32 v91, v58, v59
s_add_u32 s44, s43, 24576
buffer_store_dwordx4 v[88:91], v245, s[16:19], s44 offen offset:0 sc1
v_cvt_pk_bf16_f32 v92, v52, v53
v_cvt_pk_bf16_f32 v93, v54, v55
v_cvt_pk_bf16_f32 v94, v60, v61
v_cvt_pk_bf16_f32 v95, v62, v63
buffer_store_dwordx4 v[92:95], v245, s[16:19], s44 offen offset:512 sc1
s_waitcnt vmcnt(0)
s_barrier
s_lshl_b32 s46, s41, 7
s_lshl_b32 s47, s48, 3
s_add_u32 s46, s46, s47
s_add_u32 s46, s46, 16384
s_cmp_eq_u32 s7, 0
s_cbranch_scc0 Lpub_42
v_mov_b32 v0, s84
v_mov_b32 v1, s85
v_mov_b32 v2, 0
s_mov_b64 exec, 1
buffer_store_dwordx2 v[0:1], v2, s[88:91], s46 offen sc1
s_nop 0
s_mov_b64 exec, -1
Lpub_42:
v_mbcnt_lo_u32_b32 v250, -1, 0
v_mbcnt_hi_u32_b32 v250, -1, v250
s_mov_b32 s52, s76
s_and_b32 s53, s77, 0xffff
s_mov_b32 s54, 0x1c0000
s_mov_b32 s55, 0x20000
s_bfe_u32 s59, s6, 0x10002
s_lshl_b32 s59, s59, 3
s_lshr_b32 s60, s6, 5
s_add_u32 s59, s59, s60
s_bfe_u32 s58, s6, 0x20003
s_lshl_b32 s58, s58, 2
s_and_b32 s60, s6, 3
s_add_u32 s58, s58, s60
s_min_u32 s58, s58, 13
s_lshl_b32 s56, s58, 8
v_lshrrev_b32 v243, 3, v250
v_lshlrev_b32 v243, 5, v243
v_and_b32 v244, 1, v250
v_lshl_add_u32 v243, v244, 4, v243
v_bfe_u32 v244, v250, 1, 2
v_mul_u32_u24 v244, 3584, v244
v_add_u32 v246, v244, v243
v_xor_b32 v245, 32, v246
v_add_u32 v247, 13312, v245
v_add_u32 v249, 39936, v245
v_add_u32 v248, 26624, v246
s_mul_i32 s58, s7, 57344
s_add_u32 s56, s56, s58
s_lshl_b32 s57, s7, 12
s_cmp_lg_u32 s7, 0
s_cbranch_scc0 Low_41
s_add_u32 m0, s57, 0
s_nop 0
buffer_load_dwordx4 v246, s[52:55], s56 offen offset:0 lds
buffer_load_dwordx4 v247, s[52:55], s56 offen offset:1024 lds
buffer_load_dwordx4 v248, s[52:55], s56 offen offset:2048 lds
buffer_load_dwordx4 v249, s[52:55], s56 offen offset:3072 lds
s_add_u32 s56, s56, 229376
s_add_u32 m0, s57, 16384
s_nop 0
buffer_load_dwordx4 v246, s[52:55], s56 offen offset:0 lds
buffer_load_dwordx4 v247, s[52:55], s56 offen offset:1024 lds
buffer_load_dwordx4 v248, s[52:55], s56 offen offset:2048 lds
buffer_load_dwordx4 v249, s[52:55], s56 offen offset:3072 lds
s_add_u32 s56, s56, 229376
s_add_u32 m0, s57, 32768
s_nop 0
buffer_load_dwordx4 v246, s[52:55], s56 offen offset:0 lds
buffer_load_dwordx4 v247, s[52:55], s56 offen offset:1024 lds
buffer_load_dwordx4 v248, s[52:55], s56 offen offset:2048 lds
buffer_load_dwordx4 v249, s[52:55], s56 offen offset:3072 lds
s_add_u32 s56, s56, 229376
s_add_u32 m0, s57, 49152
s_nop 0
buffer_load_dwordx4 v246, s[52:55], s56 offen offset:0 lds
buffer_load_dwordx4 v247, s[52:55], s56 offen offset:1024 lds
buffer_load_dwordx4 v248, s[52:55], s56 offen offset:2048 lds
buffer_load_dwordx4 v249, s[52:55], s56 offen offset:3072 lds
s_add_u32 s56, s56, 229376
s_add_u32 m0, s57, 65536
s_nop 0
buffer_load_dwordx4 v246, s[52:55], s56 offen offset:0 lds
buffer_load_dwordx4 v247, s[52:55], s56 offen offset:1024 lds
buffer_load_dwordx4 v248, s[52:55], s56 offen offset:2048 lds
buffer_load_dwordx4 v249, s[52:55], s56 offen offset:3072 lds
s_add_u32 s56, s56, 229376
s_add_u32 m0, s57, 81920
s_nop 0
buffer_load_dwordx4 v246, s[52:55], s56 offen offset:0 lds
buffer_load_dwordx4 v247, s[52:55], s56 offen offset:1024 lds
buffer_load_dwordx4 v248, s[52:55], s56 offen offset:2048 lds
buffer_load_dwordx4 v249, s[52:55], s56 offen offset:3072 lds
s_add_u32 s56, s56, 229376
s_mul_i32 s58, s7, 57344
s_sub_u32 s58, s56, s58
s_sub_u32 s58, s58, 1376256
s_add_u32 s59, s7, 1
s_mul_i32 s60, s59, 229376
s_add_u32 s58, s58, s60
s_lshl_b32 s59, s59, 14
s_mov_b32 m0, s59
s_nop 0
buffer_load_dwordx4 v246, s[52:55], s58 offen offset:0 lds
buffer_load_dwordx4 v247, s[52:55], s58 offen offset:1024 lds
buffer_load_dwordx4 v248, s[52:55], s58 offen offset:2048 lds
buffer_load_dwordx4 v249, s[52:55], s58 offen offset:3072 lds
s_add_u32 s58, s58, 688128
s_add_u32 s59, s59, 49152
s_cmp_le_u32 s7, 1
s_cbranch_scc0 Low_40
s_mov_b32 m0, s59
s_nop 0
buffer_load_dwordx4 v246, s[52:55], s58 offen offset:0 lds
buffer_load_dwordx4 v247, s[52:55], s58 offen offset:1024 lds
buffer_load_dwordx4 v248, s[52:55], s58 offen offset:2048 lds
buffer_load_dwordx4 v249, s[52:55], s58 offen offset:3072 lds
Low_40:
Low_41:
v_mbcnt_lo_u32_b32 v250, -1, 0
v_mbcnt_hi_u32_b32 v250, -1, v250
s_add_u32 s8, s82, 0x3c00000
s_addc_u32 s9, s83, 0
s_and_b32 s9, s9, 0xffff
s_mov_b32 s10, 0x800000
s_mov_b32 s11, 0x20000
s_mov_b32 s12, s64
s_and_b32 s13, s65, 0xffff
s_mov_b32 s14, 0x200000
s_mov_b32 s15, 0x20000
s_add_u32 s16, s82, 0x4600000
s_addc_u32 s17, s83, 0
s_and_b32 s17, s17, 0xffff
s_mov_b32 s18, 0x200000
s_mov_b32 s19, 0x20000
s_add_u32 s20, s82, 0x4a00000
s_addc_u32 s21, s83, 0
s_and_b32 s21, s21, 0xffff
s_mov_b32 s22, 0x200000
s_mov_b32 s23, 0x20000
v_and_b32 v100, 7, v250
v_bfe_u32 v101, v250, 3, 1
v_lshrrev_b32 v102, 4, v250
v_lshlrev_b32 v103, 4, v100
v_lshl_add_u32 v103, v102, 8, v103
v_lshl_add_u32 v103, v101, 13, v103
v_lshlrev_b32 v99, 10, v100
v_lshl_add_u32 v99, v101, 6, v99
v_lshl_add_u32 v99, v102, 3, v99
s_lshr_b32 s29, s48, 1
s_and_b32 s30, s48, 1
s_lshl_b32 s26, s41, 7
s_lshl_b32 s27, s29, 4
s_add_u32 s26, s26, s27
s_lshl_b32 s27, s30, 3
s_add_u32 s26, s26, s27
s_lshl_b32 s26, s26, 10
s_lshl_b32 s27, s7, 8
s_add_u32 s26, s26, s27
s_cmp_lg_u32 s7, 0
s_cbranch_scc0 Low_43
buffer_load_dwordx2 v[64:65], v99, s[12:15], s26 offen offset:0
buffer_load_dwordx2 v[66:67], v99, s[12:15], s26 offen offset:32
buffer_load_dwordx2 v[68:69], v99, s[12:15], s26 offen offset:128
buffer_load_dwordx2 v[70:71], v99, s[12:15], s26 offen offset:160
Low_43:
s_lshl_b32 s28, s41, 4
s_lshl_b32 s27, s7, 2
s_add_u32 s28, s28, s27
s_lshl_b32 s28, s28, 3
s_add_u32 s28, s28, s29
s_lshl_b32 s28, s28, 10
s_lshl_b32 s27, s30, 7
s_add_u32 s28, s28, s27
v_and_b32 v98, 15, v250
v_lshlrev_b32 v98, 3, v98
s_lshl_b32 s46, s41, 7
s_add_u32 s46, s46, 16384
s_cmp_eq_u32 s7, 0
s_cbranch_scc0 Lp0_44
s_mov_b32 s86, 0
Lpoll_45:
buffer_load_dwordx2 v[200:201], v98, s[88:91], s46 offen sc1
s_waitcnt vmcnt(0)
v_cmp_eq_u64 vcc, s[84:85], v[200:201]
s_nop 1
s_cmp_eq_u64 vcc, exec
s_cbranch_scc1 Lpdone_46
s_sleep 1
s_add_u32 s86, s86, 1
s_cmp_lt_u32 s86, 0x40000
s_cbranch_scc1 Lpoll_45
Lpdone_46:
Lp0_44:
s_barrier
s_cmp_eq_u32 s7, 0
s_cbranch_scc0 Low_47
buffer_load_dwordx2 v[64:65], v99, s[12:15], s26 offen offset:0
buffer_load_dwordx2 v[66:67], v99, s[12:15], s26 offen offset:32
buffer_load_dwordx2 v[68:69], v99, s[12:15], s26 offen offset:128
buffer_load_dwordx2 v[70:71], v99, s[12:15], s26 offen offset:160
Low_47:
s_add_u32 s27, s28, 0x0
buffer_load_dwordx4 v[0:3], v103, s[8:11], s27 offen sc1
s_add_u32 s27, s28, 0x200000
buffer_load_dwordx4 v[4:7], v103, s[8:11], s27 offen sc1
s_add_u32 s27, s28, 0x400000
buffer_load_dwordx4 v[8:11], v103, s[8:11], s27 offen sc1
s_add_u32 s27, s28, 0x600000
buffer_load_dwordx4 v[12:15], v103, s[8:11], s27 offen sc1
s_add_u32 s27, s28, 0x4000
buffer_load_dwordx4 v[16:19], v103, s[8:11], s27 offen sc1
s_add_u32 s27, s28, 0x204000
buffer_load_dwordx4 v[20:23], v103, s[8:11], s27 offen sc1
s_add_u32 s27, s28, 0x404000
buffer_load_dwordx4 v[24:27], v103, s[8:11], s27 offen sc1
s_add_u32 s27, s28, 0x604000
buffer_load_dwordx4 v[28:31], v103, s[8:11], s27 offen sc1
s_waitcnt vmcnt(4)
v_lshlrev_b32 v112, 16, v64
v_and_b32 v113, 0xffff0000, v64
v_lshlrev_b32 v32, 16, v0
v_and_b32 v33, 0xffff0000, v0
v_pk_add_f32 v[112:113], v[112:113], v[32:33]
v_lshlrev_b32 v34, 16, v4
v_and_b32 v35, 0xffff0000, v4
v_pk_add_f32 v[112:113], v[112:113], v[34:35]
v_lshlrev_b32 v32, 16, v8
v_and_b32 v33, 0xffff0000, v8
v_pk_add_f32 v[112:113], v[112:113], v[32:33]
v_lshlrev_b32 v34, 16, v12
v_and_b32 v35, 0xffff0000, v12
v_pk_add_f32 v[112:113], v[112:113], v[34:35]
v_lshlrev_b32 v114, 16, v65
v_and_b32 v115, 0xffff0000, v65
v_lshlrev_b32 v32, 16, v1
v_and_b32 v33, 0xffff0000, v1
v_pk_add_f32 v[114:115], v[114:115], v[32:33]
v_lshlrev_b32 v34, 16, v5
v_and_b32 v35, 0xffff0000, v5
v_pk_add_f32 v[114:115], v[114:115], v[34:35]
v_lshlrev_b32 v32, 16, v9
v_and_b32 v33, 0xffff0000, v9
v_pk_add_f32 v[114:115], v[114:115], v[32:33]
v_lshlrev_b32 v34, 16, v13
v_and_b32 v35, 0xffff0000, v13
v_pk_add_f32 v[114:115], v[114:115], v[34:35]
v_lshlrev_b32 v116, 16, v66
v_and_b32 v117, 0xffff0000, v66
v_lshlrev_b32 v32, 16, v2
v_and_b32 v33, 0xffff0000, v2
v_pk_add_f32 v[116:117], v[116:117], v[32:33]
v_lshlrev_b32 v34, 16, v6
v_and_b32 v35, 0xffff0000, v6
v_pk_add_f32 v[116:117], v[116:117], v[34:35]
v_lshlrev_b32 v32, 16, v10
v_and_b32 v33, 0xffff0000, v10
v_pk_add_f32 v[116:117], v[116:117], v[32:33]
v_lshlrev_b32 v34, 16, v14
v_and_b32 v35, 0xffff0000, v14
v_pk_add_f32 v[116:117], v[116:117], v[34:35]
v_lshlrev_b32 v118, 16, v67
v_and_b32 v119, 0xffff0000, v67
v_lshlrev_b32 v32, 16, v3
v_and_b32 v33, 0xffff0000, v3
v_pk_add_f32 v[118:119], v[118:119], v[32:33]
v_lshlrev_b32 v34, 16, v7
v_and_b32 v35, 0xffff0000, v7
v_pk_add_f32 v[118:119], v[118:119], v[34:35]
v_lshlrev_b32 v32, 16, v11
v_and_b32 v33, 0xffff0000, v11
v_pk_add_f32 v[118:119], v[118:119], v[32:33]
v_lshlrev_b32 v34, 16, v15
v_and_b32 v35, 0xffff0000, v15
v_pk_add_f32 v[118:119], v[118:119], v[34:35]
v_cvt_pk_bf16_f32 v40, v112, v113
v_cvt_pk_bf16_f32 v41, v114, v115
v_cvt_pk_bf16_f32 v42, v116, v117
v_cvt_pk_bf16_f32 v43, v118, v119
s_add_u32 s27, s28, 0
buffer_store_dwordx4 v[40:43], v103, s[16:19], s27 offen sc1
s_waitcnt vmcnt(0)
v_lshlrev_b32 v120, 16, v68
v_and_b32 v121, 0xffff0000, v68
v_lshlrev_b32 v32, 16, v16
v_and_b32 v33, 0xffff0000, v16
v_pk_add_f32 v[120:121], v[120:121], v[32:33]
v_lshlrev_b32 v34, 16, v20
v_and_b32 v35, 0xffff0000, v20
v_pk_add_f32 v[120:121], v[120:121], v[34:35]
v_lshlrev_b32 v32, 16, v24
v_and_b32 v33, 0xffff0000, v24
v_pk_add_f32 v[120:121], v[120:121], v[32:33]
v_lshlrev_b32 v34, 16, v28
v_and_b32 v35, 0xffff0000, v28
v_pk_add_f32 v[120:121], v[120:121], v[34:35]
v_lshlrev_b32 v122, 16, v69
v_and_b32 v123, 0xffff0000, v69
v_lshlrev_b32 v32, 16, v17
v_and_b32 v33, 0xffff0000, v17
v_pk_add_f32 v[122:123], v[122:123], v[32:33]
v_lshlrev_b32 v34, 16, v21
v_and_b32 v35, 0xffff0000, v21
v_pk_add_f32 v[122:123], v[122:123], v[34:35]
v_lshlrev_b32 v32, 16, v25
v_and_b32 v33, 0xffff0000, v25
v_pk_add_f32 v[122:123], v[122:123], v[32:33]
v_lshlrev_b32 v34, 16, v29
v_and_b32 v35, 0xffff0000, v29
v_pk_add_f32 v[122:123], v[122:123], v[34:35]
v_lshlrev_b32 v124, 16, v70
v_and_b32 v125, 0xffff0000, v70
v_lshlrev_b32 v32, 16, v18
v_and_b32 v33, 0xffff0000, v18
v_pk_add_f32 v[124:125], v[124:125], v[32:33]
v_lshlrev_b32 v34, 16, v22
v_and_b32 v35, 0xffff0000, v22
v_pk_add_f32 v[124:125], v[124:125], v[34:35]
v_lshlrev_b32 v32, 16, v26
v_and_b32 v33, 0xffff0000, v26
v_pk_add_f32 v[124:125], v[124:125], v[32:33]
v_lshlrev_b32 v34, 16, v30
v_and_b32 v35, 0xffff0000, v30
v_pk_add_f32 v[124:125], v[124:125], v[34:35]
v_lshlrev_b32 v126, 16, v71
v_and_b32 v127, 0xffff0000, v71
v_lshlrev_b32 v32, 16, v19
v_and_b32 v33, 0xffff0000, v19
v_pk_add_f32 v[126:127], v[126:127], v[32:33]
v_lshlrev_b32 v34, 16, v23
v_and_b32 v35, 0xffff0000, v23
v_pk_add_f32 v[126:127], v[126:127], v[34:35]
v_lshlrev_b32 v32, 16, v27
v_and_b32 v33, 0xffff0000, v27
v_pk_add_f32 v[126:127], v[126:127], v[32:33]
v_lshlrev_b32 v34, 16, v31
v_and_b32 v35, 0xffff0000, v31
v_pk_add_f32 v[126:127], v[126:127], v[34:35]
v_cvt_pk_bf16_f32 v44, v120, v121
v_cvt_pk_bf16_f32 v45, v122, v123
v_cvt_pk_bf16_f32 v46, v124, v125
v_cvt_pk_bf16_f32 v47, v126, v127
s_add_u32 s27, s28, 16384
buffer_store_dwordx4 v[44:47], v103, s[16:19], s27 offen sc1
v_pk_add_f32 v[104:105], v[112:113], v[114:115]
v_pk_mul_f32 v[106:107], v[112:113], v[112:113]
v_pk_fma_f32 v[106:107], v[114:115], v[114:115], v[106:107]
v_pk_add_f32 v[104:105], v[104:105], v[116:117]
v_pk_fma_f32 v[106:107], v[116:117], v[116:117], v[106:107]
v_pk_add_f32 v[104:105], v[104:105], v[118:119]
v_pk_fma_f32 v[106:107], v[118:119], v[118:119], v[106:107]
v_pk_add_f32 v[104:105], v[104:105], v[120:121]
v_pk_fma_f32 v[106:107], v[120:121], v[120:121], v[106:107]
v_pk_add_f32 v[104:105], v[104:105], v[122:123]
v_pk_fma_f32 v[106:107], v[122:123], v[122:123], v[106:107]
v_pk_add_f32 v[104:105], v[104:105], v[124:125]
v_pk_fma_f32 v[106:107], v[124:125], v[124:125], v[106:107]
v_pk_add_f32 v[104:105], v[104:105], v[126:127]
v_pk_fma_f32 v[106:107], v[126:127], v[126:127], v[106:107]
v_add_f32 v104, v104, v105
v_add_f32 v105, v106, v107
s_nop 1
v_add_f32_dpp v104, v104, v104 row_ror:8 row_mask:0xf bank_mask:0xf
v_add_f32_dpp v105, v105, v105 row_ror:8 row_mask:0xf bank_mask:0xf
v_mov_b32 v108, v104
v_mov_b32 v109, v105
s_nop 1
v_permlane16_swap_b32 v108, v104
v_permlane16_swap_b32 v109, v105
v_add_f32 v104, v104, v108
v_add_f32 v105, v105, v109
v_mov_b32 v108, v104
v_mov_b32 v109, v105
s_nop 1
v_permlane32_swap_b32 v108, v104
v_permlane32_swap_b32 v109, v105
v_add_f32 v104, v104, v108
v_add_f32 v105, v105, v109
s_lshl_b32 s27, s7, 6
s_add_u32 s27, s27, 131072
v_lshlrev_b32 v102, 3, v100
v_add_u32 v98, s27, v102
v_add_u32 v102, 131072, v102
ds_write_b64 v98, v[104:105]
s_waitcnt lgkmcnt(0)
s_barrier
ds_read_b64 v[80:81], v102 offset:0
ds_read_b64 v[82:83], v102 offset:64
ds_read_b64 v[84:85], v102 offset:128
ds_read_b64 v[86:87], v102 offset:192
s_waitcnt lgkmcnt(0)
v_pk_add_f32 v[80:81], v[80:81], v[82:83]
v_pk_add_f32 v[84:85], v[84:85], v[86:87]
v_pk_add_f32 v[80:81], v[80:81], v[84:85]
v_mul_f32 v80, 0x3b000000, v80
v_mul_f32 v81, 0x3b000000, v81
v_fma_f32 v81, -v80, v80, v81
v_max_f32 v81, 0, v81
v_add_f32 v81, 0x3727c5ac, v81
v_rsq_f32 v82, v81
s_nop 0
v_pk_add_f32 v[112:113], v[112:113], v[80:81] op_sel_hi:[1,0] neg_lo:[0,1] neg_hi:[0,1]
v_pk_mul_f32 v[112:113], v[112:113], v[82:83] op_sel_hi:[1,0]
v_pk_add_f32 v[114:115], v[114:115], v[80:81] op_sel_hi:[1,0] neg_lo:[0,1] neg_hi:[0,1]
v_pk_mul_f32 v[114:115], v[114:115], v[82:83] op_sel_hi:[1,0]
v_pk_add_f32 v[116:117], v[116:117], v[80:81] op_sel_hi:[1,0] neg_lo:[0,1] neg_hi:[0,1]
v_pk_mul_f32 v[116:117], v[116:117], v[82:83] op_sel_hi:[1,0]
v_pk_add_f32 v[118:119], v[118:119], v[80:81] op_sel_hi:[1,0] neg_lo:[0,1] neg_hi:[0,1]
v_pk_mul_f32 v[118:119], v[118:119], v[82:83] op_sel_hi:[1,0]
v_pk_add_f32 v[120:121], v[120:121], v[80:81] op_sel_hi:[1,0] neg_lo:[0,1] neg_hi:[0,1]
v_pk_mul_f32 v[120:121], v[120:121], v[82:83] op_sel_hi:[1,0]
v_pk_add_f32 v[122:123], v[122:123], v[80:81] op_sel_hi:[1,0] neg_lo:[0,1] neg_hi:[0,1]
v_pk_mul_f32 v[122:123], v[122:123], v[82:83] op_sel_hi:[1,0]
v_pk_add_f32 v[124:125], v[124:125], v[80:81] op_sel_hi:[1,0] neg_lo:[0,1] neg_hi:[0,1]
v_pk_mul_f32 v[124:125], v[124:125], v[82:83] op_sel_hi:[1,0]
v_pk_add_f32 v[126:127], v[126:127], v[80:81] op_sel_hi:[1,0] neg_lo:[0,1] neg_hi:[0,1]
v_pk_mul_f32 v[126:127], v[126:127], v[82:83] op_sel_hi:[1,0]
v_cvt_pk_bf16_f32 v48, v112, v113
v_cvt_pk_bf16_f32 v49, v114, v115
v_cvt_pk_bf16_f32 v50, v116, v117
v_cvt_pk_bf16_f32 v51, v118, v119
s_add_u32 s27, s28, 0
buffer_store_dwordx4 v[48:51], v103, s[20:23], s27 offen sc1
v_cvt_pk_bf16_f32 v52, v120, v121
v_cvt_pk_bf16_f32 v53, v122, v123
v_cvt_pk_bf16_f32 v54, v124, v125
v_cvt_pk_bf16_f32 v55, v126, v127
s_add_u32 s27, s28, 16384
buffer_store_dwordx4 v[52:55], v103, s[20:23], s27 offen sc1
s_waitcnt vmcnt(0)
s_barrier
s_lshl_b32 s46, s41, 7
s_lshl_b32 s47, s48, 3
s_add_u32 s46, s46, s47
s_add_u32 s46, s46, 20480
s_cmp_eq_u32 s7, 0
s_cbranch_scc0 Lpub_48
v_mov_b32 v0, s84
v_mov_b32 v1, s85
v_mov_b32 v2, 0
s_mov_b64 exec, 1
buffer_store_dwordx2 v[0:1], v2, s[88:91], s46 offen sc1
s_nop 0
s_mov_b64 exec, -1
Lpub_48:
s_cmp_lt_u32 s48, 14
s_cbranch_scc0 PD_SKIP
v_mbcnt_lo_u32_b32 v250, -1, 0
v_mbcnt_hi_u32_b32 v250, -1, v250
s_add_u32 s8, s82, 0x4a00000
s_addc_u32 s9, s83, 0
s_and_b32 s9, s9, 0xffff
s_mov_b32 s10, 0x200000
s_mov_b32 s11, 0x20000
s_add_u32 s16, s82, 0x5000000
s_addc_u32 s17, s83, 0
s_and_b32 s17, s17, 0xffff
s_mov_b32 s18, 0x700000
s_mov_b32 s19, 0x20000
s_lshl_b32 s28, s41, 17
v_lshlrev_b32 v251, 4, v250
v_bfe_u32 v0, v250, 4, 1
v_lshlrev_b32 v1, 10, v0
v_lshrrev_b32 v2, 5, v250
v_lshl_add_u32 v1, v2, 8, v1
v_and_b32 v245, 15, v250
v_lshl_add_u32 v245, v245, 4, v1
v_xor_b32 v0, v0, v2
v_lshlrev_b32 v0, 7, v0
v_mul_u32_u24 v1, 1024, v2
v_add_u32 v1, v1, v0
v_bfe_u32 v2, v250, 2, 2
v_lshl_add_u32 v1, v2, 5, v1
v_and_b32 v2, 3, v250
v_lshl_add_u32 v244, v2, 3, v1
s_lshl_b32 s26, s7, 11
s_add_u32 s28, s28, s26
v_and_b32 v200, 15, v250
v_lshlrev_b32 v200, 3, v200
s_lshl_b32 s46, s41, 7
s_add_u32 s46, s46, 20480
s_cmp_eq_u32 s7, 0
s_cbranch_scc0 Lp0_49
s_mov_b32 s86, 0
Lpoll_50:
buffer_load_dwordx2 v[202:203], v200, s[88:91], s46 offen sc1
s_waitcnt vmcnt(0)
v_cmp_eq_u64 vcc, s[84:85], v[202:203]
s_nop 1
s_cmp_eq_u64 vcc, exec
s_cbranch_scc1 Lpdone_51
s_sleep 1
s_add_u32 s86, s86, 1
s_cmp_lt_u32 s86, 0x40000
s_cbranch_scc1 Lpoll_50
Lpdone_51:
Lp0_49:
s_barrier
s_cmp_eq_u32 s7, 0
s_cbranch_scc1 Lw0_52
buffer_load_dwordx4 a[128:131], v245, s[8:11], s28 offen offset:0 sc1
buffer_load_dwordx4 a[132:135], v245, s[8:11], s28 offen offset:512 sc1
s_add_u32 s27, s28, 8192
s_nop 0
buffer_load_dwordx4 a[136:139], v245, s[8:11], s27 offen offset:0 sc1
s_add_u32 s27, s28, 8192
s_nop 0
buffer_load_dwordx4 a[140:143], v245, s[8:11], s27 offen offset:512 sc1
s_add_u32 s28, s28, 16384
buffer_load_dwordx4 a[144:147], v245, s[8:11], s28 offen offset:0 sc1
buffer_load_dwordx4 a[148:151], v245, s[8:11], s28 offen offset:512 sc1
s_add_u32 s27, s28, 8192
s_nop 0
buffer_load_dwordx4 a[152:155], v245, s[8:11], s27 offen offset:0 sc1
s_add_u32 s27, s28, 8192
s_nop 0
buffer_load_dwordx4 a[156:159], v245, s[8:11], s27 offen offset:512 sc1
s_add_u32 s28, s28, 16384
buffer_load_dwordx4 a[160:163], v245, s[8:11], s28 offen offset:0 sc1
buffer_load_dwordx4 a[164:167], v245, s[8:11], s28 offen offset:512 sc1
s_add_u32 s27, s28, 8192
s_nop 0
buffer_load_dwordx4 a[168:171], v245, s[8:11], s27 offen offset:0 sc1
s_add_u32 s27, s28, 8192
s_nop 0
buffer_load_dwordx4 a[172:175], v245, s[8:11], s27 offen offset:512 sc1
s_add_u32 s28, s28, 16384
buffer_load_dwordx4 a[176:179], v245, s[8:11], s28 offen offset:0 sc1
buffer_load_dwordx4 a[180:183], v245, s[8:11], s28 offen offset:512 sc1
s_add_u32 s27, s28, 8192
s_nop 0
buffer_load_dwordx4 a[184:187], v245, s[8:11], s27 offen offset:0 sc1
s_add_u32 s27, s28, 8192
s_nop 0
buffer_load_dwordx4 a[188:191], v245, s[8:11], s27 offen offset:512 sc1
s_add_u32 s28, s28, 16384
s_branch Lw0_53
Lw0_52:
s_add_u32 m0, s57, 0
s_nop 0
buffer_load_dwordx4 v246, s[52:55], s56 offen offset:0 lds
buffer_load_dwordx4 v247, s[52:55], s56 offen offset:1024 lds
buffer_load_dwordx4 v248, s[52:55], s56 offen offset:2048 lds
buffer_load_dwordx4 v249, s[52:55], s56 offen offset:3072 lds
s_add_u32 s56, s56, 229376
s_add_u32 m0, s57, 16384
s_nop 0
buffer_load_dwordx4 v246, s[52:55], s56 offen offset:0 lds
buffer_load_dwordx4 v247, s[52:55], s56 offen offset:1024 lds
buffer_load_dwordx4 v248, s[52:55], s56 offen offset:2048 lds
buffer_load_dwordx4 v249, s[52:55], s56 offen offset:3072 lds
s_add_u32 s56, s56, 229376
s_add_u32 s56, s56, 917504
buffer_load_dwordx4 a[128:131], v245, s[8:11], s28 offen offset:0 sc1
buffer_load_dwordx4 a[132:135], v245, s[8:11], s28 offen offset:512 sc1
s_add_u32 s27, s28, 8192
s_nop 0
buffer_load_dwordx4 a[136:139], v245, s[8:11], s27 offen offset:0 sc1
s_add_u32 s27, s28, 8192
s_nop 0
buffer_load_dwordx4 a[140:143], v245, s[8:11], s27 offen offset:512 sc1
s_add_u32 s28, s28, 16384
buffer_load_dwordx4 a[144:147], v245, s[8:11], s28 offen offset:0 sc1
buffer_load_dwordx4 a[148:151], v245, s[8:11], s28 offen offset:512 sc1
s_add_u32 s27, s28, 8192
s_nop 0
buffer_load_dwordx4 a[152:155], v245, s[8:11], s27 offen offset:0 sc1
s_add_u32 s27, s28, 8192
s_nop 0
buffer_load_dwordx4 a[156:159], v245, s[8:11], s27 offen offset:512 sc1
s_add_u32 s28, s28, 16384
buffer_load_dwordx4 a[160:163], v245, s[8:11], s28 offen offset:0 sc1
buffer_load_dwordx4 a[164:167], v245, s[8:11], s28 offen offset:512 sc1
s_add_u32 s27, s28, 8192
s_nop 0
buffer_load_dwordx4 a[168:171], v245, s[8:11], s27 offen offset:0 sc1
s_add_u32 s27, s28, 8192
s_nop 0
buffer_load_dwordx4 a[172:175], v245, s[8:11], s27 offen offset:512 sc1
s_add_u32 s28, s28, 16384
buffer_load_dwordx4 a[176:179], v245, s[8:11], s28 offen offset:0 sc1
buffer_load_dwordx4 a[180:183], v245, s[8:11], s28 offen offset:512 sc1
s_add_u32 s27, s28, 8192
s_nop 0
buffer_load_dwordx4 a[184:187], v245, s[8:11], s27 offen offset:0 sc1
s_add_u32 s27, s28, 8192
s_nop 0
buffer_load_dwordx4 a[188:191], v245, s[8:11], s27 offen offset:512 sc1
s_add_u32 s28, s28, 16384
Lw0_53:
s_waitcnt vmcnt(12)
s_barrier
ds_read_b64_tr_b16 a[0:1], v244 offset:0
ds_read_b64_tr_b16 a[2:3], v244 offset:4096
ds_read_b64_tr_b16 a[4:5], v244 offset:256
ds_read_b64_tr_b16 a[6:7], v244 offset:4352
ds_read_b64_tr_b16 a[8:9], v244 offset:512
ds_read_b64_tr_b16 a[10:11], v244 offset:4608
ds_read_b64_tr_b16 a[12:13], v244 offset:768
ds_read_b64_tr_b16 a[14:15], v244 offset:4864
ds_read_b64_tr_b16 a[16:17], v244 offset:2048
ds_read_b64_tr_b16 a[18:19], v244 offset:6144
ds_read_b64_tr_b16 a[20:21], v244 offset:2304
ds_read_b64_tr_b16 a[22:23], v244 offset:6400
ds_read_b64_tr_b16 a[24:25], v244 offset:2560
ds_read_b64_tr_b16 a[26:27], v244 offset:6656
ds_read_b64_tr_b16 a[28:29], v244 offset:2816
ds_read_b64_tr_b16 a[30:31], v244 offset:6912
ds_read_b64_tr_b16 a[32:33], v244 offset:8192
ds_read_b64_tr_b16 a[34:35], v244 offset:12288
ds_read_b64_tr_b16 a[36:37], v244 offset:8448
ds_read_b64_tr_b16 a[38:39], v244 offset:12544
ds_read_b64_tr_b16 a[40:41], v244 offset:8704
ds_read_b64_tr_b16 a[42:43], v244 offset:12800
ds_read_b64_tr_b16 a[44:45], v244 offset:8960
ds_read_b64_tr_b16 a[46:47], v244 offset:13056
ds_read_b64_tr_b16 a[48:49], v244 offset:10240
ds_read_b64_tr_b16 a[50:51], v244 offset:14336
ds_read_b64_tr_b16 a[52:53], v244 offset:10496
ds_read_b64_tr_b16 a[54:55], v244 offset:14592
ds_read_b64_tr_b16 a[56:57], v244 offset:10752
ds_read_b64_tr_b16 a[58:59], v244 offset:14848
ds_read_b64_tr_b16 a[60:61], v244 offset:11008
ds_read_b64_tr_b16 a[62:63], v244 offset:15104
v_add_u32 v244, 0x4000, v244
s_waitcnt vmcnt(12) lgkmcnt(0)
s_barrier
v_mfma_f32_32x32x16_bf16 v[0:15], a[0:3], a[128:131], 0
ds_read_b64_tr_b16 a[64:65], v244 offset:0
ds_read_b64_tr_b16 a[66:67], v244 offset:4096
ds_read_b64_tr_b16 a[68:69], v244 offset:256
buffer_load_dwordx4 a[192:195], v245, s[8:11], s28 offen offset:0 sc1
v_mfma_f32_32x32x16_bf16 v[16:31], a[4:7], a[128:131], 0
ds_read_b64_tr_b16 a[70:71], v244 offset:4352
ds_read_b64_tr_b16 a[72:73], v244 offset:512
ds_read_b64_tr_b16 a[74:75], v244 offset:4608
v_mfma_f32_32x32x16_bf16 v[32:47], a[8:11], a[128:131], 0
ds_read_b64_tr_b16 a[76:77], v244 offset:768
ds_read_b64_tr_b16 a[78:79], v244 offset:4864
v_mfma_f32_32x32x16_bf16 v[48:63], a[12:15], a[128:131], 0
ds_read_b64_tr_b16 a[80:81], v244 offset:2048
ds_read_b64_tr_b16 a[82:83], v244 offset:6144
ds_read_b64_tr_b16 a[84:85], v244 offset:2304
v_mfma_f32_32x32x16_bf16 v[0:15], a[16:19], a[132:135], v[0:15]
ds_read_b64_tr_b16 a[86:87], v244 offset:6400
ds_read_b64_tr_b16 a[88:89], v244 offset:2560
ds_read_b64_tr_b16 a[90:91], v244 offset:6656
buffer_load_dwordx4 a[196:199], v245, s[8:11], s28 offen offset:512 sc1
v_mfma_f32_32x32x16_bf16 v[16:31], a[20:23], a[132:135], v[16:31]
ds_read_b64_tr_b16 a[92:93], v244 offset:2816
ds_read_b64_tr_b16 a[94:95], v244 offset:6912
v_mfma_f32_32x32x16_bf16 v[32:47], a[24:27], a[132:135], v[32:47]
ds_read_b64_tr_b16 a[96:97], v244 offset:8192
ds_read_b64_tr_b16 a[98:99], v244 offset:12288
ds_read_b64_tr_b16 a[100:101], v244 offset:8448
v_mfma_f32_32x32x16_bf16 v[48:63], a[28:31], a[132:135], v[48:63]
ds_read_b64_tr_b16 a[102:103], v244 offset:12544
ds_read_b64_tr_b16 a[104:105], v244 offset:8704
ds_read_b64_tr_b16 a[106:107], v244 offset:12800
v_mfma_f32_32x32x16_bf16 v[0:15], a[32:35], a[136:139], v[0:15]
ds_read_b64_tr_b16 a[108:109], v244 offset:8960
ds_read_b64_tr_b16 a[110:111], v244 offset:13056
s_add_u32 s27, s28, 8192
buffer_load_dwordx4 a[200:203], v245, s[8:11], s27 offen offset:0 sc1
v_mfma_f32_32x32x16_bf16 v[16:31], a[36:39], a[136:139], v[16:31]
ds_read_b64_tr_b16 a[112:113], v244 offset:10240
ds_read_b64_tr_b16 a[114:115], v244 offset:14336
ds_read_b64_tr_b16 a[116:117], v244 offset:10496
v_mfma_f32_32x32x16_bf16 v[32:47], a[40:43], a[136:139], v[32:47]
ds_read_b64_tr_b16 a[118:119], v244 offset:14592
ds_read_b64_tr_b16 a[120:121], v244 offset:10752
ds_read_b64_tr_b16 a[122:123], v244 offset:14848
v_mfma_f32_32x32x16_bf16 v[48:63], a[44:47], a[136:139], v[48:63]
ds_read_b64_tr_b16 a[124:125], v244 offset:11008
ds_read_b64_tr_b16 a[126:127], v244 offset:15104
v_mfma_f32_32x32x16_bf16 v[0:15], a[48:51], a[140:143], v[0:15]
s_add_u32 s27, s28, 8192
buffer_load_dwordx4 a[204:207], v245, s[8:11], s27 offen offset:512 sc1
v_mfma_f32_32x32x16_bf16 v[16:31], a[52:55], a[140:143], v[16:31]
v_mfma_f32_32x32x16_bf16 v[32:47], a[56:59], a[140:143], v[32:47]
v_mfma_f32_32x32x16_bf16 v[48:63], a[60:63], a[140:143], v[48:63]
s_add_u32 s28, s28, 16384
v_add_u32 v244, 0x4000, v244
s_waitcnt vmcnt(12) lgkmcnt(0)
v_mfma_f32_32x32x16_bf16 v[0:15], a[64:67], a[144:147], v[0:15]
ds_read_b64_tr_b16 a[0:1], v244 offset:0
ds_read_b64_tr_b16 a[2:3], v244 offset:4096
ds_read_b64_tr_b16 a[4:5], v244 offset:256
buffer_load_dwordx4 a[128:131], v245, s[8:11], s28 offen offset:0 sc1
v_mfma_f32_32x32x16_bf16 v[16:31], a[68:71], a[144:147], v[16:31]
ds_read_b64_tr_b16 a[6:7], v244 offset:4352
ds_read_b64_tr_b16 a[8:9], v244 offset:512
ds_read_b64_tr_b16 a[10:11], v244 offset:4608
v_mfma_f32_32x32x16_bf16 v[32:47], a[72:75], a[144:147], v[32:47]
ds_read_b64_tr_b16 a[12:13], v244 offset:768
ds_read_b64_tr_b16 a[14:15], v244 offset:4864
buffer_load_dwordx4 a[132:135], v245, s[8:11], s28 offen offset:512 sc1
v_mfma_f32_32x32x16_bf16 v[48:63], a[76:79], a[144:147], v[48:63]
ds_read_b64_tr_b16 a[16:17], v244 offset:2048
ds_read_b64_tr_b16 a[18:19], v244 offset:6144
ds_read_b64_tr_b16 a[20:21], v244 offset:2304
v_mfma_f32_32x32x16_bf16 v[0:15], a[80:83], a[148:151], v[0:15]
ds_read_b64_tr_b16 a[22:23], v244 offset:6400
ds_read_b64_tr_b16 a[24:25], v244 offset:2560
ds_read_b64_tr_b16 a[26:27], v244 offset:6656
s_add_u32 s27, s28, 8192
buffer_load_dwordx4 a[136:139], v245, s[8:11], s27 offen offset:0 sc1
v_mfma_f32_32x32x16_bf16 v[16:31], a[84:87], a[148:151], v[16:31]
ds_read_b64_tr_b16 a[28:29], v244 offset:2816
ds_read_b64_tr_b16 a[30:31], v244 offset:6912
v_mfma_f32_32x32x16_bf16 v[32:47], a[88:91], a[148:151], v[32:47]
ds_read_b64_tr_b16 a[32:33], v244 offset:8192
ds_read_b64_tr_b16 a[34:35], v244 offset:12288
ds_read_b64_tr_b16 a[36:37], v244 offset:8448
s_add_u32 s27, s28, 8192
buffer_load_dwordx4 a[140:143], v245, s[8:11], s27 offen offset:512 sc1
v_mfma_f32_32x32x16_bf16 v[48:63], a[92:95], a[148:151], v[48:63]
ds_read_b64_tr_b16 a[38:39], v244 offset:12544
ds_read_b64_tr_b16 a[40:41], v244 offset:8704
ds_read_b64_tr_b16 a[42:43], v244 offset:12800
v_mfma_f32_32x32x16_bf16 v[0:15], a[96:99], a[152:155], v[0:15]
ds_read_b64_tr_b16 a[44:45], v244 offset:8960
ds_read_b64_tr_b16 a[46:47], v244 offset:13056
s_add_u32 m0, s57, 0
s_nop 0
buffer_load_dwordx4 v246, s[52:55], s56 offen offset:0 lds
v_mfma_f32_32x32x16_bf16 v[16:31], a[100:103], a[152:155], v[16:31]
ds_read_b64_tr_b16 a[48:49], v244 offset:10240
ds_read_b64_tr_b16 a[50:51], v244 offset:14336
ds_read_b64_tr_b16 a[52:53], v244 offset:10496
v_mfma_f32_32x32x16_bf16 v[32:47], a[104:107], a[152:155], v[32:47]
ds_read_b64_tr_b16 a[54:55], v244 offset:14592
ds_read_b64_tr_b16 a[56:57], v244 offset:10752
ds_read_b64_tr_b16 a[58:59], v244 offset:14848
buffer_load_dwordx4 v247, s[52:55], s56 offen offset:1024 lds
v_mfma_f32_32x32x16_bf16 v[48:63], a[108:111], a[152:155], v[48:63]
ds_read_b64_tr_b16 a[60:61], v244 offset:11008
ds_read_b64_tr_b16 a[62:63], v244 offset:15104
v_mfma_f32_32x32x16_bf16 v[0:15], a[112:115], a[156:159], v[0:15]
buffer_load_dwordx4 v248, s[52:55], s56 offen offset:2048 lds
v_mfma_f32_32x32x16_bf16 v[16:31], a[116:119], a[156:159], v[16:31]
v_mfma_f32_32x32x16_bf16 v[32:47], a[120:123], a[156:159], v[32:47]
buffer_load_dwordx4 v249, s[52:55], s56 offen offset:3072 lds
v_mfma_f32_32x32x16_bf16 v[48:63], a[124:127], a[156:159], v[48:63]
s_add_u32 s28, s28, 16384
s_add_u32 s56, s56, 229376
v_add_u32 v244, 0x4000, v244
s_waitcnt vmcnt(16) lgkmcnt(0)
s_barrier
v_mfma_f32_32x32x16_bf16 v[0:15], a[0:3], a[160:163], v[0:15]
ds_read_b64_tr_b16 a[64:65], v244 offset:0
ds_read_b64_tr_b16 a[66:67], v244 offset:4096
ds_read_b64_tr_b16 a[68:69], v244 offset:256
buffer_load_dwordx4 a[144:147], v245, s[8:11], s28 offen offset:0 sc1
v_mfma_f32_32x32x16_bf16 v[16:31], a[4:7], a[160:163], v[16:31]
ds_read_b64_tr_b16 a[70:71], v244 offset:4352
ds_read_b64_tr_b16 a[72:73], v244 offset:512
ds_read_b64_tr_b16 a[74:75], v244 offset:4608
v_mfma_f32_32x32x16_bf16 v[32:47], a[8:11], a[160:163], v[32:47]
ds_read_b64_tr_b16 a[76:77], v244 offset:768
ds_read_b64_tr_b16 a[78:79], v244 offset:4864
buffer_load_dwordx4 a[148:151], v245, s[8:11], s28 offen offset:512 sc1
v_mfma_f32_32x32x16_bf16 v[48:63], a[12:15], a[160:163], v[48:63]
ds_read_b64_tr_b16 a[80:81], v244 offset:2048
ds_read_b64_tr_b16 a[82:83], v244 offset:6144
ds_read_b64_tr_b16 a[84:85], v244 offset:2304
v_mfma_f32_32x32x16_bf16 v[0:15], a[16:19], a[164:167], v[0:15]
ds_read_b64_tr_b16 a[86:87], v244 offset:6400
ds_read_b64_tr_b16 a[88:89], v244 offset:2560
ds_read_b64_tr_b16 a[90:91], v244 offset:6656
s_add_u32 s27, s28, 8192
buffer_load_dwordx4 a[152:155], v245, s[8:11], s27 offen offset:0 sc1
v_mfma_f32_32x32x16_bf16 v[16:31], a[20:23], a[164:167], v[16:31]
ds_read_b64_tr_b16 a[92:93], v244 offset:2816
ds_read_b64_tr_b16 a[94:95], v244 offset:6912
v_mfma_f32_32x32x16_bf16 v[32:47], a[24:27], a[164:167], v[32:47]
ds_read_b64_tr_b16 a[96:97], v244 offset:8192
ds_read_b64_tr_b16 a[98:99], v244 offset:12288
ds_read_b64_tr_b16 a[100:101], v244 offset:8448
s_add_u32 s27, s28, 8192
buffer_load_dwordx4 a[156:159], v245, s[8:11], s27 offen offset:512 sc1
v_mfma_f32_32x32x16_bf16 v[48:63], a[28:31], a[164:167], v[48:63]
ds_read_b64_tr_b16 a[102:103], v244 offset:12544
ds_read_b64_tr_b16 a[104:105], v244 offset:8704
ds_read_b64_tr_b16 a[106:107], v244 offset:12800
v_mfma_f32_32x32x16_bf16 v[0:15], a[32:35], a[168:171], v[0:15]
ds_read_b64_tr_b16 a[108:109], v244 offset:8960
ds_read_b64_tr_b16 a[110:111], v244 offset:13056
s_add_u32 m0, s57, 16384
s_nop 0
buffer_load_dwordx4 v246, s[52:55], s56 offen offset:0 lds
v_mfma_f32_32x32x16_bf16 v[16:31], a[36:39], a[168:171], v[16:31]
ds_read_b64_tr_b16 a[112:113], v244 offset:10240
ds_read_b64_tr_b16 a[114:115], v244 offset:14336
ds_read_b64_tr_b16 a[116:117], v244 offset:10496
v_mfma_f32_32x32x16_bf16 v[32:47], a[40:43], a[168:171], v[32:47]
ds_read_b64_tr_b16 a[118:119], v244 offset:14592
ds_read_b64_tr_b16 a[120:121], v244 offset:10752
ds_read_b64_tr_b16 a[122:123], v244 offset:14848
buffer_load_dwordx4 v247, s[52:55], s56 offen offset:1024 lds
v_mfma_f32_32x32x16_bf16 v[48:63], a[44:47], a[168:171], v[48:63]
ds_read_b64_tr_b16 a[124:125], v244 offset:11008
ds_read_b64_tr_b16 a[126:127], v244 offset:15104
v_mfma_f32_32x32x16_bf16 v[0:15], a[48:51], a[172:175], v[0:15]
buffer_load_dwordx4 v248, s[52:55], s56 offen offset:2048 lds
v_mfma_f32_32x32x16_bf16 v[16:31], a[52:55], a[172:175], v[16:31]
v_mfma_f32_32x32x16_bf16 v[32:47], a[56:59], a[172:175], v[32:47]
buffer_load_dwordx4 v249, s[52:55], s56 offen offset:3072 lds
v_mfma_f32_32x32x16_bf16 v[48:63], a[60:63], a[172:175], v[48:63]
s_add_u32 s28, s28, 16384
s_add_u32 s56, s56, 229376
v_add_u32 v244, 0x4000, v244
s_waitcnt vmcnt(20) lgkmcnt(0)
v_mfma_f32_32x32x16_bf16 v[0:15], a[64:67], a[176:179], v[0:15]
ds_read_b64_tr_b16 a[0:1], v244 offset:0
ds_read_b64_tr_b16 a[2:3], v244 offset:4096
ds_read_b64_tr_b16 a[4:5], v244 offset:256
buffer_load_dwordx4 a[160:163], v245, s[8:11], s28 offen offset:0 sc1
v_mfma_f32_32x32x16_bf16 v[16:31], a[68:71], a[176:179], v[16:31]
ds_read_b64_tr_b16 a[6:7], v244 offset:4352
ds_read_b64_tr_b16 a[8:9], v244 offset:512
ds_read_b64_tr_b16 a[10:11], v244 offset:4608
v_mfma_f32_32x32x16_bf16 v[32:47], a[72:75], a[176:179], v[32:47]
ds_read_b64_tr_b16 a[12:13], v244 offset:768
ds_read_b64_tr_b16 a[14:15], v244 offset:4864
v_mfma_f32_32x32x16_bf16 v[48:63], a[76:79], a[176:179], v[48:63]
ds_read_b64_tr_b16 a[16:17], v244 offset:2048
ds_read_b64_tr_b16 a[18:19], v244 offset:6144
ds_read_b64_tr_b16 a[20:21], v244 offset:2304
v_mfma_f32_32x32x16_bf16 v[0:15], a[80:83], a[180:183], v[0:15]
ds_read_b64_tr_b16 a[22:23], v244 offset:6400
ds_read_b64_tr_b16 a[24:25], v244 offset:2560
ds_read_b64_tr_b16 a[26:27], v244 offset:6656
)ASM"
R"ASM(buffer_load_dwordx4 a[164:167], v245, s[8:11], s28 offen offset:512 sc1
v_mfma_f32_32x32x16_bf16 v[16:31], a[84:87], a[180:183], v[16:31]
ds_read_b64_tr_b16 a[28:29], v244 offset:2816
ds_read_b64_tr_b16 a[30:31], v244 offset:6912
v_mfma_f32_32x32x16_bf16 v[32:47], a[88:91], a[180:183], v[32:47]
ds_read_b64_tr_b16 a[32:33], v244 offset:8192
ds_read_b64_tr_b16 a[34:35], v244 offset:12288
ds_read_b64_tr_b16 a[36:37], v244 offset:8448
v_mfma_f32_32x32x16_bf16 v[48:63], a[92:95], a[180:183], v[48:63]
ds_read_b64_tr_b16 a[38:39], v244 offset:12544
ds_read_b64_tr_b16 a[40:41], v244 offset:8704
ds_read_b64_tr_b16 a[42:43], v244 offset:12800
v_mfma_f32_32x32x16_bf16 v[0:15], a[96:99], a[184:187], v[0:15]
ds_read_b64_tr_b16 a[44:45], v244 offset:8960
ds_read_b64_tr_b16 a[46:47], v244 offset:13056
s_add_u32 s27, s28, 8192
buffer_load_dwordx4 a[168:171], v245, s[8:11], s27 offen offset:0 sc1
v_mfma_f32_32x32x16_bf16 v[16:31], a[100:103], a[184:187], v[16:31]
ds_read_b64_tr_b16 a[48:49], v244 offset:10240
ds_read_b64_tr_b16 a[50:51], v244 offset:14336
ds_read_b64_tr_b16 a[52:53], v244 offset:10496
v_mfma_f32_32x32x16_bf16 v[32:47], a[104:107], a[184:187], v[32:47]
ds_read_b64_tr_b16 a[54:55], v244 offset:14592
ds_read_b64_tr_b16 a[56:57], v244 offset:10752
ds_read_b64_tr_b16 a[58:59], v244 offset:14848
v_mfma_f32_32x32x16_bf16 v[48:63], a[108:111], a[184:187], v[48:63]
ds_read_b64_tr_b16 a[60:61], v244 offset:11008
ds_read_b64_tr_b16 a[62:63], v244 offset:15104
v_mfma_f32_32x32x16_bf16 v[0:15], a[112:115], a[188:191], v[0:15]
s_add_u32 s27, s28, 8192
buffer_load_dwordx4 a[172:175], v245, s[8:11], s27 offen offset:512 sc1
v_mfma_f32_32x32x16_bf16 v[16:31], a[116:119], a[188:191], v[16:31]
v_mfma_f32_32x32x16_bf16 v[32:47], a[120:123], a[188:191], v[32:47]
v_mfma_f32_32x32x16_bf16 v[48:63], a[124:127], a[188:191], v[48:63]
s_add_u32 s28, s28, 16384
v_add_u32 v244, 0x4000, v244
s_waitcnt vmcnt(12) lgkmcnt(0)
s_barrier
v_mfma_f32_32x32x16_bf16 v[0:15], a[0:3], a[192:195], v[0:15]
ds_read_b64_tr_b16 a[64:65], v244 offset:0
ds_read_b64_tr_b16 a[66:67], v244 offset:4096
ds_read_b64_tr_b16 a[68:69], v244 offset:256
v_mfma_f32_32x32x16_bf16 v[16:31], a[4:7], a[192:195], v[16:31]
ds_read_b64_tr_b16 a[70:71], v244 offset:4352
ds_read_b64_tr_b16 a[72:73], v244 offset:512
ds_read_b64_tr_b16 a[74:75], v244 offset:4608
v_mfma_f32_32x32x16_bf16 v[32:47], a[8:11], a[192:195], v[32:47]
ds_read_b64_tr_b16 a[76:77], v244 offset:768
ds_read_b64_tr_b16 a[78:79], v244 offset:4864
v_mfma_f32_32x32x16_bf16 v[48:63], a[12:15], a[192:195], v[48:63]
ds_read_b64_tr_b16 a[80:81], v244 offset:2048
ds_read_b64_tr_b16 a[82:83], v244 offset:6144
ds_read_b64_tr_b16 a[84:85], v244 offset:2304
v_mfma_f32_32x32x16_bf16 v[0:15], a[16:19], a[196:199], v[0:15]
ds_read_b64_tr_b16 a[86:87], v244 offset:6400
ds_read_b64_tr_b16 a[88:89], v244 offset:2560
ds_read_b64_tr_b16 a[90:91], v244 offset:6656
v_mfma_f32_32x32x16_bf16 v[16:31], a[20:23], a[196:199], v[16:31]
ds_read_b64_tr_b16 a[92:93], v244 offset:2816
ds_read_b64_tr_b16 a[94:95], v244 offset:6912
v_mfma_f32_32x32x16_bf16 v[32:47], a[24:27], a[196:199], v[32:47]
ds_read_b64_tr_b16 a[96:97], v244 offset:8192
ds_read_b64_tr_b16 a[98:99], v244 offset:12288
ds_read_b64_tr_b16 a[100:101], v244 offset:8448
v_mfma_f32_32x32x16_bf16 v[48:63], a[28:31], a[196:199], v[48:63]
ds_read_b64_tr_b16 a[102:103], v244 offset:12544
ds_read_b64_tr_b16 a[104:105], v244 offset:8704
ds_read_b64_tr_b16 a[106:107], v244 offset:12800
v_mfma_f32_32x32x16_bf16 v[0:15], a[32:35], a[200:203], v[0:15]
ds_read_b64_tr_b16 a[108:109], v244 offset:8960
ds_read_b64_tr_b16 a[110:111], v244 offset:13056
v_mfma_f32_32x32x16_bf16 v[16:31], a[36:39], a[200:203], v[16:31]
ds_read_b64_tr_b16 a[112:113], v244 offset:10240
ds_read_b64_tr_b16 a[114:115], v244 offset:14336
ds_read_b64_tr_b16 a[116:117], v244 offset:10496
v_mfma_f32_32x32x16_bf16 v[32:47], a[40:43], a[200:203], v[32:47]
ds_read_b64_tr_b16 a[118:119], v244 offset:14592
ds_read_b64_tr_b16 a[120:121], v244 offset:10752
ds_read_b64_tr_b16 a[122:123], v244 offset:14848
v_mfma_f32_32x32x16_bf16 v[48:63], a[44:47], a[200:203], v[48:63]
ds_read_b64_tr_b16 a[124:125], v244 offset:11008
ds_read_b64_tr_b16 a[126:127], v244 offset:15104
v_mfma_f32_32x32x16_bf16 v[0:15], a[48:51], a[204:207], v[0:15]
v_mfma_f32_32x32x16_bf16 v[16:31], a[52:55], a[204:207], v[16:31]
v_mfma_f32_32x32x16_bf16 v[32:47], a[56:59], a[204:207], v[32:47]
v_mfma_f32_32x32x16_bf16 v[48:63], a[60:63], a[204:207], v[48:63]
v_add_u32 v244, 0xfffec000, v244
s_waitcnt vmcnt(16) lgkmcnt(0)
v_mfma_f32_32x32x16_bf16 v[0:15], a[64:67], a[128:131], v[0:15]
ds_read_b64_tr_b16 a[0:1], v244 offset:0
ds_read_b64_tr_b16 a[2:3], v244 offset:4096
ds_read_b64_tr_b16 a[4:5], v244 offset:256
v_mfma_f32_32x32x16_bf16 v[16:31], a[68:71], a[128:131], v[16:31]
ds_read_b64_tr_b16 a[6:7], v244 offset:4352
ds_read_b64_tr_b16 a[8:9], v244 offset:512
ds_read_b64_tr_b16 a[10:11], v244 offset:4608
v_mfma_f32_32x32x16_bf16 v[32:47], a[72:75], a[128:131], v[32:47]
ds_read_b64_tr_b16 a[12:13], v244 offset:768
ds_read_b64_tr_b16 a[14:15], v244 offset:4864
v_mfma_f32_32x32x16_bf16 v[48:63], a[76:79], a[128:131], v[48:63]
ds_read_b64_tr_b16 a[16:17], v244 offset:2048
ds_read_b64_tr_b16 a[18:19], v244 offset:6144
ds_read_b64_tr_b16 a[20:21], v244 offset:2304
v_mfma_f32_32x32x16_bf16 v[0:15], a[80:83], a[132:135], v[0:15]
ds_read_b64_tr_b16 a[22:23], v244 offset:6400
ds_read_b64_tr_b16 a[24:25], v244 offset:2560
ds_read_b64_tr_b16 a[26:27], v244 offset:6656
v_mfma_f32_32x32x16_bf16 v[16:31], a[84:87], a[132:135], v[16:31]
ds_read_b64_tr_b16 a[28:29], v244 offset:2816
ds_read_b64_tr_b16 a[30:31], v244 offset:6912
v_mfma_f32_32x32x16_bf16 v[32:47], a[88:91], a[132:135], v[32:47]
ds_read_b64_tr_b16 a[32:33], v244 offset:8192
ds_read_b64_tr_b16 a[34:35], v244 offset:12288
ds_read_b64_tr_b16 a[36:37], v244 offset:8448
v_mfma_f32_32x32x16_bf16 v[48:63], a[92:95], a[132:135], v[48:63]
ds_read_b64_tr_b16 a[38:39], v244 offset:12544
ds_read_b64_tr_b16 a[40:41], v244 offset:8704
ds_read_b64_tr_b16 a[42:43], v244 offset:12800
v_mfma_f32_32x32x16_bf16 v[0:15], a[96:99], a[136:139], v[0:15]
ds_read_b64_tr_b16 a[44:45], v244 offset:8960
ds_read_b64_tr_b16 a[46:47], v244 offset:13056
v_mfma_f32_32x32x16_bf16 v[16:31], a[100:103], a[136:139], v[16:31]
ds_read_b64_tr_b16 a[48:49], v244 offset:10240
ds_read_b64_tr_b16 a[50:51], v244 offset:14336
ds_read_b64_tr_b16 a[52:53], v244 offset:10496
v_mfma_f32_32x32x16_bf16 v[32:47], a[104:107], a[136:139], v[32:47]
ds_read_b64_tr_b16 a[54:55], v244 offset:14592
ds_read_b64_tr_b16 a[56:57], v244 offset:10752
ds_read_b64_tr_b16 a[58:59], v244 offset:14848
v_mfma_f32_32x32x16_bf16 v[48:63], a[108:111], a[136:139], v[48:63]
ds_read_b64_tr_b16 a[60:61], v244 offset:11008
ds_read_b64_tr_b16 a[62:63], v244 offset:15104
v_mfma_f32_32x32x16_bf16 v[0:15], a[112:115], a[140:143], v[0:15]
v_mfma_f32_32x32x16_bf16 v[16:31], a[116:119], a[140:143], v[16:31]
v_mfma_f32_32x32x16_bf16 v[32:47], a[120:123], a[140:143], v[32:47]
v_mfma_f32_32x32x16_bf16 v[48:63], a[124:127], a[140:143], v[48:63]
v_add_u32 v244, 0x4000, v244
s_waitcnt vmcnt(4) lgkmcnt(0)
s_barrier
v_mfma_f32_32x32x16_bf16 v[0:15], a[0:3], a[144:147], v[0:15]
ds_read_b64_tr_b16 a[64:65], v244 offset:0
ds_read_b64_tr_b16 a[66:67], v244 offset:4096
ds_read_b64_tr_b16 a[68:69], v244 offset:256
v_mfma_f32_32x32x16_bf16 v[16:31], a[4:7], a[144:147], v[16:31]
ds_read_b64_tr_b16 a[70:71], v244 offset:4352
ds_read_b64_tr_b16 a[72:73], v244 offset:512
ds_read_b64_tr_b16 a[74:75], v244 offset:4608
v_mfma_f32_32x32x16_bf16 v[32:47], a[8:11], a[144:147], v[32:47]
ds_read_b64_tr_b16 a[76:77], v244 offset:768
ds_read_b64_tr_b16 a[78:79], v244 offset:4864
v_mfma_f32_32x32x16_bf16 v[48:63], a[12:15], a[144:147], v[48:63]
ds_read_b64_tr_b16 a[80:81], v244 offset:2048
ds_read_b64_tr_b16 a[82:83], v244 offset:6144
ds_read_b64_tr_b16 a[84:85], v244 offset:2304
v_mfma_f32_32x32x16_bf16 v[0:15], a[16:19], a[148:151], v[0:15]
ds_read_b64_tr_b16 a[86:87], v244 offset:6400
ds_read_b64_tr_b16 a[88:89], v244 offset:2560
ds_read_b64_tr_b16 a[90:91], v244 offset:6656
v_mfma_f32_32x32x16_bf16 v[16:31], a[20:23], a[148:151], v[16:31]
ds_read_b64_tr_b16 a[92:93], v244 offset:2816
ds_read_b64_tr_b16 a[94:95], v244 offset:6912
v_mfma_f32_32x32x16_bf16 v[32:47], a[24:27], a[148:151], v[32:47]
ds_read_b64_tr_b16 a[96:97], v244 offset:8192
ds_read_b64_tr_b16 a[98:99], v244 offset:12288
ds_read_b64_tr_b16 a[100:101], v244 offset:8448
v_mfma_f32_32x32x16_bf16 v[48:63], a[28:31], a[148:151], v[48:63]
ds_read_b64_tr_b16 a[102:103], v244 offset:12544
ds_read_b64_tr_b16 a[104:105], v244 offset:8704
ds_read_b64_tr_b16 a[106:107], v244 offset:12800
v_mfma_f32_32x32x16_bf16 v[0:15], a[32:35], a[152:155], v[0:15]
ds_read_b64_tr_b16 a[108:109], v244 offset:8960
ds_read_b64_tr_b16 a[110:111], v244 offset:13056
v_mfma_f32_32x32x16_bf16 v[16:31], a[36:39], a[152:155], v[16:31]
ds_read_b64_tr_b16 a[112:113], v244 offset:10240
ds_read_b64_tr_b16 a[114:115], v244 offset:14336
ds_read_b64_tr_b16 a[116:117], v244 offset:10496
v_mfma_f32_32x32x16_bf16 v[32:47], a[40:43], a[152:155], v[32:47]
ds_read_b64_tr_b16 a[118:119], v244 offset:14592
ds_read_b64_tr_b16 a[120:121], v244 offset:10752
ds_read_b64_tr_b16 a[122:123], v244 offset:14848
v_mfma_f32_32x32x16_bf16 v[48:63], a[44:47], a[152:155], v[48:63]
ds_read_b64_tr_b16 a[124:125], v244 offset:11008
ds_read_b64_tr_b16 a[126:127], v244 offset:15104
v_mfma_f32_32x32x16_bf16 v[0:15], a[48:51], a[156:159], v[0:15]
v_mfma_f32_32x32x16_bf16 v[16:31], a[52:55], a[156:159], v[16:31]
v_mfma_f32_32x32x16_bf16 v[32:47], a[56:59], a[156:159], v[32:47]
v_mfma_f32_32x32x16_bf16 v[48:63], a[60:63], a[156:159], v[48:63]
s_waitcnt vmcnt(0) lgkmcnt(0)
s_mul_i32 s43, s41, 56
s_lshl_b32 s44, s48, 2
s_add_u32 s43, s43, s44
s_lshl_b32 s43, s43, 13
s_lshl_b32 s44, s7, 11
s_add_u32 s43, s43, s44
v_mfma_f32_32x32x16_bf16 v[0:15], a[64:67], a[160:163], v[0:15]
v_mfma_f32_32x32x16_bf16 v[0:15], a[80:83], a[164:167], v[0:15]
v_mfma_f32_32x32x16_bf16 v[0:15], a[96:99], a[168:171], v[0:15]
v_mfma_f32_32x32x16_bf16 v[0:15], a[112:115], a[172:175], v[0:15]
v_mfma_f32_32x32x16_bf16 v[16:31], a[68:71], a[160:163], v[16:31]
s_nop 7
v_mfma_f32_32x32x16_bf16 v[16:31], a[84:87], a[164:167], v[16:31]
s_nop 1
v_cvt_pk_bf16_f32 v64, v0, v1
v_cvt_pk_bf16_f32 v65, v2, v3
v_cvt_pk_bf16_f32 v66, v8, v9
v_cvt_pk_bf16_f32 v67, v10, v11
v_pk_max_i16 v64, v64, 0
v_pk_max_i16 v65, v65, 0
v_pk_max_i16 v66, v66, 0
v_mfma_f32_32x32x16_bf16 v[16:31], a[100:103], a[168:171], v[16:31]
v_pk_max_i16 v67, v67, 0
s_add_u32 s44, s43, 0
buffer_store_dwordx4 v[64:67], v245, s[16:19], s44 offen offset:0 sc1
v_cvt_pk_bf16_f32 v68, v4, v5
v_cvt_pk_bf16_f32 v69, v6, v7
v_cvt_pk_bf16_f32 v70, v12, v13
v_cvt_pk_bf16_f32 v71, v14, v15
v_mfma_f32_32x32x16_bf16 v[16:31], a[116:119], a[172:175], v[16:31]
v_pk_max_i16 v68, v68, 0
v_pk_max_i16 v69, v69, 0
v_pk_max_i16 v70, v70, 0
v_pk_max_i16 v71, v71, 0
buffer_store_dwordx4 v[68:71], v245, s[16:19], s44 offen offset:512 sc1
v_mfma_f32_32x32x16_bf16 v[32:47], a[72:75], a[160:163], v[32:47]
s_nop 7
v_mfma_f32_32x32x16_bf16 v[32:47], a[88:91], a[164:167], v[32:47]
v_cvt_pk_bf16_f32 v72, v16, v17
v_cvt_pk_bf16_f32 v73, v18, v19
v_cvt_pk_bf16_f32 v74, v24, v25
v_cvt_pk_bf16_f32 v75, v26, v27
v_pk_max_i16 v72, v72, 0
v_pk_max_i16 v73, v73, 0
v_pk_max_i16 v74, v74, 0
v_mfma_f32_32x32x16_bf16 v[32:47], a[104:107], a[168:171], v[32:47]
v_pk_max_i16 v75, v75, 0
s_add_u32 s44, s43, 8192
buffer_store_dwordx4 v[72:75], v245, s[16:19], s44 offen offset:0 sc1
v_cvt_pk_bf16_f32 v76, v20, v21
v_cvt_pk_bf16_f32 v77, v22, v23
v_cvt_pk_bf16_f32 v78, v28, v29
v_cvt_pk_bf16_f32 v79, v30, v31
v_mfma_f32_32x32x16_bf16 v[32:47], a[120:123], a[172:175], v[32:47]
v_pk_max_i16 v76, v76, 0
v_pk_max_i16 v77, v77, 0
v_pk_max_i16 v78, v78, 0
v_pk_max_i16 v79, v79, 0
buffer_store_dwordx4 v[76:79], v245, s[16:19], s44 offen offset:512 sc1
v_mfma_f32_32x32x16_bf16 v[48:63], a[76:79], a[160:163], v[48:63]
s_nop 7
v_mfma_f32_32x32x16_bf16 v[48:63], a[92:95], a[164:167], v[48:63]
v_cvt_pk_bf16_f32 v80, v32, v33
v_cvt_pk_bf16_f32 v81, v34, v35
v_cvt_pk_bf16_f32 v82, v40, v41
v_cvt_pk_bf16_f32 v83, v42, v43
v_pk_max_i16 v80, v80, 0
v_pk_max_i16 v81, v81, 0
v_pk_max_i16 v82, v82, 0
v_mfma_f32_32x32x16_bf16 v[48:63], a[108:111], a[168:171], v[48:63]
v_pk_max_i16 v83, v83, 0
s_add_u32 s44, s43, 16384
buffer_store_dwordx4 v[80:83], v245, s[16:19], s44 offen offset:0 sc1
v_cvt_pk_bf16_f32 v84, v36, v37
v_cvt_pk_bf16_f32 v85, v38, v39
v_cvt_pk_bf16_f32 v86, v44, v45
v_cvt_pk_bf16_f32 v87, v46, v47
v_mfma_f32_32x32x16_bf16 v[48:63], a[124:127], a[172:175], v[48:63]
v_pk_max_i16 v84, v84, 0
v_pk_max_i16 v85, v85, 0
v_pk_max_i16 v86, v86, 0
v_pk_max_i16 v87, v87, 0
buffer_store_dwordx4 v[84:87], v245, s[16:19], s44 offen offset:512 sc1
s_nop 15
v_cvt_pk_bf16_f32 v88, v48, v49
v_cvt_pk_bf16_f32 v89, v50, v51
v_cvt_pk_bf16_f32 v90, v56, v57
v_cvt_pk_bf16_f32 v91, v58, v59
v_pk_max_i16 v88, v88, 0
v_pk_max_i16 v89, v89, 0
v_pk_max_i16 v90, v90, 0
v_pk_max_i16 v91, v91, 0
s_add_u32 s44, s43, 24576
buffer_store_dwordx4 v[88:91], v245, s[16:19], s44 offen offset:0 sc1
v_cvt_pk_bf16_f32 v92, v52, v53
v_cvt_pk_bf16_f32 v93, v54, v55
v_cvt_pk_bf16_f32 v94, v60, v61
v_cvt_pk_bf16_f32 v95, v62, v63
v_pk_max_i16 v92, v92, 0
v_pk_max_i16 v93, v93, 0
v_pk_max_i16 v94, v94, 0
v_pk_max_i16 v95, v95, 0
buffer_store_dwordx4 v[92:95], v245, s[16:19], s44 offen offset:512 sc1
s_waitcnt vmcnt(0)
s_barrier
s_lshl_b32 s46, s41, 7
s_lshl_b32 s47, s48, 3
s_add_u32 s46, s46, s47
s_add_u32 s46, s46, 24576
s_cmp_eq_u32 s7, 0
s_cbranch_scc0 Lpub_56
v_mov_b32 v0, s84
v_mov_b32 v1, s85
v_mov_b32 v2, 0
s_mov_b64 exec, 1
buffer_store_dwordx2 v[0:1], v2, s[88:91], s46 offen sc1
s_nop 0
s_mov_b64 exec, -1
Lpub_56:
v_mbcnt_lo_u32_b32 v250, -1, 0
v_mbcnt_hi_u32_b32 v250, -1, v250
s_mov_b32 s52, s78
s_and_b32 s53, s79, 0xffff
s_mov_b32 s54, 0x1c0000
s_mov_b32 s55, 0x20000
s_lshr_b32 s58, s6, 5
s_lshl_b32 s56, s58, 7
v_lshrrev_b32 v243, 3, v250
v_lshrrev_b32 v244, 2, v243
v_and_b32 v243, 3, v243
v_xor_b32 v243, v243, v244
v_lshlrev_b32 v243, 5, v243
v_lshlrev_b32 v244, 2, v244
v_bfe_u32 v245, v250, 1, 2
v_add_u32 v244, v244, v245
v_mul_u32_u24 v244, 1024, v244
v_add_u32 v243, v243, v244
v_and_b32 v244, 1, v250
v_lshl_add_u32 v246, v244, 4, v243
v_add_u32 v247, 7168, v246
v_add_u32 v248, 14336, v246
v_add_u32 v249, 21504, v246
s_mul_i32 s58, s7, 32768
s_add_u32 s56, s56, s58
s_lshl_b32 s57, s7, 12
s_cmp_lg_u32 s7, 0
s_cbranch_scc0 Low_55
s_add_u32 m0, s57, 0
s_nop 0
buffer_load_dwordx4 v246, s[52:55], s56 offen offset:0 lds
buffer_load_dwordx4 v247, s[52:55], s56 offen offset:1024 lds
buffer_load_dwordx4 v248, s[52:55], s56 offen offset:2048 lds
buffer_load_dwordx4 v249, s[52:55], s56 offen offset:3072 lds
s_add_u32 s56, s56, 131072
s_add_u32 m0, s57, 16384
s_nop 0
buffer_load_dwordx4 v246, s[52:55], s56 offen offset:0 lds
buffer_load_dwordx4 v247, s[52:55], s56 offen offset:1024 lds
buffer_load_dwordx4 v248, s[52:55], s56 offen offset:2048 lds
buffer_load_dwordx4 v249, s[52:55], s56 offen offset:3072 lds
s_add_u32 s56, s56, 131072
s_add_u32 m0, s57, 32768
s_nop 0
buffer_load_dwordx4 v246, s[52:55], s56 offen offset:0 lds
buffer_load_dwordx4 v247, s[52:55], s56 offen offset:1024 lds
buffer_load_dwordx4 v248, s[52:55], s56 offen offset:2048 lds
buffer_load_dwordx4 v249, s[52:55], s56 offen offset:3072 lds
s_add_u32 s56, s56, 131072
s_add_u32 m0, s57, 49152
s_nop 0
buffer_load_dwordx4 v246, s[52:55], s56 offen offset:0 lds
buffer_load_dwordx4 v247, s[52:55], s56 offen offset:1024 lds
buffer_load_dwordx4 v248, s[52:55], s56 offen offset:2048 lds
buffer_load_dwordx4 v249, s[52:55], s56 offen offset:3072 lds
s_add_u32 s56, s56, 131072
s_add_u32 m0, s57, 65536
s_nop 0
buffer_load_dwordx4 v246, s[52:55], s56 offen offset:0 lds
buffer_load_dwordx4 v247, s[52:55], s56 offen offset:1024 lds
buffer_load_dwordx4 v248, s[52:55], s56 offen offset:2048 lds
buffer_load_dwordx4 v249, s[52:55], s56 offen offset:3072 lds
s_add_u32 s56, s56, 131072
s_add_u32 m0, s57, 81920
s_nop 0
buffer_load_dwordx4 v246, s[52:55], s56 offen offset:0 lds
buffer_load_dwordx4 v247, s[52:55], s56 offen offset:1024 lds
buffer_load_dwordx4 v248, s[52:55], s56 offen offset:2048 lds
buffer_load_dwordx4 v249, s[52:55], s56 offen offset:3072 lds
s_add_u32 s56, s56, 131072
s_mul_i32 s58, s7, 32768
s_sub_u32 s58, s56, s58
s_sub_u32 s58, s58, 786432
s_add_u32 s59, s7, 1
s_mul_i32 s60, s59, 131072
s_add_u32 s58, s58, s60
s_lshl_b32 s59, s59, 14
s_mov_b32 m0, s59
s_nop 0
buffer_load_dwordx4 v246, s[52:55], s58 offen offset:0 lds
buffer_load_dwordx4 v247, s[52:55], s58 offen offset:1024 lds
buffer_load_dwordx4 v248, s[52:55], s58 offen offset:2048 lds
buffer_load_dwordx4 v249, s[52:55], s58 offen offset:3072 lds
s_add_u32 s58, s58, 393216
s_add_u32 s59, s59, 49152
s_cmp_le_u32 s7, 1
s_cbranch_scc0 Low_54
s_mov_b32 m0, s59
s_nop 0
buffer_load_dwordx4 v246, s[52:55], s58 offen offset:0 lds
buffer_load_dwordx4 v247, s[52:55], s58 offen offset:1024 lds
buffer_load_dwordx4 v248, s[52:55], s58 offen offset:2048 lds
buffer_load_dwordx4 v249, s[52:55], s58 offen offset:3072 lds
Low_54:
Low_55:
s_branch PD_END
PD_SKIP:
s_waitcnt vmcnt(0)
s_barrier
v_mbcnt_lo_u32_b32 v250, -1, 0
v_mbcnt_hi_u32_b32 v250, -1, v250
s_mov_b32 s52, s78
s_and_b32 s53, s79, 0xffff
s_mov_b32 s54, 0x1c0000
s_mov_b32 s55, 0x20000
s_lshr_b32 s58, s6, 5
s_lshl_b32 s56, s58, 7
v_lshrrev_b32 v243, 3, v250
v_lshrrev_b32 v244, 2, v243
v_and_b32 v243, 3, v243
v_xor_b32 v243, v243, v244
v_lshlrev_b32 v243, 5, v243
v_lshlrev_b32 v244, 2, v244
v_bfe_u32 v245, v250, 1, 2
v_add_u32 v244, v244, v245
v_mul_u32_u24 v244, 1024, v244
v_add_u32 v243, v243, v244
v_and_b32 v244, 1, v250
v_lshl_add_u32 v246, v244, 4, v243
v_add_u32 v247, 7168, v246
v_add_u32 v248, 14336, v246
v_add_u32 v249, 21504, v246
s_mul_i32 s58, s7, 32768
s_add_u32 s56, s56, s58
s_lshl_b32 s57, s7, 12
s_cmp_lg_u32 s7, 0
s_cbranch_scc0 Low_58
s_add_u32 m0, s57, 0
s_nop 0
buffer_load_dwordx4 v246, s[52:55], s56 offen offset:0 lds
buffer_load_dwordx4 v247, s[52:55], s56 offen offset:1024 lds
buffer_load_dwordx4 v248, s[52:55], s56 offen offset:2048 lds
buffer_load_dwordx4 v249, s[52:55], s56 offen offset:3072 lds
s_add_u32 s56, s56, 131072
s_add_u32 m0, s57, 16384
s_nop 0
buffer_load_dwordx4 v246, s[52:55], s56 offen offset:0 lds
buffer_load_dwordx4 v247, s[52:55], s56 offen offset:1024 lds
buffer_load_dwordx4 v248, s[52:55], s56 offen offset:2048 lds
buffer_load_dwordx4 v249, s[52:55], s56 offen offset:3072 lds
s_add_u32 s56, s56, 131072
s_add_u32 m0, s57, 32768
s_nop 0
buffer_load_dwordx4 v246, s[52:55], s56 offen offset:0 lds
buffer_load_dwordx4 v247, s[52:55], s56 offen offset:1024 lds
buffer_load_dwordx4 v248, s[52:55], s56 offen offset:2048 lds
buffer_load_dwordx4 v249, s[52:55], s56 offen offset:3072 lds
s_add_u32 s56, s56, 131072
s_add_u32 m0, s57, 49152
s_nop 0
buffer_load_dwordx4 v246, s[52:55], s56 offen offset:0 lds
buffer_load_dwordx4 v247, s[52:55], s56 offen offset:1024 lds
buffer_load_dwordx4 v248, s[52:55], s56 offen offset:2048 lds
buffer_load_dwordx4 v249, s[52:55], s56 offen offset:3072 lds
s_add_u32 s56, s56, 131072
s_add_u32 m0, s57, 65536
s_nop 0
buffer_load_dwordx4 v246, s[52:55], s56 offen offset:0 lds
buffer_load_dwordx4 v247, s[52:55], s56 offen offset:1024 lds
buffer_load_dwordx4 v248, s[52:55], s56 offen offset:2048 lds
buffer_load_dwordx4 v249, s[52:55], s56 offen offset:3072 lds
s_add_u32 s56, s56, 131072
s_add_u32 m0, s57, 81920
s_nop 0
buffer_load_dwordx4 v246, s[52:55], s56 offen offset:0 lds
buffer_load_dwordx4 v247, s[52:55], s56 offen offset:1024 lds
buffer_load_dwordx4 v248, s[52:55], s56 offen offset:2048 lds
buffer_load_dwordx4 v249, s[52:55], s56 offen offset:3072 lds
s_add_u32 s56, s56, 131072
s_mul_i32 s58, s7, 32768
s_sub_u32 s58, s56, s58
s_sub_u32 s58, s58, 786432
s_add_u32 s59, s7, 1
s_mul_i32 s60, s59, 131072
s_add_u32 s58, s58, s60
s_lshl_b32 s59, s59, 14
s_mov_b32 m0, s59
s_nop 0
buffer_load_dwordx4 v246, s[52:55], s58 offen offset:0 lds
buffer_load_dwordx4 v247, s[52:55], s58 offen offset:1024 lds
buffer_load_dwordx4 v248, s[52:55], s58 offen offset:2048 lds
buffer_load_dwordx4 v249, s[52:55], s58 offen offset:3072 lds
s_add_u32 s58, s58, 393216
s_add_u32 s59, s59, 49152
s_cmp_le_u32 s7, 1
s_cbranch_scc0 Low_57
s_mov_b32 m0, s59
s_nop 0
buffer_load_dwordx4 v246, s[52:55], s58 offen offset:0 lds
buffer_load_dwordx4 v247, s[52:55], s58 offen offset:1024 lds
buffer_load_dwordx4 v248, s[52:55], s58 offen offset:2048 lds
buffer_load_dwordx4 v249, s[52:55], s58 offen offset:3072 lds
Low_57:
Low_58:
PD_END:
v_mbcnt_lo_u32_b32 v250, -1, 0
v_mbcnt_hi_u32_b32 v250, -1, v250
s_add_u32 s8, s82, 0x5000000
s_addc_u32 s9, s83, 0
s_and_b32 s9, s9, 0xffff
s_mov_b32 s10, 0x700000
s_mov_b32 s11, 0x20000
s_add_u32 s16, s82, 0x4600000
s_addc_u32 s17, s83, 0
s_and_b32 s17, s17, 0xffff
s_mov_b32 s18, 0x200000
s_mov_b32 s19, 0x20000
s_mov_b32 s20, s80
s_and_b32 s21, s81, 0xffff
s_mov_b32 s22, 0x200000
s_mov_b32 s23, 0x20000
s_and_b32 s41, s6, 7
s_lshl_b32 s41, s41, 2
s_bfe_u32 s42, s6, 0x20003
s_add_u32 s41, s41, s42
s_lshr_b32 s42, s6, 5
s_lshr_b32 s49, s41, 1
s_mul_i32 s28, s49, 458752
s_and_b32 s43, s41, 1
s_lshl_b32 s43, s43, 12
s_add_u32 s28, s28, s43
v_lshlrev_b32 v251, 4, v250
v_bfe_u32 v0, v250, 4, 1
v_lshlrev_b32 v1, 10, v0
v_lshrrev_b32 v2, 5, v250
v_lshl_add_u32 v1, v2, 8, v1
v_and_b32 v245, 15, v250
v_lshl_add_u32 v245, v245, 4, v1
v_lshrrev_b32 v0, 4, v250
v_mul_u32_u24 v1, 512, v0
v_bfe_u32 v2, v250, 2, 2
v_lshl_add_u32 v1, v2, 5, v1
v_and_b32 v2, 3, v250
v_lshl_add_u32 v1, v2, 3, v1
v_and_b32 v0, 1, v0
v_lshlrev_b32 v0, 7, v0
v_add_u32 v244, v1, v0
v_sub_u32 v243, v1, v0
s_lshl_b32 s26, s7, 10
s_add_u32 s28, s28, s26
v_min_u32 v200, 13, v250
v_lshlrev_b32 v200, 3, v200
s_lshl_b32 s46, s49, 7
s_add_u32 s46, s46, 24576
s_cmp_eq_u32 s7, 0
s_cbranch_scc0 Lp0_59
s_mov_b32 s86, 0
Lpoll_60:
buffer_load_dwordx2 v[202:203], v200, s[88:91], s46 offen sc1
s_waitcnt vmcnt(0)
v_cmp_eq_u64 vcc, s[84:85], v[202:203]
s_nop 1
s_cmp_eq_u64 vcc, exec
s_cbranch_scc1 Lpdone_61
s_sleep 1
s_add_u32 s86, s86, 1
s_cmp_lt_u32 s86, 0x40000
s_cbranch_scc1 Lpoll_60
Lpdone_61:
Lp0_59:
s_barrier
s_cmp_eq_u32 s7, 0
s_cbranch_scc1 Lw0_62
buffer_load_dwordx4 a[128:131], v251, s[8:11], s28 offen offset:0 sc1
s_add_u32 s27, s28, 8192
s_nop 0
buffer_load_dwordx4 a[132:135], v251, s[8:11], s27 offen offset:0 sc1
s_add_u32 s27, s28, 16384
s_nop 0
buffer_load_dwordx4 a[136:139], v251, s[8:11], s27 offen offset:0 sc1
s_add_u32 s27, s28, 24576
s_nop 0
buffer_load_dwordx4 a[140:143], v251, s[8:11], s27 offen offset:0 sc1
s_add_u32 s28, s28, 32768
buffer_load_dwordx4 a[144:147], v251, s[8:11], s28 offen offset:0 sc1
s_add_u32 s27, s28, 8192
s_nop 0
buffer_load_dwordx4 a[148:151], v251, s[8:11], s27 offen offset:0 sc1
s_add_u32 s27, s28, 16384
s_nop 0
buffer_load_dwordx4 a[152:155], v251, s[8:11], s27 offen offset:0 sc1
s_add_u32 s27, s28, 24576
s_nop 0
buffer_load_dwordx4 a[156:159], v251, s[8:11], s27 offen offset:0 sc1
s_add_u32 s28, s28, 32768
buffer_load_dwordx4 a[160:163], v251, s[8:11], s28 offen offset:0 sc1
s_add_u32 s27, s28, 8192
s_nop 0
buffer_load_dwordx4 a[164:167], v251, s[8:11], s27 offen offset:0 sc1
s_add_u32 s27, s28, 16384
s_nop 0
buffer_load_dwordx4 a[168:171], v251, s[8:11], s27 offen offset:0 sc1
s_add_u32 s27, s28, 24576
s_nop 0
buffer_load_dwordx4 a[172:175], v251, s[8:11], s27 offen offset:0 sc1
s_add_u32 s28, s28, 32768
buffer_load_dwordx4 a[176:179], v251, s[8:11], s28 offen offset:0 sc1
s_add_u32 s27, s28, 8192
s_nop 0
buffer_load_dwordx4 a[180:183], v251, s[8:11], s27 offen offset:0 sc1
s_add_u32 s27, s28, 16384
s_nop 0
buffer_load_dwordx4 a[184:187], v251, s[8:11], s27 offen offset:0 sc1
s_add_u32 s27, s28, 24576
s_nop 0
buffer_load_dwordx4 a[188:191], v251, s[8:11], s27 offen offset:0 sc1
s_add_u32 s28, s28, 32768
s_branch Lw0_63
Lw0_62:
s_add_u32 m0, s57, 0
s_nop 0
buffer_load_dwordx4 v246, s[52:55], s56 offen offset:0 lds
buffer_load_dwordx4 v247, s[52:55], s56 offen offset:1024 lds
buffer_load_dwordx4 v248, s[52:55], s56 offen offset:2048 lds
buffer_load_dwordx4 v249, s[52:55], s56 offen offset:3072 lds
s_add_u32 s56, s56, 131072
s_add_u32 m0, s57, 16384
s_nop 0
buffer_load_dwordx4 v246, s[52:55], s56 offen offset:0 lds
buffer_load_dwordx4 v247, s[52:55], s56 offen offset:1024 lds
buffer_load_dwordx4 v248, s[52:55], s56 offen offset:2048 lds
buffer_load_dwordx4 v249, s[52:55], s56 offen offset:3072 lds
s_add_u32 s56, s56, 131072
s_add_u32 s56, s56, 524288
buffer_load_dwordx4 a[128:131], v251, s[8:11], s28 offen offset:0 sc1
s_add_u32 s27, s28, 8192
s_nop 0
buffer_load_dwordx4 a[132:135], v251, s[8:11], s27 offen offset:0 sc1
s_add_u32 s27, s28, 16384
s_nop 0
buffer_load_dwordx4 a[136:139], v251, s[8:11], s27 offen offset:0 sc1
s_add_u32 s27, s28, 24576
s_nop 0
buffer_load_dwordx4 a[140:143], v251, s[8:11], s27 offen offset:0 sc1
s_add_u32 s28, s28, 32768
buffer_load_dwordx4 a[144:147], v251, s[8:11], s28 offen offset:0 sc1
s_add_u32 s27, s28, 8192
s_nop 0
buffer_load_dwordx4 a[148:151], v251, s[8:11], s27 offen offset:0 sc1
s_add_u32 s27, s28, 16384
s_nop 0
buffer_load_dwordx4 a[152:155], v251, s[8:11], s27 offen offset:0 sc1
s_add_u32 s27, s28, 24576
s_nop 0
buffer_load_dwordx4 a[156:159], v251, s[8:11], s27 offen offset:0 sc1
s_add_u32 s28, s28, 32768
buffer_load_dwordx4 a[160:163], v251, s[8:11], s28 offen offset:0 sc1
s_add_u32 s27, s28, 8192
s_nop 0
buffer_load_dwordx4 a[164:167], v251, s[8:11], s27 offen offset:0 sc1
s_add_u32 s27, s28, 16384
s_nop 0
buffer_load_dwordx4 a[168:171], v251, s[8:11], s27 offen offset:0 sc1
s_add_u32 s27, s28, 24576
s_nop 0
buffer_load_dwordx4 a[172:175], v251, s[8:11], s27 offen offset:0 sc1
s_add_u32 s28, s28, 32768
buffer_load_dwordx4 a[176:179], v251, s[8:11], s28 offen offset:0 sc1
s_add_u32 s27, s28, 8192
s_nop 0
buffer_load_dwordx4 a[180:183], v251, s[8:11], s27 offen offset:0 sc1
s_add_u32 s27, s28, 16384
s_nop 0
buffer_load_dwordx4 a[184:187], v251, s[8:11], s27 offen offset:0 sc1
s_add_u32 s27, s28, 24576
s_nop 0
buffer_load_dwordx4 a[188:191], v251, s[8:11], s27 offen offset:0 sc1
s_add_u32 s28, s28, 32768
Lw0_63:
s_waitcnt vmcnt(12)
s_barrier
ds_read_b64_tr_b16 a[0:1], v244 offset:0
ds_read_b64_tr_b16 a[2:3], v244 offset:2048
ds_read_b64_tr_b16 a[4:5], v243 offset:128
ds_read_b64_tr_b16 a[6:7], v243 offset:2176
ds_read_b64_tr_b16 a[8:9], v244 offset:256
ds_read_b64_tr_b16 a[10:11], v244 offset:2304
ds_read_b64_tr_b16 a[12:13], v243 offset:384
ds_read_b64_tr_b16 a[14:15], v243 offset:2432
ds_read_b64_tr_b16 a[16:17], v244 offset:4096
ds_read_b64_tr_b16 a[18:19], v244 offset:6144
ds_read_b64_tr_b16 a[20:21], v243 offset:4224
ds_read_b64_tr_b16 a[22:23], v243 offset:6272
ds_read_b64_tr_b16 a[24:25], v244 offset:4352
ds_read_b64_tr_b16 a[26:27], v244 offset:6400
ds_read_b64_tr_b16 a[28:29], v243 offset:4480
ds_read_b64_tr_b16 a[30:31], v243 offset:6528
ds_read_b64_tr_b16 a[32:33], v244 offset:8192
ds_read_b64_tr_b16 a[34:35], v244 offset:10240
ds_read_b64_tr_b16 a[36:37], v243 offset:8320
ds_read_b64_tr_b16 a[38:39], v243 offset:10368
ds_read_b64_tr_b16 a[40:41], v244 offset:8448
ds_read_b64_tr_b16 a[42:43], v244 offset:10496
ds_read_b64_tr_b16 a[44:45], v243 offset:8576
ds_read_b64_tr_b16 a[46:47], v243 offset:10624
ds_read_b64_tr_b16 a[48:49], v244 offset:12288
ds_read_b64_tr_b16 a[50:51], v244 offset:14336
ds_read_b64_tr_b16 a[52:53], v243 offset:12416
ds_read_b64_tr_b16 a[54:55], v243 offset:14464
ds_read_b64_tr_b16 a[56:57], v244 offset:12544
ds_read_b64_tr_b16 a[58:59], v244 offset:14592
ds_read_b64_tr_b16 a[60:61], v243 offset:12672
ds_read_b64_tr_b16 a[62:63], v243 offset:14720
v_add_u32 v244, 0x4000, v244
v_add_u32 v243, 0x4000, v243
s_waitcnt vmcnt(12) lgkmcnt(0)
s_barrier
v_mfma_f32_16x16x32_bf16 v[0:3], a[0:3], a[128:131], 0
ds_read_b64_tr_b16 a[64:65], v244 offset:0
ds_read_b64_tr_b16 a[66:67], v244 offset:2048
ds_read_b64_tr_b16 a[68:69], v243 offset:128
buffer_load_dwordx4 a[192:195], v251, s[8:11], s28 offen offset:0 sc1
v_mfma_f32_16x16x32_bf16 v[4:7], a[4:7], a[128:131], 0
ds_read_b64_tr_b16 a[70:71], v243 offset:2176
ds_read_b64_tr_b16 a[72:73], v244 offset:256
ds_read_b64_tr_b16 a[74:75], v244 offset:2304
v_mfma_f32_16x16x32_bf16 v[8:11], a[8:11], a[128:131], 0
ds_read_b64_tr_b16 a[76:77], v243 offset:384
ds_read_b64_tr_b16 a[78:79], v243 offset:2432
v_mfma_f32_16x16x32_bf16 v[12:15], a[12:15], a[128:131], 0
ds_read_b64_tr_b16 a[80:81], v244 offset:4096
ds_read_b64_tr_b16 a[82:83], v244 offset:6144
ds_read_b64_tr_b16 a[84:85], v243 offset:4224
v_mfma_f32_16x16x32_bf16 v[0:3], a[16:19], a[132:135], v[0:3]
ds_read_b64_tr_b16 a[86:87], v243 offset:6272
ds_read_b64_tr_b16 a[88:89], v244 offset:4352
ds_read_b64_tr_b16 a[90:91], v244 offset:6400
s_add_u32 s27, s28, 8192
buffer_load_dwordx4 a[196:199], v251, s[8:11], s27 offen offset:0 sc1
v_mfma_f32_16x16x32_bf16 v[4:7], a[20:23], a[132:135], v[4:7]
ds_read_b64_tr_b16 a[92:93], v243 offset:4480
ds_read_b64_tr_b16 a[94:95], v243 offset:6528
v_mfma_f32_16x16x32_bf16 v[8:11], a[24:27], a[132:135], v[8:11]
ds_read_b64_tr_b16 a[96:97], v244 offset:8192
ds_read_b64_tr_b16 a[98:99], v244 offset:10240
ds_read_b64_tr_b16 a[100:101], v243 offset:8320
v_mfma_f32_16x16x32_bf16 v[12:15], a[28:31], a[132:135], v[12:15]
ds_read_b64_tr_b16 a[102:103], v243 offset:10368
ds_read_b64_tr_b16 a[104:105], v244 offset:8448
ds_read_b64_tr_b16 a[106:107], v244 offset:10496
v_mfma_f32_16x16x32_bf16 v[0:3], a[32:35], a[136:139], v[0:3]
ds_read_b64_tr_b16 a[108:109], v243 offset:8576
ds_read_b64_tr_b16 a[110:111], v243 offset:10624
s_add_u32 s27, s28, 16384
buffer_load_dwordx4 a[200:203], v251, s[8:11], s27 offen offset:0 sc1
v_mfma_f32_16x16x32_bf16 v[4:7], a[36:39], a[136:139], v[4:7]
ds_read_b64_tr_b16 a[112:113], v244 offset:12288
ds_read_b64_tr_b16 a[114:115], v244 offset:14336
ds_read_b64_tr_b16 a[116:117], v243 offset:12416
v_mfma_f32_16x16x32_bf16 v[8:11], a[40:43], a[136:139], v[8:11]
ds_read_b64_tr_b16 a[118:119], v243 offset:14464
ds_read_b64_tr_b16 a[120:121], v244 offset:12544
ds_read_b64_tr_b16 a[122:123], v244 offset:14592
v_mfma_f32_16x16x32_bf16 v[12:15], a[44:47], a[136:139], v[12:15]
ds_read_b64_tr_b16 a[124:125], v243 offset:12672
ds_read_b64_tr_b16 a[126:127], v243 offset:14720
v_mfma_f32_16x16x32_bf16 v[0:3], a[48:51], a[140:143], v[0:3]
s_add_u32 s27, s28, 24576
buffer_load_dwordx4 a[204:207], v251, s[8:11], s27 offen offset:0 sc1
v_mfma_f32_16x16x32_bf16 v[4:7], a[52:55], a[140:143], v[4:7]
v_mfma_f32_16x16x32_bf16 v[8:11], a[56:59], a[140:143], v[8:11]
v_mfma_f32_16x16x32_bf16 v[12:15], a[60:63], a[140:143], v[12:15]
s_add_u32 s28, s28, 32768
v_add_u32 v244, 0x4000, v244
v_add_u32 v243, 0x4000, v243
s_waitcnt vmcnt(12) lgkmcnt(0)
v_mfma_f32_16x16x32_bf16 v[0:3], a[64:67], a[144:147], v[0:3]
ds_read_b64_tr_b16 a[0:1], v244 offset:0
ds_read_b64_tr_b16 a[2:3], v244 offset:2048
ds_read_b64_tr_b16 a[4:5], v243 offset:128
buffer_load_dwordx4 a[128:131], v251, s[8:11], s28 offen offset:0 sc1
v_mfma_f32_16x16x32_bf16 v[4:7], a[68:71], a[144:147], v[4:7]
ds_read_b64_tr_b16 a[6:7], v243 offset:2176
ds_read_b64_tr_b16 a[8:9], v244 offset:256
ds_read_b64_tr_b16 a[10:11], v244 offset:2304
v_mfma_f32_16x16x32_bf16 v[8:11], a[72:75], a[144:147], v[8:11]
ds_read_b64_tr_b16 a[12:13], v243 offset:384
ds_read_b64_tr_b16 a[14:15], v243 offset:2432
s_add_u32 s27, s28, 8192
buffer_load_dwordx4 a[132:135], v251, s[8:11], s27 offen offset:0 sc1
v_mfma_f32_16x16x32_bf16 v[12:15], a[76:79], a[144:147], v[12:15]
ds_read_b64_tr_b16 a[16:17], v244 offset:4096
ds_read_b64_tr_b16 a[18:19], v244 offset:6144
ds_read_b64_tr_b16 a[20:21], v243 offset:4224
v_mfma_f32_16x16x32_bf16 v[0:3], a[80:83], a[148:151], v[0:3]
ds_read_b64_tr_b16 a[22:23], v243 offset:6272
ds_read_b64_tr_b16 a[24:25], v244 offset:4352
ds_read_b64_tr_b16 a[26:27], v244 offset:6400
s_add_u32 s27, s28, 16384
buffer_load_dwordx4 a[136:139], v251, s[8:11], s27 offen offset:0 sc1
v_mfma_f32_16x16x32_bf16 v[4:7], a[84:87], a[148:151], v[4:7]
ds_read_b64_tr_b16 a[28:29], v243 offset:4480
ds_read_b64_tr_b16 a[30:31], v243 offset:6528
v_mfma_f32_16x16x32_bf16 v[8:11], a[88:91], a[148:151], v[8:11]
ds_read_b64_tr_b16 a[32:33], v244 offset:8192
ds_read_b64_tr_b16 a[34:35], v244 offset:10240
ds_read_b64_tr_b16 a[36:37], v243 offset:8320
s_add_u32 s27, s28, 24576
buffer_load_dwordx4 a[140:143], v251, s[8:11], s27 offen offset:0 sc1
v_mfma_f32_16x16x32_bf16 v[12:15], a[92:95], a[148:151], v[12:15]
ds_read_b64_tr_b16 a[38:39], v243 offset:10368
ds_read_b64_tr_b16 a[40:41], v244 offset:8448
ds_read_b64_tr_b16 a[42:43], v244 offset:10496
v_mfma_f32_16x16x32_bf16 v[0:3], a[96:99], a[152:155], v[0:3]
ds_read_b64_tr_b16 a[44:45], v243 offset:8576
ds_read_b64_tr_b16 a[46:47], v243 offset:10624
s_add_u32 m0, s57, 0
s_nop 0
buffer_load_dwordx4 v246, s[52:55], s56 offen offset:0 lds
v_mfma_f32_16x16x32_bf16 v[4:7], a[100:103], a[152:155], v[4:7]
ds_read_b64_tr_b16 a[48:49], v244 offset:12288
ds_read_b64_tr_b16 a[50:51], v244 offset:14336
ds_read_b64_tr_b16 a[52:53], v243 offset:12416
v_mfma_f32_16x16x32_bf16 v[8:11], a[104:107], a[152:155], v[8:11]
ds_read_b64_tr_b16 a[54:55], v243 offset:14464
ds_read_b64_tr_b16 a[56:57], v244 offset:12544
ds_read_b64_tr_b16 a[58:59], v244 offset:14592
buffer_load_dwordx4 v247, s[52:55], s56 offen offset:1024 lds
v_mfma_f32_16x16x32_bf16 v[12:15], a[108:111], a[152:155], v[12:15]
ds_read_b64_tr_b16 a[60:61], v243 offset:12672
ds_read_b64_tr_b16 a[62:63], v243 offset:14720
v_mfma_f32_16x16x32_bf16 v[0:3], a[112:115], a[156:159], v[0:3]
buffer_load_dwordx4 v248, s[52:55], s56 offen offset:2048 lds
v_mfma_f32_16x16x32_bf16 v[4:7], a[116:119], a[156:159], v[4:7]
v_mfma_f32_16x16x32_bf16 v[8:11], a[120:123], a[156:159], v[8:11]
buffer_load_dwordx4 v249, s[52:55], s56 offen offset:3072 lds
v_mfma_f32_16x16x32_bf16 v[12:15], a[124:127], a[156:159], v[12:15]
s_add_u32 s28, s28, 32768
s_add_u32 s56, s56, 131072
v_add_u32 v244, 0x4000, v244
v_add_u32 v243, 0x4000, v243
s_waitcnt vmcnt(16) lgkmcnt(0)
s_barrier
v_mfma_f32_16x16x32_bf16 v[0:3], a[0:3], a[160:163], v[0:3]
ds_read_b64_tr_b16 a[64:65], v244 offset:0
ds_read_b64_tr_b16 a[66:67], v244 offset:2048
ds_read_b64_tr_b16 a[68:69], v243 offset:128
buffer_load_dwordx4 a[144:147], v251, s[8:11], s28 offen offset:0 sc1
v_mfma_f32_16x16x32_bf16 v[4:7], a[4:7], a[160:163], v[4:7]
ds_read_b64_tr_b16 a[70:71], v243 offset:2176
ds_read_b64_tr_b16 a[72:73], v244 offset:256
ds_read_b64_tr_b16 a[74:75], v244 offset:2304
v_mfma_f32_16x16x32_bf16 v[8:11], a[8:11], a[160:163], v[8:11]
ds_read_b64_tr_b16 a[76:77], v243 offset:384
ds_read_b64_tr_b16 a[78:79], v243 offset:2432
s_add_u32 s27, s28, 8192
buffer_load_dwordx4 a[148:151], v251, s[8:11], s27 offen offset:0 sc1
v_mfma_f32_16x16x32_bf16 v[12:15], a[12:15], a[160:163], v[12:15]
ds_read_b64_tr_b16 a[80:81], v244 offset:4096
ds_read_b64_tr_b16 a[82:83], v244 offset:6144
ds_read_b64_tr_b16 a[84:85], v243 offset:4224
v_mfma_f32_16x16x32_bf16 v[0:3], a[16:19], a[164:167], v[0:3]
ds_read_b64_tr_b16 a[86:87], v243 offset:6272
ds_read_b64_tr_b16 a[88:89], v244 offset:4352
ds_read_b64_tr_b16 a[90:91], v244 offset:6400
s_add_u32 s27, s28, 16384
buffer_load_dwordx4 a[152:155], v251, s[8:11], s27 offen offset:0 sc1
v_mfma_f32_16x16x32_bf16 v[4:7], a[20:23], a[164:167], v[4:7]
ds_read_b64_tr_b16 a[92:93], v243 offset:4480
ds_read_b64_tr_b16 a[94:95], v243 offset:6528
v_mfma_f32_16x16x32_bf16 v[8:11], a[24:27], a[164:167], v[8:11]
ds_read_b64_tr_b16 a[96:97], v244 offset:8192
ds_read_b64_tr_b16 a[98:99], v244 offset:10240
ds_read_b64_tr_b16 a[100:101], v243 offset:8320
s_add_u32 s27, s28, 24576
buffer_load_dwordx4 a[156:159], v251, s[8:11], s27 offen offset:0 sc1
v_mfma_f32_16x16x32_bf16 v[12:15], a[28:31], a[164:167], v[12:15]
ds_read_b64_tr_b16 a[102:103], v243 offset:10368
ds_read_b64_tr_b16 a[104:105], v244 offset:8448
ds_read_b64_tr_b16 a[106:107], v244 offset:10496
v_mfma_f32_16x16x32_bf16 v[0:3], a[32:35], a[168:171], v[0:3]
ds_read_b64_tr_b16 a[108:109], v243 offset:8576
ds_read_b64_tr_b16 a[110:111], v243 offset:10624
s_add_u32 m0, s57, 16384
s_nop 0
buffer_load_dwordx4 v246, s[52:55], s56 offen offset:0 lds
v_mfma_f32_16x16x32_bf16 v[4:7], a[36:39], a[168:171], v[4:7]
ds_read_b64_tr_b16 a[112:113], v244 offset:12288
ds_read_b64_tr_b16 a[114:115], v244 offset:14336
ds_read_b64_tr_b16 a[116:117], v243 offset:12416
v_mfma_f32_16x16x32_bf16 v[8:11], a[40:43], a[168:171], v[8:11]
ds_read_b64_tr_b16 a[118:119], v243 offset:14464
ds_read_b64_tr_b16 a[120:121], v244 offset:12544
ds_read_b64_tr_b16 a[122:123], v244 offset:14592
buffer_load_dwordx4 v247, s[52:55], s56 offen offset:1024 lds
v_mfma_f32_16x16x32_bf16 v[12:15], a[44:47], a[168:171], v[12:15]
ds_read_b64_tr_b16 a[124:125], v243 offset:12672
ds_read_b64_tr_b16 a[126:127], v243 offset:14720
v_mfma_f32_16x16x32_bf16 v[0:3], a[48:51], a[172:175], v[0:3]
buffer_load_dwordx4 v248, s[52:55], s56 offen offset:2048 lds
v_mfma_f32_16x16x32_bf16 v[4:7], a[52:55], a[172:175], v[4:7]
v_mfma_f32_16x16x32_bf16 v[8:11], a[56:59], a[172:175], v[8:11]
buffer_load_dwordx4 v249, s[52:55], s56 offen offset:3072 lds
v_mfma_f32_16x16x32_bf16 v[12:15], a[60:63], a[172:175], v[12:15]
s_add_u32 s28, s28, 32768
s_add_u32 s56, s56, 131072
v_add_u32 v244, 0x4000, v244
v_add_u32 v243, 0x4000, v243
s_waitcnt vmcnt(20) lgkmcnt(0)
v_mfma_f32_16x16x32_bf16 v[0:3], a[64:67], a[176:179], v[0:3]
ds_read_b64_tr_b16 a[0:1], v244 offset:0
ds_read_b64_tr_b16 a[2:3], v244 offset:2048
ds_read_b64_tr_b16 a[4:5], v243 offset:128
buffer_load_dwordx4 a[160:163], v251, s[8:11], s28 offen offset:0 sc1
v_mfma_f32_16x16x32_bf16 v[4:7], a[68:71], a[176:179], v[4:7]
ds_read_b64_tr_b16 a[6:7], v243 offset:2176
ds_read_b64_tr_b16 a[8:9], v244 offset:256
ds_read_b64_tr_b16 a[10:11], v244 offset:2304
v_mfma_f32_16x16x32_bf16 v[8:11], a[72:75], a[176:179], v[8:11]
ds_read_b64_tr_b16 a[12:13], v243 offset:384
ds_read_b64_tr_b16 a[14:15], v243 offset:2432
s_add_u32 s27, s28, 8192
buffer_load_dwordx4 a[164:167], v251, s[8:11], s27 offen offset:0 sc1
v_mfma_f32_16x16x32_bf16 v[12:15], a[76:79], a[176:179], v[12:15]
ds_read_b64_tr_b16 a[16:17], v244 offset:4096
ds_read_b64_tr_b16 a[18:19], v244 offset:6144
ds_read_b64_tr_b16 a[20:21], v243 offset:4224
v_mfma_f32_16x16x32_bf16 v[0:3], a[80:83], a[180:183], v[0:3]
ds_read_b64_tr_b16 a[22:23], v243 offset:6272
ds_read_b64_tr_b16 a[24:25], v244 offset:4352
ds_read_b64_tr_b16 a[26:27], v244 offset:6400
s_add_u32 s27, s28, 16384
buffer_load_dwordx4 a[168:171], v251, s[8:11], s27 offen offset:0 sc1
v_mfma_f32_16x16x32_bf16 v[4:7], a[84:87], a[180:183], v[4:7]
ds_read_b64_tr_b16 a[28:29], v243 offset:4480
ds_read_b64_tr_b16 a[30:31], v243 offset:6528
v_mfma_f32_16x16x32_bf16 v[8:11], a[88:91], a[180:183], v[8:11]
ds_read_b64_tr_b16 a[32:33], v244 offset:8192
ds_read_b64_tr_b16 a[34:35], v244 offset:10240
ds_read_b64_tr_b16 a[36:37], v243 offset:8320
s_add_u32 s27, s28, 24576
buffer_load_dwordx4 a[172:175], v251, s[8:11], s27 offen offset:0 sc1
v_mfma_f32_16x16x32_bf16 v[12:15], a[92:95], a[180:183], v[12:15]
ds_read_b64_tr_b16 a[38:39], v243 offset:10368
ds_read_b64_tr_b16 a[40:41], v244 offset:8448
ds_read_b64_tr_b16 a[42:43], v244 offset:10496
v_mfma_f32_16x16x32_bf16 v[0:3], a[96:99], a[184:187], v[0:3]
ds_read_b64_tr_b16 a[44:45], v243 offset:8576
ds_read_b64_tr_b16 a[46:47], v243 offset:10624
s_add_u32 m0, s57, 32768
s_nop 0
buffer_load_dwordx4 v246, s[52:55], s56 offen offset:0 lds
v_mfma_f32_16x16x32_bf16 v[4:7], a[100:103], a[184:187], v[4:7]
ds_read_b64_tr_b16 a[48:49], v244 offset:12288
ds_read_b64_tr_b16 a[50:51], v244 offset:14336
ds_read_b64_tr_b16 a[52:53], v243 offset:12416
v_mfma_f32_16x16x32_bf16 v[8:11], a[104:107], a[184:187], v[8:11]
ds_read_b64_tr_b16 a[54:55], v243 offset:14464
ds_read_b64_tr_b16 a[56:57], v244 offset:12544
ds_read_b64_tr_b16 a[58:59], v244 offset:14592
buffer_load_dwordx4 v247, s[52:55], s56 offen offset:1024 lds
v_mfma_f32_16x16x32_bf16 v[12:15], a[108:111], a[184:187], v[12:15]
ds_read_b64_tr_b16 a[60:61], v243 offset:12672
ds_read_b64_tr_b16 a[62:63], v243 offset:14720
v_mfma_f32_16x16x32_bf16 v[0:3], a[112:115], a[188:191], v[0:3]
buffer_load_dwordx4 v248, s[52:55], s56 offen offset:2048 lds
v_mfma_f32_16x16x32_bf16 v[4:7], a[116:119], a[188:191], v[4:7]
v_mfma_f32_16x16x32_bf16 v[8:11], a[120:123], a[188:191], v[8:11]
buffer_load_dwordx4 v249, s[52:55], s56 offen offset:3072 lds
v_mfma_f32_16x16x32_bf16 v[12:15], a[124:127], a[188:191], v[12:15]
s_add_u32 s28, s28, 32768
s_add_u32 s56, s56, 131072
v_add_u32 v244, 0x4000, v244
v_add_u32 v243, 0x4000, v243
s_waitcnt vmcnt(16) lgkmcnt(0)
s_barrier
v_mfma_f32_16x16x32_bf16 v[0:3], a[0:3], a[192:195], v[0:3]
ds_read_b64_tr_b16 a[64:65], v244 offset:0
ds_read_b64_tr_b16 a[66:67], v244 offset:2048
ds_read_b64_tr_b16 a[68:69], v243 offset:128
buffer_load_dwordx4 a[176:179], v251, s[8:11], s28 offen offset:0 sc1
v_mfma_f32_16x16x32_bf16 v[4:7], a[4:7], a[192:195], v[4:7]
ds_read_b64_tr_b16 a[70:71], v243 offset:2176
ds_read_b64_tr_b16 a[72:73], v244 offset:256
ds_read_b64_tr_b16 a[74:75], v244 offset:2304
v_mfma_f32_16x16x32_bf16 v[8:11], a[8:11], a[192:195], v[8:11]
ds_read_b64_tr_b16 a[76:77], v243 offset:384
ds_read_b64_tr_b16 a[78:79], v243 offset:2432
s_add_u32 s27, s28, 8192
buffer_load_dwordx4 a[180:183], v251, s[8:11], s27 offen offset:0 sc1
v_mfma_f32_16x16x32_bf16 v[12:15], a[12:15], a[192:195], v[12:15]
ds_read_b64_tr_b16 a[80:81], v244 offset:4096
ds_read_b64_tr_b16 a[82:83], v244 offset:6144
ds_read_b64_tr_b16 a[84:85], v243 offset:4224
v_mfma_f32_16x16x32_bf16 v[0:3], a[16:19], a[196:199], v[0:3]
ds_read_b64_tr_b16 a[86:87], v243 offset:6272
ds_read_b64_tr_b16 a[88:89], v244 offset:4352
ds_read_b64_tr_b16 a[90:91], v244 offset:6400
s_add_u32 s27, s28, 16384
buffer_load_dwordx4 a[184:187], v251, s[8:11], s27 offen offset:0 sc1
v_mfma_f32_16x16x32_bf16 v[4:7], a[20:23], a[196:199], v[4:7]
ds_read_b64_tr_b16 a[92:93], v243 offset:4480
ds_read_b64_tr_b16 a[94:95], v243 offset:6528
v_mfma_f32_16x16x32_bf16 v[8:11], a[24:27], a[196:199], v[8:11]
ds_read_b64_tr_b16 a[96:97], v244 offset:8192
ds_read_b64_tr_b16 a[98:99], v244 offset:10240
ds_read_b64_tr_b16 a[100:101], v243 offset:8320
s_add_u32 s27, s28, 24576
buffer_load_dwordx4 a[188:191], v251, s[8:11], s27 offen offset:0 sc1
v_mfma_f32_16x16x32_bf16 v[12:15], a[28:31], a[196:199], v[12:15]
ds_read_b64_tr_b16 a[102:103], v243 offset:10368
ds_read_b64_tr_b16 a[104:105], v244 offset:8448
ds_read_b64_tr_b16 a[106:107], v244 offset:10496
v_mfma_f32_16x16x32_bf16 v[0:3], a[32:35], a[200:203], v[0:3]
ds_read_b64_tr_b16 a[108:109], v243 offset:8576
ds_read_b64_tr_b16 a[110:111], v243 offset:10624
s_add_u32 m0, s57, 49152
s_nop 0
buffer_load_dwordx4 v246, s[52:55], s56 offen offset:0 lds
v_mfma_f32_16x16x32_bf16 v[4:7], a[36:39], a[200:203], v[4:7]
ds_read_b64_tr_b16 a[112:113], v244 offset:12288
ds_read_b64_tr_b16 a[114:115], v244 offset:14336
ds_read_b64_tr_b16 a[116:117], v243 offset:12416
v_mfma_f32_16x16x32_bf16 v[8:11], a[40:43], a[200:203], v[8:11]
ds_read_b64_tr_b16 a[118:119], v243 offset:14464
ds_read_b64_tr_b16 a[120:121], v244 offset:12544
ds_read_b64_tr_b16 a[122:123], v244 offset:14592
buffer_load_dwordx4 v247, s[52:55], s56 offen offset:1024 lds
v_mfma_f32_16x16x32_bf16 v[12:15], a[44:47], a[200:203], v[12:15]
ds_read_b64_tr_b16 a[124:125], v243 offset:12672
ds_read_b64_tr_b16 a[126:127], v243 offset:14720
v_mfma_f32_16x16x32_bf16 v[0:3], a[48:51], a[204:207], v[0:3]
buffer_load_dwordx4 v248, s[52:55], s56 offen offset:2048 lds
v_mfma_f32_16x16x32_bf16 v[4:7], a[52:55], a[204:207], v[4:7]
v_mfma_f32_16x16x32_bf16 v[8:11], a[56:59], a[204:207], v[8:11]
buffer_load_dwordx4 v249, s[52:55], s56 offen offset:3072 lds
v_mfma_f32_16x16x32_bf16 v[12:15], a[60:63], a[204:207], v[12:15]
s_add_u32 s28, s28, 32768
s_add_u32 s56, s56, 131072
v_add_u32 v244, 0xfffec000, v244
v_add_u32 v243, 0xfffec000, v243
s_waitcnt vmcnt(28) lgkmcnt(0)
v_mfma_f32_16x16x32_bf16 v[0:3], a[64:67], a[128:131], v[0:3]
ds_read_b64_tr_b16 a[0:1], v244 offset:0
ds_read_b64_tr_b16 a[2:3], v244 offset:2048
ds_read_b64_tr_b16 a[4:5], v243 offset:128
buffer_load_dwordx4 a[192:195], v251, s[8:11], s28 offen offset:0 sc1
v_mfma_f32_16x16x32_bf16 v[4:7], a[68:71], a[128:131], v[4:7]
ds_read_b64_tr_b16 a[6:7], v243 offset:2176
ds_read_b64_tr_b16 a[8:9], v244 offset:256
ds_read_b64_tr_b16 a[10:11], v244 offset:2304
v_mfma_f32_16x16x32_bf16 v[8:11], a[72:75], a[128:131], v[8:11]
ds_read_b64_tr_b16 a[12:13], v243 offset:384
ds_read_b64_tr_b16 a[14:15], v243 offset:2432
s_add_u32 s27, s28, 8192
buffer_load_dwordx4 a[196:199], v251, s[8:11], s27 offen offset:0 sc1
v_mfma_f32_16x16x32_bf16 v[12:15], a[76:79], a[128:131], v[12:15]
ds_read_b64_tr_b16 a[16:17], v244 offset:4096
ds_read_b64_tr_b16 a[18:19], v244 offset:6144
ds_read_b64_tr_b16 a[20:21], v243 offset:4224
v_mfma_f32_16x16x32_bf16 v[0:3], a[80:83], a[132:135], v[0:3]
ds_read_b64_tr_b16 a[22:23], v243 offset:6272
ds_read_b64_tr_b16 a[24:25], v244 offset:4352
ds_read_b64_tr_b16 a[26:27], v244 offset:6400
s_add_u32 s27, s28, 16384
buffer_load_dwordx4 a[200:203], v251, s[8:11], s27 offen offset:0 sc1
v_mfma_f32_16x16x32_bf16 v[4:7], a[84:87], a[132:135], v[4:7]
ds_read_b64_tr_b16 a[28:29], v243 offset:4480
ds_read_b64_tr_b16 a[30:31], v243 offset:6528
v_mfma_f32_16x16x32_bf16 v[8:11], a[88:91], a[132:135], v[8:11]
ds_read_b64_tr_b16 a[32:33], v244 offset:8192
ds_read_b64_tr_b16 a[34:35], v244 offset:10240
ds_read_b64_tr_b16 a[36:37], v243 offset:8320
s_add_u32 s27, s28, 24576
buffer_load_dwordx4 a[204:207], v251, s[8:11], s27 offen offset:0 sc1
v_mfma_f32_16x16x32_bf16 v[12:15], a[92:95], a[132:135], v[12:15]
ds_read_b64_tr_b16 a[38:39], v243 offset:10368
ds_read_b64_tr_b16 a[40:41], v244 offset:8448
ds_read_b64_tr_b16 a[42:43], v244 offset:10496
v_mfma_f32_16x16x32_bf16 v[0:3], a[96:99], a[136:139], v[0:3]
ds_read_b64_tr_b16 a[44:45], v243 offset:8576
ds_read_b64_tr_b16 a[46:47], v243 offset:10624
s_add_u32 m0, s57, 65536
s_nop 0
buffer_load_dwordx4 v246, s[52:55], s56 offen offset:0 lds
v_mfma_f32_16x16x32_bf16 v[4:7], a[100:103], a[136:139], v[4:7]
ds_read_b64_tr_b16 a[48:49], v244 offset:12288
ds_read_b64_tr_b16 a[50:51], v244 offset:14336
ds_read_b64_tr_b16 a[52:53], v243 offset:12416
v_mfma_f32_16x16x32_bf16 v[8:11], a[104:107], a[136:139], v[8:11]
ds_read_b64_tr_b16 a[54:55], v243 offset:14464
ds_read_b64_tr_b16 a[56:57], v244 offset:12544
ds_read_b64_tr_b16 a[58:59], v244 offset:14592
buffer_load_dwordx4 v247, s[52:55], s56 offen offset:1024 lds
v_mfma_f32_16x16x32_bf16 v[12:15], a[108:111], a[136:139], v[12:15]
ds_read_b64_tr_b16 a[60:61], v243 offset:12672
ds_read_b64_tr_b16 a[62:63], v243 offset:14720
v_mfma_f32_16x16x32_bf16 v[0:3], a[112:115], a[140:143], v[0:3]
buffer_load_dwordx4 v248, s[52:55], s56 offen offset:2048 lds
v_mfma_f32_16x16x32_bf16 v[4:7], a[116:119], a[140:143], v[4:7]
v_mfma_f32_16x16x32_bf16 v[8:11], a[120:123], a[140:143], v[8:11]
buffer_load_dwordx4 v249, s[52:55], s56 offen offset:3072 lds
v_mfma_f32_16x16x32_bf16 v[12:15], a[124:127], a[140:143], v[12:15]
s_add_u32 s28, s28, 32768
s_add_u32 s56, s56, 131072
v_add_u32 v244, 0x4000, v244
v_add_u32 v243, 0x4000, v243
s_waitcnt vmcnt(16) lgkmcnt(0)
s_barrier
v_mfma_f32_16x16x32_bf16 v[0:3], a[0:3], a[144:147], v[0:3]
ds_read_b64_tr_b16 a[64:65], v244 offset:0
ds_read_b64_tr_b16 a[66:67], v244 offset:2048
ds_read_b64_tr_b16 a[68:69], v243 offset:128
buffer_load_dwordx4 a[128:131], v251, s[8:11], s28 offen offset:0 sc1
v_mfma_f32_16x16x32_bf16 v[4:7], a[4:7], a[144:147], v[4:7]
ds_read_b64_tr_b16 a[70:71], v243 offset:2176
ds_read_b64_tr_b16 a[72:73], v244 offset:256
ds_read_b64_tr_b16 a[74:75], v244 offset:2304
v_mfma_f32_16x16x32_bf16 v[8:11], a[8:11], a[144:147], v[8:11]
ds_read_b64_tr_b16 a[76:77], v243 offset:384
ds_read_b64_tr_b16 a[78:79], v243 offset:2432
s_add_u32 s27, s28, 8192
buffer_load_dwordx4 a[132:135], v251, s[8:11], s27 offen offset:0 sc1
v_mfma_f32_16x16x32_bf16 v[12:15], a[12:15], a[144:147], v[12:15]
ds_read_b64_tr_b16 a[80:81], v244 offset:4096
ds_read_b64_tr_b16 a[82:83], v244 offset:6144
ds_read_b64_tr_b16 a[84:85], v243 offset:4224
v_mfma_f32_16x16x32_bf16 v[0:3], a[16:19], a[148:151], v[0:3]
ds_read_b64_tr_b16 a[86:87], v243 offset:6272
ds_read_b64_tr_b16 a[88:89], v244 offset:4352
ds_read_b64_tr_b16 a[90:91], v244 offset:6400
s_add_u32 s27, s28, 16384
buffer_load_dwordx4 a[136:139], v251, s[8:11], s27 offen offset:0 sc1
v_mfma_f32_16x16x32_bf16 v[4:7], a[20:23], a[148:151], v[4:7]
ds_read_b64_tr_b16 a[92:93], v243 offset:4480
ds_read_b64_tr_b16 a[94:95], v243 offset:6528
v_mfma_f32_16x16x32_bf16 v[8:11], a[24:27], a[148:151], v[8:11]
ds_read_b64_tr_b16 a[96:97], v244 offset:8192
ds_read_b64_tr_b16 a[98:99], v244 offset:10240
ds_read_b64_tr_b16 a[100:101], v243 offset:8320
s_add_u32 s27, s28, 24576
buffer_load_dwordx4 a[140:143], v251, s[8:11], s27 offen offset:0 sc1
v_mfma_f32_16x16x32_bf16 v[12:15], a[28:31], a[148:151], v[12:15]
ds_read_b64_tr_b16 a[102:103], v243 offset:10368
ds_read_b64_tr_b16 a[104:105], v244 offset:8448
ds_read_b64_tr_b16 a[106:107], v244 offset:10496
v_mfma_f32_16x16x32_bf16 v[0:3], a[32:35], a[152:155], v[0:3]
ds_read_b64_tr_b16 a[108:109], v243 offset:8576
ds_read_b64_tr_b16 a[110:111], v243 offset:10624
s_add_u32 m0, s57, 81920
s_nop 0
buffer_load_dwordx4 v246, s[52:55], s56 offen offset:0 lds
v_mfma_f32_16x16x32_bf16 v[4:7], a[36:39], a[152:155], v[4:7]
ds_read_b64_tr_b16 a[112:113], v244 offset:12288
ds_read_b64_tr_b16 a[114:115], v244 offset:14336
ds_read_b64_tr_b16 a[116:117], v243 offset:12416
v_mfma_f32_16x16x32_bf16 v[8:11], a[40:43], a[152:155], v[8:11]
ds_read_b64_tr_b16 a[118:119], v243 offset:14464
ds_read_b64_tr_b16 a[120:121], v244 offset:12544
ds_read_b64_tr_b16 a[122:123], v244 offset:14592
buffer_load_dwordx4 v247, s[52:55], s56 offen offset:1024 lds
v_mfma_f32_16x16x32_bf16 v[12:15], a[44:47], a[152:155], v[12:15]
ds_read_b64_tr_b16 a[124:125], v243 offset:12672
ds_read_b64_tr_b16 a[126:127], v243 offset:14720
v_mfma_f32_16x16x32_bf16 v[0:3], a[48:51], a[156:159], v[0:3]
)ASM"
R"ASM(buffer_load_dwordx4 v248, s[52:55], s56 offen offset:2048 lds
v_mfma_f32_16x16x32_bf16 v[4:7], a[52:55], a[156:159], v[4:7]
v_mfma_f32_16x16x32_bf16 v[8:11], a[56:59], a[156:159], v[8:11]
buffer_load_dwordx4 v249, s[52:55], s56 offen offset:3072 lds
v_mfma_f32_16x16x32_bf16 v[12:15], a[60:63], a[156:159], v[12:15]
s_add_u32 s28, s28, 32768
s_add_u32 s56, s56, 131072
v_add_u32 v244, 0x4000, v244
v_add_u32 v243, 0x4000, v243
s_waitcnt vmcnt(28) lgkmcnt(0)
v_mfma_f32_16x16x32_bf16 v[0:3], a[64:67], a[160:163], v[0:3]
ds_read_b64_tr_b16 a[0:1], v244 offset:0
ds_read_b64_tr_b16 a[2:3], v244 offset:2048
ds_read_b64_tr_b16 a[4:5], v243 offset:128
buffer_load_dwordx4 a[144:147], v251, s[8:11], s28 offen offset:0 sc1
v_mfma_f32_16x16x32_bf16 v[4:7], a[68:71], a[160:163], v[4:7]
ds_read_b64_tr_b16 a[6:7], v243 offset:2176
ds_read_b64_tr_b16 a[8:9], v244 offset:256
ds_read_b64_tr_b16 a[10:11], v244 offset:2304
v_mfma_f32_16x16x32_bf16 v[8:11], a[72:75], a[160:163], v[8:11]
ds_read_b64_tr_b16 a[12:13], v243 offset:384
ds_read_b64_tr_b16 a[14:15], v243 offset:2432
s_add_u32 s27, s28, 8192
buffer_load_dwordx4 a[148:151], v251, s[8:11], s27 offen offset:0 sc1
v_mfma_f32_16x16x32_bf16 v[12:15], a[76:79], a[160:163], v[12:15]
ds_read_b64_tr_b16 a[16:17], v244 offset:4096
ds_read_b64_tr_b16 a[18:19], v244 offset:6144
ds_read_b64_tr_b16 a[20:21], v243 offset:4224
v_mfma_f32_16x16x32_bf16 v[0:3], a[80:83], a[164:167], v[0:3]
ds_read_b64_tr_b16 a[22:23], v243 offset:6272
ds_read_b64_tr_b16 a[24:25], v244 offset:4352
ds_read_b64_tr_b16 a[26:27], v244 offset:6400
s_add_u32 s27, s28, 16384
buffer_load_dwordx4 a[152:155], v251, s[8:11], s27 offen offset:0 sc1
v_mfma_f32_16x16x32_bf16 v[4:7], a[84:87], a[164:167], v[4:7]
ds_read_b64_tr_b16 a[28:29], v243 offset:4480
ds_read_b64_tr_b16 a[30:31], v243 offset:6528
v_mfma_f32_16x16x32_bf16 v[8:11], a[88:91], a[164:167], v[8:11]
ds_read_b64_tr_b16 a[32:33], v244 offset:8192
ds_read_b64_tr_b16 a[34:35], v244 offset:10240
ds_read_b64_tr_b16 a[36:37], v243 offset:8320
s_add_u32 s27, s28, 24576
buffer_load_dwordx4 a[156:159], v251, s[8:11], s27 offen offset:0 sc1
v_mfma_f32_16x16x32_bf16 v[12:15], a[92:95], a[164:167], v[12:15]
ds_read_b64_tr_b16 a[38:39], v243 offset:10368
ds_read_b64_tr_b16 a[40:41], v244 offset:8448
ds_read_b64_tr_b16 a[42:43], v244 offset:10496
v_mfma_f32_16x16x32_bf16 v[0:3], a[96:99], a[168:171], v[0:3]
ds_read_b64_tr_b16 a[44:45], v243 offset:8576
ds_read_b64_tr_b16 a[46:47], v243 offset:10624
s_add_u32 m0, s57, 0
s_nop 0
buffer_load_dwordx4 v246, s[52:55], s56 offen offset:0 lds
v_mfma_f32_16x16x32_bf16 v[4:7], a[100:103], a[168:171], v[4:7]
ds_read_b64_tr_b16 a[48:49], v244 offset:12288
ds_read_b64_tr_b16 a[50:51], v244 offset:14336
ds_read_b64_tr_b16 a[52:53], v243 offset:12416
v_mfma_f32_16x16x32_bf16 v[8:11], a[104:107], a[168:171], v[8:11]
ds_read_b64_tr_b16 a[54:55], v243 offset:14464
ds_read_b64_tr_b16 a[56:57], v244 offset:12544
ds_read_b64_tr_b16 a[58:59], v244 offset:14592
buffer_load_dwordx4 v247, s[52:55], s56 offen offset:1024 lds
v_mfma_f32_16x16x32_bf16 v[12:15], a[108:111], a[168:171], v[12:15]
ds_read_b64_tr_b16 a[60:61], v243 offset:12672
ds_read_b64_tr_b16 a[62:63], v243 offset:14720
v_mfma_f32_16x16x32_bf16 v[0:3], a[112:115], a[172:175], v[0:3]
buffer_load_dwordx4 v248, s[52:55], s56 offen offset:2048 lds
v_mfma_f32_16x16x32_bf16 v[4:7], a[116:119], a[172:175], v[4:7]
v_mfma_f32_16x16x32_bf16 v[8:11], a[120:123], a[172:175], v[8:11]
buffer_load_dwordx4 v249, s[52:55], s56 offen offset:3072 lds
v_mfma_f32_16x16x32_bf16 v[12:15], a[124:127], a[172:175], v[12:15]
s_add_u32 s28, s28, 32768
s_add_u32 s56, s56, 131072
v_add_u32 v244, 0x4000, v244
v_add_u32 v243, 0x4000, v243
s_waitcnt vmcnt(16) lgkmcnt(0)
s_barrier
v_mfma_f32_16x16x32_bf16 v[0:3], a[0:3], a[176:179], v[0:3]
ds_read_b64_tr_b16 a[64:65], v244 offset:0
ds_read_b64_tr_b16 a[66:67], v244 offset:2048
ds_read_b64_tr_b16 a[68:69], v243 offset:128
buffer_load_dwordx4 a[160:163], v251, s[8:11], s28 offen offset:0 sc1
v_mfma_f32_16x16x32_bf16 v[4:7], a[4:7], a[176:179], v[4:7]
ds_read_b64_tr_b16 a[70:71], v243 offset:2176
ds_read_b64_tr_b16 a[72:73], v244 offset:256
ds_read_b64_tr_b16 a[74:75], v244 offset:2304
v_mfma_f32_16x16x32_bf16 v[8:11], a[8:11], a[176:179], v[8:11]
ds_read_b64_tr_b16 a[76:77], v243 offset:384
ds_read_b64_tr_b16 a[78:79], v243 offset:2432
s_add_u32 s27, s28, 8192
buffer_load_dwordx4 a[164:167], v251, s[8:11], s27 offen offset:0 sc1
v_mfma_f32_16x16x32_bf16 v[12:15], a[12:15], a[176:179], v[12:15]
ds_read_b64_tr_b16 a[80:81], v244 offset:4096
ds_read_b64_tr_b16 a[82:83], v244 offset:6144
ds_read_b64_tr_b16 a[84:85], v243 offset:4224
v_mfma_f32_16x16x32_bf16 v[0:3], a[16:19], a[180:183], v[0:3]
ds_read_b64_tr_b16 a[86:87], v243 offset:6272
ds_read_b64_tr_b16 a[88:89], v244 offset:4352
ds_read_b64_tr_b16 a[90:91], v244 offset:6400
s_add_u32 s27, s28, 16384
buffer_load_dwordx4 a[168:171], v251, s[8:11], s27 offen offset:0 sc1
v_mfma_f32_16x16x32_bf16 v[4:7], a[20:23], a[180:183], v[4:7]
ds_read_b64_tr_b16 a[92:93], v243 offset:4480
ds_read_b64_tr_b16 a[94:95], v243 offset:6528
v_mfma_f32_16x16x32_bf16 v[8:11], a[24:27], a[180:183], v[8:11]
ds_read_b64_tr_b16 a[96:97], v244 offset:8192
ds_read_b64_tr_b16 a[98:99], v244 offset:10240
ds_read_b64_tr_b16 a[100:101], v243 offset:8320
s_add_u32 s27, s28, 24576
buffer_load_dwordx4 a[172:175], v251, s[8:11], s27 offen offset:0 sc1
v_mfma_f32_16x16x32_bf16 v[12:15], a[28:31], a[180:183], v[12:15]
ds_read_b64_tr_b16 a[102:103], v243 offset:10368
ds_read_b64_tr_b16 a[104:105], v244 offset:8448
ds_read_b64_tr_b16 a[106:107], v244 offset:10496
v_mfma_f32_16x16x32_bf16 v[0:3], a[32:35], a[184:187], v[0:3]
ds_read_b64_tr_b16 a[108:109], v243 offset:8576
ds_read_b64_tr_b16 a[110:111], v243 offset:10624
s_add_u32 m0, s57, 16384
s_nop 0
buffer_load_dwordx4 v246, s[52:55], s56 offen offset:0 lds
v_mfma_f32_16x16x32_bf16 v[4:7], a[36:39], a[184:187], v[4:7]
ds_read_b64_tr_b16 a[112:113], v244 offset:12288
ds_read_b64_tr_b16 a[114:115], v244 offset:14336
ds_read_b64_tr_b16 a[116:117], v243 offset:12416
v_mfma_f32_16x16x32_bf16 v[8:11], a[40:43], a[184:187], v[8:11]
ds_read_b64_tr_b16 a[118:119], v243 offset:14464
ds_read_b64_tr_b16 a[120:121], v244 offset:12544
ds_read_b64_tr_b16 a[122:123], v244 offset:14592
buffer_load_dwordx4 v247, s[52:55], s56 offen offset:1024 lds
v_mfma_f32_16x16x32_bf16 v[12:15], a[44:47], a[184:187], v[12:15]
ds_read_b64_tr_b16 a[124:125], v243 offset:12672
ds_read_b64_tr_b16 a[126:127], v243 offset:14720
v_mfma_f32_16x16x32_bf16 v[0:3], a[48:51], a[188:191], v[0:3]
buffer_load_dwordx4 v248, s[52:55], s56 offen offset:2048 lds
v_mfma_f32_16x16x32_bf16 v[4:7], a[52:55], a[188:191], v[4:7]
v_mfma_f32_16x16x32_bf16 v[8:11], a[56:59], a[188:191], v[8:11]
buffer_load_dwordx4 v249, s[52:55], s56 offen offset:3072 lds
v_mfma_f32_16x16x32_bf16 v[12:15], a[60:63], a[188:191], v[12:15]
s_add_u32 s28, s28, 32768
s_add_u32 s56, s56, 131072
v_add_u32 v244, 0x4000, v244
v_add_u32 v243, 0x4000, v243
s_waitcnt vmcnt(28) lgkmcnt(0)
v_mfma_f32_16x16x32_bf16 v[0:3], a[64:67], a[192:195], v[0:3]
ds_read_b64_tr_b16 a[0:1], v244 offset:0
ds_read_b64_tr_b16 a[2:3], v244 offset:2048
ds_read_b64_tr_b16 a[4:5], v243 offset:128
buffer_load_dwordx4 a[176:179], v251, s[8:11], s28 offen offset:0 sc1
v_mfma_f32_16x16x32_bf16 v[4:7], a[68:71], a[192:195], v[4:7]
ds_read_b64_tr_b16 a[6:7], v243 offset:2176
ds_read_b64_tr_b16 a[8:9], v244 offset:256
ds_read_b64_tr_b16 a[10:11], v244 offset:2304
v_mfma_f32_16x16x32_bf16 v[8:11], a[72:75], a[192:195], v[8:11]
ds_read_b64_tr_b16 a[12:13], v243 offset:384
ds_read_b64_tr_b16 a[14:15], v243 offset:2432
v_mfma_f32_16x16x32_bf16 v[12:15], a[76:79], a[192:195], v[12:15]
ds_read_b64_tr_b16 a[16:17], v244 offset:4096
ds_read_b64_tr_b16 a[18:19], v244 offset:6144
ds_read_b64_tr_b16 a[20:21], v243 offset:4224
v_mfma_f32_16x16x32_bf16 v[0:3], a[80:83], a[196:199], v[0:3]
ds_read_b64_tr_b16 a[22:23], v243 offset:6272
ds_read_b64_tr_b16 a[24:25], v244 offset:4352
ds_read_b64_tr_b16 a[26:27], v244 offset:6400
s_add_u32 s27, s28, 8192
buffer_load_dwordx4 a[180:183], v251, s[8:11], s27 offen offset:0 sc1
v_mfma_f32_16x16x32_bf16 v[4:7], a[84:87], a[196:199], v[4:7]
ds_read_b64_tr_b16 a[28:29], v243 offset:4480
ds_read_b64_tr_b16 a[30:31], v243 offset:6528
v_mfma_f32_16x16x32_bf16 v[8:11], a[88:91], a[196:199], v[8:11]
ds_read_b64_tr_b16 a[32:33], v244 offset:8192
ds_read_b64_tr_b16 a[34:35], v244 offset:10240
ds_read_b64_tr_b16 a[36:37], v243 offset:8320
v_mfma_f32_16x16x32_bf16 v[12:15], a[92:95], a[196:199], v[12:15]
ds_read_b64_tr_b16 a[38:39], v243 offset:10368
ds_read_b64_tr_b16 a[40:41], v244 offset:8448
ds_read_b64_tr_b16 a[42:43], v244 offset:10496
v_mfma_f32_16x16x32_bf16 v[0:3], a[96:99], a[200:203], v[0:3]
ds_read_b64_tr_b16 a[44:45], v243 offset:8576
ds_read_b64_tr_b16 a[46:47], v243 offset:10624
s_add_u32 s27, s28, 16384
buffer_load_dwordx4 a[184:187], v251, s[8:11], s27 offen offset:0 sc1
v_mfma_f32_16x16x32_bf16 v[4:7], a[100:103], a[200:203], v[4:7]
ds_read_b64_tr_b16 a[48:49], v244 offset:12288
ds_read_b64_tr_b16 a[50:51], v244 offset:14336
ds_read_b64_tr_b16 a[52:53], v243 offset:12416
v_mfma_f32_16x16x32_bf16 v[8:11], a[104:107], a[200:203], v[8:11]
ds_read_b64_tr_b16 a[54:55], v243 offset:14464
ds_read_b64_tr_b16 a[56:57], v244 offset:12544
ds_read_b64_tr_b16 a[58:59], v244 offset:14592
v_mfma_f32_16x16x32_bf16 v[12:15], a[108:111], a[200:203], v[12:15]
ds_read_b64_tr_b16 a[60:61], v243 offset:12672
ds_read_b64_tr_b16 a[62:63], v243 offset:14720
v_mfma_f32_16x16x32_bf16 v[0:3], a[112:115], a[204:207], v[0:3]
s_add_u32 s27, s28, 24576
buffer_load_dwordx4 a[188:191], v251, s[8:11], s27 offen offset:0 sc1
v_mfma_f32_16x16x32_bf16 v[4:7], a[116:119], a[204:207], v[4:7]
v_mfma_f32_16x16x32_bf16 v[8:11], a[120:123], a[204:207], v[8:11]
v_mfma_f32_16x16x32_bf16 v[12:15], a[124:127], a[204:207], v[12:15]
s_add_u32 s28, s28, 32768
v_add_u32 v244, 0x4000, v244
v_add_u32 v243, 0x4000, v243
s_waitcnt vmcnt(12) lgkmcnt(0)
s_barrier
v_mfma_f32_16x16x32_bf16 v[0:3], a[0:3], a[128:131], v[0:3]
ds_read_b64_tr_b16 a[64:65], v244 offset:0
ds_read_b64_tr_b16 a[66:67], v244 offset:2048
ds_read_b64_tr_b16 a[68:69], v243 offset:128
v_mfma_f32_16x16x32_bf16 v[4:7], a[4:7], a[128:131], v[4:7]
ds_read_b64_tr_b16 a[70:71], v243 offset:2176
ds_read_b64_tr_b16 a[72:73], v244 offset:256
ds_read_b64_tr_b16 a[74:75], v244 offset:2304
v_mfma_f32_16x16x32_bf16 v[8:11], a[8:11], a[128:131], v[8:11]
ds_read_b64_tr_b16 a[76:77], v243 offset:384
ds_read_b64_tr_b16 a[78:79], v243 offset:2432
v_mfma_f32_16x16x32_bf16 v[12:15], a[12:15], a[128:131], v[12:15]
ds_read_b64_tr_b16 a[80:81], v244 offset:4096
ds_read_b64_tr_b16 a[82:83], v244 offset:6144
ds_read_b64_tr_b16 a[84:85], v243 offset:4224
v_mfma_f32_16x16x32_bf16 v[0:3], a[16:19], a[132:135], v[0:3]
ds_read_b64_tr_b16 a[86:87], v243 offset:6272
ds_read_b64_tr_b16 a[88:89], v244 offset:4352
ds_read_b64_tr_b16 a[90:91], v244 offset:6400
v_mfma_f32_16x16x32_bf16 v[4:7], a[20:23], a[132:135], v[4:7]
ds_read_b64_tr_b16 a[92:93], v243 offset:4480
ds_read_b64_tr_b16 a[94:95], v243 offset:6528
v_mfma_f32_16x16x32_bf16 v[8:11], a[24:27], a[132:135], v[8:11]
ds_read_b64_tr_b16 a[96:97], v244 offset:8192
ds_read_b64_tr_b16 a[98:99], v244 offset:10240
ds_read_b64_tr_b16 a[100:101], v243 offset:8320
v_mfma_f32_16x16x32_bf16 v[12:15], a[28:31], a[132:135], v[12:15]
ds_read_b64_tr_b16 a[102:103], v243 offset:10368
ds_read_b64_tr_b16 a[104:105], v244 offset:8448
ds_read_b64_tr_b16 a[106:107], v244 offset:10496
v_mfma_f32_16x16x32_bf16 v[0:3], a[32:35], a[136:139], v[0:3]
ds_read_b64_tr_b16 a[108:109], v243 offset:8576
ds_read_b64_tr_b16 a[110:111], v243 offset:10624
v_mfma_f32_16x16x32_bf16 v[4:7], a[36:39], a[136:139], v[4:7]
ds_read_b64_tr_b16 a[112:113], v244 offset:12288
ds_read_b64_tr_b16 a[114:115], v244 offset:14336
ds_read_b64_tr_b16 a[116:117], v243 offset:12416
v_mfma_f32_16x16x32_bf16 v[8:11], a[40:43], a[136:139], v[8:11]
ds_read_b64_tr_b16 a[118:119], v243 offset:14464
ds_read_b64_tr_b16 a[120:121], v244 offset:12544
ds_read_b64_tr_b16 a[122:123], v244 offset:14592
v_mfma_f32_16x16x32_bf16 v[12:15], a[44:47], a[136:139], v[12:15]
ds_read_b64_tr_b16 a[124:125], v243 offset:12672
ds_read_b64_tr_b16 a[126:127], v243 offset:14720
v_mfma_f32_16x16x32_bf16 v[0:3], a[48:51], a[140:143], v[0:3]
v_mfma_f32_16x16x32_bf16 v[4:7], a[52:55], a[140:143], v[4:7]
v_mfma_f32_16x16x32_bf16 v[8:11], a[56:59], a[140:143], v[8:11]
v_mfma_f32_16x16x32_bf16 v[12:15], a[60:63], a[140:143], v[12:15]
v_add_u32 v244, 0xfffec000, v244
v_add_u32 v243, 0xfffec000, v243
s_waitcnt vmcnt(16) lgkmcnt(0)
v_mfma_f32_16x16x32_bf16 v[0:3], a[64:67], a[144:147], v[0:3]
ds_read_b64_tr_b16 a[0:1], v244 offset:0
ds_read_b64_tr_b16 a[2:3], v244 offset:2048
ds_read_b64_tr_b16 a[4:5], v243 offset:128
v_mfma_f32_16x16x32_bf16 v[4:7], a[68:71], a[144:147], v[4:7]
ds_read_b64_tr_b16 a[6:7], v243 offset:2176
ds_read_b64_tr_b16 a[8:9], v244 offset:256
ds_read_b64_tr_b16 a[10:11], v244 offset:2304
v_mfma_f32_16x16x32_bf16 v[8:11], a[72:75], a[144:147], v[8:11]
ds_read_b64_tr_b16 a[12:13], v243 offset:384
ds_read_b64_tr_b16 a[14:15], v243 offset:2432
v_mfma_f32_16x16x32_bf16 v[12:15], a[76:79], a[144:147], v[12:15]
ds_read_b64_tr_b16 a[16:17], v244 offset:4096
ds_read_b64_tr_b16 a[18:19], v244 offset:6144
ds_read_b64_tr_b16 a[20:21], v243 offset:4224
v_mfma_f32_16x16x32_bf16 v[0:3], a[80:83], a[148:151], v[0:3]
ds_read_b64_tr_b16 a[22:23], v243 offset:6272
ds_read_b64_tr_b16 a[24:25], v244 offset:4352
ds_read_b64_tr_b16 a[26:27], v244 offset:6400
v_mfma_f32_16x16x32_bf16 v[4:7], a[84:87], a[148:151], v[4:7]
ds_read_b64_tr_b16 a[28:29], v243 offset:4480
ds_read_b64_tr_b16 a[30:31], v243 offset:6528
v_mfma_f32_16x16x32_bf16 v[8:11], a[88:91], a[148:151], v[8:11]
ds_read_b64_tr_b16 a[32:33], v244 offset:8192
ds_read_b64_tr_b16 a[34:35], v244 offset:10240
ds_read_b64_tr_b16 a[36:37], v243 offset:8320
v_mfma_f32_16x16x32_bf16 v[12:15], a[92:95], a[148:151], v[12:15]
ds_read_b64_tr_b16 a[38:39], v243 offset:10368
ds_read_b64_tr_b16 a[40:41], v244 offset:8448
ds_read_b64_tr_b16 a[42:43], v244 offset:10496
v_mfma_f32_16x16x32_bf16 v[0:3], a[96:99], a[152:155], v[0:3]
ds_read_b64_tr_b16 a[44:45], v243 offset:8576
ds_read_b64_tr_b16 a[46:47], v243 offset:10624
v_mfma_f32_16x16x32_bf16 v[4:7], a[100:103], a[152:155], v[4:7]
ds_read_b64_tr_b16 a[48:49], v244 offset:12288
ds_read_b64_tr_b16 a[50:51], v244 offset:14336
ds_read_b64_tr_b16 a[52:53], v243 offset:12416
v_mfma_f32_16x16x32_bf16 v[8:11], a[104:107], a[152:155], v[8:11]
ds_read_b64_tr_b16 a[54:55], v243 offset:14464
ds_read_b64_tr_b16 a[56:57], v244 offset:12544
ds_read_b64_tr_b16 a[58:59], v244 offset:14592
v_mfma_f32_16x16x32_bf16 v[12:15], a[108:111], a[152:155], v[12:15]
ds_read_b64_tr_b16 a[60:61], v243 offset:12672
ds_read_b64_tr_b16 a[62:63], v243 offset:14720
v_mfma_f32_16x16x32_bf16 v[0:3], a[112:115], a[156:159], v[0:3]
v_mfma_f32_16x16x32_bf16 v[4:7], a[116:119], a[156:159], v[4:7]
v_mfma_f32_16x16x32_bf16 v[8:11], a[120:123], a[156:159], v[8:11]
v_mfma_f32_16x16x32_bf16 v[12:15], a[124:127], a[156:159], v[12:15]
v_add_u32 v244, 0x4000, v244
v_add_u32 v243, 0x4000, v243
s_waitcnt vmcnt(4) lgkmcnt(0)
s_barrier
v_mfma_f32_16x16x32_bf16 v[0:3], a[0:3], a[160:163], v[0:3]
ds_read_b64_tr_b16 a[64:65], v244 offset:0
ds_read_b64_tr_b16 a[66:67], v244 offset:2048
ds_read_b64_tr_b16 a[68:69], v243 offset:128
v_mfma_f32_16x16x32_bf16 v[4:7], a[4:7], a[160:163], v[4:7]
ds_read_b64_tr_b16 a[70:71], v243 offset:2176
ds_read_b64_tr_b16 a[72:73], v244 offset:256
ds_read_b64_tr_b16 a[74:75], v244 offset:2304
v_mfma_f32_16x16x32_bf16 v[8:11], a[8:11], a[160:163], v[8:11]
ds_read_b64_tr_b16 a[76:77], v243 offset:384
ds_read_b64_tr_b16 a[78:79], v243 offset:2432
v_mfma_f32_16x16x32_bf16 v[12:15], a[12:15], a[160:163], v[12:15]
ds_read_b64_tr_b16 a[80:81], v244 offset:4096
ds_read_b64_tr_b16 a[82:83], v244 offset:6144
ds_read_b64_tr_b16 a[84:85], v243 offset:4224
v_mfma_f32_16x16x32_bf16 v[0:3], a[16:19], a[164:167], v[0:3]
ds_read_b64_tr_b16 a[86:87], v243 offset:6272
ds_read_b64_tr_b16 a[88:89], v244 offset:4352
ds_read_b64_tr_b16 a[90:91], v244 offset:6400
v_mfma_f32_16x16x32_bf16 v[4:7], a[20:23], a[164:167], v[4:7]
ds_read_b64_tr_b16 a[92:93], v243 offset:4480
ds_read_b64_tr_b16 a[94:95], v243 offset:6528
v_mfma_f32_16x16x32_bf16 v[8:11], a[24:27], a[164:167], v[8:11]
ds_read_b64_tr_b16 a[96:97], v244 offset:8192
ds_read_b64_tr_b16 a[98:99], v244 offset:10240
ds_read_b64_tr_b16 a[100:101], v243 offset:8320
v_mfma_f32_16x16x32_bf16 v[12:15], a[28:31], a[164:167], v[12:15]
ds_read_b64_tr_b16 a[102:103], v243 offset:10368
ds_read_b64_tr_b16 a[104:105], v244 offset:8448
ds_read_b64_tr_b16 a[106:107], v244 offset:10496
v_mfma_f32_16x16x32_bf16 v[0:3], a[32:35], a[168:171], v[0:3]
ds_read_b64_tr_b16 a[108:109], v243 offset:8576
ds_read_b64_tr_b16 a[110:111], v243 offset:10624
v_mfma_f32_16x16x32_bf16 v[4:7], a[36:39], a[168:171], v[4:7]
ds_read_b64_tr_b16 a[112:113], v244 offset:12288
ds_read_b64_tr_b16 a[114:115], v244 offset:14336
ds_read_b64_tr_b16 a[116:117], v243 offset:12416
v_mfma_f32_16x16x32_bf16 v[8:11], a[40:43], a[168:171], v[8:11]
ds_read_b64_tr_b16 a[118:119], v243 offset:14464
ds_read_b64_tr_b16 a[120:121], v244 offset:12544
ds_read_b64_tr_b16 a[122:123], v244 offset:14592
v_mfma_f32_16x16x32_bf16 v[12:15], a[44:47], a[168:171], v[12:15]
ds_read_b64_tr_b16 a[124:125], v243 offset:12672
ds_read_b64_tr_b16 a[126:127], v243 offset:14720
v_mfma_f32_16x16x32_bf16 v[0:3], a[48:51], a[172:175], v[0:3]
v_mfma_f32_16x16x32_bf16 v[4:7], a[52:55], a[172:175], v[4:7]
v_mfma_f32_16x16x32_bf16 v[8:11], a[56:59], a[172:175], v[8:11]
v_mfma_f32_16x16x32_bf16 v[12:15], a[60:63], a[172:175], v[12:15]
s_waitcnt vmcnt(0) lgkmcnt(0)
v_mfma_f32_16x16x32_bf16 v[0:3], a[64:67], a[176:179], v[0:3]
v_mfma_f32_16x16x32_bf16 v[4:7], a[68:71], a[176:179], v[4:7]
v_mfma_f32_16x16x32_bf16 v[8:11], a[72:75], a[176:179], v[8:11]
v_mfma_f32_16x16x32_bf16 v[12:15], a[76:79], a[176:179], v[12:15]
v_mfma_f32_16x16x32_bf16 v[0:3], a[80:83], a[180:183], v[0:3]
v_mfma_f32_16x16x32_bf16 v[4:7], a[84:87], a[180:183], v[4:7]
v_mfma_f32_16x16x32_bf16 v[8:11], a[88:91], a[180:183], v[8:11]
v_mfma_f32_16x16x32_bf16 v[12:15], a[92:95], a[180:183], v[12:15]
v_mfma_f32_16x16x32_bf16 v[0:3], a[96:99], a[184:187], v[0:3]
v_mfma_f32_16x16x32_bf16 v[4:7], a[100:103], a[184:187], v[4:7]
v_mfma_f32_16x16x32_bf16 v[8:11], a[104:107], a[184:187], v[8:11]
v_mfma_f32_16x16x32_bf16 v[12:15], a[108:111], a[184:187], v[12:15]
v_mfma_f32_16x16x32_bf16 v[0:3], a[112:115], a[188:191], v[0:3]
v_mfma_f32_16x16x32_bf16 v[4:7], a[116:119], a[188:191], v[4:7]
v_mfma_f32_16x16x32_bf16 v[8:11], a[120:123], a[188:191], v[8:11]
v_mfma_f32_16x16x32_bf16 v[12:15], a[124:127], a[188:191], v[12:15]
s_lshr_b32 s43, s41, 1
s_lshl_b32 s43, s43, 4
s_lshl_b32 s44, s42, 1
s_add_u32 s43, s43, s44
s_lshl_b32 s43, s43, 13
s_and_b32 s44, s41, 1
s_lshl_b32 s44, s44, 12
s_add_u32 s43, s43, s44
s_lshl_b32 s44, s7, 10
s_add_u32 s43, s43, s44
s_add_u32 s44, s43, 8192
buffer_load_dwordx4 v[32:35], v251, s[16:19], s43 offen sc1
buffer_load_dwordx4 v[36:39], v251, s[16:19], s44 offen sc1
v_and_b32 v40, 15, v250
v_lshlrev_b32 v40, 10, v40
v_bfe_u32 v41, v250, 4, 1
v_lshl_add_u32 v40, v41, 5, v40
v_lshrrev_b32 v41, 5, v250
v_lshl_add_u32 v40, v41, 4, v40
s_lshl_b32 s43, s41, 16
s_lshl_b32 s44, s7, 14
s_add_u32 s43, s43, s44
s_lshl_b32 s44, s42, 7
s_add_u32 s43, s43, s44
s_waitcnt vmcnt(0)
s_nop 4
v_lshlrev_b32 v42, 16, v32
v_and_b32 v43, 0xffff0000, v32
v_add_f32 v0, v0, v42
v_add_f32 v1, v1, v43
v_lshlrev_b32 v42, 16, v33
v_and_b32 v43, 0xffff0000, v33
v_add_f32 v2, v2, v42
v_add_f32 v3, v3, v43
v_lshlrev_b32 v42, 16, v34
v_and_b32 v43, 0xffff0000, v34
v_add_f32 v4, v4, v42
v_add_f32 v5, v5, v43
v_lshlrev_b32 v42, 16, v35
v_and_b32 v43, 0xffff0000, v35
v_add_f32 v6, v6, v42
v_add_f32 v7, v7, v43
v_cvt_pk_bf16_f32 v44, v0, v1
v_cvt_pk_bf16_f32 v45, v2, v3
v_cvt_pk_bf16_f32 v46, v4, v5
v_cvt_pk_bf16_f32 v47, v6, v7
s_nop 1
v_permlane16_swap_b32 v44, v46
v_permlane16_swap_b32 v45, v47
buffer_store_dwordx4 v[44:47], v40, s[20:23], s43 offen offset:0
v_lshlrev_b32 v42, 16, v36
v_and_b32 v43, 0xffff0000, v36
v_add_f32 v8, v8, v42
v_add_f32 v9, v9, v43
v_lshlrev_b32 v42, 16, v37
v_and_b32 v43, 0xffff0000, v37
v_add_f32 v10, v10, v42
v_add_f32 v11, v11, v43
v_lshlrev_b32 v42, 16, v38
v_and_b32 v43, 0xffff0000, v38
v_add_f32 v12, v12, v42
v_add_f32 v13, v13, v43
v_lshlrev_b32 v42, 16, v39
v_and_b32 v43, 0xffff0000, v39
v_add_f32 v14, v14, v42
v_add_f32 v15, v15, v43
v_cvt_pk_bf16_f32 v48, v8, v9
v_cvt_pk_bf16_f32 v49, v10, v11
v_cvt_pk_bf16_f32 v50, v12, v13
v_cvt_pk_bf16_f32 v51, v14, v15
s_nop 1
v_permlane16_swap_b32 v48, v50
v_permlane16_swap_b32 v49, v51
buffer_store_dwordx4 v[48:51], v40, s[20:23], s43 offen offset:64
s_waitcnt vmcnt(0)
KEND:
)ASM"
    :: "s"(__builtin_amdgcn_kernarg_segment_ptr()), "s"(blockIdx.x), "s"(__builtin_amdgcn_readfirstlane(threadIdx.x>>6)), "s"(ldsb)
    : "memory", "vcc", "scc", "v0", "v1", "v2", "v3", "v4", "v5", "v6", "v7", "v8", "v9", "v10", "v11", "v12", "v13", "v14", "v15", "v16", "v17", "v18", "v19", "v20", "v21", "v22", "v23", "v24", "v25", "v26", "v27", "v28", "v29", "v30", "v31", "v32", "v33", "v34", "v35", "v36", "v37", "v38", "v39", "v40", "v41", "v42", "v43", "v44", "v45", "v46", "v47", "v48", "v49", "v50", "v51", "v52", "v53", "v54", "v55", "v56", "v57", "v58", "v59", "v60", "v61", "v62", "v63", "v64", "v65", "v66", "v67", "v68", "v69", "v70", "v71", "v72", "v73", "v74", "v75", "v76", "v77", "v78", "v79", "v80", "v81", "v82", "v83", "v84", "v85", "v86", "v87", "v88", "v89", "v90", "v91", "v92", "v93", "v94", "v95", "v96", "v97", "v98", "v99", "v100", "v101", "v102", "v103", "v104", "v105", "v106", "v107", "v108", "v109", "v110", "v111", "v112", "v113", "v114", "v115", "v116", "v117", "v118", "v119", "v120", "v121", "v122", "v123", "v124", "v125", "v126", "v127", "v128", "v129", "v130", "v131", "v132", "v133", "v134", "v135", "v136", "v137", "v138", "v139", "v140", "v141", "v142", "v143", "v144", "v145", "v146", "v147", "v148", "v149", "v150", "v151", "v152", "v153", "v154", "v155", "v156", "v157", "v158", "v159", "v160", "v161", "v162", "v163", "v164", "v165", "v166", "v167", "v168", "v169", "v170", "v171", "v172", "v173", "v174", "v175", "v176", "v177", "v178", "v179", "v180", "v181", "v182", "v183", "v184", "v185", "v186", "v187", "v188", "v189", "v190", "v191", "v192", "v193", "v194", "v195", "v196", "v197", "v198", "v199", "v200", "v201", "v202", "v203", "v204", "v205", "v206", "v207", "v208", "v209", "v210", "v211", "v212", "v213", "v214", "v215", "v216", "v217", "v218", "v219", "v220", "v221", "v222", "v223", "v224", "v225", "v226", "v227", "v228", "v229", "v230", "v231", "v232", "v233", "v234", "v235", "v236", "v237", "v238", "v239", "v240", "v241", "v242", "v243", "v244", "v245", "v246", "v247", "v248", "v249", "v250", "v251", "v252", "v253", "v254", "v255", "a0", "a1", "a2", "a3", "a4", "a5", "a6", "a7", "a8", "a9", "a10", "a11", "a12", "a13", "a14", "a15", "a16", "a17", "a18", "a19", "a20", "a21", "a22", "a23", "a24", "a25", "a26", "a27", "a28", "a29", "a30", "a31", "a32", "a33", "a34", "a35", "a36", "a37", "a38", "a39", "a40", "a41", "a42", "a43", "a44", "a45", "a46", "a47", "a48", "a49", "a50", "a51", "a52", "a53", "a54", "a55", "a56", "a57", "a58", "a59", "a60", "a61", "a62", "a63", "a64", "a65", "a66", "a67", "a68", "a69", "a70", "a71", "a72", "a73", "a74", "a75", "a76", "a77", "a78", "a79", "a80", "a81", "a82", "a83", "a84", "a85", "a86", "a87", "a88", "a89", "a90", "a91", "a92", "a93", "a94", "a95", "a96", "a97", "a98", "a99", "a100", "a101", "a102", "a103", "a104", "a105", "a106", "a107", "a108", "a109", "a110", "a111", "a112", "a113", "a114", "a115", "a116", "a117", "a118", "a119", "a120", "a121", "a122", "a123", "a124", "a125", "a126", "a127", "a128", "a129", "a130", "a131", "a132", "a133", "a134", "a135", "a136", "a137", "a138", "a139", "a140", "a141", "a142", "a143", "a144", "a145", "a146", "a147", "a148", "a149", "a150", "a151", "a152", "a153", "a154", "a155", "a156", "a157", "a158", "a159", "a160", "a161", "a162", "a163", "a164", "a165", "a166", "a167", "a168", "a169", "a170", "a171", "a172", "a173", "a174", "a175", "a176", "a177", "a178", "a179", "a180", "a181", "a182", "a183", "a184", "a185", "a186", "a187", "a188", "a189", "a190", "a191", "a192", "a193", "a194", "a195", "a196", "a197", "a198", "a199", "a200", "a201", "a202", "a203", "a204", "a205", "a206", "a207", "a208", "a209", "a210", "a211", "a212", "a213", "a214", "a215", "a216", "a217", "a218", "a219", "a220", "a221", "a222", "a223", "a224", "a225", "a226", "a227", "a228", "a229", "a230", "a231", "a232", "a233", "a234", "a235", "a236", "a237", "a238", "a239", "a240", "a241", "a242", "a243", "a244", "a245", "a246", "a247", "a248", "a249", "a250", "a251", "a252", "a253", "a254", "a255", "s8", "s9", "s10", "s11", "s12", "s13", "s14", "s15", "s16", "s17", "s18", "s19", "s20", "s21", "s22", "s23", "s24", "s25", "s26", "s27", "s28", "s29", "s30", "s31", "s32", "s33", "s34", "s35", "s36", "s37", "s38", "s39", "s40", "s41", "s42", "s43", "s44", "s45", "s46", "s47", "s48", "s49", "s50", "s51", "s52", "s53", "s54", "s55", "s56", "s57", "s58", "s59", "s60", "s61", "s62", "s63", "s64", "s65", "s66", "s67", "s68", "s69", "s70", "s71", "s72", "s73", "s74", "s75", "s76", "s77", "s78", "s79", "s80", "s81", "s82", "s83", "s84", "s85", "s86", "s87", "s88", "s89", "s90", "s91", "s92", "s93", "s94", "s95", "s96", "s97", "s98", "s99");
}

extern "C" void kernel_launch(void* const* d_in, const int* in_sizes, int n_in, void* d_out, int out_size, void* d_ws, size_t ws_size, hipStream_t stream){
  fused_k<<<256,256,0,stream>>>(d_in[0],d_in[1],d_in[2],d_in[3],d_in[4],d_in[5],d_in[7],d_in[8],d_out,d_ws);
}
```
